```python
import torch

def _softmax(x, axis=-1):
    return torch.softmax(x, dim=axis)
def _gelu(x):
    return torch.nn.functional.gelu(x, approximate="tanh")
def _silu(x):
    return torch.nn.functional.silu(x)
def _sigmoid(x):
    return torch.sigmoid(x)
def _layernorm(x, axis=-1, eps=1e-5):
    m = x.mean(dim=axis, keepdim=True)
    v = x.var(dim=axis, keepdim=True, unbiased=False)
    return (x - m) / torch.sqrt(v + eps)
def _scatter_add(tgt, idx, src, axis=0):
    return tgt.clone().index_add_(axis, idx, src)
def _segment_sum(src, seg, n):
    out = torch.zeros((n,) + src.shape[1:], dtype=src.dtype)
    return out.index_add_(0, seg, src)
def _pad(x, pw):
    flat = tuple(v for pair in reversed(pw) for v in pair)
    return torch.nn.functional.pad(x, flat)

kernel_name = 'concat_heads__ssm_conv_h3__ssm_conv_s5__prompt'

def setup_inputs():

    _w = (lambda a: (lambda t: t.to(torch.bfloat16) if t.is_floating_point() else t)(torch.as_tensor(a)))
    def _A_setup():
        B, S, D = 1, 2048, 1024
        ks = 4
        return {
            "q": _w((lambda *s: torch.randn(*s, dtype=torch.bfloat16) * 0.05)(B, S, D)),
            "k": _w((lambda *s: torch.randn(*s, dtype=torch.bfloat16) * 0.2)(B, S, D)),
            "v": _w((lambda *s: torch.randn(*s, dtype=torch.bfloat16) * 0.2)(B, S, D)),
            "shift_k": _w((lambda *s: torch.randn(*s, dtype=torch.bfloat16) * 0.2)(D, ks) * 0.5),
            "ssm_k": _w((lambda *s: torch.randn(*s, dtype=torch.bfloat16) * 0.8)(D, S) * (1.0 / S ** 0.5)),
            "ssm_D": _w((lambda *s: torch.randn(*s, dtype=torch.bfloat16) * 0.2)(D)),
        }
    _A_in = _A_setup()

    def _B_setup():
        B, S, D = 1, 2048, 1024
        P = D
        Lambda_re = -0.5 * (lambda _x: torch.exp(torch.as_tensor(_x)))((lambda *s: torch.randn(*s, dtype=torch.bfloat16) * 0.2)(P))
        Lambda_im = (lambda *s: torch.randn(*s, dtype=torch.bfloat16) * 0.4)(P) * 1.0
        B_re = (lambda *s: torch.randn(*s, dtype=torch.bfloat16) * 0.2)(P, D) * (D ** -0.5)
        B_im = (lambda *s: torch.randn(*s, dtype=torch.bfloat16) * 0.2)(P, D) * (D ** -0.5)
        C_re = (lambda *s: torch.randn(*s, dtype=torch.bfloat16) * 0.2)(D, P) * (P ** -0.5)
        C_im = (lambda *s: torch.randn(*s, dtype=torch.bfloat16) * 0.2)(D, P) * (P ** -0.5)
        log_dt = (lambda *s: torch.randn(*s, dtype=torch.bfloat16) * 0.2)(P) * 0.3 - 2.0
        Dskip = (lambda *s: torch.randn(*s, dtype=torch.bfloat16) * 0.05)(D)
        return {
            "x": _w((lambda *s: torch.randn(*s, dtype=torch.bfloat16) * 0.05)(B, S, D)),
            "Lambda_re": _w(Lambda_re),
            "Lambda_im": _w(Lambda_im),
            "B_re": _w(B_re),
            "B_im": _w(B_im),
            "C_re": _w(C_re),
            "C_im": _w(C_im),
            "log_dt": _w(log_dt),
            "Dskip": _w(Dskip),
        }
    _B_in = _B_setup()

    return {
        "A_q": _A_in["q"],
        "A_k": _A_in["k"],
        "A_v": _A_in["v"],
        "A_shift_k": _A_in["shift_k"],
        "A_ssm_k": _A_in["ssm_k"],
        "A_ssm_D": _A_in["ssm_D"],
        "B_x": _B_in["x"],
        "B_Lambda_re": _B_in["Lambda_re"],
        "B_Lambda_im": _B_in["Lambda_im"],
        "B_B_re": _B_in["B_re"],
        "B_B_im": _B_in["B_im"],
        "B_C_re": _B_in["C_re"],
        "B_C_im": _B_in["C_im"],
        "B_log_dt": _B_in["log_dt"],
        "B_Dskip": _B_in["Dskip"],
    }

def reference(A_q, A_k, A_v, A_shift_k, A_ssm_k, A_ssm_D, B_x, B_Lambda_re, B_Lambda_im, B_B_re, B_B_im, B_C_re, B_C_im, B_log_dt, B_Dskip):
    def _A_ref(q, k, v, shift_k, ssm_k, ssm_D):
        B, S, D = q.shape
        ks = shift_k.shape[-1]
        k_pad = _pad(k, ((0, 0), (ks - 1, 0), (0, 0)))
        k_shift = torch.zeros_like(k)
        for t in range(ks):
            k_shift = k_shift + k_pad[:, t:t + S, :] * shift_k[:, ks - 1 - t][None, None, :]
        kv = k_shift * v
        L = 2 * S
        kv_pad = _pad(kv, ((0, 0), (0, S), (0, 0)))
        kf_pad = _pad(ssm_k, ((0, 0), (0, S)))
        kv_f = (lambda x, axes=(-2,-1): torch.fft.rfft2(x.float(), dim=axes))(kv_pad[:, None, :, :], axes=(1, 2))
        kf_f = (lambda x, axes=(-2,-1): torch.fft.rfft2(x.float(), dim=axes))(kf_pad[:, None, :], axes=(1, 2))
        kf_f = (lambda x, *d: x.permute(*d))(kf_f, 1, 2, 0)
        y_f = kv_f * kf_f[None, :, :, :]
        y = (lambda x, s, axes=(-2,-1): torch.fft.irfft2(x, s=s, dim=axes).to(torch.bfloat16))(y_f, (1, L), axes=(1, 2))[:, 0, :S, :]
        y = y + kv * ssm_D[None, None, :]
        return q * y

    def _B_ref(x, Lambda_re, Lambda_im, B_re, B_im, C_re, C_im, log_dt, Dskip):
        Bsz, L, H = x.shape
        Lam = Lambda_re + 1j * Lambda_im
        Bmat = B_re + 1j * B_im
        Cmat = C_re + 1j * C_im
        dt = torch.exp(log_dt)
        Lam_bar = torch.exp(dt * Lam)
        B_bar = ((Lam_bar - 1.0) / Lam)[:, None] * Bmat
        Bu = torch.einsum("bld,pd->blp", x + 0j, B_bar)
        h = torch.zeros_like(Bu[:, 0, :])
        hs = []
        for t in range(L):
            h = Lam_bar[None, :] * h + Bu[:, t, :]
            hs.append(h)
        hseq = torch.stack(hs, 1)
        y = 2.0 * torch.einsum("blp,dp->bld", hseq, Cmat).real + x * Dskip[None, None, :]
        return y

    a = _A_ref(A_q, A_k, A_v, A_shift_k, A_ssm_k, A_ssm_D)
    b = _B_ref(B_x, B_Lambda_re, B_Lambda_im, B_B_re, B_B_im, B_C_re, B_C_im, B_log_dt, B_Dskip)
    return torch.concatenate([a, b], axis=-1)
```

```cpp
#include <hip/hip_runtime.h>
#include <stdint.h>
#include <type_traits>

#if defined(__HIP_DEVICE_COMPILE__)
#pragma clang attribute push(__attribute__((target("no-packed-fp32-ops"))), apply_to = function)
#endif

namespace fk {

typedef __attribute__((ext_vector_type(8))) short bf16x8;
typedef __attribute__((ext_vector_type(4))) short bf16x4;
typedef __attribute__((ext_vector_type(4))) float f32x4;
typedef __attribute__((ext_vector_type(4))) unsigned u32x4;
typedef __attribute__((ext_vector_type(2))) unsigned u32x2;
typedef __attribute__((ext_vector_type(2))) __bf16 bf16x2_t;
typedef __attribute__((address_space(1))) unsigned gu32;
typedef __attribute__((address_space(1))) unsigned long long gu64;
#define RLX_AGENT __ATOMIC_RELAXED, __HIP_MEMORY_SCOPE_AGENT

__device__ __forceinline__ float u2f(unsigned v) { return __builtin_bit_cast(float, v); }
__device__ __forceinline__ unsigned f2u(float v) { return __builtin_bit_cast(unsigned, v); }
__device__ __forceinline__ bool wall(bool p) { return __builtin_amdgcn_ballot_w64(p) == ~0ull; }
__device__ __forceinline__ float bflo(unsigned w) { return u2f(w << 16); }
__device__ __forceinline__ float bfhi(unsigned w) { return u2f(w & 0xffff0000u); }
__device__ __forceinline__ unsigned pack2(float lo, float hi) {
    bf16x2_t v; v[0] = (__bf16)lo; v[1] = (__bf16)hi; return __builtin_bit_cast(unsigned, v);
}
__device__ __forceinline__ float rbf(float f) { float r; asm("v_cvt_pk_bf16_f32 %0, 0, %1" : "=v"(r) : "v"(f)); return r; }
__device__ __forceinline__ float fma_(float a, float b, float c) { return __builtin_fmaf(a, b, c); }

template <int I, int N, class F>
__device__ __forceinline__ void static_for(F&& f) {
    if constexpr (I + 1 == N) f(std::integral_constant<int, I>{});
    else if constexpr (I < N) { static_for<I, I + (N - I) / 2>(f); static_for<I + (N - I) / 2, N>(f); }
}
#define IC(v) std::integral_constant<int, (v)>{}
#define CV(c) decltype(c)::value

#define TS_ARG
#define TS_PASS(off)
#define TS_DECL
#define TS(i)
#define TSV(i, v)
#define TL_DECL
#define TL(i)
#define TS_FLUSH(slot)

template <int N> __device__ __forceinline__ void wait_v() { asm volatile("s_waitcnt vmcnt(%0)" ::"n"(N > 63 ? 63 : N) : "memory"); }
template <int N> __device__ __forceinline__ void wait_l() { asm volatile("s_waitcnt lgkmcnt(%0)" ::"n"(N > 14 ? 14 : N) : "memory"); }
template <int N> __device__ __forceinline__ void wait_lr() { __builtin_amdgcn_s_waitcnt(0xc07f | ((N > 14 ? 14 : N) << 8)); }
template <class T> __device__ __forceinline__ void tie(T& x) { asm volatile("" : "+v"(x)); }
template <int OFF, class T> __device__ __forceinline__ void dsr128(T& d, unsigned a) { asm volatile("ds_read_b128 %0, %1 offset:%2" : "=v"(d) : "v"(a), "n"(OFF)); }
template <int OFF, class T> __device__ __forceinline__ void dsr64(T& d, unsigned a) { asm volatile("ds_read_b64 %0, %1 offset:%2" : "=v"(d) : "v"(a), "n"(OFF)); }
template <int OFF> __device__ __forceinline__ void dsr32(unsigned& d, unsigned a) { asm volatile("ds_read_b32 %0, %1 offset:%2" : "=v"(d) : "v"(a), "n"(OFF)); }
template <int O0, int O1, class T> __device__ __forceinline__ void dsr2(T& d, unsigned a) { asm volatile("ds_read2_b32 %0, %1 offset0:%2 offset1:%3" : "=v"(d) : "v"(a), "n"(O0), "n"(O1)); }
template <int OFF> __device__ __forceinline__ void dsr64tr(bf16x4& d, unsigned a) { asm volatile("ds_read_b64_tr_b16 %0, %1 offset:%2" : "=v"(d) : "v"(a), "n"(OFF)); }
template <int OFF, class T> __device__ __forceinline__ void dsw128(unsigned a, const T& v) { asm volatile("ds_write_b128 %0, %1 offset:%2" :: "v"(a), "v"(v), "n"(OFF) : "memory"); }
__device__ __forceinline__ bf16x8 cat(const bf16x4& lo, const bf16x4& hi) { return __builtin_shufflevector(lo, hi, 0, 1, 2, 3, 4, 5, 6, 7); }
__device__ __forceinline__ void barrier() { asm volatile("" ::: "memory"); __builtin_amdgcn_s_barrier(); asm volatile("" ::: "memory"); }
__device__ __forceinline__ int fresh(int v) { asm volatile("" : "+s"(v)); return v; }
__device__ __forceinline__ void sched() { __builtin_amdgcn_sched_barrier(0); }
template <bool FIRST> __device__ __forceinline__ void mfma(f32x4& c, const bf16x8& a, const bf16x8& b) {
    if constexpr (FIRST) asm volatile("v_mfma_f32_16x16x32_bf16 %0, %1, %2, 0" : "=v"(c) : "v"(a), "v"(b));
    else asm volatile("v_mfma_f32_16x16x32_bf16 %0, %1, %2, %0" : "+v"(c) : "v"(a), "v"(b));
}
struct cf32 { float r, i; };
__device__ __forceinline__ cf32 cmul(cf32 a, cf32 b) { return cf32{fma_(a.r, b.r, -(a.i * b.i)), fma_(a.r, b.i, a.i * b.r)}; }
__device__ __forceinline__ cf32 cfma(cf32 a, cf32 b, cf32 c) { return cf32{fma_(a.r, b.r, fma_(-a.i, b.i, c.r)), fma_(a.r, b.i, fma_(a.i, b.r, c.i))}; }
__device__ __forceinline__ void sw16(float v, float& e, float& o) { const auto r = __builtin_amdgcn_permlane16_swap(f2u(v), f2u(v), false, false); e = u2f(r[0]); o = u2f(r[1]); }
__device__ __forceinline__ void sw32(float v, float& e, float& o) { const auto r = __builtin_amdgcn_permlane32_swap(f2u(v), f2u(v), false, false); e = u2f(r[0]); o = u2f(r[1]); }
constexpr unsigned SPIN_CAP = 1u << 16;
constexpr unsigned OOB = 0x40000000u;

constexpr size_t WS_H = 0;
constexpr size_t WS_KVT = (size_t)8 << 20;
constexpr size_t WS_ZT = (size_t)12 << 20;
constexpr size_t WS_PP = (size_t)16 << 20;
constexpr size_t WS_E = (size_t)24 << 20;
constexpr size_t WS_F = WS_E + (256 << 10);
constexpr int F_H = 0;
constexpr int F_Z = 8192;
constexpr int F_K = 16384;
constexpr int F_P = 24576;
constexpr int F_EP = F_P + 32768;
constexpr int LDS_TOTAL = 163840, BUF = 32768;
constexpr int KC = 98304, KCH = 8320, KCP = 4160;
constexpr int U0 = KC + 4 * KCH;
constexpr int CT = U0 + 18432;
constexpr int FULL = CT + 8192, FST = 388;
constexpr int HGO = FULL + 1792, DSK = HGO + 256, POLL = DSK + 256;
static_assert(POLL + 8 * 256 <= LDS_TOTAL && 4 * FST <= 1792, "lds");
constexpr int g1_buf(int s) { return ((0x0213 >> ((s & 3) * 4)) & 3) * BUF; }
constexpr int IMG = KC, QST = KC + 16384, ZST = U0, PLD = 2 * BUF;
#define CODE_KB 40
#define EXACT 1
#define HP 1
#define CP 2
#define FLQ 11
#define TOUCH 0
#define R01 1
#define ROTX 0
#define FINE 1
#define G2P 0
#define OSC 16

__device__ __forceinline__ u32x4 mk_rsrc(const void* p, unsigned bytes) {
    const unsigned long long a = (unsigned long long)p;
    return u32x4{(unsigned)a, (unsigned)(a >> 32) & 0xffffu, bytes, 0x00020000u};
}
__device__ __forceinline__ __amdgpu_buffer_rsrc_t mk_brs(const void* p, unsigned bytes) { return __builtin_amdgcn_make_buffer_rsrc((void*)p, (short)0, bytes, 0x00020000); }
template <int IMM, bool SC1> __device__ __forceinline__ void dma(unsigned lds, unsigned voff, const u32x4& rs, unsigned soff) {
    if constexpr (SC1) asm volatile("s_add_u32 m0, %0, %1\n\ts_nop 0\n\tbuffer_load_dwordx4 %2, %3, %4 offen sc1 lds" :: "s"(lds), "n"(IMM), "v"(voff), "s"(rs), "s"(soff) : "memory", "scc");
    else asm volatile("s_add_u32 m0, %0, %1\n\ts_nop 0\n\tbuffer_load_dwordx4 %2, %3, %4 offen lds" :: "s"(lds), "n"(IMM), "v"(voff), "s"(rs), "s"(soff) : "memory", "scc");
}
template <int IMM> __device__ __forceinline__ void dma1(unsigned lds, unsigned voff, const u32x4& rs, unsigned soff) {
    asm volatile("s_add_u32 m0, %0, %1\n\ts_nop 0\n\tbuffer_load_dword %2, %3, %4 offen sc1 lds" :: "s"(lds), "n"(IMM), "v"(voff), "s"(rs), "s"(soff) : "memory", "scc");
}
__device__ __forceinline__ void flag_set(unsigned waddr, unsigned v) { asm volatile("ds_write_b32 %0, %1" :: "v"(waddr), "v"(v) : "memory"); }
__device__ __forceinline__ void flag_chk(unsigned& f, unsigned raddr, unsigned need) {
    unsigned cnt;
    asm volatile("v_cmp_le_u32 vcc, %3, %0\n\t"
                 "s_cmp_eq_u64 vcc, -1\n\t"
                 "s_cbranch_scc1 1f\n\t"
                 "s_mov_b32 %1, 0x100000\n"
                 "0:\n\t"
                 "ds_read_b32 %0, %2\n\t"
                 "s_sub_u32 %1, %1, 1\n\t"
                 "s_waitcnt lgkmcnt(0)\n\t"
                 "s_cbranch_scc1 1f\n\t"
                 "v_cmp_le_u32 vcc, %3, %0\n\t"
                 "s_cmp_eq_u64 vcc, -1\n\t"
                 "s_cbranch_scc0 0b\n"
                 "1:" : "+v"(f), "=&s"(cnt) : "v"(raddr), "s"(need) : "vcc", "scc", "memory");
}
template <int OFF> __device__ __forceinline__ void zero_chk(unsigned& f, unsigned raddr) {
    unsigned cnt;
    asm volatile("v_cmp_ne_u32 vcc, 0, %0\n\t"
                 "s_cbranch_vccz 1f\n\t"
                 "s_mov_b32 %1, 0x100000\n"
                 "0:\n\t"
                 "ds_read_b32 %0, %2 offset:%3\n\t"
                 "s_sub_u32 %1, %1, 1\n\t"
                 "s_waitcnt lgkmcnt(0)\n\t"
                 "s_cbranch_scc1 1f\n\t"
                 "v_cmp_ne_u32 vcc, 0, %0\n\t"
                 "s_cbranch_vccnz 0b\n"
                 "1:" : "+v"(f), "=&s"(cnt) : "v"(raddr), "n"(OFF) : "vcc", "scc", "memory");
}
template <int IMM, class T> __device__ __forceinline__ void gld(T& d, unsigned voff, const u32x4& rs, unsigned soff) {
    if constexpr (sizeof(T) == 16) asm volatile("buffer_load_dwordx4 %0, %1, %2, %3 offen offset:%4" : "=v"(d) : "v"(voff), "s"(rs), "s"(soff), "n"(IMM) : "memory");
    else if constexpr (sizeof(T) == 8) asm volatile("buffer_load_dwordx2 %0, %1, %2, %3 offen offset:%4" : "=v"(d) : "v"(voff), "s"(rs), "s"(soff), "n"(IMM) : "memory");
    else asm volatile("buffer_load_ushort %0, %1, %2, %3 offen offset:%4" : "=v"(d) : "v"(voff), "s"(rs), "s"(soff), "n"(IMM) : "memory");
}
__device__ __forceinline__ void gld32(unsigned& d, unsigned voff, const u32x4& rs, unsigned soff) { asm volatile("buffer_load_dword %0, %1, %2, %3 offen" : "=v"(d) : "v"(voff), "s"(rs), "s"(soff) : "memory"); }
__device__ __forceinline__ void sld(unsigned& d, const void* p) { asm volatile("s_load_dword %0, %1, 0x0" : "=s"(d) : "s"(p) : "memory"); }
__device__ __forceinline__ void ties(unsigned& x) { asm volatile("" : "+s"(x)); }
template <int SLP> __device__ __forceinline__ void gate(unsigned raddr, unsigned need) {
    unsigned cnt, f;
    asm volatile("s_mov_b32 %1, 0x40000\n"
                 "0:\n\t"
                 "ds_read_b32 %0, %2\n\t"
                 "s_sub_u32 %1, %1, 1\n\t"
                 "s_waitcnt lgkmcnt(0)\n\t"
                 "s_cbranch_scc1 1f\n\t"
                 "v_cmp_le_u32 vcc, %3, %0\n\t"
                 "s_cmp_eq_u64 vcc, -1\n\t"
                 "s_cbranch_scc1 1f\n\t"
                 "s_sleep %4\n\t"
                 "s_branch 0b\n"
                 "1:" : "=&v"(f), "=&s"(cnt) : "v"(raddr), "s"(need), "n"(SLP) : "vcc", "scc", "memory");
}
__device__ __forceinline__ void tag_chk(unsigned& f, unsigned expect, unsigned voff, const u32x4& rs, unsigned soff) {
    unsigned cnt;
    asm volatile("v_cmp_eq_u32 vcc, %2, %0\n\t"
                 "s_cmp_eq_u64 vcc, -1\n\t"
                 "s_cbranch_scc1 1f\n\t"
                 "s_mov_b32 %1, 0x10000\n"
                 "0:\n\t"
                 "buffer_load_dword %0, %3, %4, %5 offen sc1\n\t"
                 "s_sub_u32 %1, %1, 1\n\t"
                 "s_waitcnt vmcnt(0)\n\t"
                 "s_cbranch_scc1 1f\n\t"
                 "v_cmp_eq_u32 vcc, %2, %0\n\t"
                 "s_cmp_eq_u64 vcc, -1\n\t"
                 "s_cbranch_scc0 0b\n"
                 "1:" : "+v"(f), "=&s"(cnt) : "v"(expect), "v"(voff), "s"(rs), "s"(soff) : "vcc", "scc", "memory");
}

__device__ __forceinline__ void pp_ld0(u32x4& d, unsigned voff, const u32x4& rs, unsigned soff) { asm volatile("buffer_load_dwordx4 v[240:243], %1, %2, %3 offen sc1" : "={v[240:243]}"(d) : "v"(voff), "s"(rs), "s"(soff) : "memory"); }
__device__ __forceinline__ void pp_ld1(u32x4& d, unsigned voff, const u32x4& rs, unsigned soff) { asm volatile("buffer_load_dwordx4 v[244:247], %1, %2, %3 offen offset:1024 sc1" : "={v[244:247]}"(d) : "v"(voff), "s"(rs), "s"(soff) : "memory"); }
__device__ __forceinline__ void pp_chk(u32x4& p0, u32x4& p1, unsigned bad, unsigned tag, unsigned voff, const u32x4& rs, unsigned soff) {
    unsigned cnt, t1, t2, t3;
    asm volatile("v_cmp_ne_u32 vcc, 0, %3\n\t"
                 "s_cbranch_vccz 1f\n\t"
                 "s_mov_b32 %2, 0x10000\n"
                 "0:\n\t"
                 "buffer_load_dwordx4 v[240:243], %8, %9, %10 offen sc1\n\t"
                 "buffer_load_dwordx4 v[244:247], %8, %9, %10 offen offset:1024 sc1\n\t"
                 "s_sub_u32 %2, %2, 1\n\t"
                 "s_waitcnt vmcnt(0)\n\t"
                 "s_cbranch_scc1 1f\n\t"
                 "v_perm_b32 %3, v241, v240, %11\n\t"
                 "v_perm_b32 %4, v243, v242, %11\n\t"
                 "v_perm_b32 %5, v245, v244, %11\n\t"
                 "v_perm_b32 %6, v247, v246, %11\n\t"
                 "v_perm_b32 %3, %4, %3, %12\n\t"
                 "v_perm_b32 %5, %6, %5, %12\n\t"
                 "v_xor_b32 %3, %7, %3\n\t"
                 "v_xor_b32 %5, %7, %5\n\t"
                 "v_or_b32 %3, %3, %5\n\t"
                 "v_cmp_ne_u32 vcc, 0, %3\n\t"
                 "s_cbranch_vccnz 0b\n"
                 "1:" : "+{v[240:243]}"(p0), "+{v[244:247]}"(p1), "=&s"(cnt), "+v"(bad), "=&v"(t1), "=&v"(t2), "=&v"(t3)
                 : "s"(tag), "v"(voff), "s"(rs), "s"(soff), "s"(0x0c0c0400u), "s"(0x05040100u) : "vcc", "scc", "memory");
}
__device__ __forceinline__ void pp_tie(u32x4& p0, u32x4& p1) { asm volatile("" : "+{v[240:243]}"(p0), "+{v[244:247]}"(p1)); }
__device__ __forceinline__ unsigned and_or(unsigned a, unsigned m, unsigned o) { unsigned r; asm("v_and_or_b32 %0, %1, %2, %3" : "=v"(r) : "v"(a), "v"(m), "s"(o)); return r; }

enum { OP_NONE, OP_A, OP_B, OP_FLAG, OP_CONST };
struct Op { int kind, a, b; };
constexpr Op g1_op(int G) {
    const int st = (G + 32) / 32 - 1, q = G - 32 * st, i = q >> 2, j = q & 3;
    if (i < 5) {
        if (st < 0) return {OP_NONE, 0, 0};
        if ((j & 1) == 0) return {OP_A, st * 8 + i + 3, j >> 1};
        if (q == FLQ && st < 15) return {OP_FLAG, 0, 0};
        if (st == 15 && q < 10) return {OP_CONST, (q + 1) >> 1, 0};
        return {OP_NONE, 0, 0};
    }
    if (st == 15) return {OP_NONE, 0, 0};
    if (G == 479) return {OP_CONST, 0, 0};
    const int n = (st + 1) * 8;
    switch (q) {
        case 20: return {OP_B, st + 1, 0}; case 21: return {OP_B, st + 1, 1}; case 22: return {OP_A, n, 0}; case 23: return {OP_B, st + 1, 2};
        case 24: return {OP_B, st + 1, 3}; case 25: return {OP_A, n, 1}; case 26: return {OP_A, n + 1, 0}; case 27: return {OP_A, n + 1, 1};
        case 28: return {OP_A, n + 2, 0}; case 30: return {OP_A, n + 2, 1};
    }
    return {OP_NONE, 0, 0};
}
constexpr int g1_need(int G) {
    const int st = G >> 5, j = G & 3;
    for (int h = G - 1; h >= -12; --h) {
        const Op o = g1_op(h);
        if (o.kind == OP_A && o.a == (G >> 2) && o.b == (j >> 1)) return h;
        if (o.kind == OP_B && o.a == st && o.b == j) return h;
        if (o.kind == OP_FLAG && (G & 31) == 20 && (h >> 5) == st) return h;
        if (o.kind == OP_CONST && o.a == 0 && G == 487) return h;
    }
    return -100;
}
constexpr int g1_wait(int G) {
    int n = -100, c = 0;
    for (int h = G; h < (G ? G + 4 : 2) && h < 512; ++h) if (g1_need(h) > n) n = g1_need(h);
    for (int h = n + 1; h < G; ++h) c += g1_op(h).kind != OP_NONE;
    return n < G ? c : -1;
}
#define KV0 0
#define KVN 0x11111112
constexpr int kv_first(int k) { int n = 0; for (int i = KV0; i < k && i < KV0 + 8; ++i) n += (KVN >> (4 * (i - KV0))) & 15; return n > 8 ? 8 : n; }
constexpr int kv_last() { int k = KV0; while (kv_first(k + 1) < 8) ++k; return k; }
constexpr int KVL = kv_last(), KFL = KVL + 3, KC0 = KVL + 1;
static_assert(KC0 + 3 <= 10 && KFL <= 11, "");
constexpr int hv_cnt(int k0, int k1) {
    int c = 0;
    for (int k = k0; k < k1; ++k) c += 9 + (kv_first(k + 1) / 2 - kv_first(k) / 2) + (k == KFL ? 1 : 0);
    return c;
}
constexpr int cv_start(int n) { return n * 8 - n * (n - 1) / 2; }
constexpr int cv_n(int q) { int n = 0; while (cv_start(n + 1) <= q) ++n; return n; }
constexpr int cv_m(int q) { return 7 - cv_n(q) - (q - cv_start(cv_n(q))); }
enum { CO_NONE, CO_A, CO_B, CO_C };
constexpr Op cv_op(int G) {
    const int s = (G + 36) / 36 - 1, q = G - 36 * s;
    if (q >= 1 && q <= 6) return s >= 0 ? Op{CO_A, s * 8 + 5 + (q - 1) / 2, (q - 1) & 1} : Op{CO_NONE, 0, 0};
    if (q >= 11 && q <= 14) return s >= 2 ? Op{CO_C, s - 2, q - 11} : Op{CO_NONE, 0, 0};
    if (s == 7) return {CO_NONE, 0, 0};
    for (int m = 0; m < 8; ++m) if (q == cv_start(7 - m)) return {CO_B, (s + 1) * 8 + m, 0};
    for (int n = 0; n < 5; ++n) if (q == cv_start(n + 1) + 1 || q == cv_start(n + 1) + 2) return {CO_A, (s + 1) * 8 + n, q - cv_start(n + 1) - 1};
    return {CO_NONE, 0, 0};
}
constexpr int cv_need(int G) {
    const int s = G / 36, q = G % 36;
    for (int h = G - 1; h >= -36; --h) {
        const Op o = cv_op(h);
        if (o.kind == CO_A && o.a == s * 8 + cv_n(q)) return h;
        if (o.kind == CO_B && o.a == s * 8 + cv_m(q)) return h;
        if (o.kind == CO_C && q == 24 && o.a == s - 2) return h;
    }
    return -100;
}
constexpr int cv_wait(int G) {
    const int n = cv_need(G);
    for (int h = G - 1; h >= 0 && h >= G - 40; --h) if (cv_need(h) >= n) return -1;
    int c = 0;
    for (int h = n + 1; h < G; ++h) c += cv_op(h).kind != CO_NONE;
    return c;
}
#define AT(n, m) ((n) * 8 + (m))
#define PC0 26
constexpr int g2_fill(int p) {
    const int n = p >> 3, m = p & 7;
    if (m != 5 && m != 7) return -1;
    if (n < 2) return 8 + 2 * n + (m == 7);
    if (n <= 20 && ((n & 3) == 3 || (n & 3) == 0)) return ((n + 1) / 4 + 2) * 4 + ((n & 3) ? 0 : 2) + (m == 7);
    return -1;
}
constexpr int g2_vm(int p) {
    if (g2_fill(p) >= 0) return 1;
    switch (p) {
        case AT(3, 4):
        case AT(2, 5): case AT(5, 5): case AT(2, 7): case AT(6, 5):
        case AT(9, 6): case AT(14, 4): case AT(14, 6):
        case AT(13, 0): case AT(13, 4): case AT(21, 0): case AT(21, 4):
        case AT(22, 1): case AT(22, 3): case AT(28, 1): case AT(28, 3):
        case AT(22, 7): case AT(23, 7): case AT(24, 7): case AT(25, 7):
        case AT(28, 5): case AT(28, 7): case AT(29, 5): case AT(29, 7):
            return 1;
    }
    return 0;
}
constexpr int g2_lx(int n) {
    switch (n) {
        case 8: case 9: case 16: case 17: case 25: case 21: case 22: case 23: return 1;
        case 11: case 24: case 27: return 2;
        case 12: case 13: return 3;
        case 19: return 6;
    }
    return 0;
}
constexpr int g2_allow(int n, int p) { int c = 0; for (int h = p + 1; h < 8 * n; ++h) c += g2_vm(h); return c; }

__global__ __launch_bounds__(512, 2) void k1(const uint16_t* __restrict__ x, const uint16_t* __restrict__ bre, const uint16_t* __restrict__ bim,
                                             const uint16_t* __restrict__ lre, const uint16_t* __restrict__ lim, const uint16_t* __restrict__ logdt,
                                             const uint16_t* __restrict__ cre, const uint16_t* __restrict__ cim, const uint16_t* __restrict__ dskip,
                                             const uint16_t* __restrict__ ssmk, const uint16_t* __restrict__ ssmD, const uint16_t* __restrict__ qin,
                                             const uint16_t* __restrict__ kin, const uint16_t* __restrict__ vin, const uint16_t* __restrict__ sk,
                                             char* __restrict__ ws, float* __restrict__ out TS_ARG) {
    unsigned long long pc0;
    asm volatile("s_getpc_b64 %0" : "=s"(pc0));
    TS_DECL TS(0);
    TL_DECL
    __shared__ __attribute__((aligned(1024))) char shm[LDS_TOTAL];
    const int tid = threadIdx.x, lane = tid & 63, wid = __builtin_amdgcn_readfirstlane(tid >> 6);
    const int b = blockIdx.x, xcd = b & 7, idx = b >> 3, hb = xcd & 1, tq = idx & 7;
    const int tm = (xcd >> 1) * 4 + (idx >> 3), tn = hb * 8 + tq;
    const int tm2 = (xcd & 2) * 4 + tq, dt = (xcd & 4) + (idx >> 3), rot2 = tq + 2 * (idx >> 3);
    const unsigned lbase = (unsigned)(uintptr_t)shm;
    const int l15 = lane & 15, g4 = lane >> 4, lw = wid & 3, kg = wid >> 2;
    dma<U0 + 4096, false>(lbase + wid * 1024, lane * 16, mk_rsrc((const void*)(pc0 & ~255ull), CODE_KB * 1024), ((idx * 8 + wid) & 63) * 1024);
    const unsigned mk0 = lbase + POLL + lw * 256 + kg * (FULL - POLL + lw * (FST - 256));
    flag_set(mk0 + lane * 4, 1u);
    flag_set(lbase + HGO + lane * 4, 0u);
    dsw128<3072>(lbase + CT + lane * 16, f32x4{1.f, 0.f, 1.f, 0.f});
    unsigned vo[4];
    const bool xw = lw < 2;
#pragma unroll
    for (int i = 0; i < 4; ++i) {
        const int v = l15 ^ (i * 4 + g4);
        vo[i] = ((xw ? 64 * (i & 1) + 4 * (i >> 1) + (lane & 32) + 2 * (g4 & 1) : 8 * (i & 1) + 2 * g4) + (v >> 3)) * 2048 + (v & 7) * 16;
    }
    const int bw = lw >> 1;
    unsigned r0 = (tm * 128 + lw * 16 + bw * (tn * 64 + lw * 16 - 64 - tm * 128)) * 2048, rsz = 229376 - bw * 163840;
    unsigned rot = ROTX ? xcd * 2 : 8 * ((tq + (idx >> 3)) & 1) + 2 * (xcd >> 1) + 4 * hb, fp = (tm + tn - rot) & 15, rst = 16384 << bw, lb = lbase + lw * 8192, oob = OOB;
    unsigned lb0 = lb + g1_buf(0) + kg * (g1_buf(1) - g1_buf(0)), so0 = ROTX == 1 ? ((rot + (kg ? fp == 0 : fp)) & 15) * 128 : ((kg + rot) & 15) * 128;
    tie(vo[0]); tie(vo[1]); tie(vo[2]); tie(vo[3]); tie(oob);
    ties(r0); ties(rsz); ties(rot); ties(fp); ties(rst); ties(lb); ties(lb0); ties(so0);
    sched();
    asm volatile("" :: "s"(x), "s"(bre), "s"(bim), "s"(lre), "s"(lim), "s"(logdt), "s"(cre), "s"(cim), "s"(dskip), "s"(ssmk), "s"(ssmD), "s"(qin),
                       "s"(kin), "s"(vin), "s"(sk), "s"(ws), "s"(out));
    const u32x4 rs0 = mk_rsrc((const char*)(xw ? x : bre) + r0, rsz), rs1 = mk_rsrc((const char*)(xw ? x : bim) + r0, rsz);
    static_for<0, 8>([&](auto ic) { constexpr int i = CV(ic); dma<i * 1024, false>(lb0, vo[i & 3], (i & 2) ? rs1 : rs0, so0 + (i >> 2) * rst); });
    wait_l<0>();
    barrier();
    dma1<0>(mk0 + kg * 120, oob, rs0, 0);
    sched();
    const unsigned a00 = (l15 >> 1) * 256 + ((((l15 & 1) * 8 + g4) ^ (l15 >> 1)) << 4);
    auto frag = [&](int par, int ks) { return (a00 ^ (ks * 64) ^ (par * 128)) + par * 2048; };
    bf16x8 cf[32];
    unsigned vc[4];
    auto c_setup = [&]() {
#pragma unroll
        for (int i = 0; i < 4; ++i) { const int v = l15 ^ (i * 4 + g4); vc[i] = (2 * (8 * (i & 1) + 2 * g4 + (v >> 3)) + (i >> 1)) * 2048 + (v & 7) * 16; }
    };
    const u32x4 rsc = mk_rsrc((kg ? cim : cre) + (size_t)dt * 131072, 262144);
    auto c_off = [&](int dg, int j) { return (unsigned)(dg * 65536 + hb * 1024 + ((rot2 + j) & 7) * 128); };
    const unsigned cst = lbase + (kg ? BUF : 2 * BUF) + lw * 8192;
    unsigned tag;
    const u32x4 rsf = mk_rsrc(ws + WS_F, 65536);
    const __amdgpu_buffer_rsrc_t rf = mk_brs(ws + WS_F, 65536);
    const u32x4 rsh = mk_rsrc(ws + WS_H + (size_t)(tm2 * 16 + hb * 8) * 32768, 262144);
    const int qx = hb ? 0 : 2, wid_ = wid, b_ = b;
    unsigned ha[4];
    auto h_setup = [&](int kg) {
        const int q = (lane >> 2) & 3, p = lane & 3;
#pragma unroll
        for (int i = 0; i < 4; ++i) {
            ha[i] = lbase + (g4 >> 1) * 2048 + kg * 1024 + ((i >> 1) ^ kg) * 512 + (p >> 1) * 256 + (((8 * (g4 & 1) + 4 * (i & 1) + q) ^ (4 * (p >> 1))) << 4) + (p & 1) * 8;
            tie(ha[i]);
        }
    };

    if (wid >= 4) {
        const int ch = xcd * 128 + (idx >> 2) * 16 + lw * 4 + (idx & 3);
        unsigned dw, tch;
        {
            const unsigned fb = lbase + FULL + lw * FST;
            auto piece = [&](auto sc, auto ic) {
                constexpr int s = CV(sc), i = CV(ic);
                dma<g1_buf(s) + i * 1024, false>(lb, vo[i & 3], (i & 2) ? rs1 : rs0, ((s + rot - (ROTX == 1 && s <= fp)) & 15) * 128 + (i >> 2) * rst);
            };
            auto mark = [&](auto sc) { dma1<(31 - CV(sc)) * 4>(fb, oob, rs0, 0); };
            auto feed = [&](auto kc, auto ic) {
                constexpr int k = CV(kc), i = CV(ic);
                if constexpr (i < 4) piece(IC(k + 3), IC(i + 4));
                if constexpr (i == 3) mark(IC(k + 3));
                if constexpr (i >= 4 && k < 12) piece(IC(k + 4), IC(i - 4));
            };
            __builtin_amdgcn_s_setprio(HP);
            TS(7);
            sld(tag, ws + WS_F + F_EP);
            sld(dw, ssmD + (ch & ~1));
            const int sb = idx * 2 + (lw >> 1), kh = lw & 1;
            u32x2 kk[11], vv[8]; u32x4 w4[2], kd[4], ke[4], kvo; unsigned c_dt, c_re, c_im;
            const unsigned ko = g4 * 16384 + l15 * 8, so = sb * 65536 + xcd * 256 + kh * 128;
            {
                const u32x4 rs = mk_rsrc(kin, 4 << 20);
                gld<0>(w4[0], l15 * 32, mk_rsrc(sk, 8192), xcd * 1024 + kh * 512);
                gld<16>(w4[1], l15 * 32, mk_rsrc(sk, 8192), xcd * 1024 + kh * 512);
                static_for<0, 3>([&](auto ic) { gld<0>(kk[CV(ic)], ko + so - 6144 + CV(ic) * 2048, rs, 0); });
                static_for<0, 8>([&](auto ic) { constexpr int i = CV(ic); gld<(i & 1) * 2048>(kk[3 + i], ko, rs, so + (i >> 1) * 4096); });
                static_for<0, 8>([&](auto ic) { constexpr int i = CV(ic); gld<(i & 1) * 2048>(vv[i], ko, mk_rsrc(vin, 4 << 20), so + (i >> 1) * 4096); });
                gld<0>(c_dt, lane * 2, mk_rsrc(logdt, 2048), tn * 128);
                gld<0>(c_re, lane * 2, mk_rsrc(lre, 2048), tn * 128);
                gld<0>(c_im, lane * 2, mk_rsrc(lim, 2048), tn * 128);
                dma1<DSK>(lbase, lane * 4, mk_rsrc(dskip, 2048), dt * 256);
                dma<U0, false>(lbase + kh * 1024, oob, rs0, 0);
            }
            static_for<4, 8>([&](auto ic) { feed(IC(-2), ic); });
            const __amdgpu_buffer_rsrc_t rk = mk_brs(ws + WS_KVT + (size_t)(xcd * 32 + kh * 16) * 16384, 262144);
            const unsigned kfo = F_K + ((xcd * 2 + (idx >> 4)) * 64 + lw * 16) * 8, K = lbase + KC + lw * KCH;
            float kf[7], pa[4], pb[4], dtv, ea, ar, ai, sn, cs;
            cf32 L, cc, M, M2, M3, N, N2, N3, N4, N8;
            auto kv_step = [&](auto uc, auto jc) {
                constexpr int u = CV(uc), j = CV(jc), c = u >> 1, h = u & 1;
                auto el = [&](const u32x2& q) -> float { return (c & 1) ? bfhi(q[c >> 1]) : bflo(q[c >> 1]); };
                auto tap = [&](int t) { const unsigned w = w4[c >> 1][(c & 1) * 2 + (t >> 1)]; return (t & 1) ? bfhi(w) : bflo(w); };
                auto all = [&](auto f) {
#pragma unroll
                    for (int i = 0; i < 4; ++i) f(i);
                };
                if constexpr (j == 0) {
#pragma unroll
                    for (int i = 0; i < 7; ++i) { kf[i] = (h && i < 3) ? kf[i + 4] : el(kk[4 * h + i]); tie(kf[i]); }
                }
                if constexpr (EXACT) {
                    if constexpr (j == 1) { const float t = tap(3); all([&](int i) { pa[i] = kf[i] * t; }); all([&](int i) { pa[i] = rbf(pa[i]); tie(pa[i]); }); }
                    if constexpr (j == 2 || j == 4 || j == 6) { const float t = tap(3 - j / 2); all([&](int i) { pb[i] = kf[i + j / 2] * t; }); all([&](int i) { pb[i] = rbf(pb[i]); tie(pb[i]); }); }
                    if constexpr (j == 3 || j == 5 || j == 7) { all([&](int i) { pa[i] += pb[i]; }); all([&](int i) { pa[i] = rbf(pa[i]); tie(pa[i]); }); }
                } else {
                    if constexpr (j == 1) { const float t = tap(3); all([&](int i) { pa[i] = kf[i] * t; tie(pa[i]); }); }
                    if constexpr (j >= 2 && j <= 4) { const float t = tap(4 - j); all([&](int i) { pa[i] = fma_(kf[i + j - 1], t, pa[i]); tie(pa[i]); }); }
                    if constexpr (j == 5) all([&](int i) { pa[i] = rbf(pa[i]); tie(pa[i]); });
                }
                if constexpr (j == (EXACT ? 7 : 6)) all([&](int i) { pb[i] = el(vv[4 * h + i]); tie(pb[i]); });
                if constexpr (j == 7) {
                    all([&](int i) { pa[i] *= pb[i]; });
                    kvo[3 - 2 * h] = pack2(pa[1], pa[0]); kvo[2 - 2 * h] = pack2(pa[3], pa[2]);
                    if constexpr (h) __builtin_amdgcn_raw_buffer_store_b128(kvo, rk, (l15 >> 2) * 65536 + g4 * 64 + (l15 & 3) * 16, c * 16384 + sb * 256, 16);
                    else tie(kvo);
                }
            };
            static_for<-1, 13>([&](auto kc) {
                constexpr int k = CV(kc);
                if constexpr (k >= 0) barrier();
                if constexpr (k == KV0) {
                    wait_v<4 + hv_cnt(-1, k)>();
                    static_for<0, 11>([&](auto ic) { tie(kk[CV(ic)]); });
                    static_for<0, 8>([&](auto ic) { tie(vv[CV(ic)]); });
                    tie(w4[0]); tie(w4[1]); tie(c_dt); tie(c_re); tie(c_im);
                    TS(8);
                }
                if constexpr (k == KFL) {
                    wait_l<0>();
                    ties(tag); ties(dw);
                    tag += 0x01010101u;
                    wait_v<hv_cnt(KVL + 1, k)>();
                    __builtin_amdgcn_raw_buffer_store_b64(u32x2{tag, ~tag}, rf, lane ? OOB : 0u, F_K + ((xcd * 2 + kh) * 64 + sb) * 8, 16);
                    TS(10);
                }
                if constexpr (k == 12) {
                    dma1<POLL>(lbase + wid * 256, (lane & 31) * 4, rsf, kfo);
                    dma1<0>(K, oob, rs0, 0);
                    dma1<KCP>(K, oob, rs0, 0);
                }
                static_for<0, 8>([&](auto ic) {
                    constexpr int i = CV(ic), u0 = kv_first(k), nu = kv_first(k + 1) - u0;
                    feed(kc, ic);
                    if constexpr (nu == 1) kv_step(IC(u0), ic);
                    if constexpr (nu == 2) { kv_step(IC(u0 + i / 4), IC(2 * (i & 3))); kv_step(IC(u0 + i / 4), IC(2 * (i & 3) + 1)); }
                    if constexpr (k == 12 && i >= 4) gld<(i - 4) * 1024>(kd[i - 4], lane * 16, mk_rsrc(ssmk, 4 << 20), ch * 4096);
                    auto tc = [&](cf32& v) { tie(v.r); tie(v.i); };
                    if constexpr (k == KC0 && i == 1) { dtv = rbf(expf(bflo(c_dt))); tie(dtv); }
                    if constexpr (k == KC0 && i == 4) { ar = bflo(c_re); ai = bflo(c_im); ea = expf(dtv * ar); tie(ea); }
                    if constexpr (k == KC0 + 1 && i == 7) { sincosf(dtv * ai, &sn, &cs); tie(sn); tie(cs); }
                    if constexpr (k == KC0 + 2) {
                        if constexpr (i == 0) { L = cf32{ea * cs, ea * sn}; M = cmul(L, L); tc(L); tc(M); }
                        if constexpr (i == 1 || i == 2) { M = cmul(M, M); M = cmul(M, M); tc(M); }
                        if constexpr (i == 3) { M2 = cmul(M, M); M3 = cmul(M2, M); tc(M2); tc(M3); }
                        if constexpr (i == 4) { N = cmul(M2, M2); N2 = cmul(N, N); tc(N); tc(N2); }
                        if constexpr (i == 5) { N3 = cmul(N2, N); N4 = cmul(N2, N2); tc(N3); tc(N4); }
                        if constexpr (i == 6) { N8 = cmul(N4, N4); tc(N8); }
                    }
                    if constexpr (k == KC0 + 3) {
                        if constexpr (i == 0) { const float nr = L.r - 1.f; dtv = ar * ar + ai * ai; cc = cf32{nr * ar + L.i * ai, L.i * ar - nr * ai}; tie(dtv); tc(cc); }
                        if constexpr (i == 2) { cc.r = 2.f * (cc.r / dtv); tie(cc.r); }
                        if constexpr (i == 5) { cc.i = 2.f * (cc.i / dtv); tie(cc.i); }
                    }
                    if constexpr (k == 11 && i == 4) c_setup();
                    sched();
                });
            });
            barrier();
            TS(1);
            unsigned pf = 1;
            const unsigned fr = lbase + FULL + (lane & 3) * FST;
            zero_chk<16 * 4>(pf, fr);
            {
                const unsigned t = lbase + CT + lane * 16;
                const cf32 ra = lw == 0 ? L : lw == 1 ? M : lw == 2 ? M2 : M3, rb = lw == 0 ? cc : lw == 1 ? N : lw == 2 ? N2 : N3;
                dsw128<0>(t + (lw ? 3072 + lw * 1024 : 0), f32x4{ra.r, ra.i, rb.r, rb.i});
                if (lw == 0) dsw128<2048>(t, f32x4{N4.r, N4.i, N8.r, N8.i});
                if (lw == 1) flag_set(lbase + CT + 1024, tag);
            }
            dsr32<POLL - 131072>(pf, lbase + 131072 + wid * 256 + lane * 4);
            wait_l<0>();
            barrier();
            tag_chk(pf, (lane & 1) ? ~tag : tag, (lane & 31) * 4, rsf, kfo);
            {
                const u32x4 rs = mk_rsrc(ws + WS_KVT + (size_t)(xcd * 32 + idx) * 16384, 16384);
                static_for<0, 4>([&](auto ic) { constexpr int i = CV(ic); dma<U0 + 2048 + i * 1024, true>(lbase + lw * 4096, (lane >> 2) * 64 + (((lane & 3) ^ g4) << 4), rs, lw * 4096 + i * 1024); });
            }
            mark(IC(16));
            if constexpr (TOUCH) gld32(tch, ((lw & 1) * 64 + lane) * 2048, mk_rsrc(((lw & 2) ? cim : cre) + (size_t)dt * 131072, 262144), c_off(0, 0));
            barrier();
            wait_v<5 + TOUCH>();
            static_for<0, 4>([&](auto ic) { tie(kd[CV(ic)]); });
#pragma unroll
            for (int n = 0; n < 4; ++n) {
                const unsigned first = n ? __builtin_amdgcn_readlane(kd[n ? n - 1 : 0][3], 63) : 0u;
                const unsigned p = __builtin_amdgcn_update_dpp(first, kd[n][3], 0x138, 0xf, 0xf, false);
                ke[n] = u32x4{__builtin_amdgcn_alignbyte(kd[n][0], p, 2), __builtin_amdgcn_alignbyte(kd[n][1], kd[n][0], 2),
                              __builtin_amdgcn_alignbyte(kd[n][2], kd[n][1], 2), __builtin_amdgcn_alignbyte(kd[n][3], kd[n][2], 2)};
                tie(ke[n]);
            }
            pf = 1;
            zero_chk<15 * 4>(pf, fr);
            TS(9);
            static_for<0, 4>([&](auto ic) { constexpr int i = CV(ic); dsw128<64 + i * 1024>(K + lane * 16, kd[i]); dsw128<KCP + 64 + i * 1024>(K + lane * 16, ke[i]); });
            wait_l<0>();
            __builtin_amdgcn_s_setprio(0);
        }
        TS(2);
        unsigned ka[4], cad[4], ub[4];
#pragma unroll
        for (int i = 0; i < 4; ++i) {
            ka[i] = lbase + KC + lw * KCH + ((l15 & 1) ? 0 : KCP) + 84 + 2 * (l15 & 14) - 16 * g4 + i * 1024;
            cad[i] = cst + frag(i & 1, i >> 1);
            const int e = 2 * l15 + g4 + 2;
            ub[i] = lbase + U0 + 64 * e + ((lw ^ (((e >> 2) + 3 - i) & 3)) << 4);
        }
        f32x4 acc[8];
        bf16x4 FA[8][2]; bf16x8 FB[8];
        auto op = [&](auto gc) {
            constexpr Op o = cv_op(CV(gc));
            if constexpr (o.kind == CO_A) { constexpr int s = o.a >> 3, n = o.a & 7, d = (n & 1) * 128 + s * 16 + o.b * 2; dsr2<d, d + 1>(FA[n][o.b], ka[n >> 1]); }
            if constexpr (o.kind == CO_B) { constexpr int s = o.a >> 3, m = o.a & 7; dsr128<2048 * m + 1792 - 256 * s>(FB[m], ub[s & 3]); }
            if constexpr (o.kind == CO_C) dsr128<(o.a & 1) * 4096>(cf[o.a * 4 + o.b], cad[o.b]);
        };
        static_for<-36, 0>(op);
        static_for<0, 288>([&](auto gc) {
            constexpr int G = CV(gc), s = G / 36, q = G % 36, n = cv_n(q), m = cv_m(q);
            if constexpr (cv_wait(G) >= 0) wait_lr<cv_wait(G)>();
            if constexpr (q == 24 && s >= 2) static_for<0, 4>([&](auto ic) { tie(cf[(s - 2) * 4 + CV(ic)]); });
            mfma<s == 0 && n == 0>(acc[n + m], cat(FA[n][0], FA[n][1]), FB[m]);
            sched();
            if constexpr (q == 11 && s >= 2) wait_v<4>();
            if constexpr (q == 11 && s == 2 && TOUCH) tie(tch);
            op(gc);
            if constexpr (q == 24 || q == 25 || q == 29 || q == 34) {
                constexpr int pc = q == 24 ? 0 : q == 25 ? 1 : q == 29 ? 2 : 3;
                dma<(s & 1) * 4096 + pc * 1024, false>(cst, vc[pc], rsc, c_off(lw, s));
            }
            sched();
        });
        TS(3);
        {
            u32x2 kvr[8];
            const unsigned kva = lbase + U0 + 2048 + 128 * l15 + 64 * (g4 >> 1) + ((lw ^ ((l15 >> 1) & 3)) << 4) + 8 * (1 - (g4 & 1));
            static_for<0, 8>([&](auto ic) { dsr64<CV(ic) * 2048>(kvr[CV(ic)], kva); });
            wait_l<0>();
            const float Dd = (ch & 1) ? bfhi(dw) : bflo(dw);
            const __amdgpu_buffer_rsrc_t rz = mk_brs(ws + WS_ZT, 4 << 20);
#pragma unroll
            for (int jj = 0; jj < 4; ++jj) {
                u32x4 o;
#pragma unroll
                for (int u = 0; u < 2; ++u) {
                    const int j = 2 * jj + u;
                    tie(kvr[j]); tie(acc[j]);
                    const float kv3 = bflo(kvr[j][0]), kv2 = bfhi(kvr[j][0]), kv1 = bflo(kvr[j][1]), kv0 = bfhi(kvr[j][1]);
                    o[2 * u] = pack2(rbf(acc[j][0]) + rbf(kv0 * Dd), rbf(acc[j][1]) + rbf(kv1 * Dd));
                    o[2 * u + 1] = pack2(rbf(acc[j][2]) + rbf(kv2 * Dd), rbf(acc[j][3]) + rbf(kv3 * Dd));
                }
                __builtin_amdgcn_raw_buffer_store_b128(o, rz, lane * 16 + jj * 1024, ch * 4096, 16);
            }
            wait_v<4>();
            static_for<0, 8>([&](auto ic) { constexpr int i = CV(ic); dsr128<(i >> 2) * 4096>(cf[24 + i], cad[i & 3]); });
            h_setup(1);
            wait_l<0>();
            static_for<0, 8>([&](auto ic) { tie(cf[24 + CV(ic)]); });
        }
        if (G2P == 2) __builtin_amdgcn_s_setprio(1);
        TS(4);
    } else {
        f32x4 acc[8][2];
        u32x4 k0, k2, k3; u32x2 k1a, k1b; unsigned tgv;
        cf32 ag;
        {
            unsigned aad[4], bad[4];
#pragma unroll
            for (int i = 0; i < 4; ++i) { aad[i] = lbase + frag(i & 1, i >> 1); bad[i] = aad[i] + 16384 + wid * 4096; }
            const unsigned ca = lbase + CT + (wid * 16 + l15) * 16, fr = lbase + FULL + (lane & 3) * FST;
            bf16x8 fa[4][2], fb[2][4];
            unsigned fl = 1;
            auto op = [&](auto gc) {
                constexpr int G = CV(gc);
                constexpr Op o = g1_op(G);
                if constexpr (o.kind == OP_A) {
                    constexpr int bo = g1_buf(o.a >> 3), hi = bo >= 65536 ? 65536 : 0, i = o.a & 7;
                    dsr128<bo - hi + (i >> 1) * 4096>(fa[i & 3][o.b], aad[(i & 1) + 2 * o.b] + hi);
                }
                if constexpr (o.kind == OP_B) { constexpr int bo = g1_buf(o.a), hi = bo >= 65536 ? 65536 : 0; dsr128<bo - hi>(fb[o.a & 1][o.b], bad[o.b] + hi); }
                if constexpr (o.kind == OP_FLAG) dsr32<(30 - (G >> 5)) * 4>(fl, fr);
                if constexpr (o.kind == OP_CONST) {
                    if constexpr (o.a == 0) dsr128<0>(k0, ca);
                    if constexpr (o.a == 1) dsr64<4096>(k1a, ca);
                    if constexpr (o.a == 2) dsr64<5120>(k1b, ca);
                    if constexpr (o.a == 3) dsr128<2048>(k2, ca);
                    if constexpr (o.a == 4) dsr128<3072>(k3, ca + g4 * 1024);
                    if constexpr (o.a == 5) dsr32<0>(tgv, lbase + CT + 1024);
                }
            };
            __builtin_amdgcn_s_setprio(CP);
            zero_chk<0>(fl, lbase + POLL + (lane & 3) * 256);
            TS(1);
            static_for<-12, 0>(op);
            static_for<0, 512>([&](auto gc) {
                constexpr int G = CV(gc), st = G >> 5, i = (G >> 2) & 7, j = G & 3;
                if constexpr ((G == 0 || j == 2) && G <= 506) { static_assert(g1_wait(G) >= 0, ""); wait_lr<G == 506 ? 0 : g1_wait(G)>(); }
                if constexpr ((G & 31) == 20 && st < 15) zero_chk<(30 - st) * 4>(fl, fr);
                if constexpr ((G & 31) == 30 && st < 15) barrier();
                if constexpr ((G & 31) == 20) TL(st);
                mfma<st == 0 && j < 2>(acc[i][j & 1], fa[i & 3][j >> 1], fb[st & 1][j]);
                sched();
                op(gc);
                if constexpr (G == 506) barrier();
                if constexpr (G >= 487) {
                    constexpr int k = G - 486;
                    if constexpr (k == 1) { asm volatile("s_nop 3"); ag = cf32{acc[0][0][0], acc[0][1][0]}; }
                    cf32 t;
                    asm volatile("v_fma_f32 %0, -%3, %5, %6\n\tv_fma_f32 %1, %3, %4, %7\n\tv_fmac_f32 %0, %2, %4\n\tv_fmac_f32 %1, %2, %5"
                                 : "=&v"(t.r), "=&v"(t.i) : "v"(k0[0]), "v"(k0[1]), "v"(ag.r), "v"(ag.i), "v"(acc[k >> 2][0][k & 3]), "v"(acc[k >> 2][1][k & 3]));
                    ag = t;
                }
                sched();
            });
        }
        __builtin_amdgcn_s_setprio(3);
        tie(k0); tie(k1a); tie(k1b); tie(k2); tie(k3); tie(tgv);
        tag = __builtin_amdgcn_readfirstlane(tgv);
        TS(2);
        const cf32 L = {u2f(k0[0]), u2f(k0[1])}, M = {u2f(k1a[0]), u2f(k1a[1])};
#pragma unroll
        for (int k = 26; k < 32; ++k) ag = cfma(L, ag, cf32{acc[k >> 2][0][k & 3], acc[k >> 2][1][k & 3]});
        cf32 ev_, od, p01, p23;
        sw16(ag.r, ev_.r, od.r); sw16(ag.i, ev_.i, od.i);
        const cf32 pp = cfma(M, ev_, od);
        sw32(pp.r, p01.r, p23.r); sw32(pp.i, p01.i, p23.i);
        cf32 E = cfma(cf32{u2f(k1b[0]), u2f(k1b[1])}, p01, p23);

        const __amdgpu_buffer_rsrc_t rse = mk_brs(ws + WS_E + (size_t)(tn * 4 + wid) * 4096, 4096);
        __builtin_amdgcn_raw_buffer_store_b128(u32x4{f2u(E.r), f2u(E.i), tag, ~tag}, rse, g4 ? OOB : l15 * 16, tm * 256, 16);
        sched();
        TS(3);
        c_setup();
        unsigned cad[4];
#pragma unroll
        for (int i = 0; i < 4; ++i) cad[i] = lb + frag(i & 1, i >> 1);
        auto c_read = [&](auto rc) { static_for<0, 8>([&](auto ic) { constexpr int r = CV(rc), i = CV(ic); dsr128<(i >> 2) * 4096>(cf[r * 8 + i], cad[i & 3] + (r == 0 || r == 3 ? 2 * BUF : 0)); }); };
        auto c_tie = [&](auto rc) { static_for<0, 8>([&](auto ic) { tie(cf[CV(rc) * 8 + CV(ic)]); }); };
        auto c_unit = [&](auto uc) { static_for<0, 4>([&](auto ic) { constexpr int u = CV(uc), i = CV(ic); dma<(u < 2 || u >= 6 ? 2 * BUF : 0) + (u & 1) * 4096 + i * 1024, false>(lb, vc[i], rsc, c_off(wid, u)); }); };
        auto c_req = [&](auto rc) { c_unit(IC(CV(rc) * 2)); c_unit(IC(CV(rc) * 2 + 1)); };
        const float c2r = u2f(k0[2]), c2i = u2f(k0[3]);
        auto scale = [&](int i) {
#pragma unroll
            for (int j = 0; j < 4; ++j) {
                const float br = acc[i][0][j], bi = acc[i][1][j];
                acc[i][0][j] = fma_(c2r, br, -(c2i * bi)); acc[i][1][j] = fma_(-c2r, bi, -(c2i * br));
            }
            tie(acc[i][0]); tie(acc[i][1]);
        };
        scale(0); scale(1); scale(2); scale(3);
        u32x4 ev[4]; unsigned eo[4]; unsigned long long none[4];
#pragma unroll
        for (int i = 0; i < 4; ++i) {
            const int t = tm - 1 - 4 * i - g4;
            eo[i] = t < 0 ? OOB : t * 256 + l15 * 16; none[i] = __builtin_amdgcn_ballot_w64(t < 0);
            ev[i] = __builtin_amdgcn_raw_buffer_load_b128(rse, eo[i], 0, 16);
        }
        asm volatile("" ::: "memory");
        sched();
        if constexpr (R01 == 0) { c_req(IC(0)); c_req(IC(1)); }
        scale(4); scale(5); scale(6); scale(7);
        cf32 X = cfma(cf32{g4 == 3 ? M.r : g4 == 2 ? 1.f : 0.f, g4 == 3 ? M.i : 0.f}, p01, cf32{(g4 & 1) ? ev_.r : 0.f, (g4 & 1) ? ev_.i : 0.f});
        tie(X.r); tie(X.i);
        sched();
        {
            const unsigned long long tag64 = (unsigned long long)tag | ((unsigned long long)~tag << 32);
#pragma unroll 1
            for (unsigned spins = 0; spins < SPIN_CAP; ++spins) {
                unsigned long long ok = ~0ull;
#pragma unroll
                for (int i = 0; i < 4; ++i) ok &= __builtin_amdgcn_ballot_w64(((unsigned long long)ev[i][2] | ((unsigned long long)ev[i][3] << 32)) == tag64) | none[i];
                TSV(11, spins);
                if (ok == ~0ull) break;
                asm volatile("" ::: "memory");
#pragma unroll
                for (int i = 0; i < 4; ++i) ev[i] = __builtin_amdgcn_raw_buffer_load_b128(rse, eo[i], 0, 16);
            }
        }
        cf32 h;
        {
            auto e = [&](int i) { return cf32{u2f(ev[i][0]), u2f(ev[i][1])}; };
            const cf32 N4 = {u2f(k2[0]), u2f(k2[1])}, N8 = {u2f(k2[2]), u2f(k2[3])};
            cf32 c = cmul(cf32{u2f(k3[2]), u2f(k3[3])}, cfma(N8, cfma(N4, e(3), e(2)), cfma(N4, e(1), e(0)))), u, v;
            sw16(c.r, u.r, v.r); sw16(c.i, u.i, v.i);
            sw32(u.r + v.r, u.r, v.r); sw32(u.i + v.i, u.i, v.i);
            c = cfma(cf32{u2f(k3[0]), u2f(k3[1])}, cf32{u.r + v.r, u.i + v.i}, X);
            h = cf32{fma_(c2r, c.r, -(c2i * c.i)), fma_(-c2r, c.i, -(c2i * c.r))};
        }
        TS(4);
        {
            const __amdgpu_buffer_rsrc_t rh = mk_brs(ws + WS_H + (size_t)(tm * 16 + tn) * 32768 + wid * 2048, 32768);
            const unsigned ho = g4 * 256 + ((l15 ^ (4 * (g4 & 1))) << 4);
            const cf32 Lc = {L.r, -L.i};
            static_for<0, 4>([&](auto qc) {
                constexpr int q = CV(qc);
                if constexpr (R01 == 1) c_unit(qc);
                u32x4 wr, wi;
#pragma unroll
                for (int u = 0; u < 4; ++u) {
                    const int i = 2 * q + (u >> 1), j = 2 * (u & 1);
                    const cf32 h0 = cfma(Lc, h, cf32{acc[i][0][j], acc[i][1][j]});
                    h = cfma(Lc, h0, cf32{acc[i][0][j + 1], acc[i][1][j + 1]});
                    wr[u] = pack2(h0.r, h.r); wi[u] = pack2(h0.i, h.i);
                }
                __builtin_amdgcn_raw_buffer_store_b128(wr, rh, ho, q * 8192, 16);
                __builtin_amdgcn_raw_buffer_store_b128(wi, rh, ho + 1024, q * 8192, 16);
                sched();
            });
        }
        wait_v<0>();
        __builtin_amdgcn_raw_buffer_store_b64(u32x2{tag, ~tag}, rf, lane ? OOB : 0u, F_H + ((tm * 16 + tn) * 4 + wid) * 8, 16);
        TS(5);
        c_read(IC(1)); c_read(IC(0));
        wait_l<8>();
        c_req(IC(2));
        wait_l<0>();
        c_req(IC(3));
        c_tie(IC(0)); c_tie(IC(1));
        wait_v<0>();
        c_read(IC(2)); c_read(IC(3));
        wait_l<0>();
        c_tie(IC(2)); c_tie(IC(3));
        if (wid == 3) {
            const unsigned long long tag64 = (unsigned long long)tag | ((unsigned long long)~tag << 32);
            gu64* hf = (gu64*)(ws + WS_F + F_H) + (tm2 * 16 + hb * 8) * 4 + (lane & 31);
#pragma unroll 1
            for (unsigned spins = 0; spins < SPIN_CAP; ++spins) {
                if (wall(__hip_atomic_load(hf, RLX_AGENT) == tag64)) break;
                __builtin_amdgcn_s_sleep(1);
            }
            flag_set(lbase + HGO + lane * 4, 1u);
        } else gate<1>(lbase + HGO, 1);
        TS(6);
        static_for<0, 16>([&](auto ic) { constexpr int i = CV(ic); dma<(i >> 3) * 2 * BUF + (i & 7) * 1024, true>(lb, lane * 16, rsh, ((rot2 + wid + (i >> 3) * 4) & 7) * 32768 + qx * 8192 + (i & 7) * 1024); });
        h_setup(0);
        wait_v<8>();
        __builtin_amdgcn_s_setprio(G2P == 1);
    }
    {
        const int wid = fresh(wid_), lw = wid & 3, kg = wid >> 2, b = fresh(b_), xcd = b & 7, idx = b >> 3, tq = (idx & 7) + 2 * (idx >> 3);
        const int dt = (xcd & 4) + (idx >> 3), qx = (~b & 1) * 2;
        const unsigned wl = lbase + wid * 4096;
        unsigned l16 = lane * 16;
        unsigned hs0, hs1;
        auto fill = [&](auto sc, auto pc) {
            constexpr int s = CV(sc), p = CV(pc);
            dma<(2 * s % 3) * BUF + p * 1024, true>(wl, l16, rsh, ((s & 1) ? hs1 : hs0) ^ ((s >> 1) * 8192 + p * 1024));
        };
        bf16x4 hf[2][8];
        auto ldh = [&](auto nc, auto ic) {
            constexpr int n = CV(nc), i = CV(ic), bo = (2 * (n >> 2) % 3) * BUF + (n & 3) * 8192 + (i >> 2) * 4096, hi = bo >= 65536 ? 65536 : 0;
            dsr64tr<bo - hi>(hf[n & 1][i], ha[i & 3] + hi);
        };
        f32x4 yc[2][2][2];
        f32x4 im[2], ao; u32x4 pl[2], po;
        u32x2 xs[4], zv[4], qv[2], tg2;
        unsigned pf, dsw_, expect, msk, sk3[4], ck[4], a_q, a_x, a_iw, a_ir, a_p, a_z, a_xr, a_zr, a_qr, a_oa, a_ob;
        float d0, d1, t0, t1;
        const __amdgpu_buffer_rsrc_t ro = mk_brs(out, 16 << 20);
        const __amdgpu_buffer_rsrc_t rp = mk_brs(ws + WS_PP, 8 << 20);
        const u32x4 rsp = mk_rsrc(ws + WS_PP, 8 << 20);
        unsigned pol, w2k, zfo, ta, tb, ppo;
        auto finB = [&](auto pc, auto rc) {
            constexpr int p = CV(pc), r = CV(rc);
            __builtin_amdgcn_raw_buffer_store_b64(u32x2{f2u(yc[p & 1][0][0][r] + im[0][r]), f2u(yc[p & 1][0][1][r] + im[1][r])}, ro, a_ob,
                                                  (tb + (p ^ qx) * 8 + r) * 8192 + 4096 + dt * 512 + lw * 128, OSC);
        };
        auto addp = [&](auto pc, auto ic) {
            constexpr int p = CV(pc), dd = CV(ic) >> 2, r = CV(ic) & 3;
            yc[p & 1][0][dd][r] += u2f(pl[dd][r] & 0xffffff00u);
            tie(yc[p & 1][0][dd]);
        };
        auto chk = [&](auto ic) {
            constexpr int i = CV(ic);
            if constexpr (i < 2) { ck[2 * i] = __builtin_amdgcn_perm(pl[i][1], pl[i][0], 0x0c0c0400u); ck[2 * i + 1] = __builtin_amdgcn_perm(pl[i][3], pl[i][2], 0x0c0c0400u); tie(ck[2 * i]); tie(ck[2 * i + 1]); }
            if constexpr (i == 2) { ck[0] = __builtin_amdgcn_perm(ck[1], ck[0], 0x05040100u); ck[2] = __builtin_amdgcn_perm(ck[3], ck[2], 0x05040100u); tie(ck[0]); tie(ck[2]); }
            if constexpr (i == 3) { ck[0] = (ck[0] ^ tag) | (ck[2] ^ tag); tie(ck[0]); }
        };
        auto side = [&](auto pc) {
            constexpr int P = CV(pc), n = P >> 3, m = P & 7, p = n >> 3;
            if constexpr (P == AT(0, 6)) { w2k = lbase + wid * 2048; ta = 512 * (idx >> 3) + 128 * (idx & 1) + 16 * wid + 4 * ((idx >> 1) & 3); ties(w2k); ties(ta); }
            if constexpr (P == AT(1, 4)) { tb = ((xcd & 2) * 4 + (idx & 7)) * 128 + 64 * kg; ties(tb); }
            if constexpr (P == AT(9, 5)) { pol = lbase + POLL + wid * 256; zfo = F_Z + (xcd * 128 + wid * 16) * 8; ties(pol); ties(zfo); }
            if constexpr (P == AT(10, 7)) { ppo = b * 32768 + wid * 2048; ties(ppo); }
            if constexpr (P == AT(0, 4)) { a_q = g4 * 2048 + l15 * 16; hs0 = ((tq + (wid >> 1)) & 7) * 32768 + (wid & 1) * 4096 + qx * 8192; hs1 = hs0 ^ 131072; tie(a_q); ties(hs0); ties(hs1); }
            if constexpr (P == AT(1, 4)) { a_x = (lane & 32) + ((lane >> 2) & 7); tie(a_x); }
            if constexpr (P == AT(1, 6)) { a_x = a_x * 2048 + (l16 & 48); tie(a_x); }
            if constexpr (P == AT(2, 4)) { tg2 = u32x2{tag, ~tag}; tie(tg2); }
            if constexpr (P == AT(2, 5) || P == AT(5, 5)) dma<QST + (n == 5) * 1024, false>(w2k, a_q, mk_rsrc(qin, 4 << 20), (ta + 256 * (n == 5)) * 2048 + xcd * 256);
            if constexpr (P == AT(2, 7) || P == AT(6, 5)) dma<ZST + (n == 6) * 1024, false>(w2k, a_x, mk_rsrc(x, 4 << 20), (tb + ((2 + (n == 6)) ^ qx) * 8) * 2048 + dt * 256 + lw * 64);
            if constexpr (P == AT(3, 4)) __builtin_amdgcn_raw_buffer_store_b64(tg2, mk_brs(ws + WS_F + F_Z + (xcd * 128 + (idx >> 2) * 16 + lw * 4 + (idx & 3)) * 8, kg * 8), l16, 0, 16);
            if constexpr (P == AT(4, 4)) { expect = (lane & 1) ? tg2[1] : tg2[0]; tie(expect); }
            if constexpr (P == AT(9, 5)) { a_p = (lane & 31) * 4; tie(a_p); }
            if constexpr (P == AT(9, 6)) dma1<0>(pol, a_p, rsf, zfo);
            if constexpr (P == AT(10, 4)) { a_z = lane ^ (lane >> 5) ^ (wid * 4); tie(a_z); }
            if constexpr (P == AT(10, 5)) { a_z = (a_z & 7) << 4; tie(a_z); }
            if constexpr (P == AT(10, 6)) { a_z |= (lane >> 3) << 12; tie(a_z); }
            if constexpr (P == AT(13, 4)) dsr32<0>(pf, pol + a_p);
            if constexpr (P == AT(14, 4)) tag_chk(pf, expect, a_p, rsf, zfo);
            if constexpr (P == AT(14, 4) || P == AT(14, 6)) dma<ZST + (m - 4) * 512, true>(w2k, a_z ^ ((m - 4) * 16), mk_rsrc(ws + WS_ZT + (size_t)xcd * 524288, 524288), idx * 128 + (wid * 16 + (m - 4) * 4) * 4096);
            if constexpr (P == AT(6, 4)) { a_iw = w2k + IMG + l16; a_ir = a_iw ^ 8192; tie(a_iw); tie(a_ir); }
            if constexpr (n >= 8 && (n & 7) < 2 && m == 4) dsw128<(n & 1) * 1024>(a_iw, yc[(p - 1) & 1][1][n & 1]);
            if constexpr (n >= 8 && (n & 7) == 3 && (m == 4 || m == 6)) dsr128<(m - 4) * 512>(im[(m - 4) / 2], a_ir);
            if constexpr (n >= 8 && (n & 7) == 4 && m == 4) { tie(im[0]); tie(im[1]); }
            if constexpr (P == AT(10, 7)) { msk = 0xffffff00u; tie(msk); }
            if constexpr (p == 1 || p == 2) {
                constexpr int j = (P & 63) - AT(4, 4);
                if constexpr (j == 4 || j == 8) __builtin_amdgcn_raw_buffer_store_b128(po, rp, l16, ppo + (p - 1) * 16384 + (j - 4) * 256, 16);
                if constexpr (j >= 0 && j < 8) { po[j & 3] = and_or(f2u(yc[(p - 1) & 1][0][j >> 2][j & 3] + im[j >> 2][j & 3]), msk, (tag >> (8 * (j & 3))) & 255u); tie(po); }
            }
            if constexpr (P == AT(22, 1) || P == AT(28, 1)) pp_ld0(pl[0], l16, rsp, ppo ^ (n == 28 ? 49152 : 32768));
            if constexpr (P == AT(22, 3) || P == AT(28, 3)) pp_ld1(pl[1], l16, rsp, ppo ^ (n == 28 ? 49152 : 32768));
            if constexpr (n == PC0 && m < 4) chk(IC(m));
            if constexpr (P == AT(PC0, 4)) pp_chk(pl[0], pl[1], ck[0], tag, l16, rsp, ppo ^ 32768);
            if constexpr (P > AT(PC0, 4) && P <= AT(PC0 + 1, 4)) addp(IC(2), IC(P - AT(PC0, 5)));
            if constexpr (P == AT(11, 0)) { a_xr = g4 * 256 + l15 * 4; tie(a_xr); }
            if constexpr (P == AT(11, 1)) { a_xr += w2k + ZST; t0 = u2f(lbase + DSK + lw * 64 + l15 * 4); tie(a_xr); tie(t0); }
            if constexpr (P == AT(12, 4)) dsr32<0>(dsw_, f2u(t0));
            if constexpr (P == AT(12, 4) || P == AT(12, 6)) dsr2<(m - 4) * 16, (m - 4) * 16 + 16>(xs[(m - 4) / 2], a_xr);
            if constexpr (P == AT(13, 5) || P == AT(13, 6)) dsr2<(m - 5) * 32, (m - 5) * 32 + 16>(xs[m - 3], a_xr + 1024);
            if constexpr (P == AT(13, 4)) { tie(xs[0]); tie(xs[1]); tie(dsw_); }
            if constexpr (P == AT(14, 4)) { tie(xs[2]); tie(xs[3]); }
            if constexpr (P == AT(13, 7)) { d0 = bflo(dsw_); d1 = bfhi(dsw_); tie(d0); tie(d1); }
            {
                constexpr int j = n == 14 ? (m < 4 ? m : (m & 1) ? (m + 3) / 2 : -1) : n == 15 ? (m < 6 ? 6 + m : -1) : n == 16 ? 12 + m : n == 17 && m < 4 ? 20 + m : -1, r = j / 3;
                if constexpr (j >= 0) {
                    if constexpr (j % 3 == 0) { t0 = bflo(xs[r >> 1][r & 1]); t1 = bfhi(xs[r >> 1][r & 1]); tie(t0); tie(t1); }
                    if constexpr (j % 3 == 1) { t0 *= d0; t1 *= d1; tie(t0); tie(t1); }
                    if constexpr (j % 3 == 2 && r < 4) { yc[0][0][0][r] = rbf(t0); yc[0][0][1][r] = rbf(t1); tie(yc[0][0][0]); tie(yc[0][0][1]); }
                    if constexpr (j % 3 == 2 && r >= 4) { sk3[r - 4] = pack2(t0, t1); tie(sk3[r - 4]); }
                }
            }
            if constexpr (n == 21 && m >= 4) { yc[1][0][0][m - 4] = bflo(sk3[m - 4]); yc[1][0][1][m - 4] = bfhi(sk3[m - 4]); tie(yc[1][0][0]); tie(yc[1][0][1]); }
            if constexpr (P == AT(18, 0)) { a_zr = (wid ^ lane) & 7; tie(a_zr); }
            if constexpr (P == AT(18, 1)) { a_zr = (a_zr << 4) + (a_p << 7); tie(a_zr); }
            if constexpr (P == AT(18, 2)) { a_qr = lane >> 5; a_zr += lbase + ZST; tie(a_qr); tie(a_zr); }
            if constexpr (P == AT(18, 3)) { a_zr += a_qr * 8; a_oa = a_p * 4; tie(a_zr); tie(a_oa); }
            if constexpr (P == AT(18, 4)) { a_oa += a_qr << 21; a_qr = (a_qr << 10) + w2k + QST; tie(a_oa); tie(a_qr); }
            if constexpr (P == AT(18, 5)) { a_qr += a_p * 2; tie(a_qr); }
            if constexpr (n == 19 && m >= 4) dsr64<(m - 4) * 128>(zv[m - 4], a_zr);
            if constexpr (P == AT(20, 4)) { tie(zv[0]); tie(zv[1]); tie(zv[2]); tie(zv[3]); }
            if constexpr (n >= 21 && n <= 24 && m == 5) dsr64<(n - 21) * 256>(qv[(n - 21) & 1], a_qr);
            if constexpr (n >= 22 && n <= 25) {
                constexpr int r = n - 22, c = m >> 1;
                if constexpr (m == 0) { wait_lr<2>(); tie(qv[r & 1]); }
                if constexpr ((m & 1) == 0) { t0 = (c & 1) ? bfhi(qv[r & 1][c >> 1]) : bflo(qv[r & 1][c >> 1]); t1 = (r & 1) ? bfhi(zv[c][r >> 1]) : bflo(zv[c][r >> 1]); tie(t0); tie(t1); }
                else { ao[c] = rbf(t0 * t1); tie(ao); }
                if constexpr (m == 7) __builtin_amdgcn_raw_buffer_store_b128(__builtin_bit_cast(u32x4, ao), ro, a_oa, (ta + r) * 8192 + xcd * 512, OSC);
            }
            if constexpr (P == AT(27, 5)) { a_ob = (lane & 32) + ((lane >> 2) & 4); tie(a_ob); }
            if constexpr (P == AT(27, 7)) { a_ob = a_ob * 8192 + l15 * 8; tie(a_ob); }
            if constexpr ((n == 28 || n == 29) && (m == 5 || m == 7)) finB(IC(2), IC((n - 28) * 2 + (m == 7)));
        };
        if (wid < 4) TS(7);
        barrier();
        static_for<0, 8>([&](auto ic) { ldh(IC(0), ic); });
        static_for<0, 32>([&](auto nc) {
            constexpr int n = CV(nc), p = n >> 3;
            wait_lr<((n & 3) == 3 && n < 31) ? 0 : 4 + g2_lx(n - 1)>();
            if constexpr ((n & 3) == 3 && n < 31) {
                constexpr int need = n == 3 ? -1 : n == 7 ? AT(1, 7) : n == 11 ? AT(6, 5) : n == 19 ? AT(14, 6) : AT(n - 7, 7);
                TL(21 + n / 4 * 3);
                wait_v<g2_allow(n, need)>();
                TL(22 + n / 4 * 3);
                barrier();
                TL(23 + n / 4 * 3);
            }
            if constexpr (n == 0) TL(20);
            if constexpr (n == 31) TL(42);
            if constexpr (n == 13) wait_v<g2_allow(13, AT(9, 6))>();
            if constexpr (n == PC0) { wait_v<g2_allow(PC0, AT(22, 3))>(); pp_tie(pl[0], pl[1]); }
            sched();
            static_for<0, 8>([&](auto mc) {
                constexpr int m = CV(mc), ks = m >> 2, tt = (m >> 1) & 1, dd = m & 1, P = n * 8 + m;
                if constexpr (m == 4 && ((n & 3) != 3 || n == 31)) wait_lr<n < 31 ? 8 : 0>();
                mfma<(n & 7) == 0 && ks == 0 && (p < 2 || tt)>(yc[p & 1][tt][dd], cat(hf[n & 1][4 * ks + 2 * tt], hf[n & 1][4 * ks + 2 * tt + 1]), cf[(n & 7) * 4 + 2 * ks + dd]);
                sched();
                if constexpr (n < 31 && m < 4) { ldh(IC(n + 1), IC(2 * m)); ldh(IC(n + 1), IC(2 * m + 1)); }
                if constexpr (g2_fill(P) >= 0) fill(IC(g2_fill(P) >> 2), IC(g2_fill(P) & 3));
                side(IC(P));
                sched();
            });
        });
        if (wid >= 4) TS(5); else TS(8);
        wait_v<4>();
        pp_tie(pl[0], pl[1]);
        static_for<0, 4>(chk);
        asm volatile("s_nop 0");
        dsw128<QST - IMG>(a_iw, yc[1][1][0]);
        dsw128<QST - IMG + 1024>(a_iw, yc[1][1][1]);
        pp_chk(pl[0], pl[1], ck[0], tag, l16, rsp, ppo ^ 49152);
        static_for<0, 8>([&](auto ic) { addp(IC(3), ic); });
        wait_l<0>();
        barrier();
        dsr128<QST - IMG>(im[0], a_ir);
        dsr128<QST - IMG + 1024>(im[1], a_ir);
        wait_l<0>();
        tie(im[0]); tie(im[1]);
        static_for<0, 4>([&](auto rc) { finB(IC(3), rc); });
        if (b == 255 && wid == 0) __builtin_amdgcn_raw_buffer_store_b32(tag, rf, lane ? OOB : 0u, F_EP, 16);
    }
}

}

#if defined(__HIP_DEVICE_COMPILE__)
#pragma clang attribute pop
#endif

extern "C" void kernel_launch(void* const* d_in, const int* in_sizes, int n_in, void* d_out, int out_size, void* d_ws, size_t ws_size,
                              hipStream_t stream) {
    (void)in_sizes; (void)n_in; (void)out_size; (void)ws_size;
    using namespace fk;
    const uint16_t* q = (const uint16_t*)d_in[0];
    const uint16_t* kin = (const uint16_t*)d_in[1];
    const uint16_t* vin = (const uint16_t*)d_in[2];
    const uint16_t* sk = (const uint16_t*)d_in[3];
    const uint16_t* ssmk = (const uint16_t*)d_in[4];
    const uint16_t* ssmD = (const uint16_t*)d_in[5];
    const uint16_t* x = (const uint16_t*)d_in[6];
    const uint16_t* lre = (const uint16_t*)d_in[7];
    const uint16_t* lim = (const uint16_t*)d_in[8];
    const uint16_t* bre = (const uint16_t*)d_in[9];
    const uint16_t* bim = (const uint16_t*)d_in[10];
    const uint16_t* cre = (const uint16_t*)d_in[11];
    const uint16_t* cim = (const uint16_t*)d_in[12];
    const uint16_t* logdt = (const uint16_t*)d_in[13];
    const uint16_t* dskip = (const uint16_t*)d_in[14];
    char* ws = (char*)d_ws;
    hipLaunchKernelGGL(k1, dim3(256), dim3(512), 0, stream, x, bre, bim, lre, lim, logdt, cre, cim, dskip, ssmk, ssmD, q, kin, vin, sk, ws, (float*)d_out TS_PASS(0));
}
```

```cpp
#include <hip/hip_runtime.h>
#include <stdint.h>
#include <type_traits>

#if defined(__HIP_DEVICE_COMPILE__)
#pragma clang attribute push(__attribute__((target("no-packed-fp32-ops"))), apply_to = function)
#endif

namespace fk {

typedef __attribute__((ext_vector_type(8))) short bf16x8;
typedef __attribute__((ext_vector_type(4))) short bf16x4;
typedef __attribute__((ext_vector_type(4))) float f32x4;
typedef __attribute__((ext_vector_type(4))) unsigned u32x4;
typedef __attribute__((ext_vector_type(2))) unsigned u32x2;
typedef __attribute__((ext_vector_type(2))) __bf16 bf16x2_t;
typedef __attribute__((address_space(1))) unsigned gu32;
typedef __attribute__((address_space(1))) unsigned long long gu64;
#define RLX_AGENT __ATOMIC_RELAXED, __HIP_MEMORY_SCOPE_AGENT

__device__ __forceinline__ float u2f(unsigned v) { return __builtin_bit_cast(float, v); }
__device__ __forceinline__ unsigned f2u(float v) { return __builtin_bit_cast(unsigned, v); }
__device__ __forceinline__ bool wall(bool p) { return __builtin_amdgcn_ballot_w64(p) == ~0ull; }
__device__ __forceinline__ float bflo(unsigned w) { return u2f(w << 16); }
__device__ __forceinline__ float bfhi(unsigned w) { return u2f(w & 0xffff0000u); }
__device__ __forceinline__ unsigned pack2(float lo, float hi) {
    bf16x2_t v; v[0] = (__bf16)lo; v[1] = (__bf16)hi; return __builtin_bit_cast(unsigned, v);
}
__device__ __forceinline__ float rbf(float f) { float r; asm("v_cvt_pk_bf16_f32 %0, 0, %1" : "=v"(r) : "v"(f)); return r; }
__device__ __forceinline__ float fma_(float a, float b, float c) { return __builtin_fmaf(a, b, c); }

template <int I, int N, class F>
__device__ __forceinline__ void static_for(F&& f) {
    if constexpr (I + 1 == N) f(std::integral_constant<int, I>{});
    else if constexpr (I < N) { static_for<I, I + (N - I) / 2>(f); static_for<I + (N - I) / 2, N>(f); }
}
#define IC(v) std::integral_constant<int, (v)>{}
#define CV(c) decltype(c)::value

#define TS_ARG
#define TS_PASS(off)
#define TS_DECL
#define TS(i)
#define TSV(i, v)
#define TL_DECL
#define TL(i)
#define TS_FLUSH(slot)

#define TG_DECL
#define TG(i)
template <int N> __device__ __forceinline__ void wait_v() { asm volatile("s_waitcnt vmcnt(%0)" ::"n"(N > 63 ? 63 : N) : "memory"); }
template <int N> __device__ __forceinline__ void wait_l() { asm volatile("s_waitcnt lgkmcnt(%0)" ::"n"(N > 14 ? 14 : N) : "memory"); }
template <int N> __device__ __forceinline__ void wait_lr() { __builtin_amdgcn_s_waitcnt(0xc07f | ((N > 14 ? 14 : N) << 8)); }
template <class T> __device__ __forceinline__ void tie(T& x) { asm volatile("" : "+v"(x)); }
template <class T> __device__ __forceinline__ void keep(const T& x) { asm volatile("" :: "v"(x)); }
template <int OFF, class T> __device__ __forceinline__ void dsr128(T& d, unsigned a) { asm volatile("ds_read_b128 %0, %1 offset:%2" : "=v"(d) : "v"(a), "n"(OFF)); }
template <int OFF, class T> __device__ __forceinline__ void dsr64(T& d, unsigned a) { asm volatile("ds_read_b64 %0, %1 offset:%2" : "=v"(d) : "v"(a), "n"(OFF)); }
template <int OFF> __device__ __forceinline__ void dsr32(unsigned& d, unsigned a) { asm volatile("ds_read_b32 %0, %1 offset:%2" : "=v"(d) : "v"(a), "n"(OFF)); }
template <int O0, int O1, class T> __device__ __forceinline__ void dsr2(T& d, unsigned a) { asm volatile("ds_read2_b32 %0, %1 offset0:%2 offset1:%3" : "=v"(d) : "v"(a), "n"(O0), "n"(O1)); }
template <int OFF> __device__ __forceinline__ void dsr64tr(bf16x4& d, unsigned a) { asm volatile("ds_read_b64_tr_b16 %0, %1 offset:%2" : "=v"(d) : "v"(a), "n"(OFF)); }
template <int OFF, class T> __device__ __forceinline__ void dsw128(unsigned a, const T& v) { asm volatile("ds_write_b128 %0, %1 offset:%2" :: "v"(a), "v"(v), "n"(OFF) : "memory"); }
__device__ __forceinline__ bf16x8 cat(const bf16x4& lo, const bf16x4& hi) { return __builtin_shufflevector(lo, hi, 0, 1, 2, 3, 4, 5, 6, 7); }
__device__ __forceinline__ void barrier() { asm volatile("" ::: "memory"); __builtin_amdgcn_s_barrier(); asm volatile("" ::: "memory"); }
__device__ __forceinline__ int fresh(int v) { asm volatile("" : "+s"(v)); return v; }
__device__ __forceinline__ void sched() { __builtin_amdgcn_sched_barrier(0); }
template <bool FIRST> __device__ __forceinline__ void mfma(f32x4& c, const bf16x8& a, const bf16x8& b) {
    if constexpr (FIRST) asm volatile("v_mfma_f32_16x16x32_bf16 %0, %1, %2, 0" : "=v"(c) : "v"(a), "v"(b));
    else asm volatile("v_mfma_f32_16x16x32_bf16 %0, %1, %2, %0" : "+v"(c) : "v"(a), "v"(b));
}
struct cf32 { float r, i; };
__device__ __forceinline__ cf32 cmul(cf32 a, cf32 b) { return cf32{fma_(a.r, b.r, -(a.i * b.i)), fma_(a.r, b.i, a.i * b.r)}; }
__device__ __forceinline__ cf32 cfma(cf32 a, cf32 b, cf32 c) { return cf32{fma_(a.r, b.r, fma_(-a.i, b.i, c.r)), fma_(a.r, b.i, fma_(a.i, b.r, c.i))}; }
__device__ __forceinline__ void sw16(float v, float& e, float& o) { const auto r = __builtin_amdgcn_permlane16_swap(f2u(v), f2u(v), false, false); e = u2f(r[0]); o = u2f(r[1]); }
__device__ __forceinline__ void sw32(float v, float& e, float& o) { const auto r = __builtin_amdgcn_permlane32_swap(f2u(v), f2u(v), false, false); e = u2f(r[0]); o = u2f(r[1]); }
constexpr unsigned SPIN_CAP = 1u << 16;
constexpr unsigned OOB = 0x40000000u;

constexpr size_t WS_H = 0;
constexpr size_t WS_KVT = (size_t)8 << 20;
constexpr size_t WS_ZT = (size_t)12 << 20;
constexpr size_t WS_PP = (size_t)16 << 20;
constexpr size_t WS_E = (size_t)24 << 20;
constexpr size_t WS_F = WS_E + (256 << 10);
constexpr int F_H = 0;
constexpr int F_K = 16384;
constexpr int F_Z = 24576;
constexpr int F_EP = F_Z + 32768;
constexpr int LDS_TOTAL = 163840, BUF = 32768;
constexpr int KC = 98304, KCH = 8320, KCP = 4160;
constexpr int U0 = KC + 4 * KCH;
constexpr int FULL = U0 + 18432 + 8192, FST = 388;
constexpr int HGO = FULL + 1792, DSK = HGO + 256, POLL = DSK + 256;
static_assert(POLL + 8 * 256 <= LDS_TOTAL && 4 * FST <= 1792, "lds");
constexpr int g1_buf(int s) { return ((0x0213 >> ((s & 3) * 4)) & 3) * BUF; }
constexpr int IMG = KC, QST = KC + 16384, ZST = U0, PLD = 2 * BUF;
#define CODE_KB 46
#define EXACT 1
#define HP 1
#define CP 2
#define FLQ 11
#define R01 3
#define ZP 7
#define SLP 8
#define STR_(x) #x
#define STR(x) STR_(x)
#define NOST 0
#define G2M 0
#define RD 2
#define PRG 1
#define OSC 16
#define NOA 0
#define PPH 0
#define NOX 0

__device__ __forceinline__ u32x4 mk_rsrc(const void* p, unsigned bytes) {
    const unsigned long long a = (unsigned long long)p;
    return u32x4{(unsigned)a, (unsigned)(a >> 32) & 0xffffu, bytes, 0x00020000u};
}
__device__ __forceinline__ __amdgpu_buffer_rsrc_t mk_brs(const void* p, unsigned bytes) { return __builtin_amdgcn_make_buffer_rsrc((void*)p, (short)0, bytes, 0x00020000); }
template <int IMM, bool SC1> __device__ __forceinline__ void dma(unsigned lds, unsigned voff, const u32x4& rs, unsigned soff) {
    if constexpr (SC1) asm volatile("s_add_u32 m0, %0, %1\n\ts_nop 0\n\tbuffer_load_dwordx4 %2, %3, %4 offen sc1 lds" :: "s"(lds), "n"(IMM), "v"(voff), "s"(rs), "s"(soff) : "memory", "scc");
    else asm volatile("s_add_u32 m0, %0, %1\n\ts_nop 0\n\tbuffer_load_dwordx4 %2, %3, %4 offen lds" :: "s"(lds), "n"(IMM), "v"(voff), "s"(rs), "s"(soff) : "memory", "scc");
}
template <int IMM> __device__ __forceinline__ void dma1(unsigned lds, unsigned voff, const u32x4& rs, unsigned soff) {
    asm volatile("s_add_u32 m0, %0, %1\n\ts_nop 0\n\tbuffer_load_dword %2, %3, %4 offen sc1 lds" :: "s"(lds), "n"(IMM), "v"(voff), "s"(rs), "s"(soff) : "memory", "scc");
}
__device__ __forceinline__ void flag_set(unsigned waddr, unsigned v) { asm volatile("ds_write_b32 %0, %1" :: "v"(waddr), "v"(v) : "memory"); }
__device__ __forceinline__ void flag_chk(unsigned& f, unsigned raddr, unsigned need) {
    unsigned cnt;
    asm volatile("v_cmp_le_u32 vcc, %3, %0\n\t"
                 "s_cmp_eq_u64 vcc, -1\n\t"
                 "s_cbranch_scc1 1f\n\t"
                 "s_mov_b32 %1, 0x100000\n"
                 "0:\n\t"
                 "ds_read_b32 %0, %2\n\t"
                 "s_sub_u32 %1, %1, 1\n\t"
                 "s_waitcnt lgkmcnt(0)\n\t"
                 "s_cbranch_scc1 1f\n\t"
                 "v_cmp_le_u32 vcc, %3, %0\n\t"
                 "s_cmp_eq_u64 vcc, -1\n\t"
                 "s_cbranch_scc0 0b\n"
                 "1:" : "+v"(f), "=&s"(cnt) : "v"(raddr), "s"(need) : "vcc", "scc", "memory");
}
template <int OFF> __device__ __forceinline__ void zero_chk(unsigned& f, unsigned raddr) {
    unsigned cnt;
    asm volatile("v_cmp_ne_u32 vcc, 0, %0\n\t"
                 "s_cbranch_vccz 1f\n\t"
                 "s_mov_b32 %1, 0x100000\n"
                 "0:\n\t"
                 "ds_read_b32 %0, %2 offset:%3\n\t"
                 "s_sub_u32 %1, %1, 1\n\t"
                 "s_waitcnt lgkmcnt(0)\n\t"
                 "s_cbranch_scc1 1f\n\t"
                 "v_cmp_ne_u32 vcc, 0, %0\n\t"
                 "s_cbranch_vccnz 0b\n"
                 "1:" : "+v"(f), "=&s"(cnt) : "v"(raddr), "n"(OFF) : "vcc", "scc", "memory");
}
template <int IMM, class T> __device__ __forceinline__ void gld(T& d, unsigned voff, const u32x4& rs, unsigned soff) {
    if constexpr (sizeof(T) == 16) asm volatile("buffer_load_dwordx4 %0, %1, %2, %3 offen offset:%4" : "=v"(d) : "v"(voff), "s"(rs), "s"(soff), "n"(IMM) : "memory");
    else if constexpr (sizeof(T) == 8) asm volatile("buffer_load_dwordx2 %0, %1, %2, %3 offen offset:%4" : "=v"(d) : "v"(voff), "s"(rs), "s"(soff), "n"(IMM) : "memory");
    else asm volatile("buffer_load_ushort %0, %1, %2, %3 offen offset:%4" : "=v"(d) : "v"(voff), "s"(rs), "s"(soff), "n"(IMM) : "memory");
}
__device__ __forceinline__ void gld32(unsigned& d, unsigned voff, const u32x4& rs, unsigned soff) { asm volatile("buffer_load_dword %0, %1, %2, %3 offen" : "=v"(d) : "v"(voff), "s"(rs), "s"(soff) : "memory"); }
__device__ __forceinline__ void sld(unsigned& d, const void* p) { asm volatile("s_load_dword %0, %1, 0x0" : "=s"(d) : "s"(p) : "memory"); }
__device__ __forceinline__ void stouch(unsigned long long p) { asm volatile("s_load_dword s101, %0, 0x0\n\ts_load_dword s101, %0, 0x80\n\ts_load_dword s101, %0, 0x100" :: "s"(p) : "s101", "memory"); }
__device__ __forceinline__ void ties(unsigned& x) { asm volatile("" : "+s"(x)); }
template <int SLQ> __device__ __forceinline__ void gate(unsigned raddr, unsigned need) {
    unsigned cnt, f;
    asm volatile("s_mov_b32 %1, 0x40000\n"
                 "0:\n\t"
                 "ds_read_b32 %0, %2\n\t"
                 "s_sub_u32 %1, %1, 1\n\t"
                 "s_waitcnt lgkmcnt(0)\n\t"
                 "s_cbranch_scc1 1f\n\t"
                 "v_cmp_le_u32 vcc, %3, %0\n\t"
                 "s_cmp_eq_u64 vcc, -1\n\t"
                 "s_cbranch_scc1 1f\n\t"
                 "s_sleep %4\n\t"
                 "s_branch 0b\n"
                 "1:" : "=&v"(f), "=&s"(cnt) : "v"(raddr), "s"(need), "n"(SLQ) : "vcc", "scc", "memory");
}
__device__ __forceinline__ void tag_chk(unsigned& f, unsigned expect, unsigned voff, const u32x4& rs, unsigned soff) {
    unsigned cnt;
    asm volatile("v_cmp_eq_u32 vcc, %2, %0\n\t"
                 "s_cmp_eq_u64 vcc, -1\n\t"
                 "s_cbranch_scc1 1f\n\t"
                 "s_mov_b32 %1, 0x10000\n"
                 "0:\n\t"
                 "s_sleep " STR(SLP) "\n\t"
                 "buffer_load_dword %0, %3, %4, %5 offen sc1\n\t"
                 "s_sub_u32 %1, %1, 1\n\t"
                 "s_waitcnt vmcnt(0)\n\t"
                 "s_cbranch_scc1 1f\n\t"
                 "v_cmp_eq_u32 vcc, %2, %0\n\t"
                 "s_cmp_eq_u64 vcc, -1\n\t"
                 "s_cbranch_scc0 0b\n"
                 "1:" : "+v"(f), "=&s"(cnt) : "v"(expect), "v"(voff), "s"(rs), "s"(soff) : "vcc", "scc", "memory");
}

__device__ __forceinline__ void pp_ld0(u32x4& d, unsigned voff, const u32x4& rs, unsigned soff) { asm volatile("buffer_load_dwordx4 v[240:243], %1, %2, %3 offen sc1" : "={v[240:243]}"(d) : "v"(voff), "s"(rs), "s"(soff) : "memory"); }
__device__ __forceinline__ void pp_ld1(u32x4& d, unsigned voff, const u32x4& rs, unsigned soff) { asm volatile("buffer_load_dwordx4 v[244:247], %1, %2, %3 offen offset:1024 sc1" : "={v[244:247]}"(d) : "v"(voff), "s"(rs), "s"(soff) : "memory"); }
__device__ __forceinline__ void pf_ld(unsigned& d, unsigned voff, const u32x4& rs, unsigned soff) { asm volatile("buffer_load_dword v248, %1, %2, %3 offen sc1" : "={v248}"(d) : "v"(voff), "s"(rs), "s"(soff) : "memory"); }
__device__ __forceinline__ void pp_chk(u32x4& p0, u32x4& p1, unsigned bad, unsigned tag, unsigned voff, const u32x4& rs, unsigned soff) {
    unsigned cnt, t;
    asm volatile("v_cmp_ne_u32 vcc, 0, %3\n\t"
                 "s_cbranch_vccz 1f\n\t"
                 "s_mov_b32 %2, 0x10000\n"
                 "0:\n\t"
                 "buffer_load_dwordx4 v[240:243], %6, %7, %8 offen sc1\n\t"
                 "buffer_load_dwordx4 v[244:247], %6, %7, %8 offen offset:1024 sc1\n\t"
                 "s_waitcnt vmcnt(0)\n\t"
                 "s_and_b32 %4, %5, 0xff\n\t"
                 "v_xor_b32 v240, %4, v240\n\t"
                 "v_xor_b32 v244, %4, v244\n\t"
                 "s_bfe_u32 %4, %5, 0x80008\n\t"
                 "v_xor_b32 v241, %4, v241\n\t"
                 "v_xor_b32 v245, %4, v245\n\t"
                 "s_bfe_u32 %4, %5, 0x80010\n\t"
                 "v_xor_b32 v242, %4, v242\n\t"
                 "v_xor_b32 v246, %4, v246\n\t"
                 "s_lshr_b32 %4, %5, 24\n\t"
                 "v_xor_b32 v243, %4, v243\n\t"
                 "v_xor_b32 v247, %4, v247\n\t"
                 "v_or3_b32 %3, v240, v241, v242\n\t"
                 "v_or3_b32 %3, %3, v243, v244\n\t"
                 "v_or3_b32 %3, %3, v245, v246\n\t"
                 "v_or_b32 %3, %3, v247\n\t"
                 "v_and_b32 %3, 0xff, %3\n\t"
                 "s_sub_u32 %2, %2, 1\n\t"
                 "s_cbranch_scc1 1f\n\t"
                 "v_cmp_ne_u32 vcc, 0, %3\n\t"
                 "s_cbranch_vccnz 0b\n"
                 "1:" : "+{v[240:243]}"(p0), "+{v[244:247]}"(p1), "=&s"(cnt), "+v"(bad), "=&s"(t)
                 : "s"(tag), "v"(voff), "s"(rs), "s"(soff) : "vcc", "scc", "memory");
}
template <int H> __device__ __forceinline__ void pp_tie(u32x4& p) { if constexpr (H) asm volatile("" : "+{v[244:247]}"(p)); else asm volatile("" : "+{v[240:243]}"(p)); }
__device__ __forceinline__ float sel8(float a, float b, unsigned long long mask) {
    float r;
    asm volatile("s_mov_b64 vcc, %3\n\tv_cndmask_b32_dpp %0, %1, %2, vcc row_ror:8 row_mask:0xf bank_mask:0xf" : "=v"(r) : "v"(a), "v"(b), "s"(mask) : "vcc");
    return r;
}
__device__ __forceinline__ unsigned sel0(unsigned a, unsigned long long mask) { unsigned r; asm volatile("v_cndmask_b32_e64 %0, 0, %1, %2" : "=v"(r) : "v"(a), "s"(mask)); return r; }
constexpr unsigned long long diag_mask(int R) {
    unsigned long long m = 0;
    for (int l = 0; l < 64; ++l) if ((l >> 4) == ((l & 7) >> 1) && 2 * (l & 1) + ((l >> 3) & 1) == R) m |= 1ull << l;
    return m;
}
template <int IMM, int X> __device__ __forceinline__ void dma_x(unsigned& so, unsigned lds, unsigned voff, const u32x4& rs, unsigned base) {
    asm volatile("s_add_u32 m0, %1, %2\n\ts_xor_b32 %0, %5, %6\n\tbuffer_load_dwordx4 %3, %4, %0 offen sc1 lds" : "=&s"(so) : "s"(lds), "n"(IMM), "v"(voff), "s"(rs), "s"(base), "n"(X) : "memory", "scc");
}
template <int OFF> __device__ __forceinline__ void dma_n(unsigned so, unsigned voff, const u32x4& rs) {
    asm volatile("buffer_load_dwordx4 %0, %1, %2 offen offset:%3 sc1 lds" :: "v"(voff), "s"(rs), "s"(so), "n"(OFF) : "memory");
}
__device__ __forceinline__ unsigned and_or(unsigned a, unsigned m, unsigned o) { unsigned r; asm("v_and_or_b32 %0, %1, %2, %3" : "=v"(r) : "v"(a), "v"(m), "s"(o)); return r; }

#define HOPS 0
constexpr int NHOP = 40;
template <int K> __device__ __forceinline__ void hop() {
    if constexpr (HOPS) asm volatile(".Lfkh%0:\n\ts_cbranch_execz .Lfkh%1" :: "n"(K), "n"(K + 4 < NHOP ? K + 4 : 63));
}
template <int K> __device__ __forceinline__ void page() {
    if constexpr (HOPS && K >= 0) asm volatile("s_branch 1f\n\t.p2align 12\n.Lfkh%0:\n\ts_cbranch_execz .Lfkh%1\n1:" :: "n"(K), "n"(K + 4 < NHOP ? K + 4 : 63));
    if constexpr (HOPS && K < 0) asm volatile("s_branch 1f\n\t.p2align 12\n1:");
}
__device__ __forceinline__ void hop_all(int w) {
    if constexpr (HOPS) asm volatile("s_mov_b64 exec, 0\n\ts_cmp_eq_u32 %0, 0\n\ts_cbranch_scc1 .Lfkh0\n\ts_cmp_eq_u32 %0, 1\n\ts_cbranch_scc1 .Lfkh1\n\t"
                                     "s_cmp_eq_u32 %0, 2\n\ts_cbranch_scc1 .Lfkh2\n\ts_branch .Lfkh3\n.Lfkh63:\n\ts_mov_b64 exec, -1" :: "s"(w) : "scc");
}
enum { OP_NONE, OP_A, OP_B, OP_FLAG };
struct Op { int kind, a, b; };
constexpr Op g1_op(int G) {
    const int st = (G + 32) / 32 - 1, q = G - 32 * st, i = q >> 2, j = q & 3;
    if (i < 5) {
        if (st < 0) return {OP_NONE, 0, 0};
        if ((j & 1) == 0) return {OP_A, st * 8 + i + 3, j >> 1};
        if (q == FLQ && st < 15) return {OP_FLAG, 0, 0};
        return {OP_NONE, 0, 0};
    }
    if (st == 15) return {OP_NONE, 0, 0};
    const int n = (st + 1) * 8;
    switch (q) {
        case 20: return {OP_B, st + 1, 0}; case 21: return {OP_B, st + 1, 1}; case 22: return {OP_A, n, 0}; case 23: return {OP_B, st + 1, 2};
        case 24: return {OP_B, st + 1, 3}; case 25: return {OP_A, n, 1}; case 26: return {OP_A, n + 1, 0}; case 27: return {OP_A, n + 1, 1};
        case 28: return {OP_A, n + 2, 0}; case 30: return {OP_A, n + 2, 1};
    }
    return {OP_NONE, 0, 0};
}
constexpr int g1_need(int G) {
    const int st = G >> 5, j = G & 3;
    for (int h = G - 1; h >= -12; --h) {
        const Op o = g1_op(h);
        if (o.kind == OP_A && o.a == (G >> 2) && o.b == (j >> 1)) return h;
        if (o.kind == OP_B && o.a == st && o.b == j) return h;
        if (o.kind == OP_FLAG && (G & 31) == 20 && (h >> 5) == st) return h;
    }
    return -100;
}
constexpr int g1_wait(int G) {
    int n = -100, c = 0;
    for (int h = G; h < (G ? G + 4 : 2) && h < 512; ++h) if (g1_need(h) > n) n = g1_need(h);
    for (int h = n + 1; h < G; ++h) c += g1_op(h).kind != OP_NONE;
    return n < G ? c : -1;
}
#define KV0 0
#define KVN 0x11111112
constexpr int kv_first(int k) { int n = 0; for (int i = KV0; i < k && i < KV0 + 8; ++i) n += (KVN >> (4 * (i - KV0))) & 15; return n > 8 ? 8 : n; }
constexpr int kv_last() { int k = KV0; while (kv_first(k + 1) < 8) ++k; return k; }
#define CQ -1
#define CTM 0
#define EC 2
#define ED 2
#define EB 1
#define KFD 3
#define KPL 11
#define CBQ 20
constexpr int KVL = kv_last(), KFL = KVL + KFD;
static_assert(KFL <= KPL && KPL <= 11, "");
constexpr int hv_cnt(int k0, int k1) {
    int c = 0;
    for (int k = k0; k < k1; ++k) c += 9 + (kv_first(k + 1) / 2 - kv_first(k) / 2) + (k == KFL) + (k == KPL);
    return c;
}
constexpr int cv_start(int n) { return n * 8 - n * (n - 1) / 2; }
constexpr int cv_n(int q) { int n = 0; while (cv_start(n + 1) <= q) ++n; return n; }
constexpr int cv_m(int q) { return 7 - cv_n(q) - (q - cv_start(cv_n(q))); }
enum { CO_NONE, CO_A, CO_B, CO_C };
constexpr Op cv_op(int G) {
    const int s = (G + 36) / 36 - 1, q = G - 36 * s;
    if (q >= 1 && q <= 6) return s >= 0 ? Op{CO_A, s * 8 + 5 + (q - 1) / 2, (q - 1) & 1} : Op{CO_NONE, 0, 0};
    if (q >= 11 && q <= 14) return s >= 2 ? Op{CO_C, s - 2, q - 11} : Op{CO_NONE, 0, 0};
    if (s == 7) return {CO_NONE, 0, 0};
    for (int m = 0; m < 8; ++m) if (q == cv_start(7 - m)) return {CO_B, (s + 1) * 8 + m, 0};
    for (int n = 0; n < 5; ++n) if (q == cv_start(n + 1) + 1 || q == cv_start(n + 1) + 2) return {CO_A, (s + 1) * 8 + n, q - cv_start(n + 1) - 1};
    return {CO_NONE, 0, 0};
}
constexpr int cv_need(int G) {
    const int s = G / 36, q = G % 36;
    for (int h = G - 1; h >= -36; --h) {
        const Op o = cv_op(h);
        if (o.kind == CO_A && o.a == s * 8 + cv_n(q)) return h;
        if (o.kind == CO_B && o.a == s * 8 + cv_m(q)) return h;
        if (o.kind == CO_C && q == 24 && o.a == s - 2) return h;
    }
    return -100;
}
constexpr int cv_wait(int G) {
    const int n = cv_need(G);
    for (int h = G - 1; h >= 0 && h >= G - 40; --h) if (cv_need(h) >= n) return -1;
    int c = 0;
    for (int h = n + 1; h < G; ++h) c += cv_op(h).kind != CO_NONE;
    return c;
}
#define AT(n, m) ((n) * 8 + (m))
constexpr int g2_fill(int p) {
    const int n = p >> 3, m = p & 7;
    if (m != 5 && m != 7) return -1;
    if (n < 2) return 8 + 2 * n + (m == 7);
    if (n <= 20 && ((n & 3) == 3 || (n & 3) == 0)) return ((n + 1) / 4 + 2) * 4 + ((n & 3) ? 0 : 2) + (m == 7);
    return -1;
}
constexpr int P_PP0 = AT(12, 4), P_PP1 = AT(20, 4), P_R0 = AT(25, 0), P_F2 = AT(28, 4), P_R1 = AT(30, 5), P_ZC = AT(10, 6);
constexpr int a_step(int p) {
    if (NOA) return -1;
    if (p >= AT(16, 4) && p < P_PP1) return p - AT(16, 4);
    if (p > P_PP1 + 16 && p <= P_PP1 + 33) return p - (P_PP1 + 17) + 32;
    return -1;
}
constexpr int g2_vm(int p) {
    if (g2_fill(p) >= 0) return 1;
    if (a_step(p) > 0 && a_step(p) % 12 == 0) return !(NOST & 1);
    if ((NOST & 2) && (p == P_PP0 + 8 || p == P_PP0 + 16 || p == P_PP1 + 8 || p == P_PP1 + 16)) return 0;
    if ((NOST & 4) && (p == P_F2 + 11 || p == P_F2 + 15)) return 0;
    if (NOA && (p == AT(3, 4) || p == AT(5, 5) || p == AT(5, 7) || p == AT(ZP, 4) || p == AT(11, 3) || p == AT(11, 4))) return 0;
    if (PPH && (p == P_PP0 + 8 || p == P_PP1 + 8 || p == AT(22, 5) || p == AT(27, 5))) return 0;
    switch (p) {
        case AT(3, 4):
        case AT(2, 5): case AT(2, 7): case AT(5, 5): case AT(5, 7):
        case AT(ZP, 4): case AT(11, 3): case AT(11, 4):
        case P_PP0 + 8: case P_PP0 + 16: case P_PP1 + 8: case P_PP1 + 16:
        case AT(22, 5): case AT(22, 7): case AT(27, 5): case AT(27, 7):
        case P_F2 + 11: case P_F2 + 15:
            return 1;
    }
    return 0;
}
constexpr int g2_lx(int n) {
    if (NOA && (n == 18 || n == 21 || n == 15)) return 0;
    if (NOA && n == 16) return 2;
    switch (n) {
        case 2: case 18: case 21: return 1;
        case 7: case 8: case 11: case 19: case 24: case 27: return 2;
        case 16: return 3;
        case 15: return 5;
    }
    return 0;
}
constexpr bool rd1(int n) { return RD == 1 || (RD == 3 && (n & 3) == 2); }
constexpr int g2_allow(int e, int p) { int c = 0; for (int h = p + 1; h < e; ++h) c += g2_vm(h); return c; }

__global__ __launch_bounds__(512, 2) __attribute__((aligned(4096))) void k1(const uint16_t* __restrict__ x, const uint16_t* __restrict__ bre, const uint16_t* __restrict__ bim,
                                             const uint16_t* __restrict__ lre, const uint16_t* __restrict__ lim, const uint16_t* __restrict__ logdt,
                                             const uint16_t* __restrict__ cre, const uint16_t* __restrict__ cim, const uint16_t* __restrict__ dskip,
                                             const uint16_t* __restrict__ ssmk, const uint16_t* __restrict__ ssmD, const uint16_t* __restrict__ qin,
                                             const uint16_t* __restrict__ kin, const uint16_t* __restrict__ vin, const uint16_t* __restrict__ sk,
                                             char* __restrict__ ws, float* __restrict__ out TS_ARG) {
    unsigned long long pc0;
    asm volatile("s_getpc_b64 %0" : "=s"(pc0));
    TS_DECL TS(0);
    TL_DECL
    __shared__ __attribute__((aligned(1024))) char shm[LDS_TOTAL];
    const int tid = threadIdx.x, lane = tid & 63, wid = __builtin_amdgcn_readfirstlane(tid >> 6);
    const int b = blockIdx.x, xcd = b & 7, idx = b >> 3, hb = xcd & 1, tq = idx & 7;
    const int tm = (xcd >> 1) * 4 + (idx >> 3), tn = hb * 8 + tq;
    const int tm2 = G2M ? tm : (xcd & 2) * 4 + tq, dt = G2M ? tq : (xcd & 4) + (idx >> 3), rot2 = tq + 2 * (idx >> 3);
    const unsigned lbase = (unsigned)(uintptr_t)shm;
    const int l15 = lane & 15, g4 = lane >> 4, lw = wid & 3, kg = wid >> 2;
    dma<U0 + 4096, false>(lbase + wid * 1024, lane * 16, mk_rsrc((const void*)(pc0 & ~255ull), CODE_KB * 1024), ((idx * 8 + wid) & 63) * 1024);
    const unsigned mk0 = lbase + POLL + lw * 256 + kg * (FULL - POLL + lw * (FST - 256));
    flag_set(mk0 + lane * 4, 1u);
    flag_set(lbase + HGO + lane * 4, 0u);
    unsigned vo[4];
    const bool xw = lw < 2;
#pragma unroll
    for (int i = 0; i < 4; ++i) {
        const int v = l15 ^ (i * 4 + g4);
        vo[i] = ((xw ? 64 * (i & 1) + 4 * (i >> 1) + (lane & 32) + 2 * (g4 & 1) : 8 * (i & 1) + 2 * g4) + (v >> 3)) * 2048 + (v & 7) * 16;
    }
    const int bw = lw >> 1;
    unsigned r0 = (tm * 128 + lw * 16 + bw * (tn * 64 + lw * 16 - 64 - tm * 128)) * 2048, rsz = 229376 - bw * 163840;
    unsigned rot = 8 * ((tq + (idx >> 3)) & 1) + 2 * (xcd >> 1) + 4 * hb, rst = 16384 << bw, lb = lbase + lw * 8192, oob = OOB;
    unsigned lb0 = lb + g1_buf(0) + kg * (g1_buf(1) - g1_buf(0)), so0 = ((kg + rot) & 15) * 128;
    unsigned co = l15 * 2 + (tn * 128 + lw * 32 + kg * 2048);
    tie(vo[0]); tie(vo[1]); tie(vo[2]); tie(vo[3]); tie(oob); tie(co);
    ties(r0); ties(rsz); ties(rot); ties(rst); ties(lb); ties(lb0); ties(so0);
    sched();
    asm volatile("" :: "s"(x), "s"(bre), "s"(bim), "s"(lre), "s"(lim), "s"(logdt), "s"(cre), "s"(cim), "s"(dskip), "s"(ssmk), "s"(ssmD), "s"(qin),
                       "s"(kin), "s"(vin), "s"(sk), "s"(ws), "s"(out));
    const u32x4 rs0 = mk_rsrc((const char*)(xw ? x : bre) + r0, rsz), rs1 = mk_rsrc((const char*)(xw ? x : bim) + r0, rsz);
    unsigned c_dt, c_re, c_im;
    gld<0>(c_dt, co, mk_rsrc(logdt, 2048), 0);
    gld<0>(c_re, co, mk_rsrc(lre, 2048), 0);
    gld<0>(c_im, co, mk_rsrc(lim, 2048), 0);
    static_for<0, 8>([&](auto ic) { constexpr int i = CV(ic); dma<i * 1024, false>(lb0, vo[i & 3], (i & 2) ? rs1 : rs0, so0 + (i >> 2) * rst); });
    wait_l<0>();
    barrier();
    dma1<0>(mk0 + kg * 120, oob, rs0, 0);
    sched();
    const unsigned a00 = (l15 >> 1) * 256 + ((((l15 & 1) * 8 + g4) ^ (l15 >> 1)) << 4);
    auto frag = [&](int par, int ks) { return (a00 ^ (ks * 64) ^ (par * 128)) + par * 2048; };
    bf16x8 cf[32];
    unsigned vc[4];
    auto c_setup = [&]() {
#pragma unroll
        for (int i = 0; i < 4; ++i) { const int v = l15 ^ (i * 4 + g4); vc[i] = (4 * (2 * g4 + (v >> 3)) + 2 * (i & 1) + (i >> 1)) * 2048 + (v & 7) * 16; }
    };
    const u32x4 rsc = mk_rsrc((kg ? cim : cre) + (size_t)dt * 131072, 262144);
    auto c_off = [&](int dg, int j) { return (unsigned)(dg * 65536 + hb * 1024 + ((rot2 + j) & 7) * 128); };
    const unsigned cst = lbase + (kg ? BUF : 2 * BUF) + lw * 8192;
    unsigned tag;
    const u32x4 rsf = mk_rsrc(ws + WS_F, 65536);
    const __amdgpu_buffer_rsrc_t rf = mk_brs(ws + WS_F, 65536);
    const u32x4 rsh = mk_rsrc(ws + WS_H + (size_t)(tm2 * 16 + hb * 8) * 32768, 262144);
    const int qx = hb ? 0 : 2, wid_ = wid, b_ = b;
    unsigned ha[4];
    auto h_setup = [&](int kg) {
        const int q = (lane >> 2) & 3, p = lane & 3;
#pragma unroll
        for (int i = 0; i < 4; ++i) {
            ha[i] = lbase + (g4 >> 1) * 2048 + kg * 1024 + ((i >> 1) ^ kg) * 512 + (p >> 1) * 256 + (((8 * (g4 & 1) + 4 * (i & 1) + q) ^ (4 * (p >> 1))) << 4) + (p & 1) * 8;
            tie(ha[i]);
        }
    };

    if (wid >= 4) {
        page<-1>();
        const int ch = xcd * 128 + (idx >> 2) * 16 + lw * 4 + (idx & 3);
        unsigned dw;
        auto ctouch = [&](int r) { const unsigned j = (fresh(idx * 4 + lw) + r * 37) & 127; stouch((pc0 & ~255ull) + (j < CODE_KB * 8 / 3 ? j : j - 64) * 384); };
        {
            const unsigned fb = lbase + FULL + lw * FST;
            auto piece = [&](auto sc, auto ic) {
                constexpr int s = CV(sc), i = CV(ic);
                dma<g1_buf(s) + i * 1024, false>(lb, vo[i & 3], (i & 2) ? rs1 : rs0, ((s + rot) & 15) * 128 + (i >> 2) * rst);
            };
            auto mark = [&](auto sc) { dma1<(31 - CV(sc)) * 4>(fb, oob, rs0, 0); };
            auto feed = [&](auto kc, auto ic) {
                constexpr int k = CV(kc), i = CV(ic);
                if constexpr (i < 4) piece(IC(k + 3), IC(i + 4));
                if constexpr (i == 3) mark(IC(k + 3));
                if constexpr (i >= 4 && k < 12) piece(IC(k + 4), IC(i - 4));
            };
            __builtin_amdgcn_s_setprio(HP);
            TS(7);
            sld(tag, ws + WS_F + F_EP);
            sld(dw, ssmD + (ch & ~1));
            const int sb = idx * 2 + (lw >> 1), kh = lw & 1;
            u32x2 kk[11], vv[8]; u32x4 w4[2], kd[4], ke[4], kvo;
            const unsigned ko = g4 * 16384 + l15 * 8, so = sb * 65536 + xcd * 256 + kh * 128;
            {
                const u32x4 rs = mk_rsrc(kin, 4 << 20);
                gld<0>(w4[0], l15 * 32, mk_rsrc(sk, 8192), xcd * 1024 + kh * 512);
                gld<16>(w4[1], l15 * 32, mk_rsrc(sk, 8192), xcd * 1024 + kh * 512);
                static_for<0, 3>([&](auto ic) { gld<0>(kk[CV(ic)], ko + so - 6144 + CV(ic) * 2048, rs, 0); });
                static_for<0, 8>([&](auto ic) { constexpr int i = CV(ic); gld<(i & 1) * 2048>(kk[3 + i], ko, rs, so + (i >> 1) * 4096); });
                static_for<0, 8>([&](auto ic) { constexpr int i = CV(ic); gld<(i & 1) * 2048>(vv[i], ko, mk_rsrc(vin, 4 << 20), so + (i >> 1) * 4096); });
                dma1<DSK>(lbase, lane * 4, mk_rsrc(dskip, 2048), dt * 256);
                dma<U0, false>(lbase + kh * 1024, oob, rs0, 0);
            }
            static_for<4, 8>([&](auto ic) { feed(IC(-2), ic); });
            const __amdgpu_buffer_rsrc_t rk = mk_brs(ws + WS_KVT + (size_t)(xcd * 32 + kh * 16) * 16384, 262144);
            const unsigned kfo = F_K + ((xcd * 2 + (idx >> 4)) * 64 + lw * 16) * 8, K = lbase + KC + lw * KCH;
            float kf[7], pa[4], pb[4];
            auto kv_step = [&](auto uc, auto jc) {
                constexpr int u = CV(uc), j = CV(jc), c = u >> 1, h = u & 1;
                auto el = [&](const u32x2& q) -> float { return (c & 1) ? bfhi(q[c >> 1]) : bflo(q[c >> 1]); };
                auto tap = [&](int t) { const unsigned w = w4[c >> 1][(c & 1) * 2 + (t >> 1)]; return (t & 1) ? bfhi(w) : bflo(w); };
                auto all = [&](auto f) {
#pragma unroll
                    for (int i = 0; i < 4; ++i) f(i);
                };
                if constexpr (j == 0) {
#pragma unroll
                    for (int i = 0; i < 7; ++i) { kf[i] = (h && i < 3) ? kf[i + 4] : el(kk[4 * h + i]); tie(kf[i]); }
                }
                if constexpr (EXACT) {
                    if constexpr (j == 1) { const float t = tap(3); all([&](int i) { pa[i] = kf[i] * t; }); all([&](int i) { pa[i] = rbf(pa[i]); tie(pa[i]); }); }
                    if constexpr (j == 2 || j == 4 || j == 6) { const float t = tap(3 - j / 2); all([&](int i) { pb[i] = kf[i + j / 2] * t; }); all([&](int i) { pb[i] = rbf(pb[i]); tie(pb[i]); }); }
                    if constexpr (j == 3 || j == 5 || j == 7) { all([&](int i) { pa[i] += pb[i]; }); all([&](int i) { pa[i] = rbf(pa[i]); tie(pa[i]); }); }
                } else {
                    if constexpr (j == 1) { const float t = tap(3); all([&](int i) { pa[i] = kf[i] * t; tie(pa[i]); }); }
                    if constexpr (j >= 2 && j <= 4) { const float t = tap(4 - j); all([&](int i) { pa[i] = fma_(kf[i + j - 1], t, pa[i]); tie(pa[i]); }); }
                    if constexpr (j == 5) all([&](int i) { pa[i] = rbf(pa[i]); tie(pa[i]); });
                }
                if constexpr (j == (EXACT ? 7 : 6)) all([&](int i) { pb[i] = el(vv[4 * h + i]); tie(pb[i]); });
                if constexpr (j == 7) {
                    all([&](int i) { pa[i] *= pb[i]; });
                    kvo[3 - 2 * h] = pack2(pa[1], pa[0]); kvo[2 - 2 * h] = pack2(pa[3], pa[2]);
                    if constexpr (h) __builtin_amdgcn_raw_buffer_store_b128(kvo, rk, (l15 >> 2) * 65536 + g4 * 64 + (l15 & 3) * 16, c * 16384 + sb * 256, 16);
                    else tie(kvo);
                }
            };
            static_for<-1, 13>([&](auto kc) {
                constexpr int k = CV(kc);
                if constexpr (k == 3) page<1>();
                if constexpr (k == 11) page<12>();
                if constexpr (k == 2) hop<0>();
                if constexpr (k == 6) hop<6>();
                if constexpr (k == 8) hop<9>();
                if constexpr (k >= 0) barrier();
                if constexpr (k >= 0 && ((CTM >> k) & 1)) ctouch(k);
                if constexpr (k == KV0) {
                    wait_v<4 + hv_cnt(-1, k)>();
                    static_for<0, 11>([&](auto ic) { tie(kk[CV(ic)]); });
                    static_for<0, 8>([&](auto ic) { tie(vv[CV(ic)]); });
                    tie(w4[0]); tie(w4[1]); keep(c_dt); keep(c_re); keep(c_im);
                    TS(8);
                }
                if constexpr (k == KFL) {
                    wait_l<0>();
                    ties(tag); ties(dw);
                    tag += 0x01010101u;
                    wait_v<hv_cnt(KVL + 1, k)>();
                    __builtin_amdgcn_raw_buffer_store_b64(u32x2{tag, ~tag}, rf, lane ? OOB : 0u, F_K + ((xcd * 2 + kh) * 64 + sb) * 8, 16);
                    TS(10);
                }
                if constexpr (k == KPL) dma1<POLL>(lbase + wid * 256, (lane & 31) * 4, rsf, kfo);
                if constexpr (k == 12) {
                    dma1<0>(K, oob, rs0, 0);
                    dma1<KCP>(K, oob, rs0, 0);
                }
                static_for<0, 8>([&](auto ic) {
                    constexpr int i = CV(ic), u0 = kv_first(k), nu = kv_first(k + 1) - u0;
                    feed(kc, ic);
                    if constexpr (k == 4 && i == 4) hop<3>();
                    if constexpr (nu == 1) kv_step(IC(u0), ic);
                    if constexpr (nu == 2) { kv_step(IC(u0 + i / 4), IC(2 * (i & 3))); kv_step(IC(u0 + i / 4), IC(2 * (i & 3) + 1)); }
                    if constexpr (k == 12 && i >= 4) gld<(i - 4) * 1024>(kd[i - 4], lane * 16, mk_rsrc(ssmk, 4 << 20), ch * 4096);
                    if constexpr (k == 11 && i == 4) c_setup();
                    sched();
                });
            });
            TS(1);
            unsigned pf;
            const unsigned fr = lbase + FULL + (lane & 3) * FST;
            wait_v<hv_cnt(KPL, 12) - 1 - (KPL == KFL) + 11>();
            dsr32<POLL - 131072>(pf, lbase + 131072 + wid * 256 + lane * 4);
            wait_l<0>();
            tag_chk(pf, (lane & 1) ? ~tag : tag, (lane & 31) * 4, rsf, kfo);
            if constexpr (EC && EB == 1) barrier();
            {
                const u32x4 rs = mk_rsrc(ws + WS_KVT + (size_t)(xcd * 32 + idx) * 16384, 16384);
                static_for<0, 4>([&](auto ic) { constexpr int i = CV(ic); dma<U0 + 2048 + i * 1024, true>(lbase + lw * 4096, (lane >> 2) * 64 + (((lane & 3) ^ g4) << 4), rs, lw * 4096 + i * 1024); });
            }
            mark(IC(16));
            if constexpr (EC) {
                if constexpr (EB == 0) barrier();
                static_for<0, 4>([&](auto ic) { constexpr int i = CV(ic); dma<i * 1024, false>(cst, vc[i], rsc, c_off(lw, 0)); });
                if constexpr (EC == 1) static_for<0, 4>([&](auto ic) { constexpr int i = CV(ic); dma<4096 + i * 1024, false>(cst, vc[i], mk_rsrc(cre + (size_t)dt * 131072, 262144), c_off(lw, 0)); });
            }
            wait_v<5 + (EC == 1 ? 8 : EC ? 4 : 0)>();
            static_for<0, 4>([&](auto ic) { tie(kd[CV(ic)]); });
#pragma unroll
            for (int n = 0; n < 4; ++n) {
                const unsigned first = n ? __builtin_amdgcn_readlane(kd[n ? n - 1 : 0][3], 63) : 0u;
                const unsigned p = __builtin_amdgcn_update_dpp(first, kd[n][3], 0x138, 0xf, 0xf, false);
                ke[n] = u32x4{__builtin_amdgcn_alignbyte(kd[n][0], p, 2), __builtin_amdgcn_alignbyte(kd[n][1], kd[n][0], 2),
                              __builtin_amdgcn_alignbyte(kd[n][2], kd[n][1], 2), __builtin_amdgcn_alignbyte(kd[n][3], kd[n][2], 2)};
                tie(ke[n]);
            }
            hop<14>();
            pf = 1;
            zero_chk<15 * 4>(pf, fr);
            TS(9);
            static_for<0, 4>([&](auto ic) { constexpr int i = CV(ic); dsw128<64 + i * 1024>(K + lane * 16, kd[i]); dsw128<KCP + 64 + i * 1024>(K + lane * 16, ke[i]); });
            wait_l<0>();
            __builtin_amdgcn_s_setprio(0);
        }
        TS(2);
        unsigned ka[4], cad[4], ub[4];
#pragma unroll
        for (int i = 0; i < 4; ++i) {
            ka[i] = lbase + KC + lw * KCH + ((l15 & 1) ? 0 : KCP) + 84 + 2 * (l15 & 14) - 16 * g4 + i * 1024;
            cad[i] = cst + frag(i & 1, i >> 1);
            const int e = 2 * l15 + g4 + 2;
            ub[i] = lbase + U0 + 64 * e + ((lw ^ (((e >> 2) + 3 - i) & 3)) << 4);
        }
        f32x4 acc[8];
        bf16x4 FA[8][2]; bf16x8 FB[8];
        auto op = [&](auto gc) {
            constexpr Op o = cv_op(CV(gc));
            if constexpr (o.kind == CO_A) { constexpr int s = o.a >> 3, n = o.a & 7, d = (n & 1) * 128 + s * 16 + o.b * 2; dsr2<d, d + 1>(FA[n][o.b], ka[n >> 1]); }
            if constexpr (o.kind == CO_B) { constexpr int s = o.a >> 3, m = o.a & 7; dsr128<2048 * m + 1792 - 256 * s>(FB[m], ub[s & 3]); }
            if constexpr (o.kind == CO_C) dsr128<(o.a & 1) * 4096>(cf[o.a * 4 + o.b], cad[o.b]);
        };
        static_for<-36, 0>(op);
        static_for<0, 288>([&](auto gc) {
            constexpr int G = CV(gc), s = G / 36, q = G % 36, n = cv_n(q), m = cv_m(q);
            if constexpr (cv_wait(G) >= 0) wait_lr<cv_wait(G)>();
            if constexpr (q == 24 && s >= 2) static_for<0, 4>([&](auto ic) { tie(cf[(s - 2) * 4 + CV(ic)]); });
            mfma<s == 0 && n == 0>(acc[n + m], cat(FA[n][0], FA[n][1]), FB[m]);
            sched();
            if constexpr (q == 11 && s >= 2) wait_v<4>();
            op(gc);
            if constexpr (q == 30 && ((CTM >> (16 + s)) & 1)) ctouch(16 + s);
            if constexpr (G == 16) hop<17>();
            if constexpr (G == 66) hop<19>();
            if constexpr (G == 107) page<21>();
            if constexpr (G == 158) hop<23>();
            if constexpr (G == 209) hop<25>();
            if constexpr (G == 254) hop<27>();
            if constexpr (G == CBQ) barrier();
            if constexpr ((q == 24 || q == 25 || q == 29 || q == 34) && (s || !EC)) {
                constexpr int pc = q == 24 ? 0 : q == 25 ? 1 : q == 29 ? 2 : 3;
                dma<(s & 1) * 4096 + pc * 1024, false>(cst, vc[pc], rsc, c_off(lw, s));
            }
            sched();
        });
        page<28>();
        TS(3);
        {
            u32x2 kvr[8];
            const unsigned kva = lbase + U0 + 2048 + 128 * l15 + 64 * (g4 >> 1) + ((lw ^ ((l15 >> 1) & 3)) << 4) + 8 * (1 - (g4 & 1));
            static_for<0, 8>([&](auto ic) { dsr64<CV(ic) * 2048>(kvr[CV(ic)], kva); });
            wait_l<0>();
            const float Dd = (ch & 1) ? bfhi(dw) : bflo(dw);
            const __amdgpu_buffer_rsrc_t rz = mk_brs(ws + WS_ZT, 4 << 20);
#pragma unroll
            for (int jj = 0; jj < 4; ++jj) {
                u32x4 o;
#pragma unroll
                for (int u = 0; u < 2; ++u) {
                    const int j = 2 * jj + u;
                    tie(kvr[j]); tie(acc[j]);
                    const float kv3 = bflo(kvr[j][0]), kv2 = bfhi(kvr[j][0]), kv1 = bflo(kvr[j][1]), kv0 = bfhi(kvr[j][1]);
                    o[2 * u] = pack2(rbf(acc[j][0]) + rbf(kv0 * Dd), rbf(acc[j][1]) + rbf(kv1 * Dd));
                    o[2 * u + 1] = pack2(rbf(acc[j][2]) + rbf(kv2 * Dd), rbf(acc[j][3]) + rbf(kv3 * Dd));
                }
                __builtin_amdgcn_raw_buffer_store_b128(o, rz, lane * 16 + jj * 1024, ch * 4096, 16);
                if (jj == 1) { sched(); hop<29>(); sched(); }
            }
            TS(8);
            hop<30>();
            wait_v<4>();
            static_for<0, 8>([&](auto ic) { constexpr int i = CV(ic); dsr128<(i >> 2) * 4096>(cf[24 + i], cad[i & 3]); });
            h_setup(1);
            wait_l<0>();
            static_for<0, 8>([&](auto ic) { tie(cf[24 + CV(ic)]); });
        }
        TS(4);
    } else {
        f32x4 acc[8][2];
        cf32 L, cc, M, M2, MG, NG, N4, N8, ag;
        {
            unsigned aad[4], bad[4];
#pragma unroll
            for (int i = 0; i < 4; ++i) { aad[i] = lbase + frag(i & 1, i >> 1); bad[i] = aad[i] + 16384 + wid * 4096; }
            const unsigned fr = lbase + FULL + (lane & 3) * FST;
            sld(tag, ws + WS_F + F_EP);
            bf16x8 fa[4][2], fb[2][4];
            unsigned fl = 1;
            auto op = [&](auto gc) {
                constexpr int G = CV(gc);
                constexpr Op o = g1_op(G);
                if constexpr (o.kind == OP_A) {
                    constexpr int bo = g1_buf(o.a >> 3), hi = bo >= 65536 ? 65536 : 0, i = o.a & 7;
                    dsr128<bo - hi + (i >> 1) * 4096>(fa[i & 3][o.b], aad[(i & 1) + 2 * o.b] + hi);
                }
                if constexpr (o.kind == OP_B) { constexpr int bo = g1_buf(o.a), hi = bo >= 65536 ? 65536 : 0; dsr128<bo - hi>(fb[o.a & 1][o.b], bad[o.b] + hi); }
                if constexpr (o.kind == OP_FLAG) dsr32<(30 - (G >> 5)) * 4>(fl, fr);
            };
            auto consts = [&]() {
                float ld, ar, ai;
                asm volatile("s_waitcnt vmcnt(9)\n\tv_lshlrev_b32 %0, 16, %3\n\tv_lshlrev_b32 %1, 16, %4\n\tv_lshlrev_b32 %2, 16, %5"
                             : "=&v"(ld), "=&v"(ar), "=&v"(ai) : "v"(c_dt), "v"(c_re), "v"(c_im) : "memory");
                const float dtv = rbf(expf(ld)), ea = expf(dtv * ar);
                float sn, cs;
                sincosf(dtv * ai, &sn, &cs);
                L = cf32{ea * cs, ea * sn};
                M = L;
#pragma unroll
                for (int e = 0; e < 5; ++e) M = cmul(M, M);
                M2 = cmul(M, M);
                const cf32 N = cmul(M2, M2), N2 = cmul(N, N), one = {1.f, 0.f};
                N4 = cmul(N2, N2); N8 = cmul(N4, N4);
                auto sel = [](bool c, cf32 a, cf32 b) { return cf32{c ? a.r : b.r, c ? a.i : b.i}; };
                MG = cmul(sel(g4 & 1, M, one), sel(g4 & 2, M2, one));
                NG = cmul(sel(g4 & 1, N, one), sel(g4 & 2, N2, one));
                const float nr = L.r - 1.f, den = ar * ar + ai * ai;
                cc = cf32{2.f * ((nr * ar + L.i * ai) / den), 2.f * ((L.i * ar - nr * ai) / den)};
                auto tc = [&](cf32& v) { tie(v.r); tie(v.i); };
                tc(L); tc(cc); tc(M); tc(M2); tc(MG); tc(NG); tc(N4); tc(N8);
                sched();
            };
            if constexpr (CQ < 0) consts();
            __builtin_amdgcn_s_setprio(CP);
            zero_chk<0>(fl, lbase + POLL + (lane & 3) * 256);
            ties(tag);
            tag += 0x01010101u;
            TS(1);
            static_for<-12, 0>(op);
            static_for<0, 512>([&](auto gc) {
                constexpr int G = CV(gc), st = G >> 5, i = (G >> 2) & 7, j = G & 3;
                if constexpr ((G == 0 || j == 2) && G <= 506) { static_assert(g1_wait(G) >= 0, ""); wait_lr<G == 506 ? 0 : g1_wait(G)>(); }
                if constexpr (G == CQ) { asm volatile("s_waitcnt lgkmcnt(0)\n\ts_nop 7" ::: "memory"); consts(); }
                if constexpr (G == 52) hop_all(wid);
                if constexpr ((G & 31) == 20 && st < 15) zero_chk<(30 - st) * 4>(fl, fr);
                if constexpr ((G & 31) == 30 && st < 13 + (EC != 0)) barrier();
                if constexpr ((G & 31) == 20) TL(st);
                mfma<st == 0 && j < 2>(acc[i][j & 1], fa[i & 3][j >> 1], fb[st & 1][j]);
                sched();
                op(gc);
                if constexpr (G == 31) page<-1>();
                if constexpr (G == 95) hop<2>();
                if constexpr (G == 159) hop<4>();
                if constexpr (G == 207) hop<5>();
                if constexpr (G == 255) page<7>();
                if constexpr (G == 319) hop<8>();
                if constexpr (G == 383) hop<10>();
                if constexpr (G == 431) hop<11>();
                if constexpr (G == 479) page<13>();
                if constexpr (G == 509) hop<15>();
                if constexpr (G >= 487) {
                    constexpr int k = G - 486;
                    if constexpr (k == 1) { asm volatile("s_nop 3"); ag = cf32{acc[0][0][0], acc[0][1][0]}; }
                    cf32 t;
                    asm volatile("v_fma_f32 %0, -%3, %5, %6\n\tv_fma_f32 %1, %3, %4, %7\n\tv_fmac_f32 %0, %2, %4\n\tv_fmac_f32 %1, %2, %5"
                                 : "=&v"(t.r), "=&v"(t.i) : "v"(L.r), "v"(L.i), "v"(ag.r), "v"(ag.i), "v"(acc[k >> 2][0][k & 3]), "v"(acc[k >> 2][1][k & 3]));
                    ag = t;
                }
                sched();
            });
        }
        __builtin_amdgcn_s_setprio(3);
        TS(2);
#pragma unroll
        for (int k = 26; k < 32; ++k) ag = cfma(L, ag, cf32{acc[k >> 2][0][k & 3], acc[k >> 2][1][k & 3]});
        cf32 ev_, od, p01, p23;
        sw16(ag.r, ev_.r, od.r); sw16(ag.i, ev_.i, od.i);
        const cf32 pp = cfma(M, ev_, od);
        sw32(pp.r, p01.r, p23.r); sw32(pp.i, p01.i, p23.i);
        cf32 E = cfma(M2, p01, p23);

        const __amdgpu_buffer_rsrc_t rse = mk_brs(ws + WS_E + (size_t)(tn * 4 + wid) * 4096, 4096);
        __builtin_amdgcn_raw_buffer_store_b128(u32x4{f2u(E.r), f2u(E.i), tag, ~tag}, rse, g4 ? OOB : l15 * 16, tm * 256, 16);
        sched();
        TS(3);
        c_setup();
        unsigned cad[4];
#pragma unroll
        for (int i = 0; i < 4; ++i) cad[i] = lb + frag(i & 1, i >> 1);
        auto c_read = [&](auto rc) { static_for<0, 8>([&](auto ic) { constexpr int r = CV(rc), i = CV(ic); dsr128<(i >> 2) * 4096>(cf[r * 8 + i], cad[i & 3] + (r == 0 || r == 3 ? 2 * BUF : 0)); }); };
        auto c_tie = [&](auto rc) { static_for<0, 8>([&](auto ic) { tie(cf[CV(rc) * 8 + CV(ic)]); }); };
        auto c_unit = [&](auto uc) { static_for<0, 4>([&](auto ic) { constexpr int u = CV(uc), i = CV(ic); dma<(u < 2 || u >= 6 ? 2 * BUF : 0) + (u & 1) * 4096 + i * 1024, false>(lb, vc[i], rsc, c_off(wid, u)); }); };
        auto c_req = [&](auto rc) { c_unit(IC(CV(rc) * 2)); c_unit(IC(CV(rc) * 2 + 1)); };
        const float c2r = cc.r, c2i = cc.i;
        auto scale = [&](int i) {
#pragma unroll
            for (int j = 0; j < 4; ++j) {
                const float br = acc[i][0][j], bi = acc[i][1][j];
                acc[i][0][j] = fma_(c2r, br, -(c2i * bi)); acc[i][1][j] = fma_(-c2r, bi, -(c2i * br));
            }
            tie(acc[i][0]); tie(acc[i][1]);
        };
        scale(0); scale(1); scale(2); scale(3);
        sched(); hop<16>(); sched();
        if constexpr (ED) { scale(4); scale(5); scale(6); scale(7); }
        if constexpr (ED == 2) barrier();
        u32x4 ev[4]; unsigned eo[4]; unsigned long long none[4];
#pragma unroll
        for (int i = 0; i < 4; ++i) {
            const int t = tm - 1 - 4 * i - g4;
            eo[i] = t < 0 ? OOB : t * 256 + l15 * 16; none[i] = __builtin_amdgcn_ballot_w64(t < 0);
            asm volatile("buffer_load_dwordx4 %0, %1, %2, 0 offen sc1" : "=v"(ev[i]) : "v"(eo[i]), "s"(mk_rsrc(ws + WS_E + (size_t)(tn * 4 + wid) * 4096, 4096)) : "memory");
        }
        asm volatile("" ::: "memory");
        sched();
        if constexpr (ED != 2) barrier();
        if constexpr (R01 == 0) { c_req(IC(0)); c_req(IC(1)); }
        if constexpr (R01 >= 2) c_unit(IC(0));
        if constexpr (!ED) { scale(4); scale(5); scale(6); scale(7); }
        cf32 X = cfma(cf32{g4 == 3 ? M.r : g4 == 2 ? 1.f : 0.f, g4 == 3 ? M.i : 0.f}, p01, cf32{(g4 & 1) ? ev_.r : 0.f, (g4 & 1) ? ev_.i : 0.f});
        tie(X.r); tie(X.i);
        sched();
        hop<24>();
        wait_v<R01 == 0 ? 16 : R01 >= 2 ? 4 : 0>();
        tie(ev[0]); tie(ev[1]); tie(ev[2]); tie(ev[3]);
        {
            const unsigned long long tag64 = (unsigned long long)tag | ((unsigned long long)~tag << 32);
#pragma unroll 1
            for (unsigned spins = 0; spins < SPIN_CAP; ++spins) {
                unsigned long long ok = ~0ull;
#pragma unroll
                for (int i = 0; i < 4; ++i) ok &= __builtin_amdgcn_ballot_w64(((unsigned long long)ev[i][2] | ((unsigned long long)ev[i][3] << 32)) == tag64) | none[i];
                TSV(11, spins);
                if (ok == ~0ull) break;
                asm volatile("buffer_load_dwordx4 %0, %4, %8, 0 offen sc1\n\tbuffer_load_dwordx4 %1, %5, %8, 0 offen sc1\n\tbuffer_load_dwordx4 %2, %6, %8, 0 offen sc1\n\t"
                             "buffer_load_dwordx4 %3, %7, %8, 0 offen sc1\n\ts_waitcnt vmcnt(0)" : "=&v"(ev[0]), "=&v"(ev[1]), "=&v"(ev[2]), "=&v"(ev[3])
                             : "v"(eo[0]), "v"(eo[1]), "v"(eo[2]), "v"(eo[3]), "s"(mk_rsrc(ws + WS_E + (size_t)(tn * 4 + wid) * 4096, 4096)) : "memory");
            }
        }
        page<18>();
        cf32 h;
        {
            auto e = [&](int i) { return cf32{u2f(ev[i][0]), u2f(ev[i][1])}; };
            cf32 c = cmul(NG, cfma(N8, cfma(N4, e(3), e(2)), cfma(N4, e(1), e(0)))), u, v;
            sw16(c.r, u.r, v.r); sw16(c.i, u.i, v.i);
            sw32(u.r + v.r, u.r, v.r); sw32(u.i + v.i, u.i, v.i);
            c = cfma(MG, cf32{u.r + v.r, u.i + v.i}, X);
            h = cf32{fma_(c2r, c.r, -(c2i * c.i)), fma_(-c2r, c.i, -(c2i * c.r))};
        }
        TS(4);
        {
            const __amdgpu_buffer_rsrc_t rh = mk_brs(ws + WS_H + (size_t)(tm * 16 + tn) * 32768 + wid * 2048, 32768);
            const unsigned ho = g4 * 256 + ((l15 ^ (4 * (g4 & 1))) << 4);
            const cf32 Lc = {L.r, -L.i};
            static_for<0, 4>([&](auto qc) {
                constexpr int q = CV(qc);
                if constexpr (R01 == 1) c_unit(qc);
                if constexpr (R01 == 2 && q < 3) c_unit(IC(q + 1));
                if constexpr (R01 == 3 && q > 0) c_unit(qc);
                if constexpr (q == 2) hop<20>();
                u32x4 wr, wi;
#pragma unroll
                for (int u = 0; u < 4; ++u) {
                    const int i = 2 * q + (u >> 1), j = 2 * (u & 1);
                    const cf32 h0 = cfma(Lc, h, cf32{acc[i][0][j], acc[i][1][j]});
                    h = cfma(Lc, h0, cf32{acc[i][0][j + 1], acc[i][1][j + 1]});
                    wr[u] = pack2(h0.r, h.r); wi[u] = pack2(h0.i, h.i);
                }
                __builtin_amdgcn_raw_buffer_store_b128(wr, rh, ho, q * 8192, 16);
                __builtin_amdgcn_raw_buffer_store_b128(wi, rh, ho + 1024, q * 8192, 16);
                sched();
            });
        }
        if constexpr (R01 == 4) { c_unit(IC(1)); c_unit(IC(2)); c_unit(IC(3)); wait_v<12>(); } else wait_v<0>();
        __builtin_amdgcn_raw_buffer_store_b64(u32x2{tag, ~tag}, rf, lane ? OOB : 0u, F_H + ((tm * 16 + tn) * 4 + wid) * 8, 16);
        TS(5);
        hop<22>();
        if constexpr (R01 == 4) wait_v<1>();
        c_read(IC(1)); c_read(IC(0));
        wait_l<8>();
        c_req(IC(2));
        wait_l<0>();
        c_req(IC(3));
        c_tie(IC(0)); c_tie(IC(1));
        wait_v<0>();
        c_read(IC(2)); c_read(IC(3));
        wait_l<0>();
        c_tie(IC(2)); c_tie(IC(3));
        if (wid == 3) {
            const unsigned long long tag64 = (unsigned long long)tag | ((unsigned long long)~tag << 32);
            gu64* hf = (gu64*)(ws + WS_F + F_H) + (tm2 * 16 + hb * 8) * 4 + (lane & 31);
#pragma unroll 1
            for (unsigned spins = 0; spins < SPIN_CAP; ++spins) {
                if (wall(__hip_atomic_load(hf, RLX_AGENT) == tag64)) break;
                __builtin_amdgcn_s_sleep(1);
            }
            flag_set(lbase + HGO + lane * 4, 1u);
        } else gate<1>(lbase + HGO, 1);
        TS(6);
        hop<26>();
        static_for<0, 16>([&](auto ic) { constexpr int i = CV(ic); dma<(i >> 3) * 2 * BUF + (i & 7) * 1024, true>(lb, lane * 16, rsh, ((rot2 + wid + (i >> 3) * 4) & 7) * 32768 + qx * 8192 + (i & 7) * 1024); });
        h_setup(0);
        wait_v<8>();
        __builtin_amdgcn_s_setprio(0);
    }
    {
        const int wid = fresh(wid_), lw = wid & 3, kg = wid >> 2, b = fresh(b_), xcd = b & 7, idx = b >> 3, tq = (idx & 7) + 2 * (idx >> 3);
        const int dt = G2M ? idx & 7 : (xcd & 4) + (idx >> 3), qx = (~b & 1) * 2;
        const unsigned wl = lbase + wid * 4096;
        unsigned l16 = lane * 16;
        unsigned hs0, hs1, fso;
        auto fill = [&](auto sc, auto pc) {
            constexpr int s = CV(sc), p = CV(pc);
            if constexpr (p == 0) dma_x<(2 * s % 3) * BUF, (s >> 1) * 8192>(fso, wl, l16, rsh, (s & 1) ? hs1 : hs0);
            else dma_n<p * 1024>(fso, l16, rsh);
        };
        bf16x4 hf[2][8];
        auto ldh = [&](auto nc, auto ic) {
            constexpr int n = CV(nc), i = CV(ic), bo = (2 * (n >> 2) % 3) * BUF + (n & 3) * 8192 + (i >> 2) * 4096, hi = bo >= 65536 ? 65536 : 0;
            dsr64tr<bo - hi>(hf[n & 1][i], ha[i & 3] + hi);
        };
        f32x4 yc[2][2][2];
        f32x4 im[2], ao, ob[2]; u32x4 pl[2], po[2], dg[2];
        bf16x8 xf[2];
        u32x2 zv[4], qv[2], tg2;
        unsigned pf, dsw_, expect, msk, ck, ck2, a_q, a_x, a_iw, a_ir, a_p, a_z, a_z2, a_xr, a_w, a_zr, a_qr, a_oa, a_ob, a_f, u0, u1, u2;
        const u32x4 rsz = mk_rsrc(ws + WS_F, wid == 1 ? 65536 : 0);
        float tq_[2], tz, sm[2];
        const __amdgpu_buffer_rsrc_t ro = mk_brs(out, 16 << 20);
        const __amdgpu_buffer_rsrc_t rp = mk_brs(ws + WS_PP, 8 << 20);
        const u32x4 rsp = mk_rsrc(ws + WS_PP, 8 << 20);
        unsigned w2k, zfo, ta, tb, ppo;
        constexpr unsigned long long LO8 = 0x00ff00ff00ff00ffull;
        auto finB = [&](auto pc, auto jc) {
            constexpr int p = CV(pc), j = CV(jc);
            if constexpr (j < 8) { yc[p & 1][0][j & 1][j >> 1] += im[j & 1][j >> 1]; tie(yc[p & 1][0][j & 1]); }
            else {
                constexpr int i = (j - 8) >> 2, k = j & 3, dd = k & 1, h = k >> 1;
                ob[i][k] = sel8(yc[p & 1][0][dd][2 * i + 1 - h], yc[p & 1][0][dd][2 * i + h], h ? ~LO8 : LO8);
                if constexpr (k == 3 && (p == 3 || !(NOST & 4))) __builtin_amdgcn_raw_buffer_store_b128(__builtin_bit_cast(u32x4, ob[i]), ro, a_ob, (tb + (p ^ qx) * 8 + 2 * i) * 8192 + 4096 + dt * 512 + lw * 128, OSC);
                if constexpr (j == 12) keep(ob[0]);
            }
        };
        auto addp = [&](auto pc, auto ic) {
            constexpr int p = CV(pc), dd = CV(ic) & 1, r = CV(ic) >> 1;
            yc[p & 1][0][dd][r] += u2f(pl[dd][r]);
            tie(yc[p & 1][0][dd]);
        };
        auto rcv = [&](auto ic) {
            constexpr int i = CV(ic);
            if constexpr (i < 8) { pl[i & 1][i >> 1] ^= (tag >> (8 * (i >> 1))) & 255u; pp_tie<i & 1>(pl[i & 1]); }
            if constexpr (i == 8) { ck = pl[0][0] | pl[0][1] | pl[0][2]; tie(ck); }
            if constexpr (i == 9) { ck2 = pl[1][0] | pl[1][1] | pl[1][2]; tie(ck2); }
            if constexpr (i == 10) { ck = ck | pl[0][3] | pl[1][3]; ck = (ck | ck2) & 255u; tie(ck); }
        };
        auto side = [&](auto pc) {
            constexpr int P = CV(pc), n = P >> 3, m = P & 7, p = n >> 3;
#define V(pos, var, expr) if constexpr (P == (pos)) { var = (expr); tie(var); }
            if constexpr (P == AT(0, 4)) { hs0 = ((tq + (wid >> 1)) & 7) * 32768 + (wid & 1) * 4096 + qx * 8192; hs1 = hs0 ^ 131072; ties(hs0); ties(hs1); }
            if constexpr (P == AT(0, 6)) { w2k = lbase + wid * 2048; ta = 512 * (idx >> 3) + 128 * (idx & 1) + 16 * wid + 4 * ((idx >> 1) & 3); ties(w2k); ties(ta); }
            if constexpr (P == AT(1, 4)) { tb = (G2M ? (xcd >> 1) * 4 + (idx >> 3) : (xcd & 2) * 4 + (idx & 7)) * 128 + 64 * kg; ties(tb); }
            if constexpr (P == AT(6, 6)) { zfo = F_Z + xcd * 4096; ties(zfo); }
            if constexpr (P == AT(9, 1)) { ppo = b * 32768 + wid * 2048; ties(ppo); }
            V(AT(0, 0), a_x, (lane >> 2) & 7)        V(AT(0, 1), u0, l16 & 48)               V(AT(0, 2), a_x, (lane & 32) | a_x)
            V(AT(0, 3), u1, l16 & 240)               V(AT(0, 4), a_x, (a_x << 11) | u0)      V(AT(0, 6), u2, lane & 48)
            V(AT(1, 0), a_w, (lane >> 1) & 4)        V(AT(1, 1), a_q, (u2 << 7) | u1)        V(AT(1, 2), u0, lane & 7)
            V(AT(1, 3), a_xr, (u1 << 2) | u2)        V(AT(1, 4), a_w, (u0 << 3) | a_w)       V(AT(1, 6), a_xr, a_xr + (w2k + IMG))
            V(AT(2, 0), a_w, a_w + (lbase + DSK + lw * 64))
            if constexpr (P == AT(2, 1)) { tg2[0] = tag; tie(tg2); }
            V(AT(2, 2), a_p, lane & 31)
            if constexpr (P == AT(2, 3)) { tg2[1] = ~tag; tie(tg2); }
            V(AT(2, 4), a_p, a_p << 2)               V(AT(2, 6), u1, (int)(lane << 31) >> 31)
            V(AT(3, 0), a_iw, w2k + IMG + l16)       V(AT(3, 1), expect, (u1 ^ tag) & (wid == 1 ? ~0u : 0u)) V(AT(4, 6), a_f, l16 << 2) V(AT(5, 4), a_f, (a_f & 0xf80) | (a_p & 4))           V(AT(3, 2), a_ir, a_iw ^ 8192)
            V(AT(3, 3), u2, lane >> 5)
            V(AT(5, 6), a_z, lane ^ u2 ^ (wid * 4))  V(AT(6, 0), u1, lane >> 3)              V(AT(6, 1), a_z, a_z & 7)
            V(AT(6, 2), a_z2, u1 << 12)              V(AT(6, 3), a_zr, (wid ^ lane) & 7)     V(AT(6, 4), a_z, (a_z << 4) | a_z2)
            V(AT(6, 5), a_zr, (a_zr << 1) | u2)      V(AT(6, 6), a_z2, a_z ^ 32)             V(AT(6, 7), a_zr, (a_zr << 3) + (lbase + ZST))
            V(AT(7, 0), a_oa, a_p << 2)              V(AT(7, 1), a_zr, (a_p << 7) + a_zr)    V(AT(7, 2), a_oa, (u2 << 21) | a_oa)
            V(AT(7, 3), a_qr, (u2 << 10) + (w2k + QST))
            V(AT(8, 0), a_ob, (lane >> 2) & 4)       V(AT(8, 1), a_qr, (a_p << 1) + a_qr)    V(AT(8, 2), a_ob, (lane & 32) | a_ob)
            V(AT(8, 3), u0, u0 << 4)                 V(AT(8, 6), a_ob, (u1 & 1) | a_ob)      V(AT(9, 0), msk, 0xffffff00u)
            V(AT(9, 2), a_ob, (a_ob << 13) | u0)
            if constexpr (P == AT(2, 5) || P == AT(2, 7)) dma<IMG + (m == 7) * 1024, false>(w2k, a_x, mk_rsrc(x, (NOX & 1) ? 0 : 4 << 20), (tb + ((m == 7) ^ qx) * 8) * 2048 + dt * 256 + lw * 64);
            if constexpr (!NOA) {
            if constexpr (P == AT(5, 5) || P == AT(5, 7)) dma<QST + (m == 7) * 1024, false>(w2k, a_q, mk_rsrc(qin, (NOX & 2) ? 0 : 4 << 20), (ta + 256 * (m == 7)) * 2048 + xcd * 256);
            if constexpr (P == AT(3, 4)) __builtin_amdgcn_raw_buffer_store_b64(tg2, mk_brs(ws + WS_F + F_Z + (xcd * 32 + idx) * 128, wid ? 0 : 8), l16, 0, 16);
            if constexpr (P == AT(ZP, 4)) pf_ld(pf, a_f, rsz, zfo);
            if constexpr (P == P_ZC) { wait_v<g2_allow(P, AT(ZP, 4))>(); tag_chk(pf, expect, a_f, rsz, zfo); }
            if constexpr (P == AT(11, 3) || P == AT(11, 4)) dma<ZST + (m - 3) * 1024, true>(w2k, m == 3 ? a_z : a_z2, mk_rsrc(ws + WS_ZT + (size_t)xcd * 524288, 524288), idx * 128 + (wid * 16 + (m - 3) * 8) * 4096);
            }
            if constexpr (P == AT(2, 4)) dsr32<0>(dsw_, a_w);
            if constexpr (P == AT(3, 6)) { tie(dsw_); u1 = dsw_ & 0xffffu; tie(u1); }
            V(AT(4, 0), dsw_, dsw_ & 0xffff0000u)
            if constexpr (n == 4 && m >= 1 && m < 5) { constexpr unsigned long long k = diag_mask(m - 1); dg[0][m - 1] = sel0(u1, k); }
            if constexpr (n == 5 && m < 4) { constexpr unsigned long long k = diag_mask(m); dg[1][m] = sel0(dsw_, k); }
            if constexpr (P == AT(7, 4) || P == AT(7, 6)) dsr128<(m - 4) * 512>(xf[(m - 4) / 2], a_xr);
            if constexpr (P == AT(8, 4)) { tie(xf[0]); tie(xf[1]); }
            if constexpr (P == AT(9, 3) || P == AT(9, 7)) static_for<0, 2>([&](auto dc) { mfma<false>(yc[m >> 2][0][CV(dc)], xf[m >> 2], __builtin_bit_cast(bf16x8, dg[CV(dc)])); });
            if constexpr (n >= 8 && (n & 7) == 0 && m == 4) { dsw128<0>(a_iw, yc[(p - 1) & 1][1][0]); dsw128<1024>(a_iw, yc[(p - 1) & 1][1][1]); }
            if constexpr (n >= 8 && (n & 7) == 3 && (m == 4 || m == 6)) dsr128<(m - 4) * 512>(im[(m - 4) / 2], a_ir);
            if constexpr (n >= 8 && (n & 7) == 4 && m == 4) { tie(im[0]); tie(im[1]); }
            if constexpr (p == 1 || p == 2) {
                constexpr int j = P - (p == 1 ? P_PP0 : P_PP1), a = (j + 1) / 2, k = j / 2 - 1;
                if constexpr (j == 0 || (j > 0 && j < 15 && (j & 1))) { sm[a & 1] = yc[(p - 1) & 1][0][a >> 2][a & 3] + im[a >> 2][a & 3]; tie(sm[a & 1]); }
                if constexpr (j >= 2 && j <= 16 && !(j & 1)) { po[k >> 2][k & 3] = (f2u(sm[k & 1]) & msk) | ((tag >> (8 * (k & 3))) & 255u); if constexpr ((k & 3) != 3) tie(po[k >> 2]); }
                if constexpr ((j == 8 || j == 16) && !(NOST & 2) && !(PPH && j == 8)) __builtin_amdgcn_raw_buffer_store_b128(po[k >> 2], rp, l16, ppo + (p - 1) * 16384 + (k >> 2) * 1024, 16);
            }
            if constexpr ((P == AT(22, 5) || P == AT(27, 5)) && !PPH) pp_ld0(pl[0], l16, rsp, ppo ^ (n == 27 ? 49152 : 32768));
            if constexpr (P == AT(22, 7) || P == AT(27, 7)) pp_ld1(pl[1], l16, rsp, ppo ^ (n == 27 ? 49152 : 32768));
            if constexpr (P == P_R0 || P == P_R1) { wait_v<g2_allow(P, P == P_R0 ? AT(22, 7) : AT(27, 7))>(); pp_tie<0>(pl[0]); pp_tie<1>(pl[1]); }
            if constexpr (P >= P_R0 && P < P_R0 + 11) rcv(IC(P - P_R0));
            if constexpr (P >= P_R1) rcv(IC(P - P_R1));
            if constexpr (P == P_R0 + 12 && !(NOST & 2) && !PPH) pp_chk(pl[0], pl[1], ck, tag, l16, rsp, ppo ^ 32768);
            if constexpr (P > P_R0 + 12 && P <= P_R0 + 20) addp(IC(2), IC(P - P_R0 - 13));
            if constexpr (!NOA) {
            if constexpr (n == 15 && m >= 4) dsr64<(m - 4) * 128>(zv[m - 4], a_zr);
            if constexpr (P == AT(15, 7) || P == AT(16, 5) || P == AT(18, 4) || P == AT(21, 5)) { constexpr int r = n == 15 ? 0 : n == 16 ? 1 : n == 18 ? 2 : 3; dsr64<r * 256>(qv[r & 1], a_qr); }
            if constexpr (P == AT(16, 4)) { tie(zv[0]); tie(zv[1]); tie(zv[2]); tie(zv[3]); tie(qv[0]); }
            if constexpr (P == AT(17, 4) || P == AT(22, 4)) tie(qv[1]);
            if constexpr (P == AT(19, 0)) tie(qv[0]);
            }
            if constexpr (a_step(P) >= 0) {
                constexpr int s = a_step(P), e = s == 0 ? 0 : s % 3 == 2 ? (s + 1) / 3 : s % 3 == 1 ? s / 3 : s / 3 - 1, r = e >> 2, c = e & 3;
                if constexpr (s == 0 || (s % 3 == 2 && e < 16)) { tq_[e & 1] = (c & 1) ? bfhi(qv[r & 1][c >> 1]) : bflo(qv[r & 1][c >> 1]); tie(tq_[e & 1]); }
                else if constexpr (s % 3 == 1) { tz = (r & 1) ? bfhi(zv[c][r >> 1]) : bflo(zv[c][r >> 1]); tie(tz); }
                else if constexpr (s % 3 == 0) {
                    ao[c] = tq_[e & 1] * tz;
                    if constexpr (c == 3 && !(NOST & 1)) __builtin_amdgcn_raw_buffer_store_b128(__builtin_bit_cast(u32x4, ao), ro, a_oa, (ta + r) * 8192 + xcd * 512, OSC);
                    else tie(ao);
                }
            }
            if constexpr (P >= P_F2 && P < P_F2 + 16) finB(IC(2), IC(P - P_F2));
            if constexpr (a_step(P - 1) > 0 && a_step(P - 1) % 12 == 0) keep(ao);
            if constexpr (P == P_PP0 + 9 || P == P_PP1 + 9) keep(po[0]);
            if constexpr (P == P_PP0 + 17 || P == P_PP1 + 17) keep(po[1]);
            if constexpr (P == P_F2 + 16) keep(ob[1]);
        };
        if (wid < 4) TS(7);
        TG_DECL
        barrier();
        static_for<0, 8>([&](auto ic) { ldh(IC(0), ic); });
        static_for<0, 32>([&](auto nc) {
            constexpr int n = CV(nc), p = n >> 3;
            if constexpr (n == 7) page<33>();
            if constexpr (n == 23) page<37>();
            wait_lr<((n & 3) == 3 && n < 31) ? 0 : 4 + g2_lx(n - 1)>();
            if constexpr ((n & 3) == 3 && n < 31) {
                constexpr int need = n == 3 ? -1 : n == 7 ? AT(2, 7) : n == 15 && !NOA ? AT(11, 4) : AT(n - 7, 7);
                TL(21 + n / 4 * 3);
                wait_v<g2_allow(8 * n, need)>();
                TL(22 + n / 4 * 3);
                barrier();
                TL(23 + n / 4 * 3);
            }
            if constexpr (n == 1) hop<31>();
            if constexpr (n == 4) hop<32>();
            if constexpr (n == 11) hop<34>();
            if constexpr (n == 15) hop<35>();
            if constexpr (n == 19) hop<36>();
            if constexpr (n == 27) hop<38>();
            if constexpr (n == 31) hop<39>();
            if constexpr (n == 0) TL(20);
            TG(20 + n);
            if constexpr (n == 31) TL(42);
            if constexpr (PRG) __builtin_amdgcn_s_setprio(n == 31 ? 0 : 3 - ((n + 1) & 3));
            sched();
            static_for<0, 8>([&](auto mc) {
                constexpr int m = CV(mc), ks = m >> 2, tt = (m >> 1) & 1, dd = m & 1, P = n * 8 + m;
                if constexpr (m == 4 && ((n & 3) != 3 || n == 31)) wait_lr<n < 31 ? (rd1(n) ? 8 : 4) : 0>();
                mfma<(n & 7) == 0 && ks == 0>(yc[p & 1][tt][dd], cat(hf[n & 1][4 * ks + 2 * tt], hf[n & 1][4 * ks + 2 * tt + 1]), cf[(n & 7) * 4 + 2 * ks + dd]);
                sched();
                if constexpr (n < 31 && rd1(n) && m < 4) { ldh(IC(n + 1), IC(2 * m)); ldh(IC(n + 1), IC(2 * m + 1)); }
                if constexpr (n < 31 && !rd1(n)) ldh(IC(n + 1), mc);
                if constexpr (g2_fill(P) >= 0) fill(IC(g2_fill(P) >> 2), IC(g2_fill(P) & 3));
                side(IC(P));
                sched();
            });
        });
        TG(52);
        if (wid >= 4) TS(5); else TS(8);
        TG(53);
        wait_l<0>();
        if constexpr (!(NOST & 2) && !PPH) pp_chk(pl[0], pl[1], ck, tag, l16, rsp, ppo ^ 49152);
        asm volatile("s_nop 5");
        dsw128<QST - IMG>(a_iw, yc[1][1][0]);
        dsw128<QST - IMG + 1024>(a_iw, yc[1][1][1]);
        static_for<0, 8>([&](auto ic) { addp(IC(3), ic); });
        wait_l<0>();
        barrier();
        dsr128<QST - IMG>(im[0], a_ir);
        dsr128<QST - IMG + 1024>(im[1], a_ir);
        wait_l<0>();
        tie(im[0]); tie(im[1]);
        static_for<0, 16>([&](auto jc) { finB(IC(3), jc); });
        if (b == 255 && wid == 0) __builtin_amdgcn_raw_buffer_store_b32(tag, rf, lane ? OOB : 0u, F_EP, 16);
    }
}

}

#if defined(__HIP_DEVICE_COMPILE__)
#pragma clang attribute pop
#endif

extern "C" void kernel_launch(void* const* d_in, const int* in_sizes, int n_in, void* d_out, int out_size, void* d_ws, size_t ws_size,
                              hipStream_t stream) {
    (void)in_sizes; (void)n_in; (void)out_size; (void)ws_size;
    using namespace fk;
    const uint16_t* q = (const uint16_t*)d_in[0];
    const uint16_t* kin = (const uint16_t*)d_in[1];
    const uint16_t* vin = (const uint16_t*)d_in[2];
    const uint16_t* sk = (const uint16_t*)d_in[3];
    const uint16_t* ssmk = (const uint16_t*)d_in[4];
    const uint16_t* ssmD = (const uint16_t*)d_in[5];
    const uint16_t* x = (const uint16_t*)d_in[6];
    const uint16_t* lre = (const uint16_t*)d_in[7];
    const uint16_t* lim = (const uint16_t*)d_in[8];
    const uint16_t* bre = (const uint16_t*)d_in[9];
    const uint16_t* bim = (const uint16_t*)d_in[10];
    const uint16_t* cre = (const uint16_t*)d_in[11];
    const uint16_t* cim = (const uint16_t*)d_in[12];
    const uint16_t* logdt = (const uint16_t*)d_in[13];
    const uint16_t* dskip = (const uint16_t*)d_in[14];
    char* ws = (char*)d_ws;
    hipLaunchKernelGGL(k1, dim3(256), dim3(512), 0, stream, x, bre, bim, lre, lim, logdt, cre, cim, dskip, ssmk, ssmD, q, kin, vin, sk, ws, (float*)d_out TS_PASS(0));
}
```

```cpp
#include <hip/hip_runtime.h>
#include <stdint.h>
#include <type_traits>

#if defined(__HIP_DEVICE_COMPILE__)
#pragma clang attribute push(__attribute__((target("no-packed-fp32-ops"))), apply_to = function)
#endif

namespace fk {

typedef __attribute__((ext_vector_type(8))) short bf16x8;
typedef __attribute__((ext_vector_type(4))) short bf16x4;
typedef __attribute__((ext_vector_type(4))) float f32x4;
typedef __attribute__((ext_vector_type(4))) unsigned u32x4;
typedef __attribute__((ext_vector_type(2))) unsigned u32x2;
typedef __attribute__((ext_vector_type(2))) __bf16 bf16x2_t;
typedef __attribute__((address_space(1))) unsigned gu32;
typedef __attribute__((address_space(1))) unsigned long long gu64;
#define RLX_AGENT __ATOMIC_RELAXED, __HIP_MEMORY_SCOPE_AGENT

__device__ __forceinline__ float u2f(unsigned v) { return __builtin_bit_cast(float, v); }
__device__ __forceinline__ unsigned f2u(float v) { return __builtin_bit_cast(unsigned, v); }
__device__ __forceinline__ bool wall(bool p) { return __builtin_amdgcn_ballot_w64(p) == ~0ull; }
__device__ __forceinline__ float bflo(unsigned w) { return u2f(w << 16); }
__device__ __forceinline__ float bfhi(unsigned w) { return u2f(w & 0xffff0000u); }
__device__ __forceinline__ unsigned pack2(float lo, float hi) {
    bf16x2_t v; v[0] = (__bf16)lo; v[1] = (__bf16)hi; return __builtin_bit_cast(unsigned, v);
}
__device__ __forceinline__ float rbf(float f) { float r; asm("v_cvt_pk_bf16_f32 %0, 0, %1" : "=v"(r) : "v"(f)); return r; }
__device__ __forceinline__ float fma_(float a, float b, float c) { return __builtin_fmaf(a, b, c); }

template <int I, int N, class F>
__device__ __forceinline__ void static_for(F&& f) {
    if constexpr (I + 1 == N) f(std::integral_constant<int, I>{});
    else if constexpr (I < N) { static_for<I, I + (N - I) / 2>(f); static_for<I + (N - I) / 2, N>(f); }
}
#define IC(v) std::integral_constant<int, (v)>{}
#define CV(c) decltype(c)::value

#define TS_ARG
#define TS_PASS(off)
#define TS_DECL
#define TS(i)
#define TSV(i, v)
#define TL_DECL
#define TL(i)
#define TS_FLUSH(slot)

#define TG_DECL
#define TG(i)
template <int N> __device__ __forceinline__ void wait_v() { asm volatile("s_waitcnt vmcnt(%0)" ::"n"(N > 63 ? 63 : N) : "memory"); }
template <int N> __device__ __forceinline__ void wait_l() { asm volatile("s_waitcnt lgkmcnt(%0)" ::"n"(N > 14 ? 14 : N) : "memory"); }
template <int N> __device__ __forceinline__ void wait_lr() { __builtin_amdgcn_s_waitcnt(0xc07f | ((N > 14 ? 14 : N) << 8)); }
template <class T> __device__ __forceinline__ void tie(T& x) { asm volatile("" : "+v"(x)); }
template <class T> __device__ __forceinline__ void keep(const T& x) { asm volatile("" :: "v"(x)); }
template <int OFF, class T> __device__ __forceinline__ void dsr128(T& d, unsigned a) { asm volatile("ds_read_b128 %0, %1 offset:%2" : "=v"(d) : "v"(a), "n"(OFF)); }
template <int OFF, class T> __device__ __forceinline__ void dsr64(T& d, unsigned a) { asm volatile("ds_read_b64 %0, %1 offset:%2" : "=v"(d) : "v"(a), "n"(OFF)); }
template <int OFF> __device__ __forceinline__ void dsr32(unsigned& d, unsigned a) { asm volatile("ds_read_b32 %0, %1 offset:%2" : "=v"(d) : "v"(a), "n"(OFF)); }
template <int O0, int O1, class T> __device__ __forceinline__ void dsr2(T& d, unsigned a) { asm volatile("ds_read2_b32 %0, %1 offset0:%2 offset1:%3" : "=v"(d) : "v"(a), "n"(O0), "n"(O1)); }
template <int OFF> __device__ __forceinline__ void dsr64tr(bf16x4& d, unsigned a) { asm volatile("ds_read_b64_tr_b16 %0, %1 offset:%2" : "=v"(d) : "v"(a), "n"(OFF)); }
template <int OFF, class T> __device__ __forceinline__ void dsw128(unsigned a, const T& v) { asm volatile("ds_write_b128 %0, %1 offset:%2" :: "v"(a), "v"(v), "n"(OFF) : "memory"); }
__device__ __forceinline__ bf16x8 cat(const bf16x4& lo, const bf16x4& hi) { return __builtin_shufflevector(lo, hi, 0, 1, 2, 3, 4, 5, 6, 7); }
__device__ __forceinline__ void barrier() { asm volatile("" ::: "memory"); __builtin_amdgcn_s_barrier(); asm volatile("" ::: "memory"); }
__device__ __forceinline__ int fresh(int v) { asm volatile("" : "+s"(v)); return v; }
__device__ __forceinline__ void sched() { __builtin_amdgcn_sched_barrier(0); }
template <bool FIRST> __device__ __forceinline__ void mfma(f32x4& c, const bf16x8& a, const bf16x8& b) {
    if constexpr (FIRST) asm volatile("v_mfma_f32_16x16x32_bf16 %0, %1, %2, 0" : "=v"(c) : "v"(a), "v"(b));
    else asm volatile("v_mfma_f32_16x16x32_bf16 %0, %1, %2, %0" : "+v"(c) : "v"(a), "v"(b));
}
struct cf32 { float r, i; };
__device__ __forceinline__ cf32 cmul(cf32 a, cf32 b) { return cf32{fma_(a.r, b.r, -(a.i * b.i)), fma_(a.r, b.i, a.i * b.r)}; }
__device__ __forceinline__ cf32 cfma(cf32 a, cf32 b, cf32 c) { return cf32{fma_(a.r, b.r, fma_(-a.i, b.i, c.r)), fma_(a.r, b.i, fma_(a.i, b.r, c.i))}; }
__device__ __forceinline__ void sw16(float v, float& e, float& o) { const auto r = __builtin_amdgcn_permlane16_swap(f2u(v), f2u(v), false, false); e = u2f(r[0]); o = u2f(r[1]); }
__device__ __forceinline__ void sw32(float v, float& e, float& o) { const auto r = __builtin_amdgcn_permlane32_swap(f2u(v), f2u(v), false, false); e = u2f(r[0]); o = u2f(r[1]); }
constexpr unsigned SPIN_CAP = 1u << 16;
constexpr unsigned OOB = 0x40000000u;

constexpr size_t WS_H = 0;
constexpr size_t WS_KVT = (size_t)8 << 20;
constexpr size_t WS_ZT = (size_t)12 << 20;
constexpr size_t WS_PP = (size_t)16 << 20;
constexpr size_t WS_E = (size_t)24 << 20;
constexpr size_t WS_F = WS_E + (256 << 10);
constexpr int F_H = 0;
constexpr int F_K = 16384;
constexpr int F_Z = 24576;
constexpr int F_EP = F_Z + 32768;
constexpr int LDS_TOTAL = 163840, BUF = 32768;
constexpr int KC = 98304, KCH = 8320, KCP = 4160;
constexpr int U0 = KC + 4 * KCH;
constexpr int FULL = U0 + 18432 + 8192, FST = 388;
constexpr int HGO = FULL + 1792, DSK = HGO + 256, POLL = DSK + 256;
static_assert(POLL + 8 * 256 <= LDS_TOTAL && 4 * FST <= 1792, "lds");
constexpr int g1_buf(int s) { return ((0x0213 >> ((s & 3) * 4)) & 3) * BUF; }
constexpr int IMG = KC, QST = KC + 16384, ZST = U0, PLD = 2 * BUF;
#define CODE_KB 46
#define EXACT 1
#define HP 1
#define CP 2
#define FLQ 11
#define R01 3
#define ZP 7
#define SLP 8
#define STR_(x) #x
#define STR(x) STR_(x)
#define NOST 0
#define G2M 0
#define RD 2
#define PRG 1
#define OSC 16
#define NOA 0
#define PPH 0
#define HS 0
#define JQ(j) (HS == 0 ? 0 : HS == 1 ? (j) & 1 : HS == 2 ? (j) : (j) & 2)
#define JR(j) (HS == 0 ? 2 * (j) : HS == 1 ? 2 * ((j) & 2) : HS == 2 ? 0 : 4 * ((j) & 1))
#define NOX 0

__device__ __forceinline__ u32x4 mk_rsrc(const void* p, unsigned bytes) {
    const unsigned long long a = (unsigned long long)p;
    return u32x4{(unsigned)a, (unsigned)(a >> 32) & 0xffffu, bytes, 0x00020000u};
}
__device__ __forceinline__ __amdgpu_buffer_rsrc_t mk_brs(const void* p, unsigned bytes) { return __builtin_amdgcn_make_buffer_rsrc((void*)p, (short)0, bytes, 0x00020000); }
template <int IMM, bool SC1> __device__ __forceinline__ void dma(unsigned lds, unsigned voff, const u32x4& rs, unsigned soff) {
    if constexpr (SC1) asm volatile("s_add_u32 m0, %0, %1\n\ts_nop 0\n\tbuffer_load_dwordx4 %2, %3, %4 offen sc1 lds" :: "s"(lds), "n"(IMM), "v"(voff), "s"(rs), "s"(soff) : "memory", "scc");
    else asm volatile("s_add_u32 m0, %0, %1\n\ts_nop 0\n\tbuffer_load_dwordx4 %2, %3, %4 offen lds" :: "s"(lds), "n"(IMM), "v"(voff), "s"(rs), "s"(soff) : "memory", "scc");
}
template <int IMM> __device__ __forceinline__ void dma1(unsigned lds, unsigned voff, const u32x4& rs, unsigned soff) {
    asm volatile("s_add_u32 m0, %0, %1\n\ts_nop 0\n\tbuffer_load_dword %2, %3, %4 offen sc1 lds" :: "s"(lds), "n"(IMM), "v"(voff), "s"(rs), "s"(soff) : "memory", "scc");
}
__device__ __forceinline__ void flag_set(unsigned waddr, unsigned v) { asm volatile("ds_write_b32 %0, %1" :: "v"(waddr), "v"(v) : "memory"); }
__device__ __forceinline__ void flag_chk(unsigned& f, unsigned raddr, unsigned need) {
    unsigned cnt;
    asm volatile("v_cmp_le_u32 vcc, %3, %0\n\t"
                 "s_cmp_eq_u64 vcc, -1\n\t"
                 "s_cbranch_scc1 1f\n\t"
                 "s_mov_b32 %1, 0x100000\n"
                 "0:\n\t"
                 "ds_read_b32 %0, %2\n\t"
                 "s_sub_u32 %1, %1, 1\n\t"
                 "s_waitcnt lgkmcnt(0)\n\t"
                 "s_cbranch_scc1 1f\n\t"
                 "v_cmp_le_u32 vcc, %3, %0\n\t"
                 "s_cmp_eq_u64 vcc, -1\n\t"
                 "s_cbranch_scc0 0b\n"
                 "1:" : "+v"(f), "=&s"(cnt) : "v"(raddr), "s"(need) : "vcc", "scc", "memory");
}
template <int OFF> __device__ __forceinline__ void zero_chk(unsigned& f, unsigned raddr) {
    unsigned cnt;
    asm volatile("v_cmp_ne_u32 vcc, 0, %0\n\t"
                 "s_cbranch_vccz 1f\n\t"
                 "s_mov_b32 %1, 0x100000\n"
                 "0:\n\t"
                 "ds_read_b32 %0, %2 offset:%3\n\t"
                 "s_sub_u32 %1, %1, 1\n\t"
                 "s_waitcnt lgkmcnt(0)\n\t"
                 "s_cbranch_scc1 1f\n\t"
                 "v_cmp_ne_u32 vcc, 0, %0\n\t"
                 "s_cbranch_vccnz 0b\n"
                 "1:" : "+v"(f), "=&s"(cnt) : "v"(raddr), "n"(OFF) : "vcc", "scc", "memory");
}
template <int IMM, class T> __device__ __forceinline__ void gld(T& d, unsigned voff, const u32x4& rs, unsigned soff) {
    if constexpr (sizeof(T) == 16) asm volatile("buffer_load_dwordx4 %0, %1, %2, %3 offen offset:%4" : "=v"(d) : "v"(voff), "s"(rs), "s"(soff), "n"(IMM) : "memory");
    else if constexpr (sizeof(T) == 8) asm volatile("buffer_load_dwordx2 %0, %1, %2, %3 offen offset:%4" : "=v"(d) : "v"(voff), "s"(rs), "s"(soff), "n"(IMM) : "memory");
    else asm volatile("buffer_load_ushort %0, %1, %2, %3 offen offset:%4" : "=v"(d) : "v"(voff), "s"(rs), "s"(soff), "n"(IMM) : "memory");
}
__device__ __forceinline__ void gld32(unsigned& d, unsigned voff, const u32x4& rs, unsigned soff) { asm volatile("buffer_load_dword %0, %1, %2, %3 offen" : "=v"(d) : "v"(voff), "s"(rs), "s"(soff) : "memory"); }
__device__ __forceinline__ void sld(unsigned& d, const void* p) { asm volatile("s_load_dword %0, %1, 0x0" : "=s"(d) : "s"(p) : "memory"); }
__device__ __forceinline__ void stouch(unsigned long long p) { asm volatile("s_load_dword s101, %0, 0x0\n\ts_load_dword s101, %0, 0x80\n\ts_load_dword s101, %0, 0x100" :: "s"(p) : "s101", "memory"); }
__device__ __forceinline__ void ties(unsigned& x) { asm volatile("" : "+s"(x)); }
template <int SLQ> __device__ __forceinline__ void gate(unsigned raddr, unsigned need) {
    unsigned cnt, f;
    asm volatile("s_mov_b32 %1, 0x40000\n"
                 "0:\n\t"
                 "ds_read_b32 %0, %2\n\t"
                 "s_sub_u32 %1, %1, 1\n\t"
                 "s_waitcnt lgkmcnt(0)\n\t"
                 "s_cbranch_scc1 1f\n\t"
                 "v_cmp_le_u32 vcc, %3, %0\n\t"
                 "s_cmp_eq_u64 vcc, -1\n\t"
                 "s_cbranch_scc1 1f\n\t"
                 "s_sleep %4\n\t"
                 "s_branch 0b\n"
                 "1:" : "=&v"(f), "=&s"(cnt) : "v"(raddr), "s"(need), "n"(SLQ) : "vcc", "scc", "memory");
}
__device__ __forceinline__ void tag_chk(unsigned& f, unsigned expect, unsigned voff, const u32x4& rs, unsigned soff) {
    unsigned cnt;
    asm volatile("v_cmp_eq_u32 vcc, %2, %0\n\t"
                 "s_cmp_eq_u64 vcc, -1\n\t"
                 "s_cbranch_scc1 1f\n\t"
                 "s_mov_b32 %1, 0x10000\n"
                 "0:\n\t"
                 "s_sleep " STR(SLP) "\n\t"
                 "buffer_load_dword %0, %3, %4, %5 offen sc1\n\t"
                 "s_sub_u32 %1, %1, 1\n\t"
                 "s_waitcnt vmcnt(0)\n\t"
                 "s_cbranch_scc1 1f\n\t"
                 "v_cmp_eq_u32 vcc, %2, %0\n\t"
                 "s_cmp_eq_u64 vcc, -1\n\t"
                 "s_cbranch_scc0 0b\n"
                 "1:" : "+v"(f), "=&s"(cnt) : "v"(expect), "v"(voff), "s"(rs), "s"(soff) : "vcc", "scc", "memory");
}

__device__ __forceinline__ void pp_ld0(u32x4& d, unsigned voff, const u32x4& rs, unsigned soff) { asm volatile("buffer_load_dwordx4 v[240:243], %1, %2, %3 offen sc1" : "={v[240:243]}"(d) : "v"(voff), "s"(rs), "s"(soff) : "memory"); }
__device__ __forceinline__ void pp_ld1(u32x4& d, unsigned voff, const u32x4& rs, unsigned soff) { asm volatile("buffer_load_dwordx4 v[244:247], %1, %2, %3 offen offset:1024 sc1" : "={v[244:247]}"(d) : "v"(voff), "s"(rs), "s"(soff) : "memory"); }
__device__ __forceinline__ void pf_ld(unsigned& d, unsigned voff, const u32x4& rs, unsigned soff) { asm volatile("buffer_load_dword v248, %1, %2, %3 offen sc1" : "={v248}"(d) : "v"(voff), "s"(rs), "s"(soff) : "memory"); }
__device__ __forceinline__ void pp_chk(u32x4& p0, u32x4& p1, unsigned bad, unsigned tag, unsigned voff, const u32x4& rs, unsigned soff) {
    unsigned cnt, t;
    asm volatile("v_cmp_ne_u32 vcc, 0, %3\n\t"
                 "s_cbranch_vccz 1f\n\t"
                 "s_mov_b32 %2, 0x10000\n"
                 "0:\n\t"
                 "buffer_load_dwordx4 v[240:243], %6, %7, %8 offen sc1\n\t"
                 "buffer_load_dwordx4 v[244:247], %6, %7, %8 offen offset:1024 sc1\n\t"
                 "s_waitcnt vmcnt(0)\n\t"
                 "s_and_b32 %4, %5, 0xff\n\t"
                 "v_xor_b32 v240, %4, v240\n\t"
                 "v_xor_b32 v244, %4, v244\n\t"
                 "s_bfe_u32 %4, %5, 0x80008\n\t"
                 "v_xor_b32 v241, %4, v241\n\t"
                 "v_xor_b32 v245, %4, v245\n\t"
                 "s_bfe_u32 %4, %5, 0x80010\n\t"
                 "v_xor_b32 v242, %4, v242\n\t"
                 "v_xor_b32 v246, %4, v246\n\t"
                 "s_lshr_b32 %4, %5, 24\n\t"
                 "v_xor_b32 v243, %4, v243\n\t"
                 "v_xor_b32 v247, %4, v247\n\t"
                 "v_or3_b32 %3, v240, v241, v242\n\t"
                 "v_or3_b32 %3, %3, v243, v244\n\t"
                 "v_or3_b32 %3, %3, v245, v246\n\t"
                 "v_or_b32 %3, %3, v247\n\t"
                 "v_and_b32 %3, 0xff, %3\n\t"
                 "s_sub_u32 %2, %2, 1\n\t"
                 "s_cbranch_scc1 1f\n\t"
                 "v_cmp_ne_u32 vcc, 0, %3\n\t"
                 "s_cbranch_vccnz 0b\n"
                 "1:" : "+{v[240:243]}"(p0), "+{v[244:247]}"(p1), "=&s"(cnt), "+v"(bad), "=&s"(t)
                 : "s"(tag), "v"(voff), "s"(rs), "s"(soff) : "vcc", "scc", "memory");
}
template <int H> __device__ __forceinline__ void pp_tie(u32x4& p) { if constexpr (H) asm volatile("" : "+{v[244:247]}"(p)); else asm volatile("" : "+{v[240:243]}"(p)); }
__device__ __forceinline__ float sel8(float a, float b, unsigned long long mask) {
    float r;
    asm volatile("s_mov_b64 vcc, %3\n\tv_cndmask_b32_dpp %0, %1, %2, vcc row_ror:8 row_mask:0xf bank_mask:0xf" : "=v"(r) : "v"(a), "v"(b), "s"(mask) : "vcc");
    return r;
}
__device__ __forceinline__ unsigned sel0(unsigned a, unsigned long long mask) { unsigned r; asm volatile("v_cndmask_b32_e64 %0, 0, %1, %2" : "=v"(r) : "v"(a), "s"(mask)); return r; }
constexpr unsigned long long diag_mask(int R) {
    unsigned long long m = 0;
    for (int l = 0; l < 64; ++l) if ((l >> 4) == ((l & 7) >> 1) && 2 * (l & 1) + ((l >> 3) & 1) == R) m |= 1ull << l;
    return m;
}
template <int IMM, int X> __device__ __forceinline__ void dma_x(unsigned& so, unsigned lds, unsigned voff, const u32x4& rs, unsigned base) {
    asm volatile("s_add_u32 m0, %1, %2\n\ts_xor_b32 %0, %5, %6\n\tbuffer_load_dwordx4 %3, %4, %0 offen sc1 lds" : "=&s"(so) : "s"(lds), "n"(IMM), "v"(voff), "s"(rs), "s"(base), "n"(X) : "memory", "scc");
}
template <int OFF> __device__ __forceinline__ void dma_n(unsigned so, unsigned voff, const u32x4& rs) {
    asm volatile("buffer_load_dwordx4 %0, %1, %2 offen offset:%3 sc1 lds" :: "v"(voff), "s"(rs), "s"(so), "n"(OFF) : "memory");
}
__device__ __forceinline__ unsigned and_or(unsigned a, unsigned m, unsigned o) { unsigned r; asm("v_and_or_b32 %0, %1, %2, %3" : "=v"(r) : "v"(a), "v"(m), "s"(o)); return r; }

#define HOPS 1
#define HM 0x2210243082ull
#define HQ 52
#define HW 4
constexpr int hop_nth(int n) { for (int k = 0; k < 40; ++k) if (((HM >> k) & 1) && n-- == 0) return k; return 63; }
constexpr int hop_next(int K) { int n = 0; for (int k = 0; k < K; ++k) n += (HM >> k) & 1; return hop_nth(n + HW); }
template <int K> __device__ __forceinline__ void hop() {
    if constexpr (HOPS && ((HM >> K) & 1)) asm volatile(".Lfkh%0:\n\ts_cbranch_execz .Lfkh%1" :: "n"(K), "n"(hop_next(K)));
}
template <int K> __device__ __forceinline__ void page() {
    if constexpr (HOPS && K >= 0 && ((HM >> (K & 63)) & 1)) asm volatile("s_branch 1f\n\t.p2align 12\n.Lfkh%0:\n\ts_cbranch_execz .Lfkh%1\n1:" :: "n"(K), "n"(hop_next(K)));
    else if constexpr (HOPS) asm volatile("s_branch 1f\n\t.p2align 12\n1:");
}
__device__ __forceinline__ void hop_all(int w) {
    if constexpr (HOPS) asm volatile("s_mov_b64 exec, 0\n\ts_cmp_eq_u32 %0, 0\n\ts_cbranch_scc1 .Lfkh%1\n\ts_cmp_eq_u32 %0, 1\n\ts_cbranch_scc1 .Lfkh%2\n\t"
                                     "s_cmp_eq_u32 %0, 2\n\ts_cbranch_scc1 .Lfkh%3\n\ts_branch .Lfkh%4\n.Lfkh63:\n\ts_mov_b64 exec, -1"
                                     :: "s"(w), "n"(hop_nth(0)), "n"(hop_nth(1)), "n"(hop_nth(2)), "n"(hop_nth(3)) : "scc");
}
enum { OP_NONE, OP_A, OP_B, OP_FLAG };
struct Op { int kind, a, b; };
constexpr Op g1_op(int G) {
    const int st = (G + 32) / 32 - 1, q = G - 32 * st, i = q >> 2, j = q & 3;
    if (i < 5) {
        if (st < 0) return {OP_NONE, 0, 0};
        if ((j & 1) == 0) return {OP_A, st * 8 + i + 3, j >> 1};
        if (q == FLQ && st < 15) return {OP_FLAG, 0, 0};
        return {OP_NONE, 0, 0};
    }
    if (st == 15) return {OP_NONE, 0, 0};
    const int n = (st + 1) * 8;
    switch (q) {
        case 20: return {OP_B, st + 1, 0}; case 21: return {OP_B, st + 1, 1}; case 22: return {OP_A, n, 0}; case 23: return {OP_B, st + 1, 2};
        case 24: return {OP_B, st + 1, 3}; case 25: return {OP_A, n, 1}; case 26: return {OP_A, n + 1, 0}; case 27: return {OP_A, n + 1, 1};
        case 28: return {OP_A, n + 2, 0}; case 30: return {OP_A, n + 2, 1};
    }
    return {OP_NONE, 0, 0};
}
constexpr int g1_need(int G) {
    const int st = G >> 5, j = G & 3;
    for (int h = G - 1; h >= -12; --h) {
        const Op o = g1_op(h);
        if (o.kind == OP_A && o.a == (G >> 2) && o.b == (j >> 1)) return h;
        if (o.kind == OP_B && o.a == st && o.b == j) return h;
        if (o.kind == OP_FLAG && (G & 31) == 20 && (h >> 5) == st) return h;
    }
    return -100;
}
constexpr int g1_wait(int G) {
    int n = -100, c = 0;
    for (int h = G; h < (G ? G + 4 : 2) && h < 512; ++h) if (g1_need(h) > n) n = g1_need(h);
    for (int h = n + 1; h < G; ++h) c += g1_op(h).kind != OP_NONE;
    return n < G ? c : -1;
}
#define KV0 0
#define KVN 0x11111112
constexpr int kv_first(int k) { int n = 0; for (int i = KV0; i < k && i < KV0 + 8; ++i) n += (KVN >> (4 * (i - KV0))) & 15; return n > 8 ? 8 : n; }
constexpr int kv_last() { int k = KV0; while (kv_first(k + 1) < 8) ++k; return k; }
#define CQ -1
#define CTM 0
#define EC 2
#define ED 2
#define EB 1
#define KFD 3
#define KPL 11
#define CBQ 20
constexpr int KVL = kv_last(), KFL = KVL + KFD;
static_assert(KFL <= KPL && KPL <= 11, "");
constexpr int hv_cnt(int k0, int k1) {
    int c = 0;
    for (int k = k0; k < k1; ++k) c += 9 + (kv_first(k + 1) / 2 - kv_first(k) / 2) + (k == KFL) + (k == KPL);
    return c;
}
constexpr int cv_start(int n) { return n * 8 - n * (n - 1) / 2; }
constexpr int cv_n(int q) { int n = 0; while (cv_start(n + 1) <= q) ++n; return n; }
constexpr int cv_m(int q) { return 7 - cv_n(q) - (q - cv_start(cv_n(q))); }
enum { CO_NONE, CO_A, CO_B, CO_C };
constexpr Op cv_op(int G) {
    const int s = (G + 36) / 36 - 1, q = G - 36 * s;
    if (q >= 1 && q <= 6) return s >= 0 ? Op{CO_A, s * 8 + 5 + (q - 1) / 2, (q - 1) & 1} : Op{CO_NONE, 0, 0};
    if (q >= 11 && q <= 14) return s >= 2 ? Op{CO_C, s - 2, q - 11} : Op{CO_NONE, 0, 0};
    if (s == 7) return {CO_NONE, 0, 0};
    for (int m = 0; m < 8; ++m) if (q == cv_start(7 - m)) return {CO_B, (s + 1) * 8 + m, 0};
    for (int n = 0; n < 5; ++n) if (q == cv_start(n + 1) + 1 || q == cv_start(n + 1) + 2) return {CO_A, (s + 1) * 8 + n, q - cv_start(n + 1) - 1};
    return {CO_NONE, 0, 0};
}
constexpr int cv_need(int G) {
    const int s = G / 36, q = G % 36;
    for (int h = G - 1; h >= -36; --h) {
        const Op o = cv_op(h);
        if (o.kind == CO_A && o.a == s * 8 + cv_n(q)) return h;
        if (o.kind == CO_B && o.a == s * 8 + cv_m(q)) return h;
        if (o.kind == CO_C && q == 24 && o.a == s - 2) return h;
    }
    return -100;
}
constexpr int cv_wait(int G) {
    const int n = cv_need(G);
    for (int h = G - 1; h >= 0 && h >= G - 40; --h) if (cv_need(h) >= n) return -1;
    int c = 0;
    for (int h = n + 1; h < G; ++h) c += cv_op(h).kind != CO_NONE;
    return c;
}
#define AT(n, m) ((n) * 8 + (m))
constexpr int g2_fill(int p) {
    const int n = p >> 3, m = p & 7;
    if (m != 5 && m != 7) return -1;
    if (n < 2) return 8 + 2 * n + (m == 7);
    if (n <= 20 && ((n & 3) == 3 || (n & 3) == 0)) return ((n + 1) / 4 + 2) * 4 + ((n & 3) ? 0 : 2) + (m == 7);
    return -1;
}
#define DBG 0
#define XW 4
#define XW2 4
#define QPP0 AT(12, 0)
#define QL0 AT(21, 1)
#define QR0 AT(25, 0)
#define QD2 AT(26, 5)
#define QF2 AT(28, 4)
#define QL1 AT(27, 5)
#define QA AT(16, 4)
#define AD 0
constexpr int P_PP0 = QPP0, P_PP1 = AT(16, 4), P_L0 = QL0, P_R0 = QR0, P_D2 = QD2, P_F2 = QF2, P_L1 = QL1, P_R1 = AT(30, 5), P_ZC = AT(10, 6), P_A = QA;
constexpr int ZW = P_A >= AT(20, 4) ? 19 : 15;
static_assert(P_D2 > P_R0 + 12 && P_D2 + 8 <= P_F2 && P_D2 + 8 <= P_L1 && ((P_D2 >= AT(20, 3) && P_D2 + 7 <= AT(21, 6)) || P_D2 >= AT(24, 3)) && P_F2 >= AT(24, 4) && P_F2 + 8 <= AT(31, 0), "");
static_assert(P_PP0 >= AT(8, 4) && P_PP0 + 14 <= AT(15, 4) && P_A >= AT(16, 4), "");
constexpr bool a_free(int p) {
    return p >= P_A && (AD || !((p >= P_PP1 && p <= P_PP1 + 16) || (p >= P_R0 && p <= P_R0 + 10) || (p >= P_D2 && p < P_D2 + 8) || (p >= P_F2 && p < P_F2 + 16)));
}
constexpr int a_step(int p) {
    if (NOA || !a_free(p)) return -1;
    int c = 0;
    for (int h = P_A; h < p; ++h) c += a_free(h);
    return c <= 48 ? c : -1;
}
constexpr int a_pos(int s) { int p = P_A; while (a_step(p) != s) ++p; return p; }
constexpr int q_rd(int r) { int p = r < 2 ? AT(ZW, 6) + r : a_pos(12 * r - 16) + 1; while ((p & 7) < 4) ++p; return p; }
constexpr int q_tie(int r) { const int n = (q_rd(r) >> 3) + 1; return AT(n, (n & 3) == 3 ? 0 : 4); }
static_assert(NOA || (a_pos(48) < 256 && q_tie(2) <= a_pos(23) && q_tie(3) <= a_pos(35)), "");
constexpr int g2_vm(int p) {
    int c = g2_fill(p) >= 0;
    c += a_step(p) > 0 && a_step(p) % 12 == 0 && !(NOST & 1);
    c += !NOA && (p == AT(3, 4) || p == AT(5, 5) || p == AT(5, 7) || p == AT(ZP, 4) || p == AT(11, 3) || p == AT(11, 4));
    c += p == AT(2, 5) || p == AT(2, 7);
    c += !(NOST & 2) && (p == P_PP0 + 16 || p == P_PP1 + 16 || (!PPH && (p == P_PP0 + 8 || p == P_PP1 + 8)));
    c += p == P_L0 + 2 || p == P_L1 + 2 || (!PPH && (p == P_L0 || p == P_L1));
    c += !(NOST & 4) && (p == P_F2 + 11 || p == P_F2 + 15);
    return c;
}
constexpr int g2_lx(int n) {
    int c = n == 2;
    if ((n & 7) >= 6) c += 2;
    if (!NOA) { c += 4 * (n == ZW); for (int r = 0; r < 4; ++r) c += (q_rd(r) >> 3) == n; }
    return c;
}
constexpr int g2_tile(int n, int j) { return (n & 4) ? (n & 2) == 0 : j; }
constexpr int g2_unit(int n, int j) { return (n & 4) ? 4 + 2 * (n & 1) + j : n & 7; }
constexpr bool rd1(int n) { return RD == 1 || (RD == 3 && (n & 3) == 2); }
constexpr int g2_allow(int e, int p) { int c = 0; for (int h = p + 1; h < e; ++h) c += g2_vm(h); return c; }

__global__ __launch_bounds__(512, 2) __attribute__((aligned(4096))) void k1(const uint16_t* __restrict__ x, const uint16_t* __restrict__ bre, const uint16_t* __restrict__ bim,
                                             const uint16_t* __restrict__ lre, const uint16_t* __restrict__ lim, const uint16_t* __restrict__ logdt,
                                             const uint16_t* __restrict__ cre, const uint16_t* __restrict__ cim, const uint16_t* __restrict__ dskip,
                                             const uint16_t* __restrict__ ssmk, const uint16_t* __restrict__ ssmD, const uint16_t* __restrict__ qin,
                                             const uint16_t* __restrict__ kin, const uint16_t* __restrict__ vin, const uint16_t* __restrict__ sk,
                                             char* __restrict__ ws, float* __restrict__ out TS_ARG) {
    unsigned long long pc0;
    asm volatile("s_getpc_b64 %0" : "=s"(pc0));
    TS_DECL TS(0);
    TL_DECL
    __shared__ __attribute__((aligned(1024))) char shm[LDS_TOTAL];
    const int tid = threadIdx.x, lane = tid & 63, wid = __builtin_amdgcn_readfirstlane(tid >> 6);
    const int b = blockIdx.x, xcd = b & 7, idx = b >> 3, hb = xcd & 1, tq = idx & 7;
    const int tm = (xcd >> 1) * 4 + (idx >> 3), tn = hb * 8 + tq;
    const int tm2 = G2M ? tm : (xcd & 2) * 4 + tq, dt = G2M ? tq : (xcd & 4) + (idx >> 3), rot2 = tq + JR(idx >> 3);
    const unsigned lbase = (unsigned)(uintptr_t)shm;
    const int l15 = lane & 15, g4 = lane >> 4, lw = wid & 3, kg = wid >> 2;
    dma<U0 + 4096, false>(lbase + wid * 1024, lane * 16, mk_rsrc((const void*)(pc0 & ~255ull), CODE_KB * 1024), ((idx * 8 + wid) & 63) * 1024);
    const unsigned mk0 = lbase + POLL + lw * 256 + kg * (FULL - POLL + lw * (FST - 256));
    flag_set(mk0 + lane * 4, 1u);
    flag_set(lbase + HGO + lane * 4, 0u);
    unsigned vo[4];
    const bool xw = lw < 2;
#pragma unroll
    for (int i = 0; i < 4; ++i) {
        const int v = l15 ^ (i * 4 + g4);
        vo[i] = ((xw ? 64 * (i & 1) + 4 * (i >> 1) + (lane & 32) + 2 * (g4 & 1) : 8 * (i & 1) + 2 * g4) + (v >> 3)) * 2048 + (v & 7) * 16;
    }
    const int bw = lw >> 1;
    unsigned r0 = (tm * 128 + lw * 16 + bw * (tn * 64 + lw * 16 - 64 - tm * 128)) * 2048, rsz = 229376 - bw * 163840;
    unsigned rot = 8 * ((tq + (idx >> 3)) & 1) + 2 * (xcd >> 1) + 4 * hb, rst = 16384 << bw, lb = lbase + lw * 8192, oob = OOB;
    unsigned lb0 = lb + g1_buf(0) + kg * (g1_buf(1) - g1_buf(0)), so0 = ((kg + rot) & 15) * 128;
    unsigned co = l15 * 2 + (tn * 128 + lw * 32 + kg * 2048);
    tie(vo[0]); tie(vo[1]); tie(vo[2]); tie(vo[3]); tie(oob); tie(co);
    ties(r0); ties(rsz); ties(rot); ties(rst); ties(lb); ties(lb0); ties(so0);
    sched();
    asm volatile("" :: "s"(x), "s"(bre), "s"(bim), "s"(lre), "s"(lim), "s"(logdt), "s"(cre), "s"(cim), "s"(dskip), "s"(ssmk), "s"(ssmD), "s"(qin),
                       "s"(kin), "s"(vin), "s"(sk), "s"(ws), "s"(out));
    const u32x4 rs0 = mk_rsrc((const char*)(xw ? x : bre) + r0, rsz), rs1 = mk_rsrc((const char*)(xw ? x : bim) + r0, rsz);
    unsigned c_dt, c_re, c_im;
    gld<0>(c_dt, co, mk_rsrc(logdt, 2048), 0);
    gld<0>(c_re, co, mk_rsrc(lre, 2048), 0);
    gld<0>(c_im, co, mk_rsrc(lim, 2048), 0);
    static_for<0, 8>([&](auto ic) { constexpr int i = CV(ic); dma<i * 1024, false>(lb0, vo[i & 3], (i & 2) ? rs1 : rs0, so0 + (i >> 2) * rst); });
    wait_l<0>();
    barrier();
    dma1<0>(mk0 + kg * 120, oob, rs0, 0);
    sched();
    const unsigned a00 = (l15 >> 1) * 256 + ((((l15 & 1) * 8 + g4) ^ (l15 >> 1)) << 4);
    auto frag = [&](int par, int ks) { return (a00 ^ (ks * 64) ^ (par * 128)) + par * 2048; };
    bf16x8 cf[32];
    unsigned vc[4];
    auto c_setup = [&]() {
#pragma unroll
        for (int i = 0; i < 4; ++i) { const int v = l15 ^ (i * 4 + g4); vc[i] = (4 * (2 * g4 + (v >> 3)) + 2 * (i & 1) + (i >> 1)) * 2048 + (v & 7) * 16; }
    };
    const u32x4 rsc = mk_rsrc((kg ? cim : cre) + (size_t)dt * 131072, 262144);
    auto c_off = [&](int dg, int j) { return (unsigned)(dg * 65536 + hb * 1024 + ((rot2 + j) & 7) * 128); };
    const unsigned cst = lbase + (kg ? BUF : 2 * BUF) + lw * 8192;
    unsigned tag;
    const u32x4 rsf = mk_rsrc(ws + WS_F, 65536);
    const __amdgpu_buffer_rsrc_t rf = mk_brs(ws + WS_F, 65536);
    const u32x4 rsh = mk_rsrc(ws + WS_H + (size_t)(tm2 * 16 + hb * 8) * 32768, 262144);
    const int qx = (hb ? 0 : 2) ^ JQ(idx >> 3), wid_ = wid, b_ = b;
    unsigned ha[4];
    auto h_setup = [&](int kg) {
        const int q = (lane >> 2) & 3, p = lane & 3;
#pragma unroll
        for (int i = 0; i < 4; ++i) {
            ha[i] = lbase + (g4 >> 1) * 2048 + kg * 1024 + ((i >> 1) ^ kg) * 512 + (p >> 1) * 256 + (((8 * (g4 & 1) + 4 * (i & 1) + q) ^ (4 * (p >> 1))) << 4) + (p & 1) * 8;
            tie(ha[i]);
        }
    };

    if (wid >= 4) {
        page<-1>();
        const int ch = xcd * 128 + (idx >> 2) * 16 + lw * 4 + (idx & 3);
        unsigned dw;
        auto ctouch = [&](int r) { const unsigned j = (fresh(idx * 4 + lw) + r * 37) & 127; stouch((pc0 & ~255ull) + (j < CODE_KB * 8 / 3 ? j : j - 64) * 384); };
        {
            const unsigned fb = lbase + FULL + lw * FST;
            auto piece = [&](auto sc, auto ic) {
                constexpr int s = CV(sc), i = CV(ic);
                dma<g1_buf(s) + i * 1024, false>(lb, vo[i & 3], (i & 2) ? rs1 : rs0, ((s + rot) & 15) * 128 + (i >> 2) * rst);
            };
            auto mark = [&](auto sc) { dma1<(31 - CV(sc)) * 4>(fb, oob, rs0, 0); };
            auto feed = [&](auto kc, auto ic) {
                constexpr int k = CV(kc), i = CV(ic);
                if constexpr (i < 4) piece(IC(k + 3), IC(i + 4));
                if constexpr (i == 3) mark(IC(k + 3));
                if constexpr (i >= 4 && k < 12) piece(IC(k + 4), IC(i - 4));
            };
            __builtin_amdgcn_s_setprio(HP);
            TS(7);
            sld(tag, ws + WS_F + F_EP);
            sld(dw, ssmD + (ch & ~1));
            const int sb = idx * 2 + (lw >> 1), kh = lw & 1;
            u32x2 kk[11], vv[8]; u32x4 w4[2], kd[4], ke[4], kvo;
            const unsigned ko = g4 * 16384 + l15 * 8, so = sb * 65536 + xcd * 256 + kh * 128;
            {
                const u32x4 rs = mk_rsrc(kin, 4 << 20);
                gld<0>(w4[0], l15 * 32, mk_rsrc(sk, 8192), xcd * 1024 + kh * 512);
                gld<16>(w4[1], l15 * 32, mk_rsrc(sk, 8192), xcd * 1024 + kh * 512);
                static_for<0, 3>([&](auto ic) { gld<0>(kk[CV(ic)], ko + so - 6144 + CV(ic) * 2048, rs, 0); });
                static_for<0, 8>([&](auto ic) { constexpr int i = CV(ic); gld<(i & 1) * 2048>(kk[3 + i], ko, rs, so + (i >> 1) * 4096); });
                static_for<0, 8>([&](auto ic) { constexpr int i = CV(ic); gld<(i & 1) * 2048>(vv[i], ko, mk_rsrc(vin, 4 << 20), so + (i >> 1) * 4096); });
                dma1<DSK>(lbase, lane * 4, mk_rsrc(dskip, 2048), dt * 256);
                dma<U0, false>(lbase + kh * 1024, oob, rs0, 0);
            }
            static_for<4, 8>([&](auto ic) { feed(IC(-2), ic); });
            const __amdgpu_buffer_rsrc_t rk = mk_brs(ws + WS_KVT + (size_t)(xcd * 32 + kh * 16) * 16384, 262144);
            const unsigned kfo = F_K + ((xcd * 2 + (idx >> 4)) * 64 + lw * 16) * 8, K = lbase + KC + lw * KCH;
            float kf[7], pa[4], pb[4];
            auto kv_step = [&](auto uc, auto jc) {
                constexpr int u = CV(uc), j = CV(jc), c = u >> 1, h = u & 1;
                auto el = [&](const u32x2& q) -> float { return (c & 1) ? bfhi(q[c >> 1]) : bflo(q[c >> 1]); };
                auto tap = [&](int t) { const unsigned w = w4[c >> 1][(c & 1) * 2 + (t >> 1)]; return (t & 1) ? bfhi(w) : bflo(w); };
                auto all = [&](auto f) {
#pragma unroll
                    for (int i = 0; i < 4; ++i) f(i);
                };
                if constexpr (j == 0) {
#pragma unroll
                    for (int i = 0; i < 7; ++i) { kf[i] = (h && i < 3) ? kf[i + 4] : el(kk[4 * h + i]); tie(kf[i]); }
                }
                if constexpr (EXACT) {
                    if constexpr (j == 1) { const float t = tap(3); all([&](int i) { pa[i] = kf[i] * t; }); all([&](int i) { pa[i] = rbf(pa[i]); tie(pa[i]); }); }
                    if constexpr (j == 2 || j == 4 || j == 6) { const float t = tap(3 - j / 2); all([&](int i) { pb[i] = kf[i + j / 2] * t; }); all([&](int i) { pb[i] = rbf(pb[i]); tie(pb[i]); }); }
                    if constexpr (j == 3 || j == 5 || j == 7) { all([&](int i) { pa[i] += pb[i]; }); all([&](int i) { pa[i] = rbf(pa[i]); tie(pa[i]); }); }
                } else {
                    if constexpr (j == 1) { const float t = tap(3); all([&](int i) { pa[i] = kf[i] * t; tie(pa[i]); }); }
                    if constexpr (j >= 2 && j <= 4) { const float t = tap(4 - j); all([&](int i) { pa[i] = fma_(kf[i + j - 1], t, pa[i]); tie(pa[i]); }); }
                    if constexpr (j == 5) all([&](int i) { pa[i] = rbf(pa[i]); tie(pa[i]); });
                }
                if constexpr (j == (EXACT ? 7 : 6)) all([&](int i) { pb[i] = el(vv[4 * h + i]); tie(pb[i]); });
                if constexpr (j == 7) {
                    all([&](int i) { pa[i] *= pb[i]; });
                    kvo[3 - 2 * h] = pack2(pa[1], pa[0]); kvo[2 - 2 * h] = pack2(pa[3], pa[2]);
                    if constexpr (h) __builtin_amdgcn_raw_buffer_store_b128(kvo, rk, (l15 >> 2) * 65536 + g4 * 64 + (l15 & 3) * 16, c * 16384 + sb * 256, 16);
                    else tie(kvo);
                }
            };
            static_for<-1, 13>([&](auto kc) {
                constexpr int k = CV(kc);
                if constexpr (k == 3) page<1>();
                if constexpr (k == 11) page<12>();
                if constexpr (k == 2) hop<0>();
                if constexpr (k == 6) hop<6>();
                if constexpr (k == 8) hop<9>();
                if constexpr (k >= 0) barrier();
                if constexpr (k >= 0 && ((CTM >> k) & 1)) ctouch(k);
                if constexpr (k == KV0) {
                    wait_v<4 + hv_cnt(-1, k)>();
                    static_for<0, 11>([&](auto ic) { tie(kk[CV(ic)]); });
                    static_for<0, 8>([&](auto ic) { tie(vv[CV(ic)]); });
                    tie(w4[0]); tie(w4[1]); keep(c_dt); keep(c_re); keep(c_im);
                    TS(8);
                }
                if constexpr (k == KFL) {
                    wait_l<0>();
                    ties(tag); ties(dw);
                    tag += 0x01010101u;
                    wait_v<hv_cnt(KVL + 1, k)>();
                    __builtin_amdgcn_raw_buffer_store_b64(u32x2{tag, ~tag}, rf, lane ? OOB : 0u, F_K + ((xcd * 2 + kh) * 64 + sb) * 8, 16);
                    TS(10);
                }
                if constexpr (k == KPL) dma1<POLL>(lbase + wid * 256, (lane & 31) * 4, rsf, kfo);
                if constexpr (k == 12) {
                    dma1<0>(K, oob, rs0, 0);
                    dma1<KCP>(K, oob, rs0, 0);
                }
                static_for<0, 8>([&](auto ic) {
                    constexpr int i = CV(ic), u0 = kv_first(k), nu = kv_first(k + 1) - u0;
                    feed(kc, ic);
                    if constexpr (k == 4 && i == 4) hop<3>();
                    if constexpr (nu == 1) kv_step(IC(u0), ic);
                    if constexpr (nu == 2) { kv_step(IC(u0 + i / 4), IC(2 * (i & 3))); kv_step(IC(u0 + i / 4), IC(2 * (i & 3) + 1)); }
                    if constexpr (k == 12 && i >= 4) gld<(i - 4) * 1024>(kd[i - 4], lane * 16, mk_rsrc(ssmk, 4 << 20), ch * 4096);
                    if constexpr (k == 11 && i == 4) c_setup();
                    sched();
                });
            });
            TS(1);
            unsigned pf;
            const unsigned fr = lbase + FULL + (lane & 3) * FST;
            wait_v<hv_cnt(KPL, 12) - 1 - (KPL == KFL) + 11>();
            dsr32<POLL - 131072>(pf, lbase + 131072 + wid * 256 + lane * 4);
            wait_l<0>();
            tag_chk(pf, (lane & 1) ? ~tag : tag, (lane & 31) * 4, rsf, kfo);
            if constexpr (EC && EB == 1) barrier();
            {
                const u32x4 rs = mk_rsrc(ws + WS_KVT + (size_t)(xcd * 32 + idx) * 16384, 16384);
                static_for<0, 4>([&](auto ic) { constexpr int i = CV(ic); dma<U0 + 2048 + i * 1024, true>(lbase + lw * 4096, (lane >> 2) * 64 + (((lane & 3) ^ g4) << 4), rs, lw * 4096 + i * 1024); });
            }
            mark(IC(16));
            if constexpr (EC) {
                if constexpr (EB == 0) barrier();
                static_for<0, 4>([&](auto ic) { constexpr int i = CV(ic); dma<i * 1024, false>(cst, vc[i], rsc, c_off(lw, 0)); });
                if constexpr (EC == 1) static_for<0, 4>([&](auto ic) { constexpr int i = CV(ic); dma<4096 + i * 1024, false>(cst, vc[i], mk_rsrc(cre + (size_t)dt * 131072, 262144), c_off(lw, 0)); });
            }
            wait_v<5 + (EC == 1 ? 8 : EC ? 4 : 0)>();
            static_for<0, 4>([&](auto ic) { tie(kd[CV(ic)]); });
#pragma unroll
            for (int n = 0; n < 4; ++n) {
                const unsigned first = n ? __builtin_amdgcn_readlane(kd[n ? n - 1 : 0][3], 63) : 0u;
                const unsigned p = __builtin_amdgcn_update_dpp(first, kd[n][3], 0x138, 0xf, 0xf, false);
                ke[n] = u32x4{__builtin_amdgcn_alignbyte(kd[n][0], p, 2), __builtin_amdgcn_alignbyte(kd[n][1], kd[n][0], 2),
                              __builtin_amdgcn_alignbyte(kd[n][2], kd[n][1], 2), __builtin_amdgcn_alignbyte(kd[n][3], kd[n][2], 2)};
                tie(ke[n]);
            }
            hop<14>();
            pf = 1;
            zero_chk<15 * 4>(pf, fr);
            TS(9);
            static_for<0, 4>([&](auto ic) { constexpr int i = CV(ic); dsw128<64 + i * 1024>(K + lane * 16, kd[i]); dsw128<KCP + 64 + i * 1024>(K + lane * 16, ke[i]); });
            wait_l<0>();
            __builtin_amdgcn_s_setprio(0);
        }
        TS(2);
        unsigned ka[4], cad[4], ub[4];
#pragma unroll
        for (int i = 0; i < 4; ++i) {
            ka[i] = lbase + KC + lw * KCH + ((l15 & 1) ? 0 : KCP) + 84 + 2 * (l15 & 14) - 16 * g4 + i * 1024;
            cad[i] = cst + frag(i & 1, i >> 1);
            const int e = 2 * l15 + g4 + 2;
            ub[i] = lbase + U0 + 64 * e + ((lw ^ (((e >> 2) + 3 - i) & 3)) << 4);
        }
        f32x4 acc[8];
        bf16x4 FA[8][2]; bf16x8 FB[8];
        auto op = [&](auto gc) {
            constexpr Op o = cv_op(CV(gc));
            if constexpr (o.kind == CO_A) { constexpr int s = o.a >> 3, n = o.a & 7, d = (n & 1) * 128 + s * 16 + o.b * 2; dsr2<d, d + 1>(FA[n][o.b], ka[n >> 1]); }
            if constexpr (o.kind == CO_B) { constexpr int s = o.a >> 3, m = o.a & 7; dsr128<2048 * m + 1792 - 256 * s>(FB[m], ub[s & 3]); }
            if constexpr (o.kind == CO_C) dsr128<(o.a & 1) * 4096>(cf[o.a * 4 + o.b], cad[o.b]);
        };
        static_for<-36, 0>(op);
        static_for<0, 288>([&](auto gc) {
            constexpr int G = CV(gc), s = G / 36, q = G % 36, n = cv_n(q), m = cv_m(q);
            if constexpr (cv_wait(G) >= 0) wait_lr<cv_wait(G)>();
            if constexpr (q == 24 && s >= 2) static_for<0, 4>([&](auto ic) { tie(cf[(s - 2) * 4 + CV(ic)]); });
            mfma<s == 0 && n == 0>(acc[n + m], cat(FA[n][0], FA[n][1]), FB[m]);
            sched();
            if constexpr (q == 11 && s >= 2) wait_v<4>();
            op(gc);
            if constexpr (q == 30 && ((CTM >> (16 + s)) & 1)) ctouch(16 + s);
            if constexpr (G == 16) hop<17>();
            if constexpr (G == 66) hop<19>();
            if constexpr (G == 107) page<21>();
            if constexpr (G == 158) hop<23>();
            if constexpr (G == 209) hop<25>();
            if constexpr (G == 254) hop<27>();
            if constexpr (G == CBQ) barrier();
            if constexpr ((q == 24 || q == 25 || q == 29 || q == 34) && (s || !EC)) {
                constexpr int pc = q == 24 ? 0 : q == 25 ? 1 : q == 29 ? 2 : 3;
                dma<(s & 1) * 4096 + pc * 1024, false>(cst, vc[pc], rsc, c_off(lw, s));
            }
            sched();
        });
        page<28>();
        TS(3);
        {
            u32x2 kvr[8];
            const unsigned kva = lbase + U0 + 2048 + 128 * l15 + 64 * (g4 >> 1) + ((lw ^ ((l15 >> 1) & 3)) << 4) + 8 * (1 - (g4 & 1));
            static_for<0, 8>([&](auto ic) { dsr64<CV(ic) * 2048>(kvr[CV(ic)], kva); });
            wait_l<0>();
            const float Dd = (ch & 1) ? bfhi(dw) : bflo(dw);
            const __amdgpu_buffer_rsrc_t rz = mk_brs(ws + WS_ZT, 4 << 20);
#pragma unroll
            for (int jj = 0; jj < 4; ++jj) {
                u32x4 o;
#pragma unroll
                for (int u = 0; u < 2; ++u) {
                    const int j = 2 * jj + u;
                    tie(kvr[j]); tie(acc[j]);
                    const float kv3 = bflo(kvr[j][0]), kv2 = bfhi(kvr[j][0]), kv1 = bflo(kvr[j][1]), kv0 = bfhi(kvr[j][1]);
                    o[2 * u] = pack2(rbf(acc[j][0]) + rbf(kv0 * Dd), rbf(acc[j][1]) + rbf(kv1 * Dd));
                    o[2 * u + 1] = pack2(rbf(acc[j][2]) + rbf(kv2 * Dd), rbf(acc[j][3]) + rbf(kv3 * Dd));
                }
                __builtin_amdgcn_raw_buffer_store_b128(o, rz, lane * 16 + jj * 1024, ch * 4096, 16);
                if (jj == 1) { sched(); hop<29>(); sched(); }
            }
            TS(8);
            hop<30>();
            wait_v<4>();
            static_for<0, 8>([&](auto ic) { constexpr int i = CV(ic); dsr128<(i >> 2) * 4096>(cf[24 + i], cad[i & 3]); });
            h_setup(1);
            wait_l<0>();
            static_for<0, 8>([&](auto ic) { tie(cf[24 + CV(ic)]); });
        }
        TS(4);
    } else {
        f32x4 acc[8][2];
        cf32 L, cc, M, M2, MG, NG, N4, N8, ag;
        {
            unsigned aad[4], bad[4];
#pragma unroll
            for (int i = 0; i < 4; ++i) { aad[i] = lbase + frag(i & 1, i >> 1); bad[i] = aad[i] + 16384 + wid * 4096; }
            const unsigned fr = lbase + FULL + (lane & 3) * FST;
            sld(tag, ws + WS_F + F_EP);
            bf16x8 fa[4][2], fb[2][4];
            unsigned fl = 1;
            auto op = [&](auto gc) {
                constexpr int G = CV(gc);
                constexpr Op o = g1_op(G);
                if constexpr (o.kind == OP_A) {
                    constexpr int bo = g1_buf(o.a >> 3), hi = bo >= 65536 ? 65536 : 0, i = o.a & 7;
                    dsr128<bo - hi + (i >> 1) * 4096>(fa[i & 3][o.b], aad[(i & 1) + 2 * o.b] + hi);
                }
                if constexpr (o.kind == OP_B) { constexpr int bo = g1_buf(o.a), hi = bo >= 65536 ? 65536 : 0; dsr128<bo - hi>(fb[o.a & 1][o.b], bad[o.b] + hi); }
                if constexpr (o.kind == OP_FLAG) dsr32<(30 - (G >> 5)) * 4>(fl, fr);
            };
            auto consts = [&]() {
                float ld, ar, ai;
                asm volatile("s_waitcnt vmcnt(9)\n\tv_lshlrev_b32 %0, 16, %3\n\tv_lshlrev_b32 %1, 16, %4\n\tv_lshlrev_b32 %2, 16, %5"
                             : "=&v"(ld), "=&v"(ar), "=&v"(ai) : "v"(c_dt), "v"(c_re), "v"(c_im) : "memory");
                const float dtv = rbf(expf(ld)), ea = expf(dtv * ar);
                float sn, cs;
                sincosf(dtv * ai, &sn, &cs);
                L = cf32{ea * cs, ea * sn};
                M = L;
#pragma unroll
                for (int e = 0; e < 5; ++e) M = cmul(M, M);
                M2 = cmul(M, M);
                const cf32 N = cmul(M2, M2), N2 = cmul(N, N), one = {1.f, 0.f};
                N4 = cmul(N2, N2); N8 = cmul(N4, N4);
                auto sel = [](bool c, cf32 a, cf32 b) { return cf32{c ? a.r : b.r, c ? a.i : b.i}; };
                MG = cmul(sel(g4 & 1, M, one), sel(g4 & 2, M2, one));
                NG = cmul(sel(g4 & 1, N, one), sel(g4 & 2, N2, one));
                const float nr = L.r - 1.f, den = ar * ar + ai * ai;
                cc = cf32{2.f * ((nr * ar + L.i * ai) / den), 2.f * ((L.i * ar - nr * ai) / den)};
                auto tc = [&](cf32& v) { tie(v.r); tie(v.i); };
                tc(L); tc(cc); tc(M); tc(M2); tc(MG); tc(NG); tc(N4); tc(N8);
                sched();
            };
            if constexpr (CQ < 0) consts();
            __builtin_amdgcn_s_setprio(CP);
            zero_chk<0>(fl, lbase + POLL + (lane & 3) * 256);
            ties(tag);
            tag += 0x01010101u;
            TS(1);
            static_for<-12, 0>(op);
            static_for<0, 512>([&](auto gc) {
                constexpr int G = CV(gc), st = G >> 5, i = (G >> 2) & 7, j = G & 3;
                if constexpr ((G == 0 || j == 2) && G <= 506) { static_assert(g1_wait(G) >= 0, ""); wait_lr<G == 506 ? 0 : g1_wait(G)>(); }
                if constexpr (G == CQ) { asm volatile("s_waitcnt lgkmcnt(0)\n\ts_nop 7" ::: "memory"); consts(); }
                if constexpr (G == HQ) hop_all(wid);
                if constexpr ((G & 31) == 20 && st < 15) zero_chk<(30 - st) * 4>(fl, fr);
                if constexpr ((G & 31) == 30 && st < 13 + (EC != 0)) barrier();
                if constexpr ((G & 31) == 20) TL(st);
                mfma<st == 0 && j < 2>(acc[i][j & 1], fa[i & 3][j >> 1], fb[st & 1][j]);
                sched();
                op(gc);
                if constexpr (G == 31) page<-1>();
                if constexpr (G == 95) hop<2>();
                if constexpr (G == 159) hop<4>();
                if constexpr (G == 207) hop<5>();
                if constexpr (G == 255) page<7>();
                if constexpr (G == 319) hop<8>();
                if constexpr (G == 383) hop<10>();
                if constexpr (G == 431) hop<11>();
                if constexpr (G == 479) page<13>();
                if constexpr (G == 509) hop<15>();
                if constexpr (G >= 487) {
                    constexpr int k = G - 486;
                    if constexpr (k == 1) { asm volatile("s_nop 3"); ag = cf32{acc[0][0][0], acc[0][1][0]}; }
                    cf32 t;
                    asm volatile("v_fma_f32 %0, -%3, %5, %6\n\tv_fma_f32 %1, %3, %4, %7\n\tv_fmac_f32 %0, %2, %4\n\tv_fmac_f32 %1, %2, %5"
                                 : "=&v"(t.r), "=&v"(t.i) : "v"(L.r), "v"(L.i), "v"(ag.r), "v"(ag.i), "v"(acc[k >> 2][0][k & 3]), "v"(acc[k >> 2][1][k & 3]));
                    ag = t;
                }
                sched();
            });
        }
        __builtin_amdgcn_s_setprio(3);
        TS(2);
#pragma unroll
        for (int k = 26; k < 32; ++k) ag = cfma(L, ag, cf32{acc[k >> 2][0][k & 3], acc[k >> 2][1][k & 3]});
        cf32 ev_, od, p01, p23;
        sw16(ag.r, ev_.r, od.r); sw16(ag.i, ev_.i, od.i);
        const cf32 pp = cfma(M, ev_, od);
        sw32(pp.r, p01.r, p23.r); sw32(pp.i, p01.i, p23.i);
        cf32 E = cfma(M2, p01, p23);

        const __amdgpu_buffer_rsrc_t rse = mk_brs(ws + WS_E + (size_t)(tn * 4 + wid) * 4096, 4096);
        __builtin_amdgcn_raw_buffer_store_b128(u32x4{f2u(E.r), f2u(E.i), tag, ~tag}, rse, g4 ? OOB : l15 * 16, tm * 256, 16);
        sched();
        TS(3);
        c_setup();
        unsigned cad[4];
#pragma unroll
        for (int i = 0; i < 4; ++i) cad[i] = lb + frag(i & 1, i >> 1);
        auto c_read = [&](auto rc) { static_for<0, 8>([&](auto ic) { constexpr int r = CV(rc), i = CV(ic); dsr128<(i >> 2) * 4096>(cf[r * 8 + i], cad[i & 3] + (r == 0 || r == 3 ? 2 * BUF : 0)); }); };
        auto c_tie = [&](auto rc) { static_for<0, 8>([&](auto ic) { tie(cf[CV(rc) * 8 + CV(ic)]); }); };
        auto c_unit = [&](auto uc) { static_for<0, 4>([&](auto ic) { constexpr int u = CV(uc), i = CV(ic); dma<(u < 2 || u >= 6 ? 2 * BUF : 0) + (u & 1) * 4096 + i * 1024, false>(lb, vc[i], rsc, c_off(wid, u)); }); };
        auto c_req = [&](auto rc) { c_unit(IC(CV(rc) * 2)); c_unit(IC(CV(rc) * 2 + 1)); };
        const float c2r = cc.r, c2i = cc.i;
        auto scale = [&](int i) {
#pragma unroll
            for (int j = 0; j < 4; ++j) {
                const float br = acc[i][0][j], bi = acc[i][1][j];
                acc[i][0][j] = fma_(c2r, br, -(c2i * bi)); acc[i][1][j] = fma_(-c2r, bi, -(c2i * br));
            }
            tie(acc[i][0]); tie(acc[i][1]);
        };
        scale(0); scale(1); scale(2); scale(3);
        sched(); hop<16>(); sched();
        if constexpr (ED) { scale(4); scale(5); scale(6); scale(7); }
        if constexpr (ED == 2) barrier();
        u32x4 ev[4]; unsigned eo[4]; unsigned long long none[4];
#pragma unroll
        for (int i = 0; i < 4; ++i) {
            const int t = tm - 1 - 4 * i - g4;
            eo[i] = t < 0 ? OOB : t * 256 + l15 * 16; none[i] = __builtin_amdgcn_ballot_w64(t < 0);
            asm volatile("buffer_load_dwordx4 %0, %1, %2, 0 offen sc1" : "=v"(ev[i]) : "v"(eo[i]), "s"(mk_rsrc(ws + WS_E + (size_t)(tn * 4 + wid) * 4096, 4096)) : "memory");
        }
        asm volatile("" ::: "memory");
        sched();
        if constexpr (ED != 2) barrier();
        if constexpr (R01 == 0) { c_req(IC(0)); c_req(IC(1)); }
        if constexpr (R01 >= 2) c_unit(IC(0));
        if constexpr (!ED) { scale(4); scale(5); scale(6); scale(7); }
        cf32 X = cfma(cf32{g4 == 3 ? M.r : g4 == 2 ? 1.f : 0.f, g4 == 3 ? M.i : 0.f}, p01, cf32{(g4 & 1) ? ev_.r : 0.f, (g4 & 1) ? ev_.i : 0.f});
        tie(X.r); tie(X.i);
        sched();
        hop<24>();
        wait_v<R01 == 0 ? 16 : R01 >= 2 ? 4 : 0>();
        tie(ev[0]); tie(ev[1]); tie(ev[2]); tie(ev[3]);
        {
            const unsigned long long tag64 = (unsigned long long)tag | ((unsigned long long)~tag << 32);
#pragma unroll 1
            for (unsigned spins = 0; spins < SPIN_CAP; ++spins) {
                unsigned long long ok = ~0ull;
#pragma unroll
                for (int i = 0; i < 4; ++i) ok &= __builtin_amdgcn_ballot_w64(((unsigned long long)ev[i][2] | ((unsigned long long)ev[i][3] << 32)) == tag64) | none[i];
                TSV(11, spins);
                if (ok == ~0ull) break;
                asm volatile("buffer_load_dwordx4 %0, %4, %8, 0 offen sc1\n\tbuffer_load_dwordx4 %1, %5, %8, 0 offen sc1\n\tbuffer_load_dwordx4 %2, %6, %8, 0 offen sc1\n\t"
                             "buffer_load_dwordx4 %3, %7, %8, 0 offen sc1\n\ts_waitcnt vmcnt(0)" : "=&v"(ev[0]), "=&v"(ev[1]), "=&v"(ev[2]), "=&v"(ev[3])
                             : "v"(eo[0]), "v"(eo[1]), "v"(eo[2]), "v"(eo[3]), "s"(mk_rsrc(ws + WS_E + (size_t)(tn * 4 + wid) * 4096, 4096)) : "memory");
            }
        }
        page<18>();
        cf32 h;
        {
            auto e = [&](int i) { return cf32{u2f(ev[i][0]), u2f(ev[i][1])}; };
            cf32 c = cmul(NG, cfma(N8, cfma(N4, e(3), e(2)), cfma(N4, e(1), e(0)))), u, v;
            sw16(c.r, u.r, v.r); sw16(c.i, u.i, v.i);
            sw32(u.r + v.r, u.r, v.r); sw32(u.i + v.i, u.i, v.i);
            c = cfma(MG, cf32{u.r + v.r, u.i + v.i}, X);
            h = cf32{fma_(c2r, c.r, -(c2i * c.i)), fma_(-c2r, c.i, -(c2i * c.r))};
        }
        TS(4);
        {
            const __amdgpu_buffer_rsrc_t rh = mk_brs(ws + WS_H + (size_t)(tm * 16 + tn) * 32768 + wid * 2048, 32768);
            const unsigned ho = g4 * 256 + ((l15 ^ (4 * (g4 & 1))) << 4);
            const cf32 Lc = {L.r, -L.i};
            static_for<0, 4>([&](auto qc) {
                constexpr int q = CV(qc);
                if constexpr (R01 == 1) c_unit(qc);
                if constexpr (R01 == 2 && q < 3) c_unit(IC(q + 1));
                if constexpr (R01 == 3 && q > 0) c_unit(qc);
                if constexpr (q == 2) hop<20>();
                u32x4 wr, wi;
#pragma unroll
                for (int u = 0; u < 4; ++u) {
                    const int i = 2 * q + (u >> 1), j = 2 * (u & 1);
                    const cf32 h0 = cfma(Lc, h, cf32{acc[i][0][j], acc[i][1][j]});
                    h = cfma(Lc, h0, cf32{acc[i][0][j + 1], acc[i][1][j + 1]});
                    wr[u] = pack2(h0.r, h.r); wi[u] = pack2(h0.i, h.i);
                }
                __builtin_amdgcn_raw_buffer_store_b128(wr, rh, ho, q * 8192, 16);
                __builtin_amdgcn_raw_buffer_store_b128(wi, rh, ho + 1024, q * 8192, 16);
                sched();
            });
        }
        if constexpr (R01 == 4) { c_unit(IC(1)); c_unit(IC(2)); c_unit(IC(3)); wait_v<12>(); } else wait_v<0>();
        __builtin_amdgcn_raw_buffer_store_b64(u32x2{tag, ~tag}, rf, lane ? OOB : 0u, F_H + ((tm * 16 + tn) * 4 + wid) * 8, 16);
        TS(5);
        hop<22>();
        if constexpr (R01 == 4) wait_v<1>();
        c_read(IC(1)); c_read(IC(0));
        wait_l<8>();
        c_req(IC(2));
        wait_l<0>();
        c_req(IC(3));
        c_tie(IC(0)); c_tie(IC(1));
        wait_v<0>();
        c_read(IC(2)); c_read(IC(3));
        wait_l<0>();
        c_tie(IC(2)); c_tie(IC(3));
        if (wid == 3) {
            const unsigned long long tag64 = (unsigned long long)tag | ((unsigned long long)~tag << 32);
            gu64* hf = (gu64*)(ws + WS_F + F_H) + (tm2 * 16 + hb * 8) * 4 + (lane & 31);
#pragma unroll 1
            for (unsigned spins = 0; spins < SPIN_CAP; ++spins) {
                if (wall(__hip_atomic_load(hf, RLX_AGENT) == tag64)) break;
                __builtin_amdgcn_s_sleep(1);
            }
            flag_set(lbase + HGO + lane * 4, 1u);
        } else gate<1>(lbase + HGO, 1);
        TS(6);
        hop<26>();
        static_for<0, 16>([&](auto ic) { constexpr int i = CV(ic); dma<(i >> 3) * 2 * BUF + (i & 7) * 1024, true>(lb, lane * 16, rsh, ((rot2 + wid + (i >> 3) * 4) & 7) * 32768 + qx * 8192 + (i & 7) * 1024); });
        h_setup(0);
        wait_v<8>();
        __builtin_amdgcn_s_setprio(0);
    }
    {
        const int wid = fresh(wid_), lw = wid & 3, kg = wid >> 2, b = fresh(b_), xcd = b & 7, idx = b >> 3, tq = (idx & 7) + JR(idx >> 3);
        const int dt = G2M ? idx & 7 : (xcd & 4) + (idx >> 3), qx = ((~b & 1) * 2) ^ JQ(idx >> 3);
        const unsigned wl = lbase + wid * 4096;
        unsigned l16 = lane * 16;
        unsigned hs0, hs1, fso;
        auto fill = [&](auto sc, auto pc) {
            constexpr int s = CV(sc), p = CV(pc);
            if constexpr (p == 0) dma_x<(2 * s % 3) * BUF, (s >> 1) * 8192>(fso, wl, l16, rsh, (s & 1) ? hs1 : hs0);
            else dma_n<p * 1024>(fso, l16, rsh);
        };
        bf16x4 hf[2][8];
        auto ldh = [&](auto nc, auto ic) {
            constexpr int n = CV(nc), i = CV(ic), j = (i >> 1) & 1, bo = (2 * (n >> 2) % 3) * BUF + (g2_unit(n, j) & 3) * 8192 + (i >> 2) * 4096, hi = bo >= 65536 ? 65536 : 0;
            dsr64tr<bo - hi>(hf[n & 1][i], ha[2 * g2_tile(n, j) + (i & 1)] + hi);
        };
        f32x4 yc[2][2][2];
        f32x4 im[2], ao, ob[2]; u32x4 pl[2], po[2], dg[2];
        bf16x8 xf[2];
        u32x2 zv[4], qv[2], tg2;
        unsigned pf, dsw_, expect, msk, ck, ck2, a_q, a_x, a_iw, a_ir, a_p, a_z, a_z2, a_w, a_zr, a_qr, a_oa, a_ob, a_f, u0, u1, u2;
        const u32x4 rsz = mk_rsrc(ws + WS_F, wid == 1 ? 65536 : 0);
        float tq_[2], tz, sm[2];
        const __amdgpu_buffer_rsrc_t ro = mk_brs(out, 16 << 20);
        const __amdgpu_buffer_rsrc_t rp = mk_brs(ws + WS_PP, 8 << 20);
        const u32x4 rsp = mk_rsrc(ws + WS_PP, 8 << 20);
        unsigned w2k, zfo, ta, tb, ppo;
        constexpr unsigned long long LO8 = 0x00ff00ff00ff00ffull;
        auto finB = [&](auto pc, auto jc) {
            constexpr int p = CV(pc), j = CV(jc);
            if constexpr (j < 8) { yc[p & 1][0][j & 1][j >> 1] += im[j & 1][j >> 1]; tie(yc[p & 1][0][j & 1]); }
            else {
                constexpr int i = (j - 8) >> 2, k = j & 3, dd = k & 1, h = k >> 1;
                ob[i][k] = sel8(yc[p & 1][0][dd][2 * i + 1 - h], yc[p & 1][0][dd][2 * i + h], h ? ~LO8 : LO8);
                if constexpr (k == 3 && (p == 3 || !(NOST & 4))) __builtin_amdgcn_raw_buffer_store_b128(__builtin_bit_cast(u32x4, ob[i]), ro, a_ob, (tb + (p ^ qx) * 8 + 2 * i) * 8192 + 4096 + dt * 512 + lw * 128, OSC);
                if constexpr (j == 12) keep(ob[0]);
            }
        };
        auto addp = [&](auto pc, auto ic) {
            constexpr int p = CV(pc), dd = CV(ic) & 1, r = CV(ic) >> 1;
            yc[p & 1][0][dd][r] += u2f(pl[dd][r]);
            tie(yc[p & 1][0][dd]);
        };
        auto rcv = [&](auto ic) {
            constexpr int i = CV(ic);
            if constexpr (i < 8) { pl[i & 1][i >> 1] ^= (tag >> (8 * (i >> 1))) & 255u; pp_tie<i & 1>(pl[i & 1]); }
            if constexpr (i == 8) { ck = pl[0][0] | pl[0][1] | pl[0][2]; tie(ck); }
            if constexpr (i == 9) { ck2 = pl[1][0] | pl[1][1] | pl[1][2]; tie(ck2); }
            if constexpr (i == 10) { ck = ck | pl[0][3] | pl[1][3]; ck = (ck | ck2) & 255u; tie(ck); }
        };
        auto side = [&](auto pc) {
            constexpr int P = CV(pc), n = P >> 3, m = P & 7, p = n >> 3;
#define V(pos, var, expr) if constexpr (P == (pos)) { var = (expr); tie(var); }
            if constexpr (P == AT(0, 4)) { hs0 = ((tq + (wid >> 1)) & 7) * 32768 + (wid & 1) * 4096 + qx * 8192; hs1 = hs0 ^ 131072; ties(hs0); ties(hs1); }
            if constexpr (P == AT(0, 6)) { w2k = lbase + wid * 2048; ta = 512 * (idx >> 3) + 128 * (idx & 1) + 16 * wid + 4 * ((idx >> 1) & 3); ties(w2k); ties(ta); }
            if constexpr (P == AT(1, 4)) { tb = (G2M ? (xcd >> 1) * 4 + (idx >> 3) : (xcd & 2) * 4 + (idx & 7)) * 128 + 64 * kg; ties(tb); }
            if constexpr (P == AT(6, 6)) { zfo = F_Z + xcd * 4096; ties(zfo); }
            if constexpr (P == AT(7, 5)) { ppo = b * 32768 + wid * 2048; ties(ppo); }
            V(AT(0, 0), a_x, (lane << 2) & 32)       V(AT(0, 1), u0, lane & 7)               V(AT(0, 2), u2, lane & 48)
            V(AT(0, 3), u1, l16 & 240)               V(AT(0, 4), a_x, a_x | u0)              V(AT(0, 6), a_q, (u2 << 7) | u1)
            V(AT(1, 0), a_w, (lane >> 1) & 4)        V(AT(1, 1), a_x, (a_x << 11) | u2)      V(AT(1, 2), a_w, (u0 << 3) | a_w)
            V(AT(1, 3), msk, 0xffffff00u)
            V(AT(2, 0), a_w, a_w + (lbase + DSK + lw * 64))
            if constexpr (P == AT(2, 1)) { tg2[0] = tag; tie(tg2); }
            V(AT(2, 2), a_p, lane & 31)
            if constexpr (P == AT(2, 3)) { tg2[1] = ~tag; tie(tg2); }
            V(AT(2, 4), a_p, a_p << 2)               V(AT(2, 6), u1, (int)(lane << 31) >> 31)
            V(AT(3, 0), a_iw, w2k + IMG + l16)       V(AT(3, 1), expect, (u1 ^ tag) & (wid == 1 ? ~0u : 0u)) V(AT(4, 6), a_f, l16 << 2) V(AT(5, 4), a_f, (a_f & 0xf80) | (a_p & 4))           V(AT(3, 2), a_ir, a_iw ^ 8192)
            V(AT(3, 3), u2, lane >> 5)
            V(AT(5, 6), a_z, lane ^ u2 ^ (wid * 4))  V(AT(6, 0), u1, lane >> 3)              V(AT(6, 1), a_z, a_z & 7)
            V(AT(6, 2), a_z2, u1 << 12)              V(AT(6, 3), a_zr, (wid ^ lane) & 7)     V(AT(6, 4), a_z, (a_z << 4) | a_z2)
            V(AT(6, 5), a_zr, (a_zr << 1) | u2)      V(AT(6, 6), a_z2, a_z ^ 32)             V(AT(6, 7), a_zr, (a_zr << 3) + (lbase + ZST))
            V(AT(7, 0), a_oa, a_p << 2)              V(AT(7, 1), a_zr, (a_p << 7) + a_zr)    V(AT(7, 2), a_oa, (u2 << 21) | a_oa)
            V(AT(7, 3), a_qr, (u2 << 10) + (w2k + QST))
            V(AT(7, 6), a_qr, (a_p << 1) + a_qr)     V(AT(7, 7), a_ob, (lane >> 2) & 4)      V(AT(8, 0), u0, u0 << 4)
            V(AT(8, 1), a_ob, (lane & 32) | a_ob)    V(AT(8, 2), a_ob, (u1 & 1) | a_ob)      V(AT(8, 3), a_ob, (a_ob << 13) | u0)
            if constexpr (P == AT(2, 5) || P == AT(2, 7)) gld<0>(xf[m == 7], a_x, mk_rsrc(x, (NOX & 1) ? 0 : 4 << 20), (tb + ((m == 7) ^ qx) * 8) * 2048 + dt * 256 + lw * 64);
            if constexpr (!NOA) {
            if constexpr (P == AT(5, 5) || P == AT(5, 7)) dma<QST + (m == 7) * 1024, false>(w2k, a_q, mk_rsrc(qin, (NOX & 2) ? 0 : 4 << 20), (ta + 256 * (m == 7)) * 2048 + xcd * 256);
            if constexpr (P == AT(3, 4)) __builtin_amdgcn_raw_buffer_store_b64(tg2, mk_brs(ws + WS_F + F_Z + (xcd * 32 + idx) * 128, wid ? 0 : 8), l16, 0, 16);
            if constexpr (P == AT(ZP, 4)) pf_ld(pf, a_f, rsz, zfo);
            if constexpr (P == P_ZC) { wait_v<g2_allow(P, AT(ZP, 4))>(); tag_chk(pf, expect, a_f, rsz, zfo); }
            if constexpr (P == AT(11, 3) || P == AT(11, 4)) dma<ZST + (m - 3) * 1024, true>(w2k, m == 3 ? a_z : a_z2, mk_rsrc(ws + WS_ZT + (size_t)xcd * 524288, 524288), idx * 128 + (wid * 16 + (m - 3) * 8) * 4096);
            }
            if constexpr (P == AT(2, 4)) dsr32<0>(dsw_, a_w);
            if constexpr (P == AT(3, 6)) { tie(dsw_); u1 = dsw_ & 0xffffu; tie(u1); }
            V(AT(4, 0), dsw_, dsw_ & 0xffff0000u)
            if constexpr (n == 4 && m >= 1 && m < 5) { constexpr unsigned long long k = diag_mask(m - 1); dg[0][m - 1] = sel0(u1, k); }
            if constexpr (n == 5 && m < 4) { constexpr unsigned long long k = diag_mask(m); dg[1][m] = sel0(dsw_, k); }
            if constexpr (P == AT(7, 0)) { tie(xf[0]); tie(xf[1]); }
            if constexpr (P == AT(7, 3) || P == AT(9, 3)) static_for<0, 2>([&](auto dc) { mfma<false>(yc[p][0][CV(dc)], xf[p], __builtin_bit_cast(bf16x8, dg[CV(dc)])); });
            if constexpr ((n & 7) == 6 && m == XW && !(DBG & 2)) dsw128<0>(a_iw, yc[p & 1][1][0]);
            if constexpr ((n & 7) == 6 && m == XW2 && !(DBG & 2)) dsw128<1024>(a_iw, yc[p & 1][1][1]);
            if constexpr ((n & 7) == 7 && (m & 5) == (n < 31 ? 4 : 0)) dsr128<(m & 2) * 512>(im[(m >> 1) & 1], a_ir);
            if constexpr (n >= 8 && (n & 7) == 0 && m == 4) { tie(im[0]); tie(im[1]); }
            if constexpr (p == 1 || p == 2) {
                constexpr int j = P - (p == 1 ? P_PP0 : P_PP1), a = (j + 1) / 2, k = j / 2 - 1;
                if constexpr (j == 0 || (j > 0 && j < 15 && (j & 1))) { sm[a & 1] = yc[(p - 1) & 1][0][a >> 2][a & 3] + im[a >> 2][a & 3]; tie(sm[a & 1]); }
                if constexpr (j >= 2 && j <= 16 && !(j & 1)) { po[k >> 2][k & 3] = (f2u(sm[k & 1]) & msk) | ((tag >> (8 * (k & 3))) & 255u); if constexpr ((k & 3) != 3) tie(po[k >> 2]); }
                if constexpr ((j == 8 || j == 16) && !(NOST & 2) && !(PPH && j == 8)) __builtin_amdgcn_raw_buffer_store_b128(po[k >> 2], rp, l16, ppo + (p - 1) * 16384 + (k >> 2) * 1024, 16);
            }
            if constexpr ((P == P_L0 || P == P_L1) && !PPH) pp_ld0(pl[0], l16, rsp, ppo ^ (P == P_L1 ? 49152 : 32768));
            if constexpr (P == P_L0 + 2 || P == P_L1 + 2) pp_ld1(pl[1], l16, rsp, ppo ^ (P == P_L1 + 2 ? 49152 : 32768));
            if constexpr (P == P_R0 || P == P_R1) { wait_v<g2_allow(P, (P == P_R0 ? P_L0 : P_L1) + 2)>(); pp_tie<0>(pl[0]); pp_tie<1>(pl[1]); }
            if constexpr (P >= P_R0 && P < P_R0 + 11) rcv(IC(P - P_R0));
            if constexpr (P >= P_R1) rcv(IC(P - P_R1));
            if constexpr (P == P_R0 + 12 && !(NOST & 2) && !PPH) pp_chk(pl[0], pl[1], ck, tag, l16, rsp, ppo ^ 32768);
            if constexpr (P >= P_D2 && P < P_D2 + 8) addp(IC(2), IC(P - P_D2));
            if constexpr (!NOA) {
            if constexpr (n == ZW && m >= 4) dsr64<(m - 4) * 128>(zv[m - 4], a_zr);
            static_for<0, 4>([&](auto rc) {
                constexpr int r = CV(rc);
                if constexpr (P == q_tie(r)) tie(qv[r & 1]);
                if constexpr (P == q_rd(r)) dsr64<r * 256>(qv[r & 1], a_qr);
            });
            if constexpr (P == AT(ZW + 1, 4)) { tie(zv[0]); tie(zv[1]); tie(zv[2]); tie(zv[3]); }
            }
            if constexpr (a_step(P) >= 0) {
                constexpr int s = a_step(P), e = s == 0 ? 0 : s % 3 == 2 ? (s + 1) / 3 : s % 3 == 1 ? s / 3 : s / 3 - 1, r = e >> 2, c = e & 3;
                if constexpr (s == 0 || (s % 3 == 2 && e < 16)) { tq_[e & 1] = (c & 1) ? bfhi(qv[r & 1][c >> 1]) : bflo(qv[r & 1][c >> 1]); tie(tq_[e & 1]); }
                else if constexpr (s % 3 == 1) { tz = (r & 1) ? bfhi(zv[c][r >> 1]) : bflo(zv[c][r >> 1]); tie(tz); }
                else if constexpr (s % 3 == 0) {
                    ao[c] = tq_[e & 1] * tz;
                    if constexpr (c == 3 && !(NOST & 1)) __builtin_amdgcn_raw_buffer_store_b128(__builtin_bit_cast(u32x4, ao), ro, a_oa, (ta + r) * 8192 + xcd * 512, OSC);
                    else tie(ao);
                }
            }
            if constexpr (P >= P_F2 && P < P_F2 + 16) finB(IC(2), IC(P - P_F2));
            if constexpr (a_step(P - 1) > 0 && a_step(P - 1) % 12 == 0) keep(ao);
            if constexpr (P == P_PP0 + 9 || P == P_PP1 + 9) keep(po[0]);
            if constexpr (P == P_PP0 + 17 || P == P_PP1 + 17) keep(po[1]);
            if constexpr (P == P_F2 + 16) keep(ob[1]);
        };
        if (wid < 4) TS(7);
        TG_DECL
        barrier();
        static_for<0, 8>([&](auto ic) { ldh(IC(0), ic); });
        static_for<0, 32>([&](auto nc) {
            constexpr int n = CV(nc), p = n >> 3;
            if constexpr (n == 7) page<33>();
            if constexpr (n == 23) page<37>();
            wait_lr<(n & 3) == 3 ? 0 : 4 + g2_lx(n - 1)>();
            if constexpr (n == 31) barrier();
            if constexpr ((n & 3) == 3 && n < 31) {
                constexpr int need = n == 3 ? -1 : n == 7 ? AT(2, 7) : n == 15 && ZW == 15 && !NOA ? AT(11, 4) : AT(n - 7, 7);
                TL(21 + n / 4 * 3);
                wait_v<g2_allow(8 * n, need)>();
                TL(22 + n / 4 * 3);
                barrier();
                TL(23 + n / 4 * 3);
            }
            if constexpr (n == 1) hop<31>();
            if constexpr (n == 4) hop<32>();
            if constexpr (n == 11) hop<34>();
            if constexpr (n == 15) hop<35>();
            if constexpr (n == 19) hop<36>();
            if constexpr (n == 27) hop<38>();
            if constexpr (n == 31) hop<39>();
            if constexpr (n == 0) TL(20);
            TG(20 + n);
            if constexpr (n == 31) TL(42);
            if constexpr (PRG) __builtin_amdgcn_s_setprio(n == 31 ? 0 : 3 - ((n + 1) & 3));
            sched();
            static_for<0, 8>([&](auto mc) {
                constexpr int m = CV(mc), ks = m >> 2, tt = (m >> 1) & 1, dd = m & 1, P = n * 8 + m;
                if constexpr (m == 4 && (n & 3) != 3) wait_lr<(rd1(n) ? 8 : 4) + ((n & 7) == 6 ? (XW < 4) + (XW2 < 4) : 0)>();
                mfma<(n & 7) == 0 && ks == 0>(yc[p & 1][(DBG & 1) ? tt : g2_tile(n, tt)][dd], cat(hf[n & 1][4 * ks + 2 * tt], hf[n & 1][4 * ks + 2 * tt + 1]), cf[g2_unit(n, tt) * 4 + 2 * ks + dd]);
                sched();
                if constexpr (n < 31 && rd1(n) && m < 4) { ldh(IC(n + 1), IC(2 * m)); ldh(IC(n + 1), IC(2 * m + 1)); }
                if constexpr (n < 31 && !rd1(n)) ldh(IC(n + 1), mc);
                if constexpr (g2_fill(P) >= 0) fill(IC(g2_fill(P) >> 2), IC(g2_fill(P) & 3));
                side(IC(P));
                sched();
            });
        });
        TG(52);
        if (wid >= 4) TS(5); else TS(8);
        TG(53);
        if constexpr (!(NOST & 2) && !PPH) pp_chk(pl[0], pl[1], ck, tag, l16, rsp, ppo ^ 49152);
        wait_l<0>();
        tie(im[0]); tie(im[1]);
        static_for<0, 8>([&](auto ic) { constexpr int dd = CV(ic) & 1, r = CV(ic) >> 1; im[dd][r] += u2f(pl[dd][r]); tie(im[dd]); });
        asm volatile("s_nop 3");
        sched();
        static_for<0, 16>([&](auto jc) { finB(IC(3), jc); });
        if (b == 255 && wid == 0) __builtin_amdgcn_raw_buffer_store_b32(tag, rf, lane ? OOB : 0u, F_EP, 16);
    }
}

}

#if defined(__HIP_DEVICE_COMPILE__)
#pragma clang attribute pop
#endif

extern "C" void kernel_launch(void* const* d_in, const int* in_sizes, int n_in, void* d_out, int out_size, void* d_ws, size_t ws_size,
                              hipStream_t stream) {
    (void)in_sizes; (void)n_in; (void)out_size; (void)ws_size;
    using namespace fk;
    const uint16_t* q = (const uint16_t*)d_in[0];
    const uint16_t* kin = (const uint16_t*)d_in[1];
    const uint16_t* vin = (const uint16_t*)d_in[2];
    const uint16_t* sk = (const uint16_t*)d_in[3];
    const uint16_t* ssmk = (const uint16_t*)d_in[4];
    const uint16_t* ssmD = (const uint16_t*)d_in[5];
    const uint16_t* x = (const uint16_t*)d_in[6];
    const uint16_t* lre = (const uint16_t*)d_in[7];
    const uint16_t* lim = (const uint16_t*)d_in[8];
    const uint16_t* bre = (const uint16_t*)d_in[9];
    const uint16_t* bim = (const uint16_t*)d_in[10];
    const uint16_t* cre = (const uint16_t*)d_in[11];
    const uint16_t* cim = (const uint16_t*)d_in[12];
    const uint16_t* logdt = (const uint16_t*)d_in[13];
    const uint16_t* dskip = (const uint16_t*)d_in[14];
    char* ws = (char*)d_ws;
    hipLaunchKernelGGL(k1, dim3(256), dim3(512), 0, stream, x, bre, bim, lre, lim, logdt, cre, cim, dskip, ssmk, ssmD, q, kin, vin, sk, ws, (float*)d_out TS_PASS(0));
}
```

```cpp
#include <hip/hip_runtime.h>
#include <stdint.h>
#include <type_traits>

#if defined(__HIP_DEVICE_COMPILE__)
#pragma clang attribute push(__attribute__((target("no-packed-fp32-ops"))), apply_to = function)
#endif

namespace fk {

typedef __attribute__((ext_vector_type(8))) short bf16x8;
typedef __attribute__((ext_vector_type(4))) short bf16x4;
typedef __attribute__((ext_vector_type(4))) float f32x4;
typedef __attribute__((ext_vector_type(4))) unsigned u32x4;
typedef __attribute__((ext_vector_type(2))) unsigned u32x2;
typedef __attribute__((ext_vector_type(2))) __bf16 bf16x2_t;
typedef __attribute__((address_space(1))) unsigned gu32;
typedef __attribute__((address_space(1))) unsigned long long gu64;
#define RLX_AGENT __ATOMIC_RELAXED, __HIP_MEMORY_SCOPE_AGENT

__device__ __forceinline__ float u2f(unsigned v) { return __builtin_bit_cast(float, v); }
__device__ __forceinline__ unsigned f2u(float v) { return __builtin_bit_cast(unsigned, v); }
__device__ __forceinline__ bool wall(bool p) { return __builtin_amdgcn_ballot_w64(p) == ~0ull; }
__device__ __forceinline__ float bflo(unsigned w) { return u2f(w << 16); }
__device__ __forceinline__ float bfhi(unsigned w) { return u2f(w & 0xffff0000u); }
__device__ __forceinline__ unsigned pack2(float lo, float hi) {
    bf16x2_t v; v[0] = (__bf16)lo; v[1] = (__bf16)hi; return __builtin_bit_cast(unsigned, v);
}
__device__ __forceinline__ float rbf(float f) { float r; asm("v_cvt_pk_bf16_f32 %0, 0, %1" : "=v"(r) : "v"(f)); return r; }
__device__ __forceinline__ float fma_(float a, float b, float c) { return __builtin_fmaf(a, b, c); }

template <int I, int N, class F>
__device__ __forceinline__ void static_for(F&& f) {
    if constexpr (I + 1 == N) f(std::integral_constant<int, I>{});
    else if constexpr (I < N) { static_for<I, I + (N - I) / 2>(f); static_for<I + (N - I) / 2, N>(f); }
}
#define IC(v) std::integral_constant<int, (v)>{}
#define CV(c) decltype(c)::value

#define TS_ARG
#define TS_PASS(off)
#define TS_DECL
#define TS(i)
#define TSV(i, v)
#define TL_DECL
#define TL(i)
#define TS_FLUSH(slot)

#define TG_DECL
#define TG(i)
template <int N> __device__ __forceinline__ void wait_v() { asm volatile("s_waitcnt vmcnt(%0)" ::"n"(N > 63 ? 63 : N) : "memory"); }
template <int N> __device__ __forceinline__ void wait_l() { asm volatile("s_waitcnt lgkmcnt(%0)" ::"n"(N > 14 ? 14 : N) : "memory"); }
template <int N> __device__ __forceinline__ void wait_lr() { __builtin_amdgcn_s_waitcnt(0xc07f | ((N > 14 ? 14 : N) << 8)); }
template <class T> __device__ __forceinline__ void tie(T& x) { asm volatile("" : "+v"(x)); }
template <class T> __device__ __forceinline__ void keep(const T& x) { asm volatile("" :: "v"(x)); }
template <int OFF, class T> __device__ __forceinline__ void dsr128(T& d, unsigned a) { asm volatile("ds_read_b128 %0, %1 offset:%2" : "=v"(d) : "v"(a), "n"(OFF)); }
template <int OFF, class T> __device__ __forceinline__ void dsr64(T& d, unsigned a) { asm volatile("ds_read_b64 %0, %1 offset:%2" : "=v"(d) : "v"(a), "n"(OFF)); }
template <int OFF> __device__ __forceinline__ void dsr32(unsigned& d, unsigned a) { asm volatile("ds_read_b32 %0, %1 offset:%2" : "=v"(d) : "v"(a), "n"(OFF)); }
template <int O0, int O1, class T> __device__ __forceinline__ void dsr2(T& d, unsigned a) { asm volatile("ds_read2_b32 %0, %1 offset0:%2 offset1:%3" : "=v"(d) : "v"(a), "n"(O0), "n"(O1)); }
template <int OFF> __device__ __forceinline__ void dsr64tr(bf16x4& d, unsigned a) { asm volatile("ds_read_b64_tr_b16 %0, %1 offset:%2" : "=v"(d) : "v"(a), "n"(OFF)); }
template <int OFF, class T> __device__ __forceinline__ void dsw128(unsigned a, const T& v) { asm volatile("ds_write_b128 %0, %1 offset:%2" :: "v"(a), "v"(v), "n"(OFF) : "memory"); }
__device__ __forceinline__ bf16x8 cat(const bf16x4& lo, const bf16x4& hi) { return __builtin_shufflevector(lo, hi, 0, 1, 2, 3, 4, 5, 6, 7); }
__device__ __forceinline__ void barrier() { asm volatile("" ::: "memory"); __builtin_amdgcn_s_barrier(); asm volatile("" ::: "memory"); }
__device__ __forceinline__ int fresh(int v) { asm volatile("" : "+s"(v)); return v; }
__device__ __forceinline__ void sched() { __builtin_amdgcn_sched_barrier(0); }
template <bool FIRST> __device__ __forceinline__ void mfma(f32x4& c, const bf16x8& a, const bf16x8& b) {
    if constexpr (FIRST) asm volatile("v_mfma_f32_16x16x32_bf16 %0, %1, %2, 0" : "=v"(c) : "v"(a), "v"(b));
    else asm volatile("v_mfma_f32_16x16x32_bf16 %0, %1, %2, %0" : "+v"(c) : "v"(a), "v"(b));
}
struct cf32 { float r, i; };
__device__ __forceinline__ cf32 cmul(cf32 a, cf32 b) { return cf32{fma_(a.r, b.r, -(a.i * b.i)), fma_(a.r, b.i, a.i * b.r)}; }
__device__ __forceinline__ cf32 cfma(cf32 a, cf32 b, cf32 c) { return cf32{fma_(a.r, b.r, fma_(-a.i, b.i, c.r)), fma_(a.r, b.i, fma_(a.i, b.r, c.i))}; }
__device__ __forceinline__ void sw16(float v, float& e, float& o) { const auto r = __builtin_amdgcn_permlane16_swap(f2u(v), f2u(v), false, false); e = u2f(r[0]); o = u2f(r[1]); }
__device__ __forceinline__ void sw32(float v, float& e, float& o) { const auto r = __builtin_amdgcn_permlane32_swap(f2u(v), f2u(v), false, false); e = u2f(r[0]); o = u2f(r[1]); }
constexpr unsigned SPIN_CAP = 1u << 16;
constexpr unsigned OOB = 0x40000000u;

constexpr size_t WS_H = 0;
constexpr size_t WS_KVT = (size_t)8 << 20;
constexpr size_t WS_ZT = (size_t)12 << 20;
constexpr size_t WS_PP = (size_t)16 << 20;
constexpr size_t WS_E = (size_t)24 << 20;
constexpr size_t WS_F = WS_E + (256 << 10);
constexpr int F_H = 0;
constexpr int F_K = 16384;
constexpr int F_Z = 24576;
constexpr int F_EP = F_Z + 32768;
constexpr int LDS_TOTAL = 163840, BUF = 32768;
constexpr int KC = 98304, KCH = 8320, KCP = 4160;
constexpr int U0 = KC + 4 * KCH;
constexpr int FULL = U0 + 18432 + 8192, FST = 388;
constexpr int HGO = FULL + 1792, DSK = HGO + 256, POLL = DSK + 256;
static_assert(POLL + 8 * 256 <= LDS_TOTAL && 4 * FST <= 1792, "lds");
constexpr int g1_buf(int s) { return ((0x0213 >> ((s & 3) * 4)) & 3) * BUF; }
constexpr int IMG = KC, QST = KC + 16384, ZST = U0, PLD = 2 * BUF;
#define CODE_KB 47
#define EXACT 0
#define HP 1
#define CP 2
#define FLQ 11
#define R01 3
#define ZP 7
#define SLP 8
#define STR_(x) #x
#define STR(x) STR_(x)
#define NOST 0
#define G2M 0
#define RD 2
#define PRG 1
#define OSC 16
#define NOA 0
#define PPH 0
#define HS 0
#define JQ(j) (HS == 0 ? 0 : HS == 1 ? (j) & 1 : HS == 2 ? (j) : (j) & 2)
#define JR(j) (HS == 0 ? 2 * (j) : HS == 1 ? 2 * ((j) & 2) : HS == 2 ? 0 : 4 * ((j) & 1))
#define NOX 0

__device__ __forceinline__ u32x4 mk_rsrc(const void* p, unsigned bytes) {
    const unsigned long long a = (unsigned long long)p;
    return u32x4{(unsigned)a, (unsigned)(a >> 32) & 0xffffu, bytes, 0x00020000u};
}
__device__ __forceinline__ __amdgpu_buffer_rsrc_t mk_brs(const void* p, unsigned bytes) { return __builtin_amdgcn_make_buffer_rsrc((void*)p, (short)0, bytes, 0x00020000); }
template <int IMM, bool SC1> __device__ __forceinline__ void dma(unsigned lds, unsigned voff, const u32x4& rs, unsigned soff) {
    if constexpr (SC1) asm volatile("s_add_u32 m0, %0, %1\n\ts_nop 0\n\tbuffer_load_dwordx4 %2, %3, %4 offen sc1 lds" :: "s"(lds), "n"(IMM), "v"(voff), "s"(rs), "s"(soff) : "memory", "scc");
    else asm volatile("s_add_u32 m0, %0, %1\n\ts_nop 0\n\tbuffer_load_dwordx4 %2, %3, %4 offen lds" :: "s"(lds), "n"(IMM), "v"(voff), "s"(rs), "s"(soff) : "memory", "scc");
}
template <int IMM> __device__ __forceinline__ void dma1(unsigned lds, unsigned voff, const u32x4& rs, unsigned soff) {
    asm volatile("s_add_u32 m0, %0, %1\n\ts_nop 0\n\tbuffer_load_dword %2, %3, %4 offen sc1 lds" :: "s"(lds), "n"(IMM), "v"(voff), "s"(rs), "s"(soff) : "memory", "scc");
}
__device__ __forceinline__ void flag_set(unsigned waddr, unsigned v) { asm volatile("ds_write_b32 %0, %1" :: "v"(waddr), "v"(v) : "memory"); }
__device__ __forceinline__ void flag_chk(unsigned& f, unsigned raddr, unsigned need) {
    unsigned cnt;
    asm volatile("v_cmp_le_u32 vcc, %3, %0\n\t"
                 "s_cmp_eq_u64 vcc, -1\n\t"
                 "s_cbranch_scc1 1f\n\t"
                 "s_mov_b32 %1, 0x100000\n"
                 "0:\n\t"
                 "ds_read_b32 %0, %2\n\t"
                 "s_sub_u32 %1, %1, 1\n\t"
                 "s_waitcnt lgkmcnt(0)\n\t"
                 "s_cbranch_scc1 1f\n\t"
                 "v_cmp_le_u32 vcc, %3, %0\n\t"
                 "s_cmp_eq_u64 vcc, -1\n\t"
                 "s_cbranch_scc0 0b\n"
                 "1:" : "+v"(f), "=&s"(cnt) : "v"(raddr), "s"(need) : "vcc", "scc", "memory");
}
template <int OFF> __device__ __forceinline__ void zero_chk(unsigned& f, unsigned raddr) {
    unsigned cnt;
    asm volatile("v_cmp_ne_u32 vcc, 0, %0\n\t"
                 "s_cbranch_vccz 1f\n\t"
                 "s_mov_b32 %1, 0x100000\n"
                 "0:\n\t"
                 "ds_read_b32 %0, %2 offset:%3\n\t"
                 "s_sub_u32 %1, %1, 1\n\t"
                 "s_waitcnt lgkmcnt(0)\n\t"
                 "s_cbranch_scc1 1f\n\t"
                 "v_cmp_ne_u32 vcc, 0, %0\n\t"
                 "s_cbranch_vccnz 0b\n"
                 "1:" : "+v"(f), "=&s"(cnt) : "v"(raddr), "n"(OFF) : "vcc", "scc", "memory");
}
template <int IMM, class T> __device__ __forceinline__ void gld(T& d, unsigned voff, const u32x4& rs, unsigned soff) {
    if constexpr (sizeof(T) == 16) asm volatile("buffer_load_dwordx4 %0, %1, %2, %3 offen offset:%4" : "=v"(d) : "v"(voff), "s"(rs), "s"(soff), "n"(IMM) : "memory");
    else if constexpr (sizeof(T) == 8) asm volatile("buffer_load_dwordx2 %0, %1, %2, %3 offen offset:%4" : "=v"(d) : "v"(voff), "s"(rs), "s"(soff), "n"(IMM) : "memory");
    else asm volatile("buffer_load_ushort %0, %1, %2, %3 offen offset:%4" : "=v"(d) : "v"(voff), "s"(rs), "s"(soff), "n"(IMM) : "memory");
}
__device__ __forceinline__ void gld32(unsigned& d, unsigned voff, const u32x4& rs, unsigned soff) { asm volatile("buffer_load_dword %0, %1, %2, %3 offen" : "=v"(d) : "v"(voff), "s"(rs), "s"(soff) : "memory"); }
__device__ __forceinline__ void sld(unsigned& d, const void* p) { asm volatile("s_load_dword %0, %1, 0x0" : "=s"(d) : "s"(p) : "memory"); }
__device__ __forceinline__ void stouch(unsigned long long p) { asm volatile("s_load_dword s101, %0, 0x0\n\ts_load_dword s101, %0, 0x80\n\ts_load_dword s101, %0, 0x100" :: "s"(p) : "s101", "memory"); }
__device__ __forceinline__ void ties(unsigned& x) { asm volatile("" : "+s"(x)); }
template <int SLQ> __device__ __forceinline__ void gate(unsigned raddr, unsigned need) {
    unsigned cnt, f;
    asm volatile("s_mov_b32 %1, 0x40000\n"
                 "0:\n\t"
                 "ds_read_b32 %0, %2\n\t"
                 "s_sub_u32 %1, %1, 1\n\t"
                 "s_waitcnt lgkmcnt(0)\n\t"
                 "s_cbranch_scc1 1f\n\t"
                 "v_cmp_le_u32 vcc, %3, %0\n\t"
                 "s_cmp_eq_u64 vcc, -1\n\t"
                 "s_cbranch_scc1 1f\n\t"
                 "s_sleep %4\n\t"
                 "s_branch 0b\n"
                 "1:" : "=&v"(f), "=&s"(cnt) : "v"(raddr), "s"(need), "n"(SLQ) : "vcc", "scc", "memory");
}
__device__ __forceinline__ void tag_chk(unsigned& f, unsigned expect, unsigned voff, const u32x4& rs, unsigned soff) {
    unsigned cnt;
    asm volatile("v_cmp_eq_u32 vcc, %2, %0\n\t"
                 "s_cmp_eq_u64 vcc, -1\n\t"
                 "s_cbranch_scc1 1f\n\t"
                 "s_mov_b32 %1, 0x10000\n"
                 "0:\n\t"
                 "s_sleep " STR(SLP) "\n\t"
                 "buffer_load_dword %0, %3, %4, %5 offen sc1\n\t"
                 "s_sub_u32 %1, %1, 1\n\t"
                 "s_waitcnt vmcnt(0)\n\t"
                 "s_cbranch_scc1 1f\n\t"
                 "v_cmp_eq_u32 vcc, %2, %0\n\t"
                 "s_cmp_eq_u64 vcc, -1\n\t"
                 "s_cbranch_scc0 0b\n"
                 "1:" : "+v"(f), "=&s"(cnt) : "v"(expect), "v"(voff), "s"(rs), "s"(soff) : "vcc", "scc", "memory");
}

template <int H> __device__ __forceinline__ void pp_ld(u32x4& d, unsigned voff, const u32x4& rs, unsigned soff) {
    if constexpr (H) asm volatile("buffer_load_dwordx4 v[244:247], %1, %2, %3 offen sc1" : "={v[244:247]}"(d) : "v"(voff), "s"(rs), "s"(soff) : "memory");
    else asm volatile("buffer_load_dwordx4 v[240:243], %1, %2, %3 offen sc1" : "={v[240:243]}"(d) : "v"(voff), "s"(rs), "s"(soff) : "memory");
}
__device__ __forceinline__ void pf_ld(unsigned& d, unsigned voff, const u32x4& rs, unsigned soff) { asm volatile("buffer_load_dword v248, %1, %2, %3 offen sc1" : "={v248}"(d) : "v"(voff), "s"(rs), "s"(soff) : "memory"); }
#define PP_CHK(R, R1, R2, R3) \
    asm volatile("v_cmp_ne_u32 vcc, 0, %2\n\t" \
                 "s_cbranch_vccz 1f\n\t" \
                 "s_mov_b32 %1, 0x10000\n" \
                 "0:\n\t" \
                 "buffer_load_dwordx4 " R ", %5, %6, %7 offen sc1\n\t" \
                 "s_waitcnt vmcnt(0)\n\t" \
                 "s_and_b32 %3, %4, 0xf000f\n\t" \
                 "v_xor_b32 " R1 ", %3, " R1 "\n\t" \
                 "s_lshr_b32 %3, %4, 8\n\t" \
                 "s_and_b32 %3, %3, 0xf000f\n\t" \
                 "v_xor_b32 " R2 ", %3, " R2 "\n\t" \
                 "s_lshr_b32 %3, %4, 4\n\t" \
                 "s_and_b32 %3, %3, 0xf000f\n\t" \
                 "v_xor_b32 " R3 ", %3, " R3 "\n\t" \
                 "v_or3_b32 %2, " R1 ", " R2 ", " R3 "\n\t" \
                 "v_and_b32 %2, 0xf000f, %2\n\t" \
                 "s_sub_u32 %1, %1, 1\n\t" \
                 "s_cbranch_scc1 1f\n\t" \
                 "v_cmp_ne_u32 vcc, 0, %2\n\t" \
                 "s_cbranch_vccnz 0b\n" \
                 "1:" : "+{" R "}"(p), "=&s"(cnt), "+v"(bad), "=&s"(t) : "s"(tag), "v"(voff), "s"(rs), "s"(soff) : "vcc", "scc", "memory")
template <int H> __device__ __forceinline__ void pp_chk(u32x4& p, unsigned bad, unsigned tag, unsigned voff, const u32x4& rs, unsigned soff) {
    unsigned cnt, t;
    if constexpr (H) PP_CHK("v[244:247]", "v245", "v246", "v247"); else PP_CHK("v[240:243]", "v241", "v242", "v243");
}
template <int H> __device__ __forceinline__ void pp_tie(u32x4& p) { if constexpr (H) asm volatile("" : "+{v[244:247]}"(p)); else asm volatile("" : "+{v[240:243]}"(p)); }
__device__ __forceinline__ float amax3(float a, float b, float c) { float r; asm volatile("v_max3_f32 %0, |%1|, |%2|, |%3|" : "=v"(r) : "v"(a), "v"(b), "v"(c)); return r; }
__device__ __forceinline__ unsigned bfi_v(unsigned m, unsigned a, unsigned b) { unsigned r; asm volatile("v_bfi_b32 %0, %1, %2, %3" : "=v"(r) : "v"(m), "v"(a), "v"(b)); return r; }
__device__ __forceinline__ unsigned bfi_s(unsigned m, unsigned a, unsigned b) { unsigned r; asm volatile("v_bfi_b32 %0, %1, %2, %3" : "=v"(r) : "v"(m), "v"(a), "s"(b)); return r; }
template <int H> __device__ __forceinline__ float cvt16(unsigned w) {
    float r;
    if constexpr (H) asm volatile("v_cvt_f32_i32_sdwa %0, sext(%1) dst_sel:DWORD dst_unused:UNUSED_PAD src0_sel:WORD_1" : "=v"(r) : "v"(w));
    else asm volatile("v_cvt_f32_i32_sdwa %0, sext(%1) dst_sel:DWORD dst_unused:UNUSED_PAD src0_sel:WORD_0" : "=v"(r) : "v"(w));
    return r;
}
__device__ __forceinline__ float sel8(float a, float b, unsigned long long mask) {
    float r;
    asm volatile("s_mov_b64 vcc, %3\n\tv_cndmask_b32_dpp %0, %1, %2, vcc row_ror:8 row_mask:0xf bank_mask:0xf" : "=v"(r) : "v"(a), "v"(b), "s"(mask) : "vcc");
    return r;
}
__device__ __forceinline__ unsigned sel0(unsigned a, unsigned long long mask) { unsigned r; asm volatile("v_cndmask_b32_e64 %0, 0, %1, %2" : "=v"(r) : "v"(a), "s"(mask)); return r; }
constexpr unsigned long long diag_mask(int R) {
    unsigned long long m = 0;
    for (int l = 0; l < 64; ++l) if ((l >> 4) == ((l & 7) >> 1) && 2 * (l & 1) + ((l >> 3) & 1) == R) m |= 1ull << l;
    return m;
}
template <int IMM, int X> __device__ __forceinline__ void dma_x(unsigned& so, unsigned lds, unsigned voff, const u32x4& rs, unsigned base) {
    asm volatile("s_add_u32 m0, %1, %2\n\ts_xor_b32 %0, %5, %6\n\tbuffer_load_dwordx4 %3, %4, %0 offen sc1 lds" : "=&s"(so) : "s"(lds), "n"(IMM), "v"(voff), "s"(rs), "s"(base), "n"(X) : "memory", "scc");
}
template <int OFF> __device__ __forceinline__ void dma_n(unsigned so, unsigned voff, const u32x4& rs) {
    asm volatile("buffer_load_dwordx4 %0, %1, %2 offen offset:%3 sc1 lds" :: "v"(voff), "s"(rs), "s"(so), "n"(OFF) : "memory");
}
template <int IMM, int OFF> __device__ __forceinline__ void dma_m(unsigned so, unsigned lds, unsigned voff, const u32x4& rs) {
    asm volatile("s_add_u32 m0, %4, %5\n\ts_nop 0\n\tbuffer_load_dwordx4 %0, %1, %2 offen offset:%3 sc1 lds" :: "v"(voff), "s"(rs), "s"(so), "n"(OFF), "s"(lds), "n"(IMM) : "memory", "scc");
}
__device__ __forceinline__ unsigned and_or(unsigned a, unsigned m, unsigned o) { unsigned r; asm("v_and_or_b32 %0, %1, %2, %3" : "=v"(r) : "v"(a), "v"(m), "s"(o)); return r; }

#define HOPS 1
#define HM 0x2210243082ull
#define HQ 52
#define HW 4
constexpr int hop_nth(int n) { for (int k = 0; k < 40; ++k) if (((HM >> k) & 1) && n-- == 0) return k; return 63; }
constexpr int hop_next(int K) { int n = 0; for (int k = 0; k < K; ++k) n += (HM >> k) & 1; return hop_nth(n + HW); }
template <int K> __device__ __forceinline__ void hop() {
    if constexpr (HOPS && ((HM >> K) & 1)) asm volatile(".Lfkh%0:\n\ts_cbranch_execz .Lfkh%1" :: "n"(K), "n"(hop_next(K)));
}
template <int K> __device__ __forceinline__ void page() {
    if constexpr (HOPS && K >= 0 && ((HM >> (K & 63)) & 1)) asm volatile("s_branch 1f\n\t.p2align 12\n.Lfkh%0:\n\ts_cbranch_execz .Lfkh%1\n1:" :: "n"(K), "n"(hop_next(K)));
    else if constexpr (HOPS) asm volatile("s_branch 1f\n\t.p2align 12\n1:");
}
__device__ __forceinline__ void hop_all(int w) {
    if constexpr (HOPS) asm volatile("s_mov_b64 exec, 0\n\ts_cmp_eq_u32 %0, 0\n\ts_cbranch_scc1 .Lfkh%1\n\ts_cmp_eq_u32 %0, 1\n\ts_cbranch_scc1 .Lfkh%2\n\t"
                                     "s_cmp_eq_u32 %0, 2\n\ts_cbranch_scc1 .Lfkh%3\n\ts_branch .Lfkh%4\n.Lfkh63:\n\ts_mov_b64 exec, -1"
                                     :: "s"(w), "n"(hop_nth(0)), "n"(hop_nth(1)), "n"(hop_nth(2)), "n"(hop_nth(3)) : "scc");
}
enum { OP_NONE, OP_A, OP_B, OP_FLAG };
struct Op { int kind, a, b; };
constexpr Op g1_op(int G) {
    const int st = (G + 32) / 32 - 1, q = G - 32 * st, i = q >> 2, j = q & 3;
    if (i < 5) {
        if (st < 0) return {OP_NONE, 0, 0};
        if ((j & 1) == 0) return {OP_A, st * 8 + i + 3, j >> 1};
        if (q == FLQ && st < 15) return {OP_FLAG, 0, 0};
        return {OP_NONE, 0, 0};
    }
    if (st == 15) return {OP_NONE, 0, 0};
    const int n = (st + 1) * 8;
    switch (q) {
        case 20: return {OP_B, st + 1, 0}; case 21: return {OP_B, st + 1, 1}; case 22: return {OP_A, n, 0}; case 23: return {OP_B, st + 1, 2};
        case 24: return {OP_B, st + 1, 3}; case 25: return {OP_A, n, 1}; case 26: return {OP_A, n + 1, 0}; case 27: return {OP_A, n + 1, 1};
        case 28: return {OP_A, n + 2, 0}; case 30: return {OP_A, n + 2, 1};
    }
    return {OP_NONE, 0, 0};
}
constexpr int g1_need(int G) {
    const int st = G >> 5, j = G & 3;
    for (int h = G - 1; h >= -12; --h) {
        const Op o = g1_op(h);
        if (o.kind == OP_A && o.a == (G >> 2) && o.b == (j >> 1)) return h;
        if (o.kind == OP_B && o.a == st && o.b == j) return h;
        if (o.kind == OP_FLAG && (G & 31) == 20 && (h >> 5) == st) return h;
    }
    return -100;
}
constexpr int g1_wait(int G) {
    int n = -100, c = 0;
    for (int h = G; h < (G ? G + 4 : 2) && h < 512; ++h) if (g1_need(h) > n) n = g1_need(h);
    for (int h = n + 1; h < G; ++h) c += g1_op(h).kind != OP_NONE;
    return n < G ? c : -1;
}
#define KV0 0
#define KVN 0x11111112
constexpr int kv_first(int k) { int n = 0; for (int i = KV0; i < k && i < KV0 + 8; ++i) n += (KVN >> (4 * (i - KV0))) & 15; return n > 8 ? 8 : n; }
constexpr int kv_last() { int k = KV0; while (kv_first(k + 1) < 8) ++k; return k; }
#define CQ -1
#define CTM 0
#define EC 2
#define ED 2
#define EB 1
#define KST 0
#define KFD 3
#define KPL 11
#define CBQ 20
constexpr int KVL = kv_last(), KFL = KVL + KFD;
static_assert(KFL <= KPL && KPL <= 11, "");
constexpr int hv_cnt(int k0, int k1) {
    int c = 0;
    for (int k = k0; k < k1; ++k) c += 9 + (KST ? 4 * (k == KVL) : kv_first(k + 1) / 2 - kv_first(k) / 2) + (k == KFL) + (k == KPL);
    return c;
}
#define ZM 1
#define CL 1
#define EP 0
constexpr unsigned long long lag0_mask(int w) {
    unsigned long long m = 0;
    for (int l = 0; l < 64; ++l) if ((l >> 4) == 2 + ((l >> 3) & 1) && 3 - ((l & 7) >> 1) == w) m |= 1ull << l;
    return m;
}
constexpr int cv_start(int n) { return n * 8 - n * (n - 1) / 2; }
constexpr int cv_n(int q) { int n = 0; while (cv_start(n + 1) <= q) ++n; return n; }
constexpr int cv_m(int q) { return 7 - cv_n(q) - (q - cv_start(cv_n(q))); }
enum { CO_NONE, CO_A, CO_B, CO_C };
constexpr Op cv_op(int G) {
    const int s = (G + 36) / 36 - 1, q = G - 36 * s;
    if (q >= 1 && q <= 6) return s >= 0 ? Op{CO_A, s * 8 + 5 + (q - 1) / 2, (q - 1) & 1} : Op{CO_NONE, 0, 0};
    if (q >= 11 && q <= 14) return s >= CL ? Op{CO_C, s - CL, q - 11} : Op{CO_NONE, 0, 0};
    if (s == 7) return {CO_NONE, 0, 0};
    for (int m = 0; m < 8; ++m) if (q == cv_start(7 - m)) return {CO_B, (s + 1) * 8 + m, 0};
    for (int n = 0; n < 5; ++n) if (q == cv_start(n + 1) + 1 || q == cv_start(n + 1) + 2) return {CO_A, (s + 1) * 8 + n, q - cv_start(n + 1) - 1};
    return {CO_NONE, 0, 0};
}
constexpr int cv_need(int G) {
    const int s = G / 36, q = G % 36;
    for (int h = G - 1; h >= -36; --h) {
        const Op o = cv_op(h);
        if (o.kind == CO_A && o.a == s * 8 + cv_n(q)) return h;
        if (o.kind == CO_B && o.a == s * 8 + cv_m(q)) return h;
        if (o.kind == CO_C && q == 24 && o.a == s - CL) return h;
    }
    return -100;
}
constexpr int cv_wait(int G) {
    const int n = cv_need(G);
    for (int h = G - 1; h >= 0 && h >= G - 40; --h) if (cv_need(h) >= n) return -1;
    int c = 0;
    for (int h = n + 1; h < G; ++h) c += cv_op(h).kind != CO_NONE;
    return c;
}
#define AT(n, m) ((n) * 8 + (m))
#define FM 0x333330
constexpr int fill_pos(int s, int i) {
    const int f = (FM >> (4 * (s - 2))) & 15, n = s == 2 ? 0 : 4 * s - 9;
    switch (f) {
        case 1: return AT(n + i, 5) - (s == 2 && i == 3) * 6;
        case 2: return AT(n + 1 + i / 2, 5 + 2 * (i & 1));
        case 3: return i < 2 ? AT(n, 5 + 2 * i) : AT(n + i - 1, 5);
        case 4: return AT(n + 2 + i / 2, 5 + 2 * (i & 1));
        case 5: return AT(n, 1 + 2 * i) + (s == 2 ? 4 : 0);
    }
    return AT(n + i / 2, 5 + 2 * (i & 1));
}
constexpr int g2_fill(int p) {
    for (int k = 8; k < 32; ++k) if (fill_pos(k >> 2, k & 3) == p) return k;
    return -1;
}
#define QX AT(2, 5)
#define QQ AT(5, 4)
#define QZ AT(11, 2)
#define QT AT(3, 4)
constexpr int P_X = QX, P_Q = QQ, P_Z = QZ, P_T = QT;
constexpr bool XL = false;
constexpr bool fill_m0(int k) {
    const int a = fill_pos(k >> 2, (k & 3) - 1), b = fill_pos(k >> 2, k & 3);
    return (P_Q + 2 >= a && P_Q < b) || (P_Z + 2 >= a && P_Z < b) || (P_X + 2 >= a && P_X < b);
}
#define DBG 0
#define XW 4
#define XW2 4
#define QPP0 AT(9, 4)
#define QL0 AT(21, 1)
#define QR0 AT(24, 6)
#define QF2 AT(28, 3)
#define QL1 AT(26, 1)
#define QR1 AT(29, 3)
#define QA AT(16, 4)
#define AD 0
#define D1 2
#define D0 1
constexpr int NS = 33, NR = 29;
constexpr int P_PP0 = QPP0, P_PP1 = AT(16, 4), P_L0 = QL0, P_R0 = QR0, P_F2 = QF2, P_L1 = QL1, P_R1 = QR1, P_ZC = AT(10, 6), P_A = QA;
constexpr int E_PP0 = P_PP0 + NS - 1, E_PP1 = P_PP1 + (NS - 1) / D1, E_R0 = P_R0 + (NR - 1) / D0;
constexpr int ZW = P_A >= AT(20, 4) ? 19 : 15;
static_assert(P_R0 + 14 / D0 >= AT(24, 3) && E_R0 < P_F2 && P_F2 >= AT(24, 4) && P_F2 + 8 <= AT(31, 0) && E_R0 < P_R1, "");
static_assert(P_PP0 >= AT(9, 2) && P_PP0 + 8 <= AT(15, 4) && E_PP0 < P_PP1 && P_A >= AT(16, 4), "");
constexpr bool a_free(int p) {
    return p >= P_A && (AD || !((p >= P_PP1 && p <= E_PP1) || (p >= P_R0 && p <= E_R0) || (p >= P_F2 && p < P_F2 + 16)));
}
constexpr int a_step(int p) {
    if (NOA || !a_free(p)) return -1;
    int c = 0;
    for (int h = P_A; h < p; ++h) c += a_free(h);
    return c <= 48 ? c : -1;
}
constexpr int a_pos(int s) { int p = P_A; while (a_step(p) != s) ++p; return p; }
constexpr int q_rd(int r) { int p = r < 2 ? AT(ZW, 6) + r : a_pos(12 * r - 16) + 1; while ((p & 7) < 4) ++p; return p; }
constexpr int q_tie(int r) { const int n = (q_rd(r) >> 3) + 1; return AT(n, (n & 3) == 3 ? 0 : 4); }
static_assert(NOA || (a_pos(48) < 256 && q_tie(2) <= a_pos(23) && q_tie(3) <= a_pos(35)), "");
constexpr int g2_vm(int p) {
    int c = g2_fill(p) >= 0;
    c += a_step(p) > 0 && a_step(p) % 12 == 0 && !(NOST & 1);
    if (!NOA) c += (p == P_T) + (p == P_Q || p == P_Q + 2) + (p == AT(ZP, 4)) + (p == P_Z || p == P_Z + 2);
    c += p == P_X || p == P_X + 2;
    c += !(NOST & 2) && (p == E_PP0 || p == E_PP1);
    c += p == P_L0 || p == P_L1;
    c += !(NOST & 4) && (p == P_F2 + 11 || p == P_F2 + 15);
    return c;
}
constexpr int g2_lx(int n) {
    int c = (n == 2) + 2 * (n == 7);
    if (n >= 0 && (n & 7) >= 6) c += 2;
    if (!NOA) { c += 4 * (n == ZW); for (int r = 0; r < 4; ++r) c += (q_rd(r) >> 3) == n; }
    return c;
}
constexpr int g2_tile(int n, int j) { return (n & 4) ? (n & 2) == 0 : j; }
constexpr int g2_unit(int n, int j) { return (n & 4) ? 4 + 2 * (n & 1) + j : n & 7; }
constexpr int imax(int a, int b) { return a > b ? a : b; }
constexpr int g2_need(int n) {
    int r = n == 3 ? -1 : fill_pos((n + 1) / 4, 3);
    if (n == 7 && !XL) r = imax(r, P_X + 2);
    if (n == ZW && !NOA) r = imax(r, imax(P_Q, P_Z) + 2);
    return r;
}
static_assert(P_Z > AT(11, 0) && P_T > AT(3, 0) && P_T < AT(ZP, 4) && P_Q >= AT(1, 2) && P_X >= AT(1, 5) && (!XL || (P_X + 2 < P_R0 && P_R0 + 2 >= AT(24, 2) && P_R0 < AT(27, 0) && D0 == 1)), "");
constexpr bool rd1(int n) { return RD == 1 || (RD == 3 && (n & 3) == 2); }
constexpr int g2_allow(int e, int p) { int c = 0; for (int h = p + 1; h < e; ++h) c += g2_vm(h); return c; }

__global__ __launch_bounds__(512, 2) __attribute__((aligned(4096))) void k1(const uint16_t* __restrict__ x, const uint16_t* __restrict__ bre, const uint16_t* __restrict__ bim,
                                             const uint16_t* __restrict__ lre, const uint16_t* __restrict__ lim, const uint16_t* __restrict__ logdt,
                                             const uint16_t* __restrict__ cre, const uint16_t* __restrict__ cim, const uint16_t* __restrict__ dskip,
                                             const uint16_t* __restrict__ ssmk, const uint16_t* __restrict__ ssmD, const uint16_t* __restrict__ qin,
                                             const uint16_t* __restrict__ kin, const uint16_t* __restrict__ vin, const uint16_t* __restrict__ sk,
                                             char* __restrict__ ws, float* __restrict__ out TS_ARG) {
    unsigned long long pc0;
    asm volatile("s_getpc_b64 %0" : "=s"(pc0));
    TS_DECL TS(0);
    TL_DECL
    __shared__ __attribute__((aligned(1024))) char shm[LDS_TOTAL];
    const int tid = threadIdx.x, lane = tid & 63, wid = __builtin_amdgcn_readfirstlane(tid >> 6);
    const int b = blockIdx.x, xcd = b & 7, idx = b >> 3, hb = xcd & 1, tq = idx & 7;
    const int tm = (xcd >> 1) * 4 + (idx >> 3), tn = hb * 8 + tq;
    const int tm2 = G2M ? tm : (xcd & 2) * 4 + tq, dt = G2M ? tq : (xcd & 4) + (idx >> 3), rot2 = tq + JR(idx >> 3);
    const unsigned lbase = (unsigned)(uintptr_t)shm;
    const int l15 = lane & 15, g4 = lane >> 4, lw = wid & 3, kg = wid >> 2;
    dma<U0 + 4096, false>(lbase + wid * 1024, lane * 16, mk_rsrc((const void*)(pc0 & ~255ull), CODE_KB * 1024), ((idx * 8 + wid) & 63) * 1024);
    const unsigned mk0 = lbase + POLL + lw * 256 + kg * (FULL - POLL + lw * (FST - 256));
    flag_set(mk0 + lane * 4, 1u);
    flag_set(lbase + HGO + lane * 4, 0u);
    unsigned vo[4];
    const bool xw = lw < 2;
#pragma unroll
    for (int i = 0; i < 4; ++i) {
        const int v = l15 ^ (i * 4 + g4);
        vo[i] = ((xw ? 64 * (i & 1) + 4 * (i >> 1) + (lane & 32) + 2 * (g4 & 1) : 8 * (i & 1) + 2 * g4) + (v >> 3)) * 2048 + (v & 7) * 16;
    }
    const int bw = lw >> 1;
    unsigned r0 = (tm * 128 + lw * 16 + bw * (tn * 64 + lw * 16 - 64 - tm * 128)) * 2048, rsz = 229376 - bw * 163840;
    unsigned rot = 8 * ((tq + (idx >> 3)) & 1) + 2 * (xcd >> 1) + 4 * hb, rst = 16384 << bw, lb = lbase + lw * 8192, oob = OOB;
    unsigned lb0 = lb + g1_buf(0) + kg * (g1_buf(1) - g1_buf(0)), so0 = ((kg + rot) & 15) * 128;
    unsigned co = l15 * 2 + (tn * 128 + lw * 32 + kg * 2048);
    tie(vo[0]); tie(vo[1]); tie(vo[2]); tie(vo[3]); tie(oob); tie(co);
    ties(r0); ties(rsz); ties(rot); ties(rst); ties(lb); ties(lb0); ties(so0);
    sched();
    asm volatile("" :: "s"(x), "s"(bre), "s"(bim), "s"(lre), "s"(lim), "s"(logdt), "s"(cre), "s"(cim), "s"(dskip), "s"(ssmk), "s"(ssmD), "s"(qin),
                       "s"(kin), "s"(vin), "s"(sk), "s"(ws), "s"(out));
    const u32x4 rs0 = mk_rsrc((const char*)(xw ? x : bre) + r0, rsz), rs1 = mk_rsrc((const char*)(xw ? x : bim) + r0, rsz);
    unsigned c_dt, c_re, c_im;
    gld<0>(c_dt, co, mk_rsrc(logdt, 2048), 0);
    gld<0>(c_re, co, mk_rsrc(lre, 2048), 0);
    gld<0>(c_im, co, mk_rsrc(lim, 2048), 0);
    static_for<0, 8>([&](auto ic) { constexpr int i = CV(ic); dma<i * 1024, false>(lb0, vo[i & 3], (i & 2) ? rs1 : rs0, so0 + (i >> 2) * rst); });
    wait_l<0>();
    barrier();
    dma1<0>(mk0 + kg * 120, oob, rs0, 0);
    sched();
    const unsigned a00 = (l15 >> 1) * 256 + ((((l15 & 1) * 8 + g4) ^ (l15 >> 1)) << 4);
    auto frag = [&](int par, int ks) { return (a00 ^ (ks * 64) ^ (par * 128)) + par * 2048; };
    bf16x8 cf[32];
    unsigned vc[4];
    auto c_setup = [&]() {
#pragma unroll
        for (int i = 0; i < 4; ++i) { const int v = l15 ^ (i * 4 + g4); vc[i] = (4 * (2 * g4 + (v >> 3)) + 2 * (i & 1) + (i >> 1)) * 2048 + (v & 7) * 16; }
    };
    const u32x4 rsc = mk_rsrc((kg ? cim : cre) + (size_t)dt * 131072, 262144);
    auto c_off = [&](int dg, int j) { return (unsigned)(dg * 65536 + hb * 1024 + ((rot2 + j) & 7) * 128); };
    const unsigned cst = lbase + (kg ? BUF : 2 * BUF) + lw * 8192;
    unsigned tag;
    const u32x4 rsf = mk_rsrc(ws + WS_F, 65536);
    const __amdgpu_buffer_rsrc_t rf = mk_brs(ws + WS_F, 65536);
    const u32x4 rsh = mk_rsrc(ws + WS_H + (size_t)(tm2 * 16 + hb * 8) * 32768, 262144);
    const int qx = (hb ? 0 : 2) ^ JQ(idx >> 3), wid_ = wid, b_ = b;
    unsigned ha[4];
    auto h_setup = [&](int kg) {
        const int q = (lane >> 2) & 3, p = lane & 3;
#pragma unroll
        for (int i = 0; i < 4; ++i) {
            ha[i] = lbase + (g4 >> 1) * 2048 + kg * 1024 + ((i >> 1) ^ kg) * 512 + (p >> 1) * 256 + (((8 * (g4 & 1) + 4 * (i & 1) + q) ^ (4 * (p >> 1))) << 4) + (p & 1) * 8;
            tie(ha[i]);
        }
    };

    if (wid >= 4) {
        page<-1>();
        const int ch = xcd * 128 + (idx >> 2) * 16 + lw * 4 + (idx & 3);
        unsigned dw; u32x4 ad;
        auto ctouch = [&](int r) { const unsigned j = (fresh(idx * 4 + lw) + r * 37) & 127; stouch((pc0 & ~255ull) + (j < CODE_KB * 8 / 3 ? j : j - 64) * 384); };
        {
            const unsigned fb = lbase + FULL + lw * FST;
            auto piece = [&](auto sc, auto ic) {
                constexpr int s = CV(sc), i = CV(ic);
                dma<g1_buf(s) + i * 1024, false>(lb, vo[i & 3], (i & 2) ? rs1 : rs0, ((s + rot) & 15) * 128 + (i >> 2) * rst);
            };
            auto mark = [&](auto sc) { dma1<(31 - CV(sc)) * 4>(fb, oob, rs0, 0); };
            auto feed = [&](auto kc, auto ic) {
                constexpr int k = CV(kc), i = CV(ic);
                if constexpr (i < 4) piece(IC(k + 3), IC(i + 4));
                if constexpr (i == 3) mark(IC(k + 3));
                if constexpr (i >= 4 && k < 12) piece(IC(k + 4), IC(i - 4));
            };
            __builtin_amdgcn_s_setprio(HP);
            TS(7);
            sld(tag, ws + WS_F + F_EP);
            sld(dw, ssmD + (ch & ~1));
            const int sb = idx * 2 + (lw >> 1), kh = lw & 1;
            u32x2 kk[11], vv[8]; u32x4 w4[2], kd[4], ke[4], kvo[4];
            const unsigned ko = g4 * 16384 + l15 * 8, so = sb * 65536 + xcd * 256 + kh * 128;
            {
                const u32x4 rs = mk_rsrc(kin, 4 << 20);
                gld<0>(w4[0], l15 * 32, mk_rsrc(sk, 8192), xcd * 1024 + kh * 512);
                gld<16>(w4[1], l15 * 32, mk_rsrc(sk, 8192), xcd * 1024 + kh * 512);
                static_for<0, 3>([&](auto ic) { gld<0>(kk[CV(ic)], ko + so - 6144 + CV(ic) * 2048, rs, 0); });
                static_for<0, 8>([&](auto ic) { constexpr int i = CV(ic); gld<(i & 1) * 2048>(kk[3 + i], ko, rs, so + (i >> 1) * 4096); });
                static_for<0, 8>([&](auto ic) { constexpr int i = CV(ic); gld<(i & 1) * 2048>(vv[i], ko, mk_rsrc(vin, 4 << 20), so + (i >> 1) * 4096); });
                dma1<DSK>(lbase, lane * 4, mk_rsrc(dskip, 2048), dt * 256);
                dma<U0, false>(lbase + kh * 1024, oob, rs0, 0);
            }
            static_for<4, 8>([&](auto ic) { feed(IC(-2), ic); });
            const __amdgpu_buffer_rsrc_t rk = mk_brs(ws + WS_KVT + (size_t)(xcd * 32 + kh * 16) * 16384, 262144);
            const unsigned kfo = F_K + ((xcd * 2 + (idx >> 4)) * 64 + lw * 16) * 8, K = lbase + KC + lw * KCH;
            float kf[7], pa[4], pb[4];
            auto kv_step = [&](auto uc, auto jc) {
                constexpr int u = CV(uc), j = CV(jc), c = u >> 1, h = u & 1;
                auto el = [&](const u32x2& q) -> float { return (c & 1) ? bfhi(q[c >> 1]) : bflo(q[c >> 1]); };
                auto tap = [&](int t) { const unsigned w = w4[c >> 1][(c & 1) * 2 + (t >> 1)]; return (t & 1) ? bfhi(w) : bflo(w); };
                auto all = [&](auto f) {
#pragma unroll
                    for (int i = 0; i < 4; ++i) f(i);
                };
                if constexpr (j == 0) {
#pragma unroll
                    for (int i = 0; i < 7; ++i) { kf[i] = (h && i < 3) ? kf[i + 4] : el(kk[4 * h + i]); tie(kf[i]); }
                }
                if constexpr (EXACT) {
                    if constexpr (j == 1) { const float t = tap(3); all([&](int i) { pa[i] = kf[i] * t; }); all([&](int i) { pa[i] = rbf(pa[i]); tie(pa[i]); }); }
                    if constexpr (j == 2 || j == 4 || j == 6) { const float t = tap(3 - j / 2); all([&](int i) { pb[i] = kf[i + j / 2] * t; }); all([&](int i) { pb[i] = rbf(pb[i]); tie(pb[i]); }); }
                    if constexpr (j == 3 || j == 5 || j == 7) { all([&](int i) { pa[i] += pb[i]; }); all([&](int i) { pa[i] = rbf(pa[i]); tie(pa[i]); }); }
                } else {
                    if constexpr (j == 1) { const float t = tap(3); all([&](int i) { pa[i] = kf[i] * t; tie(pa[i]); }); }
                    if constexpr (j >= 2 && j <= 4) { const float t = tap(4 - j); all([&](int i) { pa[i] = fma_(kf[i + j - 1], t, pa[i]); tie(pa[i]); }); }
                    if constexpr (j == 5) all([&](int i) { pa[i] = rbf(pa[i]); tie(pa[i]); });
                }
                if constexpr (j == (EXACT ? 7 : 6)) all([&](int i) { pb[i] = el(vv[4 * h + i]); tie(pb[i]); });
                if constexpr (j == 7) {
                    all([&](int i) { pa[i] *= pb[i]; });
                    kvo[c][3 - 2 * h] = pack2(pa[1], pa[0]); kvo[c][2 - 2 * h] = pack2(pa[3], pa[2]);
                    auto st = [&](int c) { __builtin_amdgcn_raw_buffer_store_b128(kvo[c], rk, (l15 >> 2) * 65536 + g4 * 64 + (l15 & 3) * 16, c * 16384 + sb * 256, 16); };
                    if constexpr (h && !KST) st(c);
                    else if constexpr (u == 7) all(st);
                    else tie(kvo[c]);
                }
            };
            static_for<-1, 13>([&](auto kc) {
                constexpr int k = CV(kc);
                if constexpr (k == 3) page<1>();
                if constexpr (k == 11) page<12>();
                if constexpr (k == 2) hop<0>();
                if constexpr (k == 6) hop<6>();
                if constexpr (k == 8) hop<9>();
                if constexpr (k >= 0) barrier();
                if constexpr (k >= 0 && ((CTM >> k) & 1)) ctouch(k);
                if constexpr (k == KV0) {
                    wait_v<4 + hv_cnt(-1, k)>();
                    static_for<0, 11>([&](auto ic) { tie(kk[CV(ic)]); });
                    static_for<0, 8>([&](auto ic) { tie(vv[CV(ic)]); });
                    tie(w4[0]); tie(w4[1]); keep(c_dt); keep(c_re); keep(c_im);
                    TS(8);
                }
                if constexpr (k == KFL) {
                    wait_l<0>();
                    ties(tag); ties(dw);
                    tag += 0x01010101u;
                    wait_v<hv_cnt(KVL + 1, k)>();
                    __builtin_amdgcn_raw_buffer_store_b64(u32x2{tag, ~tag}, rf, lane ? OOB : 0u, F_K + ((xcd * 2 + kh) * 64 + sb) * 8, 16);
                    TS(10);
                }
                if constexpr (k == KPL) dma1<POLL>(lbase + wid * 256, (lane & 31) * 4, rsf, kfo);
                if constexpr (k == 12) {
                    dma1<0>(K, oob, rs0, 0);
                    dma1<KCP>(K, oob, rs0, 0);
                }
                static_for<0, 8>([&](auto ic) {
                    constexpr int i = CV(ic), u0 = kv_first(k), nu = kv_first(k + 1) - u0;
                    feed(kc, ic);
                    if constexpr (k == 4 && i == 4) hop<3>();
                    if constexpr (nu == 1) kv_step(IC(u0), ic);
                    if constexpr (nu == 2) { kv_step(IC(u0 + i / 4), IC(2 * (i & 3))); kv_step(IC(u0 + i / 4), IC(2 * (i & 3) + 1)); }
                    if constexpr (k == 12 && i >= 4) gld<(i - 4) * 1024>(kd[i - 4], lane * 16, mk_rsrc(ssmk, 4 << 20), ch * 4096);
                    if constexpr (k == 11 && i == 4) c_setup();
                    sched();
                });
            });
            TS(1);
            unsigned pf;
            const unsigned fr = lbase + FULL + (lane & 3) * FST;
            wait_v<hv_cnt(KPL, 12) - 1 - (KPL == KFL) + 11>();
            dsr32<POLL - 131072>(pf, lbase + 131072 + wid * 256 + lane * 4);
            wait_l<0>();
            tag_chk(pf, (lane & 1) ? ~tag : tag, (lane & 31) * 4, rsf, kfo);
            if constexpr (EC && EB == 1) barrier();
            {
                const u32x4 rs = mk_rsrc(ws + WS_KVT + (size_t)(xcd * 32 + idx) * 16384, 16384);
                static_for<0, 4>([&](auto ic) { constexpr int i = CV(ic); dma<U0 + 2048 + i * 1024, true>(lbase + lw * 4096, (lane >> 2) * 64 + (((lane & 3) ^ g4) << 4), rs, lw * 4096 + i * 1024); });
            }
            mark(IC(16));
            if constexpr (EC) {
                if constexpr (EB == 0) barrier();
                static_for<0, 4>([&](auto ic) { constexpr int i = CV(ic); dma<i * 1024, false>(cst, vc[i], rsc, c_off(lw, 0)); });
                if constexpr (EC == 1) static_for<0, 4>([&](auto ic) { constexpr int i = CV(ic); dma<4096 + i * 1024, false>(cst, vc[i], mk_rsrc(cre + (size_t)dt * 131072, 262144), c_off(lw, 0)); });
            }
            wait_v<5 + (EC == 1 ? 8 : EC ? 4 : 0)>();
            static_for<0, 4>([&](auto ic) { tie(kd[CV(ic)]); });
#pragma unroll
            for (int n = 0; n < 4; ++n) {
                const unsigned first = n ? __builtin_amdgcn_readlane(kd[n ? n - 1 : 0][3], 63) : 0u;
                const unsigned p = __builtin_amdgcn_update_dpp(first, kd[n][3], 0x138, 0xf, 0xf, false);
                ke[n] = u32x4{__builtin_amdgcn_alignbyte(kd[n][0], p, 2), __builtin_amdgcn_alignbyte(kd[n][1], kd[n][0], 2),
                              __builtin_amdgcn_alignbyte(kd[n][2], kd[n][1], 2), __builtin_amdgcn_alignbyte(kd[n][3], kd[n][2], 2)};
                tie(ke[n]);
            }
            if constexpr (ZM) {
                const unsigned dl = (ch & 1) ? dw >> 16 : dw & 0xffffu, dv = dl << ((~lane & 1) * 16);
                static_for<0, 4>([&](auto ic) { constexpr unsigned long long k = lag0_mask(CV(ic)); ad[CV(ic)] = sel0(dv, k); });
                tie(ad);
            }
            hop<14>();
            pf = 1;
            zero_chk<15 * 4>(pf, fr);
            TS(9);
            static_for<0, 4>([&](auto ic) { constexpr int i = CV(ic); dsw128<64 + i * 1024>(K + lane * 16, kd[i]); dsw128<KCP + 64 + i * 1024>(K + lane * 16, ke[i]); });
            wait_l<0>();
            __builtin_amdgcn_s_setprio(0);
        }
        TS(2);
        unsigned ka[4], cad[4], ub[4];
#pragma unroll
        for (int i = 0; i < 4; ++i) {
            ka[i] = lbase + KC + lw * KCH + ((l15 & 1) ? 0 : KCP) + 84 + 2 * (l15 & 14) - 16 * g4 + i * 1024;
            cad[i] = cst + frag(i & 1, i >> 1);
            const int e = 2 * l15 + g4 + 2;
            ub[i] = lbase + U0 + 64 * e + ((lw ^ (((e >> 2) + 3 - i) & 3)) << 4);
        }
        f32x4 acc[8];
        bf16x4 FA[8][2]; bf16x8 FB[8];
        auto op = [&](auto gc) {
            constexpr Op o = cv_op(CV(gc));
            if constexpr (o.kind == CO_A) { constexpr int s = o.a >> 3, n = o.a & 7, d = (n & 1) * 128 + s * 16 + o.b * 2; dsr2<d, d + 1>(FA[n][o.b], ka[n >> 1]); }
            if constexpr (o.kind == CO_B) { constexpr int s = o.a >> 3, m = o.a & 7; dsr128<2048 * m + 1792 - 256 * s>(FB[m], ub[s & 3]); }
            if constexpr (o.kind == CO_C) dsr128<(o.a & 1) * 4096>(cf[o.a * 4 + o.b], cad[o.b]);
        };
        static_for<-36, 0>(op);
        static_for<0, 288>([&](auto gc) {
            constexpr int G = CV(gc), s = G / 36, q = G % 36, n = cv_n(q), m = cv_m(q);
            if constexpr (cv_wait(G) >= 0) wait_lr<cv_wait(G)>();
            if constexpr (q == 24 && s >= CL) static_for<0, 4>([&](auto ic) { tie(cf[(s - CL) * 4 + CV(ic)]); });
            if constexpr (ZM && G < 8) mfma<true>(acc[m], __builtin_bit_cast(bf16x8, ad), FB[m]);
            mfma<s == 0 && n == 0 && !ZM>(acc[n + m], cat(FA[n][0], FA[n][1]), FB[m]);
            sched();
            if constexpr (q == 11 && s >= CL) wait_v<4>();
            op(gc);
            if constexpr (q == 30 && ((CTM >> (16 + s)) & 1)) ctouch(16 + s);
            if constexpr (G == 16) hop<17>();
            if constexpr (G == 66) hop<19>();
            if constexpr (G == 107) page<21>();
            if constexpr (G == 158) hop<23>();
            if constexpr (G == 209) hop<25>();
            if constexpr (G == 254) hop<27>();
            if constexpr (G == CBQ) barrier();
            if constexpr ((q == 24 || q == 25 || q == 29 || q == 34) && (s + 2 - CL || !EC) && s + 2 - CL < 8) {
                constexpr int pc = q == 24 ? 0 : q == 25 ? 1 : q == 29 ? 2 : 3, u = s + 2 - CL;
                dma<(u & 1) * 4096 + pc * 1024, false>(cst, vc[pc], rsc, c_off(lw, u));
            }
            sched();
        });
        page<28>();
        TS(3);
        {
            const __amdgpu_buffer_rsrc_t rz = mk_brs(ws + WS_ZT, 4 << 20);
            if constexpr (ZM) {
                h_setup(1);
                sched();
#pragma unroll
                for (int jj = 0; jj < 4; ++jj) {
                    tie(acc[2 * jj]); tie(acc[2 * jj + 1]);
                    const f32x4 a = acc[2 * jj], c = acc[2 * jj + 1];
                    __builtin_amdgcn_raw_buffer_store_b128(u32x4{pack2(a[0], a[1]), pack2(a[2], a[3]), pack2(c[0], c[1]), pack2(c[2], c[3])}, rz, lane * 16 + jj * 1024, ch * 4096, 16);
                }
            } else {
            u32x2 kvr[8];
            const unsigned kva = lbase + U0 + 2048 + 128 * l15 + 64 * (g4 >> 1) + ((lw ^ ((l15 >> 1) & 3)) << 4) + 8 * (1 - (g4 & 1));
            static_for<0, 8>([&](auto ic) { dsr64<CV(ic) * 2048>(kvr[CV(ic)], kva); });
            wait_l<0>();
            const float Dd = (ch & 1) ? bfhi(dw) : bflo(dw);
#pragma unroll
            for (int jj = 0; jj < 4; ++jj) {
                u32x4 o;
#pragma unroll
                for (int u = 0; u < 2; ++u) {
                    const int j = 2 * jj + u;
                    tie(kvr[j]); tie(acc[j]);
                    const float kv3 = bflo(kvr[j][0]), kv2 = bfhi(kvr[j][0]), kv1 = bflo(kvr[j][1]), kv0 = bfhi(kvr[j][1]);
                    o[2 * u] = pack2(rbf(acc[j][0]) + rbf(kv0 * Dd), rbf(acc[j][1]) + rbf(kv1 * Dd));
                    o[2 * u + 1] = pack2(rbf(acc[j][2]) + rbf(kv2 * Dd), rbf(acc[j][3]) + rbf(kv3 * Dd));
                }
                __builtin_amdgcn_raw_buffer_store_b128(o, rz, lane * 16 + jj * 1024, ch * 4096, 16);
                if (jj == 1) { sched(); hop<29>(); sched(); }
            }
            h_setup(1);
            }
            TS(8);
            hop<30>();
            wait_v<4>();
            static_for<32 - 4 * CL, 32>([&](auto ic) { constexpr int i = CV(ic); dsr128<((i >> 2) & 1) * 4096>(cf[i], cad[i & 3]); });
            wait_l<0>();
            static_for<32 - 4 * CL, 32>([&](auto ic) { tie(cf[CV(ic)]); });
        }
        TS(4);
    } else {
        f32x4 acc[8][2];
        cf32 L, cc, M, M2, MG, NG, N4, N8, ag;
        {
            unsigned aad[4], bad[4];
#pragma unroll
            for (int i = 0; i < 4; ++i) { aad[i] = lbase + frag(i & 1, i >> 1); bad[i] = aad[i] + 16384 + wid * 4096; }
            const unsigned fr = lbase + FULL + (lane & 3) * FST;
            sld(tag, ws + WS_F + F_EP);
            bf16x8 fa[4][2], fb[2][4];
            unsigned fl = 1;
            auto op = [&](auto gc) {
                constexpr int G = CV(gc);
                constexpr Op o = g1_op(G);
                if constexpr (o.kind == OP_A) {
                    constexpr int bo = g1_buf(o.a >> 3), hi = bo >= 65536 ? 65536 : 0, i = o.a & 7;
                    dsr128<bo - hi + (i >> 1) * 4096>(fa[i & 3][o.b], aad[(i & 1) + 2 * o.b] + hi);
                }
                if constexpr (o.kind == OP_B) { constexpr int bo = g1_buf(o.a), hi = bo >= 65536 ? 65536 : 0; dsr128<bo - hi>(fb[o.a & 1][o.b], bad[o.b] + hi); }
                if constexpr (o.kind == OP_FLAG) dsr32<(30 - (G >> 5)) * 4>(fl, fr);
            };
            auto consts = [&]() {
                float ld, ar, ai;
                asm volatile("s_waitcnt vmcnt(9)\n\tv_lshlrev_b32 %0, 16, %3\n\tv_lshlrev_b32 %1, 16, %4\n\tv_lshlrev_b32 %2, 16, %5"
                             : "=&v"(ld), "=&v"(ar), "=&v"(ai) : "v"(c_dt), "v"(c_re), "v"(c_im) : "memory");
                const float dtv = rbf(expf(ld)), ea = expf(dtv * ar);
                float sn, cs;
                sincosf(dtv * ai, &sn, &cs);
                L = cf32{ea * cs, ea * sn};
                M = L;
#pragma unroll
                for (int e = 0; e < 5; ++e) M = cmul(M, M);
                M2 = cmul(M, M);
                const cf32 N = cmul(M2, M2), N2 = cmul(N, N), one = {1.f, 0.f};
                N4 = cmul(N2, N2); N8 = cmul(N4, N4);
                auto sel = [](bool c, cf32 a, cf32 b) { return cf32{c ? a.r : b.r, c ? a.i : b.i}; };
                MG = cmul(sel(g4 & 1, M, one), sel(g4 & 2, M2, one));
                NG = cmul(sel(g4 & 1, N, one), sel(g4 & 2, N2, one));
                const float nr = L.r - 1.f, den = ar * ar + ai * ai;
                cc = cf32{2.f * ((nr * ar + L.i * ai) / den), 2.f * ((L.i * ar - nr * ai) / den)};
                auto tc = [&](cf32& v) { tie(v.r); tie(v.i); };
                tc(L); tc(cc); tc(M); tc(M2); tc(MG); tc(NG); tc(N4); tc(N8);
                sched();
            };
            if constexpr (CQ < 0) consts();
            __builtin_amdgcn_s_setprio(CP);
            zero_chk<0>(fl, lbase + POLL + (lane & 3) * 256);
            ties(tag);
            tag += 0x01010101u;
            TS(1);
            static_for<-12, 0>(op);
            static_for<0, 512>([&](auto gc) {
                constexpr int G = CV(gc), st = G >> 5, i = (G >> 2) & 7, j = G & 3;
                if constexpr ((G == 0 || j == 2) && G <= 506) { static_assert(g1_wait(G) >= 0, ""); wait_lr<G == 506 ? 0 : g1_wait(G)>(); }
                if constexpr (G == CQ) { asm volatile("s_waitcnt lgkmcnt(0)\n\ts_nop 7" ::: "memory"); consts(); }
                if constexpr (G == HQ) hop_all(wid);
                if constexpr ((G & 31) == 20 && st < 15) zero_chk<(30 - st) * 4>(fl, fr);
                if constexpr ((G & 31) == 30 && st < 13 + (EC != 0)) barrier();
                if constexpr ((G & 31) == 20) TL(st);
                mfma<st == 0 && j < 2>(acc[i][j & 1], fa[i & 3][j >> 1], fb[st & 1][j]);
                sched();
                op(gc);
                if constexpr (G == 31) page<-1>();
                if constexpr (G == 95) hop<2>();
                if constexpr (G == 159) hop<4>();
                if constexpr (G == 207) hop<5>();
                if constexpr (G == 255) page<7>();
                if constexpr (G == 319) hop<8>();
                if constexpr (G == 383) hop<10>();
                if constexpr (G == 431) hop<11>();
                if constexpr (G == 479) page<13>();
                if constexpr (G == 509) hop<15>();
                if constexpr (G >= 487) {
                    constexpr int k = G - 486;
                    if constexpr (k == 1) { asm volatile("s_nop 3"); ag = cf32{acc[0][0][0], acc[0][1][0]}; }
                    cf32 t;
                    asm volatile("v_fma_f32 %0, -%3, %5, %6\n\tv_fma_f32 %1, %3, %4, %7\n\tv_fmac_f32 %0, %2, %4\n\tv_fmac_f32 %1, %2, %5"
                                 : "=&v"(t.r), "=&v"(t.i) : "v"(L.r), "v"(L.i), "v"(ag.r), "v"(ag.i), "v"(acc[k >> 2][0][k & 3]), "v"(acc[k >> 2][1][k & 3]));
                    ag = t;
                }
                sched();
            });
        }
        __builtin_amdgcn_s_setprio(3);
        TS(2);
#pragma unroll
        for (int k = 26; k < 32; ++k) ag = cfma(L, ag, cf32{acc[k >> 2][0][k & 3], acc[k >> 2][1][k & 3]});
        cf32 ev_, od, p01, p23;
        sw16(ag.r, ev_.r, od.r); sw16(ag.i, ev_.i, od.i);
        const cf32 pp = cfma(M, ev_, od);
        sw32(pp.r, p01.r, p23.r); sw32(pp.i, p01.i, p23.i);
        cf32 E = cfma(M2, p01, p23);

        const __amdgpu_buffer_rsrc_t rse = mk_brs(ws + WS_E + (size_t)(tn * 4 + wid) * 4096, 4096);
        __builtin_amdgcn_raw_buffer_store_b128(u32x4{f2u(E.r), f2u(E.i), tag, ~tag}, rse, g4 ? OOB : l15 * 16, tm * 256, 16);
        sched();
        TS(3);
        c_setup();
        unsigned cad[4];
#pragma unroll
        for (int i = 0; i < 4; ++i) cad[i] = lb + frag(i & 1, i >> 1);
        auto c_read = [&](auto rc) { static_for<0, 8>([&](auto ic) { constexpr int r = CV(rc), i = CV(ic); dsr128<(i >> 2) * 4096>(cf[r * 8 + i], cad[i & 3] + (r == 0 || r == 3 ? 2 * BUF : 0)); }); };
        auto c_tie = [&](auto rc) { static_for<0, 8>([&](auto ic) { tie(cf[CV(rc) * 8 + CV(ic)]); }); };
        auto c_unit = [&](auto uc) { static_for<0, 4>([&](auto ic) { constexpr int u = CV(uc), i = CV(ic); dma<(u < 2 || u >= 6 ? 2 * BUF : 0) + (u & 1) * 4096 + i * 1024, false>(lb, vc[i], rsc, c_off(wid, u)); }); };
        auto c_req = [&](auto rc) { c_unit(IC(CV(rc) * 2)); c_unit(IC(CV(rc) * 2 + 1)); };
        const float c2r = cc.r, c2i = cc.i;
        auto scale = [&](int i) {
#pragma unroll
            for (int j = 0; j < 4; ++j) {
                const float br = acc[i][0][j], bi = acc[i][1][j];
                acc[i][0][j] = fma_(c2r, br, -(c2i * bi)); acc[i][1][j] = fma_(-c2r, bi, -(c2i * br));
            }
            tie(acc[i][0]); tie(acc[i][1]);
        };
        scale(0); scale(1); scale(2); scale(3);
        sched(); hop<16>(); sched();
        if constexpr (ED) { scale(4); scale(5); scale(6); scale(7); }
        if constexpr (ED == 2) barrier();
        u32x4 ev[4]; unsigned eo[4]; unsigned long long none[4];
#pragma unroll
        for (int i = 0; i < 4; ++i) {
            const int t = tm - 1 - 4 * i - g4;
            eo[i] = t < 0 ? OOB : t * 256 + l15 * 16; none[i] = __builtin_amdgcn_ballot_w64(t < 0);
            asm volatile("buffer_load_dwordx4 %0, %1, %2, 0 offen sc1" : "=v"(ev[i]) : "v"(eo[i]), "s"(mk_rsrc(ws + WS_E + (size_t)(tn * 4 + wid) * 4096, 4096)) : "memory");
        }
        asm volatile("" ::: "memory");
        sched();
        if constexpr (ED != 2) barrier();
        if constexpr (R01 == 0) { c_req(IC(0)); c_req(IC(1)); }
        if constexpr (R01 >= 2) c_unit(IC(0));
        if constexpr (!ED) { scale(4); scale(5); scale(6); scale(7); }
        cf32 X = cfma(cf32{g4 == 3 ? M.r : g4 == 2 ? 1.f : 0.f, g4 == 3 ? M.i : 0.f}, p01, cf32{(g4 & 1) ? ev_.r : 0.f, (g4 & 1) ? ev_.i : 0.f});
        tie(X.r); tie(X.i);
        sched();
        hop<24>();
        wait_v<R01 == 0 ? 16 : R01 >= 2 ? 4 : 0>();
        tie(ev[0]); tie(ev[1]); tie(ev[2]); tie(ev[3]);
        {
            const unsigned long long tag64 = (unsigned long long)tag | ((unsigned long long)~tag << 32);
#pragma unroll 1
            for (unsigned spins = 0; spins < SPIN_CAP; ++spins) {
                unsigned long long ok = ~0ull;
#pragma unroll
                for (int i = 0; i < 4; ++i) ok &= __builtin_amdgcn_ballot_w64(((unsigned long long)ev[i][2] | ((unsigned long long)ev[i][3] << 32)) == tag64) | none[i];
                TSV(11, spins);
                if (ok == ~0ull) break;
                asm volatile("buffer_load_dwordx4 %0, %4, %8, 0 offen sc1\n\tbuffer_load_dwordx4 %1, %5, %8, 0 offen sc1\n\tbuffer_load_dwordx4 %2, %6, %8, 0 offen sc1\n\t"
                             "buffer_load_dwordx4 %3, %7, %8, 0 offen sc1\n\ts_waitcnt vmcnt(0)" : "=&v"(ev[0]), "=&v"(ev[1]), "=&v"(ev[2]), "=&v"(ev[3])
                             : "v"(eo[0]), "v"(eo[1]), "v"(eo[2]), "v"(eo[3]), "s"(mk_rsrc(ws + WS_E + (size_t)(tn * 4 + wid) * 4096, 4096)) : "memory");
            }
        }
        page<18>();
        cf32 h;
        {
            auto e = [&](int i) { return cf32{u2f(ev[i][0]), u2f(ev[i][1])}; };
            cf32 c = cmul(NG, cfma(N8, cfma(N4, e(3), e(2)), cfma(N4, e(1), e(0)))), u, v;
            sw16(c.r, u.r, v.r); sw16(c.i, u.i, v.i);
            sw32(u.r + v.r, u.r, v.r); sw32(u.i + v.i, u.i, v.i);
            c = cfma(MG, cf32{u.r + v.r, u.i + v.i}, X);
            h = cf32{fma_(c2r, c.r, -(c2i * c.i)), fma_(-c2r, c.i, -(c2i * c.r))};
        }
        TS(4);
        {
            const __amdgpu_buffer_rsrc_t rh = mk_brs(ws + WS_H + (size_t)(tm * 16 + tn) * 32768 + wid * 2048, 32768);
            const unsigned ho = g4 * 256 + ((l15 ^ (4 * (g4 & 1))) << 4);
            const cf32 Lc = {L.r, -L.i};
            static_for<0, 4>([&](auto qc) {
                constexpr int q = CV(qc);
                if constexpr (R01 == 1) c_unit(qc);
                if constexpr (R01 == 2 && q < 3) c_unit(IC(q + 1));
                if constexpr (R01 == 3 && q > 0) c_unit(qc);
                if constexpr (q == 2) hop<20>();
                u32x4 wr, wi;
#pragma unroll
                for (int u = 0; u < 4; ++u) {
                    const int i = 2 * q + (u >> 1), j = 2 * (u & 1);
                    const cf32 h0 = cfma(Lc, h, cf32{acc[i][0][j], acc[i][1][j]});
                    h = cfma(Lc, h0, cf32{acc[i][0][j + 1], acc[i][1][j + 1]});
                    wr[u] = pack2(h0.r, h.r); wi[u] = pack2(h0.i, h.i);
                }
                __builtin_amdgcn_raw_buffer_store_b128(wr, rh, ho, q * 8192, 16);
                __builtin_amdgcn_raw_buffer_store_b128(wi, rh, ho + 1024, q * 8192, 16);
                sched();
            });
        }
        if constexpr (R01 == 4) { c_unit(IC(1)); c_unit(IC(2)); c_unit(IC(3)); wait_v<12>(); } else wait_v<0>();
        __builtin_amdgcn_raw_buffer_store_b64(u32x2{tag, ~tag}, rf, lane ? OOB : 0u, F_H + ((tm * 16 + tn) * 4 + wid) * 8, 16);
        TS(5);
        hop<22>();
        if constexpr (R01 == 4) wait_v<1>();
        c_read(IC(1)); c_read(IC(0));
        wait_l<8>();
        c_req(IC(2));
        wait_l<0>();
        c_req(IC(3));
        u32x2 ht;
        const unsigned hto = (lane & 31) * 8, hts = F_H + (tm2 * 16 + hb * 8) * 32;
        if constexpr (EP) asm volatile("buffer_load_dwordx2 %0, %1, %2, %3 offen sc1" : "=v"(ht) : "v"(hto), "s"(rsf), "s"(hts) : "memory");
        c_tie(IC(0)); c_tie(IC(1));
        wait_v<0>();
        if constexpr (EP) tie(ht);
        c_read(IC(2)); c_read(IC(3));
        wait_l<0>();
        c_tie(IC(2)); c_tie(IC(3));
        {
            const unsigned long long tag64 = (unsigned long long)tag | ((unsigned long long)~tag << 32);
            const bool early = EP && wall(((unsigned long long)ht[0] | ((unsigned long long)ht[1] << 32)) == tag64);
            if (wid == 3) {
                gu64* hf = (gu64*)(ws + WS_F + F_H) + (tm2 * 16 + hb * 8) * 4 + (lane & 31);
                if (!early) {
#pragma unroll 1
                    for (unsigned spins = 0; spins < SPIN_CAP; ++spins) {
                        if (wall(__hip_atomic_load(hf, RLX_AGENT) == tag64)) break;
                        __builtin_amdgcn_s_sleep(1);
                    }
                }
                flag_set(lbase + HGO + lane * 4, 1u);
            } else if (!early) gate<1>(lbase + HGO, 1);
        }
        TS(6);
        hop<26>();
        static_for<0, 16>([&](auto ic) { constexpr int i = CV(ic); dma<(i >> 3) * 2 * BUF + (i & 7) * 1024, true>(lb, lane * 16, rsh, ((rot2 + wid + (i >> 3) * 4) & 7) * 32768 + qx * 8192 + (i & 7) * 1024); });
        h_setup(0);
        wait_v<8>();
        __builtin_amdgcn_s_setprio(0);
    }
    {
        const int wid = fresh(wid_), lw = wid & 3, kg = wid >> 2, b = fresh(b_), xcd = b & 7, idx = b >> 3, tq = (idx & 7) + JR(idx >> 3);
        const int dt = G2M ? idx & 7 : (xcd & 4) + (idx >> 3), qx = ((~b & 1) * 2) ^ JQ(idx >> 3);
        const unsigned wl = lbase + wid * 4096;
        tag = fresh(__builtin_amdgcn_readfirstlane(tag));
        unsigned l16 = lane * 16;
        unsigned hs0, hs1, fso;
        auto fill = [&](auto sc, auto pc) {
            constexpr int s = CV(sc), p = CV(pc);
            if constexpr (p == 0) dma_x<(2 * s % 3) * BUF, (s >> 1) * 8192>(fso, wl, l16, rsh, (s & 1) ? hs1 : hs0);
            else if constexpr (fill_m0(4 * s + p)) dma_m<(2 * s % 3) * BUF, p * 1024>(fso, wl, l16, rsh);
            else dma_n<p * 1024>(fso, l16, rsh);
        };
        bf16x4 hf[2][8];
        auto ldh = [&](auto nc, auto ic) {
            constexpr int n = CV(nc), i = CV(ic), j = (i >> 1) & 1, bo = (2 * (n >> 2) % 3) * BUF + (g2_unit(n, j) & 3) * 8192 + (i >> 2) * 4096, hi = bo >= 65536 ? 65536 : 0;
            dsr64tr<bo - hi>(hf[n & 1][i], ha[2 * g2_tile(n, j) + (i & 1)] + hi);
        };
        f32x4 yc[2][2][2];
        f32x4 im[2], ao, ob[2]; u32x4 pl[2], po, dg[2];
        bf16x8 xf[2];
        u32x2 zv[4], qv[2], tg2;
        unsigned pf, dsw_, dlo, expect, msk, ck, ex, a_q, a_x, a_iw, a_ir, a_p, a_z, a_z2, a_xr, a_w, a_zr, a_qr, a_oa, a_ob, a_f, u0, u1, u2;
        const u32x4 rsz = mk_rsrc(ws + WS_F, wid == 1 ? 65536 : 0);
        float tq_[2], tz, sm[8], mx[2], cv[8];
        const __amdgpu_buffer_rsrc_t ro = mk_brs(out, 16 << 20);
        const __amdgpu_buffer_rsrc_t rp = mk_brs(ws + WS_PP, 8 << 20);
        const u32x4 rsp = mk_rsrc(ws + WS_PP, 8 << 20);
        unsigned w2k, zfo, ta, tb, ppo;
        constexpr unsigned long long LO8 = 0x00ff00ff00ff00ffull;
        auto finB = [&](auto pc, auto jc) {
            constexpr int p = CV(pc), j = CV(jc);
            if constexpr (j < 8) { yc[p & 1][0][j & 1][j >> 1] += im[j & 1][j >> 1]; tie(yc[p & 1][0][j & 1]); }
            else {
                constexpr int i = (j - 8) >> 2, k = j & 3, dd = k & 1, h = k >> 1;
                ob[i][k] = sel8(yc[p & 1][0][dd][2 * i + 1 - h], yc[p & 1][0][dd][2 * i + h], h ? ~LO8 : LO8);
                if constexpr (k == 3 && (p == 3 || !(NOST & 4))) __builtin_amdgcn_raw_buffer_store_b128(__builtin_bit_cast(u32x4, ob[i]), ro, a_ob, (tb + (p ^ qx) * 8 + 2 * i) * 8192 + 4096 + dt * 512 + lw * 128, OSC);
                if constexpr (j == 12) keep(ob[0]);
            }
        };
        auto snd = [&](auto pc, auto jc) {
            constexpr int p = CV(pc), j = CV(jc);
            if constexpr (j < 8) { sm[j] = yc[p][0][j >> 2][j & 3] + im[j >> 2][j & 3]; tie(sm[j]); }
            if constexpr (j == 8) mx[0] = amax3(sm[0], sm[1], sm[2]);
            if constexpr (j == 9) mx[1] = amax3(sm[3], sm[4], sm[5]);
            if constexpr (j == 10) mx[0] = amax3(mx[0], sm[6], sm[7]);
            if constexpr (j == 11) mx[0] = amax3(mx[0], mx[1], mx[1]);
            if constexpr (j == 12) { ex = f2u(mx[0]) + 0x4000u; tie(ex); }
            if constexpr (j == 13) { ex &= 0x7f800000u; tie(ex); }
            if constexpr (j == 14) { mx[0] = u2f(ex + 0x04c00008u); tie(mx[0]); }
            if constexpr (j == 15) { ex >>= 23; tie(ex); }
            if constexpr (j >= 16 && j < 24) { sm[j - 16] += mx[0]; tie(sm[j - 16]); }
            if constexpr (j == 24) { ex |= ex << 12; tie(ex); }
            if constexpr (j >= 25 && j < 29) { po[j - 25] = __builtin_amdgcn_perm(f2u(sm[2 * (j - 25) + 1]), f2u(sm[2 * (j - 25)]), 0x05040100u); tie(po); }
            if constexpr (j == 29) { po[0] = bfi_v(msk, po[0], ex); tie(po); }
            if constexpr (j >= 30 && j < NS) { po[j - 29] = bfi_s(msk, po[j - 29], tag >> (j == 30 ? 0 : j == 31 ? 8 : 4)); if constexpr (j < NS - 1) tie(po); }
            if constexpr (j == NS - 1 && !(NOST & 2)) __builtin_amdgcn_raw_buffer_store_b128(po, rp, l16, ppo + p * 8192, 16);
        };
        auto rcv = [&](auto hc, auto jc) {
            constexpr int h = CV(hc), j = CV(jc);
            if constexpr (j < 3) { pl[h][j + 1] ^= (tag >> (j == 0 ? 0 : j == 1 ? 8 : 4)) & 0xf000fu; pp_tie<h>(pl[h]); }
            if constexpr (j == 3) { ck = pl[h][1] | pl[h][2] | pl[h][3]; tie(ck); }
            if constexpr (j == 4) { ck &= 0xf000fu; tie(ck); }
            if constexpr (j == 6 && !(NOST & 2)) pp_chk<h>(pl[h], ck, tag, l16, rsp, ppo ^ (16384 + h * 8192));
            if constexpr (j == 7) { ex = pl[h][0] >> 12; tie(ex); }
            if constexpr (j == 8) ex = bfi_v(msk, ex, pl[h][0]);
            if constexpr (j == 9) { pl[h][0] &= msk; pp_tie<h>(pl[h]); }
            if constexpr (j == 10) { ex <<= 23; tie(ex); }
            if constexpr (j == 11) { ex &= 0x7f800000u; tie(ex); }
            if constexpr (j == 12) { mx[1] = u2f(ex) * 0x1p-14f; tie(mx[1]); }
            if constexpr (h == 0 && j >= 13) {
                constexpr int k = j - 13, a = (k + 1) / 2, e = k == 15 ? 7 : k / 2 - 1;
                if constexpr (k == 0 || ((k & 1) && k < 15)) cv[a] = cvt16<a & 1>(pl[h][a >> 1]);
                else { yc[0][0][e >> 2][e & 3] = fma_(cv[e], mx[1], yc[0][0][e >> 2][e & 3]); tie(yc[0][0][e >> 2]); }
            }
            if constexpr (h == 1 && j >= 13 && j < 21) cv[j - 13] = cvt16<(j - 13) & 1>(pl[h][(j - 13) >> 1]);
            if constexpr (h == 1 && j >= 21) { constexpr int e = j - 21; im[e >> 2][e & 3] = fma_(cv[e], mx[1], im[e >> 2][e & 3]); tie(im[e >> 2]); }
        };
        auto side = [&](auto pc) {
            constexpr int P = CV(pc), n = P >> 3, m = P & 7, p = n >> 3;
#define V(pos, var, expr) if constexpr (P == (pos)) { var = (expr); tie(var); }
            if constexpr (P == AT(0, 4)) { hs0 = ((tq + (wid >> 1)) & 7) * 32768 + (wid & 1) * 4096 + qx * 8192; hs1 = hs0 ^ 131072; ties(hs0); ties(hs1); }
            if constexpr (P == AT(0, 6)) { w2k = lbase + wid * 2048; ta = 512 * (idx >> 3) + 128 * (idx & 1) + 16 * wid + 4 * ((idx >> 1) & 3); ties(w2k); ties(ta); }
            if constexpr (P == AT(1, 4)) { tb = (G2M ? (xcd >> 1) * 4 + (idx >> 3) : (xcd & 2) * 4 + (idx & 7)) * 128 + 64 * kg; ties(tb); }
            if constexpr (P == AT(6, 6)) { zfo = F_Z + xcd * 4096; ties(zfo); }
            if constexpr (P == AT(7, 5)) { ppo = b * 16384 + wid * 1024; ties(ppo); }
            V(AT(0, 0), a_x, (lane >> 2) & 7)        V(AT(0, 1), u0, l16 & 48)               V(AT(0, 2), a_x, (lane & 32) | a_x)
            V(AT(0, 3), u1, l16 & 240)               V(AT(0, 4), a_x, (a_x << 11) | u0)      V(AT(0, 6), u2, lane & 48)
            V(AT(1, 0), a_w, (lane >> 1) & 4)        V(AT(1, 1), a_q, (u2 << 7) | u1)        V(AT(1, 2), u0, lane & 7)
            V(AT(1, 3), a_xr, (u1 << 2) | u2)        V(AT(1, 4), a_w, (u0 << 3) | a_w)       V(AT(1, 6), a_xr, a_xr + (w2k + ZST))
            V(AT(8, 6), msk, 0xfff0fff0u)
            V(AT(2, 0), a_w, a_w + (lbase + DSK + lw * 64))
            if constexpr (P == AT(2, 1)) { tg2[0] = tag; tie(tg2); }
            V(AT(2, 2), a_p, lane & 31)
            if constexpr (P == AT(2, 3)) { tg2[1] = ~tag; tie(tg2); }
            V(AT(2, 4), a_p, a_p << 2)               V(AT(2, 6), u1, (int)(lane << 31) >> 31)
            V(AT(3, 0), a_iw, w2k + IMG + l16)       V(AT(3, 1), expect, (u1 ^ tag) & (wid == 1 ? ~0u : 0u)) V(AT(4, 6), a_f, l16 << 2) V(AT(5, 4), a_f, (a_f & 0xf80) | (a_p & 4))           V(AT(3, 2), a_ir, a_iw ^ 8192)
            V(AT(3, 3), u2, lane >> 5)
            V(AT(5, 6), a_z, lane ^ u2 ^ (wid * 4))  V(AT(6, 0), u1, lane >> 3)              V(AT(6, 1), a_z, a_z & 7)
            V(AT(6, 2), a_z2, u1 << 12)              V(AT(6, 3), a_zr, (wid ^ lane) & 7)     V(AT(6, 4), a_z, (a_z << 4) | a_z2)
            V(AT(6, 5), a_zr, (a_zr << 1) | u2)      V(AT(6, 6), a_z2, a_z ^ 32)             V(AT(6, 7), a_zr, (a_zr << 3) + (lbase + ZST))
            V(AT(7, 0), a_oa, a_p << 2)              V(AT(7, 1), a_zr, (a_p << 7) + a_zr)    V(AT(7, 2), a_oa, (u2 << 21) | a_oa)
            V(AT(7, 3), a_qr, (u2 << 10) + (w2k + QST))
            V(AT(7, 6), a_qr, (a_p << 1) + a_qr)     V(AT(7, 7), a_ob, (lane >> 2) & 4)      V(AT(8, 0), u0, u0 << 4)
            V(AT(8, 1), a_ob, (lane & 32) | a_ob)    V(AT(8, 2), a_ob, (u1 & 1) | a_ob)      V(AT(8, 3), a_ob, (a_ob << 13) | u0)
            if constexpr (P == P_X || P == P_X + 2) dma<ZST + (P != P_X) * 1024, false>(w2k, a_x, mk_rsrc(x, (NOX & 1) ? 0 : 4 << 20), (tb + ((2 * XL + (P != P_X)) ^ qx) * 8) * 2048 + dt * 256 + lw * 64);
            if constexpr (!NOA) {
            if constexpr (P == P_Q || P == P_Q + 2) dma<QST + (P != P_Q) * 1024, false>(w2k, a_q, mk_rsrc(qin, (NOX & 2) ? 0 : 4 << 20), (ta + 256 * (P != P_Q)) * 2048 + xcd * 256);
            if constexpr (P == P_T) __builtin_amdgcn_raw_buffer_store_b64(tg2, mk_brs(ws + WS_F + F_Z + (xcd * 32 + idx) * 128, wid ? 0 : 8), l16, 0, 16);
            if constexpr (P == AT(ZP, 4)) pf_ld(pf, a_f, rsz, zfo);
            if constexpr (P == P_ZC) { wait_v<g2_allow(P, AT(ZP, 4))>(); tag_chk(pf, expect, a_f, rsz, zfo); }
            if constexpr (P == P_Z || P == P_Z + 2) dma<ZST + (P != P_Z) * 1024, true>(w2k, P == P_Z ? a_z : a_z2, mk_rsrc(ws + WS_ZT + (size_t)xcd * 524288, 524288), idx * 128 + (wid * 16 + (P != P_Z) * 8) * 4096);
            }
            {
                constexpr int d0 = XL ? AT(14, 4) : AT(3, 7), j = P - d0, m0 = AT(8, 5), m1 = XL ? AT((P_R0 >> 3) + 1, 3) : AT(9, 3);
                if constexpr (P == (XL ? AT(13, 6) : AT(2, 4))) dsr32<0>(dsw_, a_w);
                if constexpr (j == 0) { tie(dsw_); dlo = dsw_ & 0xffffu; tie(dlo); }
                if constexpr (j == 1) { dsw_ &= 0xffff0000u; tie(dsw_); }
                if constexpr (j >= 2 && j < 6) { constexpr unsigned long long k = diag_mask(j - 2); dg[0][j - 2] = sel0(dlo, k); }
                if constexpr (j >= 9 && j < 13) { constexpr unsigned long long k = diag_mask(j - 9); dg[1][j - 9] = sel0(dsw_, k); }
                if constexpr (P == AT(7, 5) || P == AT(7, 7)) dsr128<(m - 5) * 512>(xf[(m - 5) / 2], a_xr);
                if constexpr (P == AT(8, 4)) { tie(xf[0]); tie(xf[1]); }
                if constexpr (P == m0 || P == m1) static_for<0, 2>([&](auto dc) { mfma<false>(yc[P == m1][0][CV(dc)], xf[P == m1], __builtin_bit_cast(bf16x8, dg[CV(dc)])); });
            }
            if constexpr ((n & 7) == 6 && m == XW && !(DBG & 2)) dsw128<0>(a_iw, yc[p & 1][1][0]);
            if constexpr ((n & 7) == 6 && m == XW2 && !(DBG & 2)) dsw128<1024>(a_iw, yc[p & 1][1][1]);
            if constexpr ((n & 7) == 7 && (m & 5) == (n < 31 ? 4 : 0)) dsr128<(m & 2) * 512>(im[(m >> 1) & 1], a_ir);
            if constexpr (n >= 8 && (n & 7) == 0 && m == 4) { tie(im[0]); tie(im[1]); }
            if constexpr (P >= P_PP0 && P <= E_PP0) snd(IC(0), IC(P - P_PP0));
            if constexpr (P >= P_PP1 && P <= E_PP1) static_for<0, D1>([&](auto ic) { snd(IC(1), IC((P - P_PP1) * D1 + CV(ic))); });
            if constexpr (P == P_L0 || P == P_L1) pp_ld<P == P_L1>(pl[P == P_L1], l16, rsp, ppo ^ (P == P_L1 ? 24576 : 16384));
            if constexpr (P == P_R0 || P == P_R1) { wait_v<g2_allow(P, P == P_R0 ? imax(P_L0, XL ? P_X + 2 : 0) : P_L1)>(); pp_tie<P == P_R1>(pl[P == P_R1]); }
            if constexpr (P >= P_R0 && P <= E_R0) static_for<0, D0>([&](auto ic) { if constexpr ((P - P_R0) * D0 + CV(ic) < NR) rcv(IC(0), IC((P - P_R0) * D0 + CV(ic))); });
            if constexpr (P >= P_R1 && P < P_R1 + 21) rcv(IC(1), IC(P - P_R1));
            if constexpr (!NOA) {
            if constexpr (n == ZW && m >= 4) dsr64<(m - 4) * 128>(zv[m - 4], a_zr);
            static_for<0, 4>([&](auto rc) {
                constexpr int r = CV(rc);
                if constexpr (P == q_tie(r)) tie(qv[r & 1]);
                if constexpr (P == q_rd(r)) dsr64<r * 256>(qv[r & 1], a_qr);
            });
            if constexpr (P == AT(ZW + 1, 4)) { tie(zv[0]); tie(zv[1]); tie(zv[2]); tie(zv[3]); }
            }
            if constexpr (a_step(P) >= 0) {
                constexpr int s = a_step(P), e = s == 0 ? 0 : s % 3 == 2 ? (s + 1) / 3 : s % 3 == 1 ? s / 3 : s / 3 - 1, r = e >> 2, c = e & 3;
                if constexpr (s == 0 || (s % 3 == 2 && e < 16)) { tq_[e & 1] = (c & 1) ? bfhi(qv[r & 1][c >> 1]) : bflo(qv[r & 1][c >> 1]); tie(tq_[e & 1]); }
                else if constexpr (s % 3 == 1) { tz = (r & 1) ? bfhi(zv[c][r >> 1]) : bflo(zv[c][r >> 1]); tie(tz); }
                else if constexpr (s % 3 == 0) {
                    ao[c] = tq_[e & 1] * tz;
                    if constexpr (c == 3 && !(NOST & 1)) __builtin_amdgcn_raw_buffer_store_b128(__builtin_bit_cast(u32x4, ao), ro, a_oa, (ta + r) * 8192 + xcd * 512, OSC);
                    else tie(ao);
                }
            }
            if constexpr (P >= P_F2 && P < P_F2 + 16) finB(IC(2), IC(P - P_F2));
            if constexpr (a_step(P - 1) > 0 && a_step(P - 1) % 12 == 0) keep(ao);
            if constexpr (P == E_PP0 + 1 || P == E_PP1 + 1) keep(po);
            if constexpr (P == P_F2 + 16) keep(ob[1]);
        };
        if (wid < 4) TS(7);
        TG_DECL
        barrier();
        static_for<0, 8>([&](auto ic) { ldh(IC(0), ic); });
        static_for<0, 32>([&](auto nc) {
            constexpr int n = CV(nc), p = n >> 3;
            if constexpr (n == 7) page<33>();
            if constexpr (n == 23) page<37>();
            wait_lr<(n & 3) == 3 ? 0 : 4 + g2_lx(n - 1)>();
            if constexpr (n == 31) barrier();
            if constexpr ((n & 3) == 3 && n < 31) {
                constexpr int need = g2_need(n);
                TL(21 + n / 4 * 3);
                wait_v<g2_allow(8 * n, need)>();
                TL(22 + n / 4 * 3);
                barrier();
                TL(23 + n / 4 * 3);
            }
            if constexpr (n == 1) hop<31>();
            if constexpr (n == 4) hop<32>();
            if constexpr (n == 11) hop<34>();
            if constexpr (n == 15) hop<35>();
            if constexpr (n == 19) hop<36>();
            if constexpr (n == 27) hop<38>();
            if constexpr (n == 31) hop<39>();
            if constexpr (n == 0) TL(20);
            TG(20 + n);
            if constexpr (n == 31) TL(42);
            if constexpr (PRG) __builtin_amdgcn_s_setprio(n == 31 ? 0 : 3 - ((n + 1) & 3));
            sched();
            static_for<0, 8>([&](auto mc) {
                constexpr int m = CV(mc), ks = m >> 2, tt = (m >> 1) & 1, dd = m & 1, P = n * 8 + m;
                if constexpr (m == 4 && (n & 3) != 3) wait_lr<(rd1(n) ? 8 : 4) + ((n & 7) == 6 ? (XW < 4) + (XW2 < 4) : 0)>();
                mfma<(n & 7) == 0 && ks == 0>(yc[p & 1][(DBG & 1) ? tt : g2_tile(n, tt)][dd], cat(hf[n & 1][4 * ks + 2 * tt], hf[n & 1][4 * ks + 2 * tt + 1]), cf[g2_unit(n, tt) * 4 + 2 * ks + dd]);
                sched();
                if constexpr (n < 31 && rd1(n) && m < 4) { ldh(IC(n + 1), IC(2 * m)); ldh(IC(n + 1), IC(2 * m + 1)); }
                if constexpr (n < 31 && !rd1(n)) ldh(IC(n + 1), mc);
                if constexpr (g2_fill(P) >= 0) fill(IC(g2_fill(P) >> 2), IC(g2_fill(P) & 3));
                side(IC(P));
                sched();
            });
        });
        TG(52);
        if (wid >= 4) TS(5); else TS(8);
        TG(53);
        static_for<256 - P_R1, 21>([&](auto jc) { rcv(IC(1), jc); });
        wait_l<0>();
        tie(im[0]); tie(im[1]);
        static_for<21, NR>([&](auto jc) { rcv(IC(1), jc); });
        asm volatile("s_nop 3");
        sched();
        static_for<0, 16>([&](auto jc) { finB(IC(3), jc); });
        if (b == 255 && wid == 0) __builtin_amdgcn_raw_buffer_store_b32(tag, rf, lane ? OOB : 0u, F_EP, 16);
    }
}

}

#if defined(__HIP_DEVICE_COMPILE__)
#pragma clang attribute pop
#endif

extern "C" void kernel_launch(void* const* d_in, const int* in_sizes, int n_in, void* d_out, int out_size, void* d_ws, size_t ws_size,
                              hipStream_t stream) {
    (void)in_sizes; (void)n_in; (void)out_size; (void)ws_size;
    using namespace fk;
    const uint16_t* q = (const uint16_t*)d_in[0];
    const uint16_t* kin = (const uint16_t*)d_in[1];
    const uint16_t* vin = (const uint16_t*)d_in[2];
    const uint16_t* sk = (const uint16_t*)d_in[3];
    const uint16_t* ssmk = (const uint16_t*)d_in[4];
    const uint16_t* ssmD = (const uint16_t*)d_in[5];
    const uint16_t* x = (const uint16_t*)d_in[6];
    const uint16_t* lre = (const uint16_t*)d_in[7];
    const uint16_t* lim = (const uint16_t*)d_in[8];
    const uint16_t* bre = (const uint16_t*)d_in[9];
    const uint16_t* bim = (const uint16_t*)d_in[10];
    const uint16_t* cre = (const uint16_t*)d_in[11];
    const uint16_t* cim = (const uint16_t*)d_in[12];
    const uint16_t* logdt = (const uint16_t*)d_in[13];
    const uint16_t* dskip = (const uint16_t*)d_in[14];
    char* ws = (char*)d_ws;
    hipLaunchKernelGGL(k1, dim3(256), dim3(512), 0, stream, x, bre, bim, lre, lim, logdt, cre, cim, dskip, ssmk, ssmD, q, kin, vin, sk, ws, (float*)d_out TS_PASS(0));
}
```

```cpp
#include <hip/hip_runtime.h>
#include <stdint.h>
#include <type_traits>

#if defined(__HIP_DEVICE_COMPILE__)
#pragma clang attribute push(__attribute__((target("no-packed-fp32-ops"))), apply_to = function)
#endif

namespace fk {

typedef __attribute__((ext_vector_type(8))) short bf16x8;
typedef __attribute__((ext_vector_type(4))) short bf16x4;
typedef __attribute__((ext_vector_type(4))) float f32x4;
typedef __attribute__((ext_vector_type(4))) unsigned u32x4;
typedef __attribute__((ext_vector_type(2))) unsigned u32x2;
typedef __attribute__((ext_vector_type(2))) __bf16 bf16x2_t;
typedef __attribute__((address_space(1))) unsigned gu32;
typedef __attribute__((address_space(1))) unsigned long long gu64;
#define RLX_AGENT __ATOMIC_RELAXED, __HIP_MEMORY_SCOPE_AGENT

__device__ __forceinline__ float u2f(unsigned v) { return __builtin_bit_cast(float, v); }
__device__ __forceinline__ unsigned f2u(float v) { return __builtin_bit_cast(unsigned, v); }
__device__ __forceinline__ bool wall(bool p) { return __builtin_amdgcn_ballot_w64(p) == ~0ull; }
__device__ __forceinline__ float bflo(unsigned w) { return u2f(w << 16); }
__device__ __forceinline__ float bfhi(unsigned w) { return u2f(w & 0xffff0000u); }
__device__ __forceinline__ unsigned pack2(float lo, float hi) {
    bf16x2_t v; v[0] = (__bf16)lo; v[1] = (__bf16)hi; return __builtin_bit_cast(unsigned, v);
}
__device__ __forceinline__ float rbf(float f) { float r; asm("v_cvt_pk_bf16_f32 %0, 0, %1" : "=v"(r) : "v"(f)); return r; }
__device__ __forceinline__ float fma_(float a, float b, float c) { return __builtin_fmaf(a, b, c); }

template <int I, int N, class F>
__device__ __forceinline__ void static_for(F&& f) {
    if constexpr (I + 1 == N) f(std::integral_constant<int, I>{});
    else if constexpr (I < N) { static_for<I, I + (N - I) / 2>(f); static_for<I + (N - I) / 2, N>(f); }
}
#define IC(v) std::integral_constant<int, (v)>{}
#define CV(c) decltype(c)::value

#define TS_ARG
#define TS_PASS(off)
#define TS_DECL
#define TS(i)
#define TSV(i, v)
#define TL_DECL
#define TL(i)
#define TS_FLUSH(slot)

#define TG_DECL
#define TG(i)
template <int N> __device__ __forceinline__ void wait_v() { asm volatile("s_waitcnt vmcnt(%0)" ::"n"(N > 63 ? 63 : N) : "memory"); }
template <int N> __device__ __forceinline__ void wait_l() { asm volatile("s_waitcnt lgkmcnt(%0)" ::"n"(N > 14 ? 14 : N) : "memory"); }
template <int N> __device__ __forceinline__ void wait_lr() { __builtin_amdgcn_s_waitcnt(0xc07f | ((N > 14 ? 14 : N) << 8)); }
template <class T> __device__ __forceinline__ void tie(T& x) { asm volatile("" : "+v"(x)); }
template <class T> __device__ __forceinline__ void undef(T& x) { asm volatile("" : "=v"(x)); }
template <int OFF, class T> __device__ __forceinline__ void dsr128u(T& d, unsigned a) { asm volatile("ds_read_b128 %0, %1 offset:%2" : "+v"(d) : "v"(a), "n"(OFF)); }
template <class T> __device__ __forceinline__ void keep(const T& x) { asm volatile("" :: "v"(x)); }
template <int OFF, class T> __device__ __forceinline__ void dsr128(T& d, unsigned a) { asm volatile("ds_read_b128 %0, %1 offset:%2" : "=v"(d) : "v"(a), "n"(OFF)); }
template <int OFF, class T> __device__ __forceinline__ void dsr64(T& d, unsigned a) { asm volatile("ds_read_b64 %0, %1 offset:%2" : "=v"(d) : "v"(a), "n"(OFF)); }
template <int OFF> __device__ __forceinline__ void dsr32(unsigned& d, unsigned a) { asm volatile("ds_read_b32 %0, %1 offset:%2" : "=v"(d) : "v"(a), "n"(OFF)); }
template <int O0, int O1, class T> __device__ __forceinline__ void dsr2(T& d, unsigned a) { asm volatile("ds_read2_b32 %0, %1 offset0:%2 offset1:%3" : "=v"(d) : "v"(a), "n"(O0), "n"(O1)); }
template <int OFF> __device__ __forceinline__ void dsr64tr(bf16x4& d, unsigned a) { asm volatile("ds_read_b64_tr_b16 %0, %1 offset:%2" : "=v"(d) : "v"(a), "n"(OFF)); }
template <int OFF, class T> __device__ __forceinline__ void dsw128(unsigned a, const T& v) { asm volatile("ds_write_b128 %0, %1 offset:%2" :: "v"(a), "v"(v), "n"(OFF) : "memory"); }
template <int OFF> __device__ __forceinline__ void dsw32(unsigned a, unsigned v) { asm volatile("ds_write_b32 %0, %1 offset:%2" :: "v"(a), "v"(v), "n"(OFF) : "memory"); }
__device__ __forceinline__ bf16x8 cat(const bf16x4& lo, const bf16x4& hi) { return __builtin_shufflevector(lo, hi, 0, 1, 2, 3, 4, 5, 6, 7); }
__device__ __forceinline__ void barrier() { asm volatile("" ::: "memory"); __builtin_amdgcn_s_barrier(); asm volatile("" ::: "memory"); }
__device__ __forceinline__ int fresh(int v) { asm volatile("" : "+s"(v)); return v; }
__device__ __forceinline__ void sched() { __builtin_amdgcn_sched_barrier(0); }
template <bool FIRST> __device__ __forceinline__ void mfma(f32x4& c, const bf16x8& a, const bf16x8& b) {
    if constexpr (FIRST) asm volatile("v_mfma_f32_16x16x32_bf16 %0, %1, %2, 0" : "=v"(c) : "v"(a), "v"(b));
    else asm volatile("v_mfma_f32_16x16x32_bf16 %0, %1, %2, %0" : "+v"(c) : "v"(a), "v"(b));
}
struct cf32 { float r, i; };
__device__ __forceinline__ cf32 cmul(cf32 a, cf32 b) { return cf32{fma_(a.r, b.r, -(a.i * b.i)), fma_(a.r, b.i, a.i * b.r)}; }
__device__ __forceinline__ cf32 cfma(cf32 a, cf32 b, cf32 c) { return cf32{fma_(a.r, b.r, fma_(-a.i, b.i, c.r)), fma_(a.r, b.i, fma_(a.i, b.r, c.i))}; }
__device__ __forceinline__ void sw16(float v, float& e, float& o) { const auto r = __builtin_amdgcn_permlane16_swap(f2u(v), f2u(v), false, false); e = u2f(r[0]); o = u2f(r[1]); }
__device__ __forceinline__ void sw32(float v, float& e, float& o) { const auto r = __builtin_amdgcn_permlane32_swap(f2u(v), f2u(v), false, false); e = u2f(r[0]); o = u2f(r[1]); }
constexpr unsigned SPIN_CAP = 1u << 16;
constexpr unsigned OOB = 0x40000000u;

constexpr size_t WS_H = 0;
constexpr size_t WS_KVT = (size_t)8 << 20;
constexpr size_t WS_ZT = (size_t)12 << 20;
constexpr size_t WS_PP = (size_t)16 << 20;
constexpr size_t WS_E = (size_t)24 << 20;
constexpr size_t WS_F = WS_E + (256 << 10);
constexpr int F_H = 0;
constexpr int F_K = 16384;
constexpr int F_Z = 24576;
constexpr int F_EP = F_Z + 32768;
constexpr int LDS_TOTAL = 163840, BUF = 32768;
constexpr int KC = 98304, KCH = 8320, KCP = 4160;
constexpr int U0 = KC + 4 * KCH;
constexpr int FULL = U0 + 18432 + 8192, FST = 388;
constexpr int DONE = U0 + 18432, DUMP = DONE + 4096;
constexpr int HGO = FULL + 1792, DSK = HGO + 256, POLL = DSK + 256;
static_assert(POLL + 8 * 256 <= LDS_TOTAL && 4 * FST <= 1792, "lds");
constexpr int g1_buf(int s) { return ((0x0213 >> ((s & 3) * 4)) & 3) * BUF; }
constexpr int IMG = KC, QST = KC + 16384, ZST = U0, PLD = 2 * BUF;
#define CODE_KB 47
#define EXACT 0
#define HP 1
#define CP 2
#define FLQ 11
#define R01 3
#define ZP 7
#define SLP 8
#define STR_(x) #x
#define STR(x) STR_(x)
#define NOST 0
#define G2M 0
#define RD 2
#define PRG 1
#define OSC 16
#define NOA 0
#define PPH 0
#define HS 0
#define JQ(j) (HS == 0 ? 0 : HS == 1 ? (j) & 1 : HS == 2 ? (j) : (j) & 2)
#define JR(j) (HS == 0 ? 2 * (j) : HS == 1 ? 2 * ((j) & 2) : HS == 2 ? 0 : 4 * ((j) & 1))
#define NOX 0

__device__ __forceinline__ u32x4 mk_rsrc(const void* p, unsigned bytes) {
    const unsigned long long a = (unsigned long long)p;
    return u32x4{(unsigned)a, (unsigned)(a >> 32) & 0xffffu, bytes, 0x00020000u};
}
__device__ __forceinline__ __amdgpu_buffer_rsrc_t mk_brs(const void* p, unsigned bytes) { return __builtin_amdgcn_make_buffer_rsrc((void*)p, (short)0, bytes, 0x00020000); }
template <int IMM, bool SC1> __device__ __forceinline__ void dma(unsigned lds, unsigned voff, const u32x4& rs, unsigned soff) {
    if constexpr (SC1) asm volatile("s_add_u32 m0, %0, %1\n\ts_nop 0\n\tbuffer_load_dwordx4 %2, %3, %4 offen sc1 lds" :: "s"(lds), "n"(IMM), "v"(voff), "s"(rs), "s"(soff) : "memory", "scc");
    else asm volatile("s_add_u32 m0, %0, %1\n\ts_nop 0\n\tbuffer_load_dwordx4 %2, %3, %4 offen lds" :: "s"(lds), "n"(IMM), "v"(voff), "s"(rs), "s"(soff) : "memory", "scc");
}
template <int IMM, int I> __device__ __forceinline__ void dma4(unsigned lds, unsigned voff, const u32x4& rs, unsigned soff) {
    if constexpr (I == 0) dma<IMM, false>(lds, voff, rs, soff);
    else asm volatile("buffer_load_dwordx4 %0, %1, %2 offen offset:%3 lds" :: "v"(voff), "s"(rs), "s"(soff), "n"(I * 1024) : "memory");
}
template <int IMM> __device__ __forceinline__ void dma1(unsigned lds, unsigned voff, const u32x4& rs, unsigned soff) {
    asm volatile("s_add_u32 m0, %0, %1\n\ts_nop 0\n\tbuffer_load_dword %2, %3, %4 offen sc1 lds" :: "s"(lds), "n"(IMM), "v"(voff), "s"(rs), "s"(soff) : "memory", "scc");
}
__device__ __forceinline__ void flag_set(unsigned waddr, unsigned v) { asm volatile("ds_write_b32 %0, %1" :: "v"(waddr), "v"(v) : "memory"); }
__device__ __forceinline__ void flag_chk(unsigned& f, unsigned raddr, unsigned need) {
    unsigned cnt;
    asm volatile("v_cmp_le_u32 vcc, %3, %0\n\t"
                 "s_cmp_eq_u64 vcc, -1\n\t"
                 "s_cbranch_scc1 1f\n\t"
                 "s_mov_b32 %1, 0x100000\n"
                 "0:\n\t"
                 "ds_read_b32 %0, %2\n\t"
                 "s_sub_u32 %1, %1, 1\n\t"
                 "s_waitcnt lgkmcnt(0)\n\t"
                 "s_cbranch_scc1 1f\n\t"
                 "v_cmp_le_u32 vcc, %3, %0\n\t"
                 "s_cmp_eq_u64 vcc, -1\n\t"
                 "s_cbranch_scc0 0b\n"
                 "1:" : "+v"(f), "=&s"(cnt) : "v"(raddr), "s"(need) : "vcc", "scc", "memory");
}
template <int OFF, int SL = 0> __device__ __forceinline__ void zero_chk(unsigned& f, unsigned raddr) {
    unsigned cnt;
    asm volatile("v_cmp_ne_u32 vcc, 0, %0\n\t"
                 "s_cbranch_vccz 1f\n\t"
                 "s_mov_b32 %1, 0x100000\n"
                 "0:\n\t"
                 ".if %4\n\ts_sleep %4\n\t.endif\n\t"
                 "ds_read_b32 %0, %2 offset:%3\n\t"
                 "s_sub_u32 %1, %1, 1\n\t"
                 "s_waitcnt lgkmcnt(0)\n\t"
                 "s_cbranch_scc1 1f\n\t"
                 "v_cmp_ne_u32 vcc, 0, %0\n\t"
                 "s_cbranch_vccnz 0b\n"
                 "1:" : "+v"(f), "=&s"(cnt) : "v"(raddr), "n"(OFF), "n"(SL) : "vcc", "scc", "memory");
}
template <int IMM, class T> __device__ __forceinline__ void gld(T& d, unsigned voff, const u32x4& rs, unsigned soff) {
    if constexpr (sizeof(T) == 16) asm volatile("buffer_load_dwordx4 %0, %1, %2, %3 offen offset:%4" : "=v"(d) : "v"(voff), "s"(rs), "s"(soff), "n"(IMM) : "memory");
    else if constexpr (sizeof(T) == 8) asm volatile("buffer_load_dwordx2 %0, %1, %2, %3 offen offset:%4" : "=v"(d) : "v"(voff), "s"(rs), "s"(soff), "n"(IMM) : "memory");
    else asm volatile("buffer_load_ushort %0, %1, %2, %3 offen offset:%4" : "=v"(d) : "v"(voff), "s"(rs), "s"(soff), "n"(IMM) : "memory");
}
__device__ __forceinline__ void gld32(unsigned& d, unsigned voff, const u32x4& rs, unsigned soff) { asm volatile("buffer_load_dword %0, %1, %2, %3 offen" : "=v"(d) : "v"(voff), "s"(rs), "s"(soff) : "memory"); }
__device__ __forceinline__ void sld(unsigned& d, const void* p) { asm volatile("s_load_dword %0, %1, 0x0" : "=s"(d) : "s"(p) : "memory"); }
__device__ __forceinline__ void stouch(unsigned long long p) { asm volatile("s_load_dword s101, %0, 0x0\n\ts_load_dword s101, %0, 0x80\n\ts_load_dword s101, %0, 0x100" :: "s"(p) : "s101", "memory"); }
template <class T> __device__ __forceinline__ void ties(T& x) { asm volatile("" : "+s"(x)); }
template <int IMM, class T> __device__ __forceinline__ void gst(const T& d, unsigned voff, const u32x4& rs, unsigned soff) {
    if constexpr (sizeof(T) == 16) asm volatile("buffer_store_dwordx4 %0, %1, %2, %3 offen offset:%4 sc1" :: "v"(d), "v"(voff), "s"(rs), "s"(soff), "n"(IMM) : "memory");
    else asm volatile("buffer_store_dwordx2 %0, %1, %2, %3 offen offset:%4 sc1" :: "v"(d), "v"(voff), "s"(rs), "s"(soff), "n"(IMM) : "memory");
}
template <int SLQ> __device__ __forceinline__ void gate(unsigned raddr, unsigned need) {
    unsigned cnt, f;
    asm volatile("s_mov_b32 %1, 0x40000\n"
                 "0:\n\t"
                 "ds_read_b32 %0, %2\n\t"
                 "s_sub_u32 %1, %1, 1\n\t"
                 "s_waitcnt lgkmcnt(0)\n\t"
                 "s_cbranch_scc1 1f\n\t"
                 "v_cmp_le_u32 vcc, %3, %0\n\t"
                 "s_cmp_eq_u64 vcc, -1\n\t"
                 "s_cbranch_scc1 1f\n\t"
                 "s_sleep %4\n\t"
                 "s_branch 0b\n"
                 "1:" : "=&v"(f), "=&s"(cnt) : "v"(raddr), "s"(need), "n"(SLQ) : "vcc", "scc", "memory");
}
template <int SL = SLP> __device__ __forceinline__ void tag_chk(unsigned& f, unsigned expect, unsigned voff, const u32x4& rs, unsigned soff) {
    unsigned cnt;
    asm volatile("v_cmp_eq_u32 vcc, %2, %0\n\t"
                 "s_cmp_eq_u64 vcc, -1\n\t"
                 "s_cbranch_scc1 1f\n\t"
                 "s_mov_b32 %1, 0x10000\n"
                 "0:\n\t"
                 "s_sleep %6\n\t"
                 "buffer_load_dword %0, %3, %4, %5 offen sc1\n\t"
                 "s_sub_u32 %1, %1, 1\n\t"
                 "s_waitcnt vmcnt(0)\n\t"
                 "s_cbranch_scc1 1f\n\t"
                 "v_cmp_eq_u32 vcc, %2, %0\n\t"
                 "s_cmp_eq_u64 vcc, -1\n\t"
                 "s_cbranch_scc0 0b\n"
                 "1:" : "+v"(f), "=&s"(cnt) : "v"(expect), "v"(voff), "s"(rs), "s"(soff), "n"(SL) : "vcc", "scc", "memory");
}

template <int H> __device__ __forceinline__ void pp_ld(u32x4& d, unsigned voff, const u32x4& rs, unsigned soff) {
    if constexpr (H) asm volatile("buffer_load_dwordx4 v[244:247], %1, %2, %3 offen offset:1024 sc1" : "={v[244:247]}"(d) : "v"(voff), "s"(rs), "s"(soff) : "memory");
    else asm volatile("buffer_load_dwordx4 v[240:243], %1, %2, %3 offen sc1" : "={v[240:243]}"(d) : "v"(voff), "s"(rs), "s"(soff) : "memory");
}
__device__ __forceinline__ void pf_ld(unsigned& d, unsigned voff, const u32x4& rs, unsigned soff) { asm volatile("buffer_load_dword v248, %1, %2, %3 offen sc1" : "={v248}"(d) : "v"(voff), "s"(rs), "s"(soff) : "memory"); }
#define PP_CHK(R, R1, R2, R3, OFF) \
    asm volatile("v_cmp_ne_u32 vcc, 0, %2\n\t" \
                 "s_cbranch_vccz 1f\n\t" \
                 "s_mov_b32 %1, 0x10000\n" \
                 "0:\n\t" \
                 "buffer_load_dwordx4 " R ", %5, %6, %7 offen offset:" OFF " sc1\n\t" \
                 "s_waitcnt vmcnt(0)\n\t" \
                 "s_and_b32 %3, %4, 0xf000f\n\t" \
                 "v_xor_b32 " R1 ", %3, " R1 "\n\t" \
                 "s_lshr_b32 %3, %4, 8\n\t" \
                 "s_and_b32 %3, %3, 0xf000f\n\t" \
                 "v_xor_b32 " R2 ", %3, " R2 "\n\t" \
                 "s_lshr_b32 %3, %4, 4\n\t" \
                 "s_and_b32 %3, %3, 0xf000f\n\t" \
                 "v_xor_b32 " R3 ", %3, " R3 "\n\t" \
                 "v_or3_b32 %2, " R1 ", " R2 ", " R3 "\n\t" \
                 "v_and_b32 %2, 0xf000f, %2\n\t" \
                 "s_sub_u32 %1, %1, 1\n\t" \
                 "s_cbranch_scc1 1f\n\t" \
                 "v_cmp_ne_u32 vcc, 0, %2\n\t" \
                 "s_cbranch_vccnz 0b\n" \
                 "1:" : "+{" R "}"(p), "=&s"(cnt), "+v"(bad), "=&s"(t) : "s"(tag), "v"(voff), "s"(rs), "s"(soff) : "vcc", "scc", "memory")
template <int H> __device__ __forceinline__ void pp_chk(u32x4& p, unsigned bad, unsigned tag, unsigned voff, const u32x4& rs, unsigned soff) {
    unsigned cnt, t;
    if constexpr (H) PP_CHK("v[244:247]", "v245", "v246", "v247", "1024"); else PP_CHK("v[240:243]", "v241", "v242", "v243", "0");
}
template <int H> __device__ __forceinline__ void pp_tie(u32x4& p) { if constexpr (H) asm volatile("" : "+{v[244:247]}"(p)); else asm volatile("" : "+{v[240:243]}"(p)); }
__device__ __forceinline__ float amax3(float a, float b, float c) { float r; asm volatile("v_max3_f32 %0, |%1|, |%2|, |%3|" : "=v"(r) : "v"(a), "v"(b), "v"(c)); return r; }
__device__ __forceinline__ unsigned bfi_v(unsigned m, unsigned a, unsigned b) { unsigned r; asm volatile("v_bfi_b32 %0, %1, %2, %3" : "=v"(r) : "v"(m), "v"(a), "v"(b)); return r; }
__device__ __forceinline__ unsigned bfi_s(unsigned m, unsigned a, unsigned b) { unsigned r; asm volatile("v_bfi_b32 %0, %1, %2, %3" : "=v"(r) : "v"(m), "v"(a), "s"(b)); return r; }
template <int H> __device__ __forceinline__ float cvt16(unsigned w) {
    float r;
    if constexpr (H) asm volatile("v_cvt_f32_i32_sdwa %0, sext(%1) dst_sel:DWORD dst_unused:UNUSED_PAD src0_sel:WORD_1" : "=v"(r) : "v"(w));
    else asm volatile("v_cvt_f32_i32_sdwa %0, sext(%1) dst_sel:DWORD dst_unused:UNUSED_PAD src0_sel:WORD_0" : "=v"(r) : "v"(w));
    return r;
}
__device__ __forceinline__ float sel8(float a, float b, unsigned long long mask) {
    float r;
    asm volatile("s_mov_b64 vcc, %3\n\tv_cndmask_b32_dpp %0, %1, %2, vcc row_ror:8 row_mask:0xf bank_mask:0xf" : "=v"(r) : "v"(a), "v"(b), "s"(mask) : "vcc");
    return r;
}
__device__ __forceinline__ unsigned sel0(unsigned a, unsigned long long mask) { unsigned r; asm volatile("v_cndmask_b32_e64 %0, 0, %1, %2" : "=v"(r) : "v"(a), "s"(mask)); return r; }
constexpr unsigned long long diag_mask(int R) {
    unsigned long long m = 0;
    for (int l = 0; l < 64; ++l) if ((l >> 4) == ((l & 7) >> 1) && 2 * (l & 1) + ((l >> 3) & 1) == R) m |= 1ull << l;
    return m;
}
template <int IMM, int X, bool SC1> __device__ __forceinline__ void dma_x(unsigned& so, unsigned lds, unsigned voff, const u32x4& rs, unsigned base) {
    if constexpr (SC1) asm volatile("s_add_u32 m0, %1, %2\n\ts_xor_b32 %0, %5, %6\n\tbuffer_load_dwordx4 %3, %4, %0 offen sc1 lds" : "=&s"(so) : "s"(lds), "n"(IMM), "v"(voff), "s"(rs), "s"(base), "n"(X) : "memory", "scc");
    else asm volatile("s_add_u32 m0, %1, %2\n\ts_xor_b32 %0, %5, %6\n\tbuffer_load_dwordx4 %3, %4, %0 offen lds" : "=&s"(so) : "s"(lds), "n"(IMM), "v"(voff), "s"(rs), "s"(base), "n"(X) : "memory", "scc");
}
template <int OFF> __device__ __forceinline__ void dma_n(unsigned so, unsigned voff, const u32x4& rs) {
    asm volatile("buffer_load_dwordx4 %0, %1, %2 offen offset:%3 sc1 lds" :: "v"(voff), "s"(rs), "s"(so), "n"(OFF) : "memory");
}
template <int IMM, int OFF> __device__ __forceinline__ void dma_m(unsigned so, unsigned lds, unsigned voff, const u32x4& rs) {
    asm volatile("s_add_u32 m0, %4, %5\n\ts_nop 0\n\tbuffer_load_dwordx4 %0, %1, %2 offen offset:%3 sc1 lds" :: "v"(voff), "s"(rs), "s"(so), "n"(OFF), "s"(lds), "n"(IMM) : "memory", "scc");
}
__device__ __forceinline__ unsigned and_or(unsigned a, unsigned m, unsigned o) { unsigned r; asm("v_and_or_b32 %0, %1, %2, %3" : "=v"(r) : "v"(a), "v"(m), "s"(o)); return r; }

#define HOPS 1
#define HM 0x2210243082ull
#define PG1 95
#define PG2 288
#define PG3 8
#define PG4 24
#define HQ 52
#define HW 4
constexpr int hop_nth(int n) { for (int k = 0; k < 62; ++k) if (((HM >> k) & 1) && n-- == 0) return k; return 63; }
constexpr int hop_next(int K) { int n = 0; for (int k = 0; k < K; ++k) n += (HM >> k) & 1; return hop_nth(n + HW); }
template <int K> __device__ __forceinline__ void hop() {
    if constexpr (HOPS && ((HM >> K) & 1)) asm volatile(".Lfkh%0:\n\ts_cbranch_execz .Lfkh%1" :: "n"(K), "n"(hop_next(K)));
}
template <int K> __device__ __forceinline__ void page() {
    if constexpr (HOPS && K >= 0 && ((HM >> (K & 63)) & 1)) asm volatile("s_branch 1f\n\t.p2align 12\n.Lfkh%0:\n\ts_cbranch_execz .Lfkh%1\n1:" :: "n"(K), "n"(hop_next(K)));
    else if constexpr (HOPS) asm volatile("s_branch 1f\n\t.p2align 12\n1:");
}
__device__ __forceinline__ void hop_all(int w) {
    if constexpr (HOPS) asm volatile("s_mov_b64 exec, 0\n\ts_cmp_eq_u32 %0, 0\n\ts_cbranch_scc1 .Lfkh%1\n\ts_cmp_eq_u32 %0, 1\n\ts_cbranch_scc1 .Lfkh%2\n\t"
                                     "s_cmp_eq_u32 %0, 2\n\ts_cbranch_scc1 .Lfkh%3\n\ts_branch .Lfkh%4\n.Lfkh63:\n\ts_mov_b64 exec, -1"
                                     :: "s"(w), "n"(hop_nth(0)), "n"(hop_nth(1)), "n"(hop_nth(2)), "n"(hop_nth(3)) : "scc");
}
#define NB 1
#define PFQ 0
#define CVR 3
#define SPR 3
#define ELP 6
#define PW 1
#define CPG 95
#define GQ -1
#define GE 3
#define KDL 0
#define GP 1
#define SG 507
#define KO 0
#define DP 0
#define B13 460
#define PRE 0
enum { OP_NONE, OP_A, OP_B, OP_FLAG, OP_SIG };
struct Op { int kind, a, b; };
constexpr Op g1_op(int G) {
    const int st = (G + 32) / 32 - 1, q = G - 32 * st, i = q >> 2, j = q & 3;
    if (i < 5) {
        if (st < 0) return {OP_NONE, 0, 0};
        if ((j & 1) == 0) return {OP_A, st * 8 + i + 3, j >> 1};
        if (q == FLQ && st < 15) return {OP_FLAG, 0, 0};
        if (NB && G == SG) return {OP_SIG, 2, 0};
        if (NB == 2 && G == 435) return {OP_SIG, 3, 0};
        return {OP_NONE, 0, 0};
    }
    if (st == 15) return NB && G == SG ? Op{OP_SIG, 2, 0} : Op{OP_NONE, 0, 0};
    const int n = (st + 1) * 8;
    switch (q) {
        case 20: return {OP_B, st + 1, 0}; case 21: return {OP_B, st + 1, 1}; case 22: return {OP_A, n, 0}; case 23: return {OP_B, st + 1, 2};
        case 24: return {OP_B, st + 1, 3}; case 25: return {OP_A, n, 1}; case 26: return {OP_A, n + 1, 0}; case 27: return {OP_A, n + 1, 1};
        case 28: return {OP_A, n + 2, 0}; case 30: return {OP_A, n + 2, 1};
    }
    return {OP_NONE, 0, 0};
}
constexpr int g1_need(int G) {
    const int st = G >> 5, j = G & 3;
    for (int h = G - 1; h >= -12; --h) {
        const Op o = g1_op(h);
        if (o.kind == OP_A && o.a == (G >> 2) && o.b == (j >> 1)) return h;
        if (o.kind == OP_B && o.a == st && o.b == j) return h;
        if (o.kind == OP_FLAG && (G & 31) == 20 && (h >> 5) == st) return h;
    }
    return -100;
}
constexpr int g1_wait(int G) {
    int n = -100, c = 0;
    for (int h = G; h < (G ? G + 4 : 2) && h < 512; ++h) if (g1_need(h) > n) n = g1_need(h);
    for (int h = n + 1; h < G; ++h) c += g1_op(h).kind != OP_NONE;
    return n < G ? c : -1;
}
#define KV0 0
#define KVN 0x11111112
constexpr int kv_first(int k) { int n = 0; for (int i = KV0; i < k && i < KV0 + 8; ++i) n += (KVN >> (4 * (i - KV0))) & 15; return n > 8 ? 8 : n; }
constexpr int kv_last() { int k = KV0; while (kv_first(k + 1) < 8) ++k; return k; }
#define CQ -1
#define CTM 0
#define EC 2
#define ED 2
#define EB 0
#define KST 0
#define KFD 3
#define KPL 11
#define CBQ 20
constexpr int KVL = kv_last(), KFL = KVL + KFD;
static_assert(KFL <= KPL && KPL <= 11, "");
constexpr int hv_cnt(int k0, int k1) {
    int c = 0;
    for (int k = k0; k < k1; ++k) c += 9 + (KST ? 4 * (k == KVL) : kv_first(k + 1) / 2 - kv_first(k) / 2) + (k == KFL) + (k == KPL);
    return c;
}
#define ZM 1
#define CL 1
#define EP 0
constexpr unsigned long long lag0_mask(int w) {
    unsigned long long m = 0;
    for (int l = 0; l < 64; ++l) if ((l >> 4) == 2 + ((l >> 3) & 1) && 3 - ((l & 7) >> 1) == w) m |= 1ull << l;
    return m;
}
constexpr int cv_start(int n) { return n * 8 - n * (n - 1) / 2; }
constexpr int cv_n(int q) { int n = 0; while (cv_start(n + 1) <= q) ++n; return n; }
constexpr int cv_m(int q) { return 7 - cv_n(q) - (q - cv_start(cv_n(q))); }
enum { CO_NONE, CO_A, CO_B, CO_C };
constexpr Op cv_op(int G) {
    const int s = (G + 36) / 36 - 1, q = G - 36 * s;
    if (q >= 1 && q <= 6) return s >= 0 ? Op{CO_A, s * 8 + 5 + (q - 1) / 2, (q - 1) & 1} : Op{CO_NONE, 0, 0};
    if (q >= 11 && q <= 14) return s >= 0 && s >= CL && s - CL < 8 ? Op{CO_C, s - CL, q - 11} : Op{CO_NONE, 0, 0};
    if (s == 7) return {CO_NONE, 0, 0};
    for (int m = 0; m < 8; ++m) if (q == cv_start(7 - m)) return {CO_B, (s + 1) * 8 + m, 0};
    for (int n = 0; n < 5; ++n) if (q == cv_start(n + 1) + 1 || q == cv_start(n + 1) + 2) return {CO_A, (s + 1) * 8 + n, q - cv_start(n + 1) - 1};
    return {CO_NONE, 0, 0};
}
constexpr int cv_need(int G) {
    const int s = G / 36, q = G % 36;
    for (int h = G - 1; h >= -36; --h) {
        const Op o = cv_op(h);
        if (o.kind == CO_A && o.a == s * 8 + cv_n(q)) return h;
        if (o.kind == CO_B && o.a == s * 8 + cv_m(q)) return h;
        if (o.kind == CO_C && q == 24 && o.a == s - CL) return h;
    }
    return -100;
}
constexpr int cv_wait(int G) {
    const int n = cv_need(G);
    for (int h = G - 1; h >= 0 && h >= G - 40; --h) if (cv_need(h) >= n) return -1;
    int c = 0;
    for (int h = n + 1; h < G; ++h) c += cv_op(h).kind != CO_NONE;
    return c;
}
#define AT(n, m) ((n) * 8 + (m))
#define FM 0x326260
constexpr int fill_pos(int s, int i) {
    const int f = (FM >> (4 * (s - 2))) & 15, n = s == 2 ? 0 : 4 * s - 9;
    switch (f) {
        case 1: return AT(n + i, 5) - (s == 2 && i == 3) * 6;
        case 2: return AT(n + 1 + i / 2, 5 + 2 * (i & 1));
        case 3: return i < 2 ? AT(n, 5 + 2 * i) : AT(n + i - 1, 5);
        case 4: return AT(n + 2 + i / 2, 5 + 2 * (i & 1));
        case 5: return AT(n, 1 + 2 * i) + (s == 2 ? 4 : 0);
        case 6: return AT(n + 3 + i / 2, 5 + 2 * (i & 1));
        case 7: return AT(n + 3, 5 + 2 * i);
        case 8: return i < 2 ? AT(n + 1, 5 + 2 * i) : AT(n + i, 5);
        case 9: return AT(n + 1 + i, 1);
        case 10: return i < 2 ? AT(n, 1 + 2 * i) : AT(n + 3, 1 + 2 * i);
    }
    return AT(n + i / 2, 5 + 2 * (i & 1));
}
constexpr int g2_fill(int p) {
    for (int k = 8; k < 32; ++k) if (fill_pos(k >> 2, k & 3) == p) return k;
    return -1;
}
#define QX AT(2, 5)
#define QQ AT(3, 2)
#define QZ AT(11, 2)
#define QT AT(3, 4)
constexpr int P_X = QX, P_Q = QQ, P_Z = QZ, P_T = QT;
constexpr bool XL = false;
constexpr bool fill_m0(int k) {
    const int a = fill_pos(k >> 2, (k & 3) - 1), b = fill_pos(k >> 2, k & 3);
    return (P_Q + 2 >= a && P_Q < b) || (P_Z + 2 >= a && P_Z < b) || (P_X + 2 >= a && P_X < b);
}
#define DBG 0
#define XW 4
#define XW2 4
#define QPP0 AT(9, 4)
#define QL0 AT(21, 1)
#define QR0 AT(24, 6)
#define QF2 AT(28, 3)
#define QL1 AT(26, 1)
#define QR1 AT(29, 3)
#define QA AT(16, 4)
#define AD 0
#define D1 2
#define D0 1
constexpr int NS = 33, NR = 29;
constexpr int P_PP0 = QPP0, P_PP1 = AT(16, 4), P_L0 = QL0, P_R0 = QR0, P_F2 = QF2, P_L1 = QL1, P_R1 = QR1, P_ZC = AT(10, 6), P_A = QA;
constexpr int E_PP0 = P_PP0 + NS - 1, E_PP1 = P_PP1 + (NS - 1) / D1, E_R0 = P_R0 + (NR - 1) / D0;
constexpr int ZW = P_A >= AT(20, 4) ? 19 : 15;
static_assert(P_R0 + 14 / D0 >= AT(24, 3) && E_R0 < P_F2 && P_F2 >= AT(24, 4) && P_F2 + 8 <= AT(31, 0) && E_R0 < P_R1, "");
static_assert(P_PP0 >= AT(9, 2) && P_PP0 + 8 <= AT(15, 4) && E_PP0 < P_PP1 && P_A >= AT(16, 4), "");
constexpr bool a_free(int p) {
    return p >= P_A && (AD || !((p >= P_PP1 && p <= E_PP1) || (p >= P_R0 && p <= E_R0) || (p >= P_F2 && p < P_F2 + 16)));
}
constexpr int a_step(int p) {
    if (NOA || !a_free(p)) return -1;
    int c = 0;
    for (int h = P_A; h < p; ++h) c += a_free(h);
    return c <= 48 ? c : -1;
}
constexpr int a_pos(int s) { int p = P_A; while (a_step(p) != s) ++p; return p; }
constexpr int q_rd(int r) { int p = r < 2 ? AT(ZW, 6) + r : a_pos(12 * r - 16) + 1; while ((p & 7) < 4) ++p; return p; }
constexpr int q_tie(int r) { const int n = (q_rd(r) >> 3) + 1; return AT(n, (n & 3) == 3 ? 0 : 4); }
static_assert(NOA || (a_pos(48) < 256 && q_tie(2) <= a_pos(23) && q_tie(3) <= a_pos(35)), "");
constexpr int g2_vm(int p) {
    int c = g2_fill(p) >= 0;
    c += a_step(p) > 0 && a_step(p) % 12 == 0 && !(NOST & 1);
    if (!NOA) c += (p == P_T) + (p == P_Q || p == P_Q + 2) + (p == AT(ZP, 4)) + (p == P_Z || p == P_Z + 2);
    c += p == P_X || p == P_X + 2;
    c += !(NOST & 2) && (p == E_PP0 || p == E_PP1);
    c += p == P_L0 || p == P_L1;
    c += !(NOST & 4) && (p == P_F2 + 11 || p == P_F2 + 15);
    return c;
}
constexpr int g2_lx(int n) {
    int c = 2 * (n == 7);
    if (n >= 0 && (n & 7) >= 6) c += 2;
    if (!NOA) { c += 4 * (n == ZW); for (int r = 0; r < 4; ++r) c += (q_rd(r) >> 3) == n; }
    return c;
}
constexpr int g2_tile(int n, int j) { return (n & 4) ? (n & 2) == 0 : j; }
constexpr int g2_unit(int n, int j) { return (n & 4) ? 4 + 2 * (n & 1) + j : n & 7; }
constexpr int imax(int a, int b) { return a > b ? a : b; }
constexpr int g2_need(int n) {
    int r = n == 3 ? -1 : fill_pos((n + 1) / 4, 3);
    if (n == 7 && !XL) r = imax(r, P_X + 2);
    if (n == ZW && !NOA) r = imax(r, imax(P_Q, P_Z) + 2);
    return r;
}
static_assert(P_Z > AT(11, 0) && P_T > AT(3, 0) && P_T < AT(ZP, 4) && (!XL || (P_X + 2 < P_R0 && P_R0 + 2 >= AT(24, 2) && P_R0 < AT(27, 0) && D0 == 1)), "");
constexpr bool rd1(int n) { return RD == 1 || (RD == 3 && (n & 3) == 2); }
constexpr int g2_allow(int e, int p) { int c = 0; for (int h = p + 1; h < e; ++h) c += g2_vm(h); return c; }

__global__ __launch_bounds__(512, 2) __attribute__((aligned(4096))) void k1(const uint16_t* __restrict__ x, const uint16_t* __restrict__ bre, const uint16_t* __restrict__ bim,
                                             const uint16_t* __restrict__ lre, const uint16_t* __restrict__ lim, const uint16_t* __restrict__ logdt,
                                             const uint16_t* __restrict__ cre, const uint16_t* __restrict__ cim, const uint16_t* __restrict__ dskip,
                                             const uint16_t* __restrict__ ssmk, const uint16_t* __restrict__ ssmD, const uint16_t* __restrict__ qin,
                                             const uint16_t* __restrict__ kin, const uint16_t* __restrict__ vin, const uint16_t* __restrict__ sk,
                                             char* __restrict__ ws, float* __restrict__ out TS_ARG) {
    unsigned long long pc0;
    asm volatile("s_getpc_b64 %0" : "=s"(pc0));
    TS_DECL TS(0);
    TL_DECL
    __shared__ __attribute__((aligned(1024))) char shm[LDS_TOTAL];
    const int tid = threadIdx.x, lane = tid & 63, wid = __builtin_amdgcn_readfirstlane(tid >> 6);
    const int b = blockIdx.x, xcd = b & 7, idx = b >> 3, hb = xcd & 1, tq = idx & 7;
    const int tm = (xcd >> 1) * 4 + (idx >> 3), tn = hb * 8 + tq;
    const int tm2 = G2M ? tm : (xcd & 2) * 4 + tq, dt = G2M ? tq : (xcd & 4) + (idx >> 3), rot2 = tq + JR(idx >> 3);
    const unsigned lbase = (unsigned)(uintptr_t)shm;
    const int l15 = lane & 15, g4 = lane >> 4, lw = wid & 3, kg = wid >> 2;
    dma<U0 + 4096, false>(lbase + wid * 1024, lane * 16, mk_rsrc((const void*)(pc0 & ~255ull), CODE_KB * 1024), ((idx * 8 + wid) & 63) * 1024);
    const unsigned mk0 = lbase + POLL + lw * 256 + kg * (FULL - POLL + lw * (FST - 256));
    flag_set(mk0 + lane * 4, 1u);
    flag_set(lbase + HGO + lane * 4, 0u);
    if constexpr (NB) flag_set(lbase + DONE + lw * FST + lane * 4, 1u);
    unsigned vo[4];
    const bool xw = lw < 2;
#pragma unroll
    for (int i = 0; i < 4; ++i) {
        const int v = l15 ^ (i * 4 + g4);
        vo[i] = ((xw ? 64 * (i & 1) + 4 * (i >> 1) + (lane & 32) + 2 * (g4 & 1) : 8 * (i & 1) + 2 * g4) + (v >> 3)) * 2048 + (v & 7) * 16 - (i & 1) * 1024;
    }
    const int bw = lw >> 1;
    unsigned r0 = (tm * 128 + lw * 16 + bw * (tn * 64 + lw * 16 - 64 - tm * 128)) * 2048, rsz = 229376 - bw * 163840;
    unsigned rot = 8 * ((tq + (idx >> 3)) & 1) + 2 * (xcd >> 1) + 4 * hb, rst = 16384 << bw, lb = lbase + lw * 8192, oob = OOB;
    unsigned lb0 = lb + g1_buf(0) + kg * (g1_buf(1) - g1_buf(0)), so0 = ((kg + rot) & 15) * 128;
    unsigned co = l15 * 2 + (tn * 128 + lw * 32 + kg * 2048);
    tie(vo[0]); tie(vo[1]); tie(vo[2]); tie(vo[3]); tie(oob); tie(co);
    ties(r0); ties(rsz); ties(rot); ties(rst); ties(lb); ties(lb0); ties(so0);
    sched();
    asm volatile("" :: "s"(x), "s"(bre), "s"(bim), "s"(lre), "s"(lim), "s"(logdt), "s"(cre), "s"(cim), "s"(dskip), "s"(ssmk), "s"(ssmD), "s"(qin),
                       "s"(kin), "s"(vin), "s"(sk), "s"(ws), "s"(out));
    const u32x4 rs0 = mk_rsrc((const char*)(xw ? x : bre) + r0, rsz), rs1 = mk_rsrc((const char*)(xw ? x : bim) + r0 - 2048, rsz + 2048);
    unsigned c_dt, c_re, c_im;
    gld<0>(c_dt, co, mk_rsrc(logdt, 2048), 0);
    gld<0>(c_re, co, mk_rsrc(lre, 2048), 0);
    gld<0>(c_im, co, mk_rsrc(lim, 2048), 0);
    static_for<0, 8>([&](auto ic) { constexpr int i = CV(ic); dma4<(i & 4) * 1024, i & 3>(lb0, vo[i & 3], (i & 2) ? rs1 : rs0, so0 + (i >> 2) * rst); });
    wait_l<0>();
    barrier();
    dma1<0>(mk0 + kg * 120, oob, rs0, 0);
    sched();
    const unsigned a00 = (l15 >> 1) * 256 + ((((l15 & 1) * 8 + g4) ^ (l15 >> 1)) << 4);
    auto frag = [&](int par, int ks) { return (a00 ^ (ks * 64) ^ (par * 128)) + par * 2048; };
    bf16x8 cf[32];
    unsigned vc[4], cad[4];
    auto c_setup = [&]() {
#pragma unroll
        for (int i = 0; i < 4; ++i) { const int v = l15 ^ (i * 4 + g4); vc[i] = (4 * (2 * g4 + (v >> 3)) + 2 * (i & 1) + (i >> 1)) * 2048 + (v & 7) * 16 - i * 1024; }
    };
    const u32x4 rsc = mk_rsrc((kg ? cim : cre) + (size_t)dt * 131072, 262144);
    auto c_off = [&](int dg, int j) { return (unsigned)(dg * 65536 + hb * 1024 + ((rot2 + j) & 7) * 128); };
    const unsigned cst = lbase + (kg ? BUF : 2 * BUF) + lw * 8192;
    unsigned tag;
    const u32x4 rsf = mk_rsrc(ws + WS_F, 65536);
    const __amdgpu_buffer_rsrc_t rf = mk_brs(ws + WS_F, 65536);
    const u32x4 rsh = mk_rsrc(ws + WS_H + (size_t)(tm2 * 16 + hb * 8) * 32768, 262144);
    const int qx = (hb ? 0 : 2) ^ JQ(idx >> 3), wid_ = wid, b_ = b;

    if (wid >= 4) {
        page<-1>();
        const int ch = xcd * 128 + (idx >> 2) * 16 + lw * 4 + (idx & 3);
        unsigned dw; u32x4 ad;
        auto c_unit = [&](auto uc) { static_for<0, 4>([&](auto ic) { constexpr int u = CV(uc), i = CV(ic); dma4<(u & 1) * 4096, i>(cst, vc[i], rsc, c_off(lw, u)); }); };
        constexpr int XV = 4 * (EC == 1) + 8 * (PFQ == 2);
        auto ctouch = [&](int r) { const unsigned j = (fresh(idx * 4 + lw) + r * 37) & 127; stouch((pc0 & ~255ull) + (j < CODE_KB * 8 / 3 ? j : j - 64) * 384); };
        {
            const unsigned fb = lbase + FULL + lw * FST;
            auto piece = [&](auto sc, auto ic) {
                constexpr int s = CV(sc), i = CV(ic);
                dma4<g1_buf(s) + (i & 4) * 1024, i & 3>(lb, vo[i & 3], (i & 2) ? rs1 : rs0, ((s + rot) & 15) * 128 + (i >> 2) * rst);
            };
            auto mark = [&](auto sc) { dma1<(31 - CV(sc)) * 4>(fb, oob, rs0, 0); };
            auto feed = [&](auto kc, auto ic) {
                constexpr int k = CV(kc), i = CV(ic);
                if constexpr (i < 4) piece(IC(k + 3), IC(i + 4));
                if constexpr (i == 3) mark(IC(k + 3));
                if constexpr (i >= 4 && k < 12) piece(IC(k + 4), IC(i - 4));
            };
            __builtin_amdgcn_s_setprio(HP);
            TS(7);
            sld(tag, ws + WS_F + F_EP);
            sld(dw, ssmD + (ch & ~1));
            const int sb = idx * 2 + (lw >> 1), kh = lw & 1;
            u32x2 kk[11], vv[8]; u32x4 w4[2], kd[4], ke[4], kvo[4];
            const unsigned ko = g4 * 16384 + l15 * 8, so = sb * 65536 + xcd * 256 + kh * 128;
            {
                const u32x4 rs = mk_rsrc(kin, 4 << 20);
                gld<0>(w4[0], l15 * 32, mk_rsrc(sk, 8192), xcd * 1024 + kh * 512);
                gld<16>(w4[1], l15 * 32, mk_rsrc(sk, 8192), xcd * 1024 + kh * 512);
                static_for<0, 3>([&](auto ic) { gld<0>(kk[CV(ic)], ko + so - 6144 + CV(ic) * 2048, rs, 0); });
                static_for<0, 8>([&](auto ic) { constexpr int i = CV(ic); gld<(i & 1) * 2048>(kk[3 + i], ko, rs, so + (i >> 1) * 4096); });
                static_for<0, 8>([&](auto ic) { constexpr int i = CV(ic); gld<(i & 1) * 2048>(vv[i], ko, mk_rsrc(vin, 4 << 20), so + (i >> 1) * 4096); });
                dma1<DSK>(lbase, lane * 4, mk_rsrc(dskip, 2048), dt * 256);
                dma<U0, false>(lbase + kh * 1024, oob, rs0, 0);
            }
            static_for<4, 8>([&](auto ic) { feed(IC(-2), ic); });
            const __amdgpu_buffer_rsrc_t rk = mk_brs(ws + WS_KVT + (size_t)(xcd * 32 + kh * 16) * 16384, 262144);
            const unsigned kfo = F_K + ((xcd * 2 + (idx >> 4)) * 64 + lw * 16) * 8, K = lbase + KC + lw * KCH;
            float kf[7], pa[4], pb[4];
            auto kv_step = [&](auto uc, auto jc) {
                constexpr int u = CV(uc), j = CV(jc), c = u >> 1, h = u & 1;
                auto el = [&](const u32x2& q) -> float { return (c & 1) ? bfhi(q[c >> 1]) : bflo(q[c >> 1]); };
                auto tap = [&](int t) { const unsigned w = w4[c >> 1][(c & 1) * 2 + (t >> 1)]; return (t & 1) ? bfhi(w) : bflo(w); };
                auto all = [&](auto f) {
#pragma unroll
                    for (int i = 0; i < 4; ++i) f(i);
                };
                if constexpr (j == 0) {
#pragma unroll
                    for (int i = 0; i < 7; ++i) { kf[i] = (h && i < 3) ? kf[i + 4] : el(kk[4 * h + i]); tie(kf[i]); }
                }
                if constexpr (EXACT) {
                    if constexpr (j == 1) { const float t = tap(3); all([&](int i) { pa[i] = kf[i] * t; }); all([&](int i) { pa[i] = rbf(pa[i]); tie(pa[i]); }); }
                    if constexpr (j == 2 || j == 4 || j == 6) { const float t = tap(3 - j / 2); all([&](int i) { pb[i] = kf[i + j / 2] * t; }); all([&](int i) { pb[i] = rbf(pb[i]); tie(pb[i]); }); }
                    if constexpr (j == 3 || j == 5 || j == 7) { all([&](int i) { pa[i] += pb[i]; }); all([&](int i) { pa[i] = rbf(pa[i]); tie(pa[i]); }); }
                } else {
                    if constexpr (j == 1) { const float t = tap(3); all([&](int i) { pa[i] = kf[i] * t; tie(pa[i]); }); }
                    if constexpr (j >= 2 && j <= 4) { const float t = tap(4 - j); all([&](int i) { pa[i] = fma_(kf[i + j - 1], t, pa[i]); tie(pa[i]); }); }
                    if constexpr (j == 5) all([&](int i) { pa[i] = rbf(pa[i]); tie(pa[i]); });
                }
                if constexpr (j == (EXACT ? 7 : 6)) all([&](int i) { pb[i] = el(vv[4 * h + i]); tie(pb[i]); });
                if constexpr (j == 7) {
                    all([&](int i) { pa[i] *= pb[i]; });
                    kvo[c][3 - 2 * h] = pack2(pa[1], pa[0]); kvo[c][2 - 2 * h] = pack2(pa[3], pa[2]);
                    auto st = [&](int c) { __builtin_amdgcn_raw_buffer_store_b128(kvo[c], rk, (l15 >> 2) * 65536 + g4 * 64 + (l15 & 3) * 16, c * 16384 + sb * 256, 16); };
                    if constexpr (h && !KST) st(c);
                    else if constexpr (u == 7) all(st);
                    else tie(kvo[c]);
                }
            };
            static_for<-1, 13>([&](auto kc) {
                constexpr int k = CV(kc);
                if constexpr (k == 3) page<1>();
                if constexpr (k == 2) hop<0>();
                if constexpr (k == 6) hop<6>();
                if constexpr (k == 8) hop<9>();
                if constexpr (k >= 0) barrier();
                if constexpr (k >= 0 && ((CTM >> k) & 1)) ctouch(k);
                if constexpr (k == KV0) {
                    wait_v<4 + hv_cnt(-1, k)>();
                    static_for<0, 11>([&](auto ic) { tie(kk[CV(ic)]); });
                    static_for<0, 8>([&](auto ic) { tie(vv[CV(ic)]); });
                    tie(w4[0]); tie(w4[1]); keep(c_dt); keep(c_re); keep(c_im);
                    TS(8);
                }
                if constexpr (k == KFL) {
                    wait_l<0>();
                    ties(tag); ties(dw);
                    tag += 0x01010101u;
                    wait_v<hv_cnt(KVL + 1, k)>();
                    __builtin_amdgcn_raw_buffer_store_b64(u32x2{tag, ~tag}, rf, lane ? OOB : 0u, F_K + ((xcd * 2 + kh) * 64 + sb) * 8, 16);
                    TS(10);
                }
                if constexpr (k == KPL) dma1<POLL>(lbase + wid * 256, (lane & 31) * 4, rsf, kfo);
                if constexpr (k == 12) {
                    dma1<0>(K, oob, rs0, 0);
                    dma1<KCP>(K, oob, rs0, 0);
                }
                static_for<0, 8>([&](auto ic) {
                    constexpr int i = CV(ic), u0 = kv_first(k), nu = kv_first(k + 1) - u0;
                    feed(kc, ic);
                    if constexpr (k == 4 && i == 4) hop<3>();
                    if constexpr (nu == 1) kv_step(IC(u0), ic);
                    if constexpr (nu == 2) { kv_step(IC(u0 + i / 4), IC(2 * (i & 3))); kv_step(IC(u0 + i / 4), IC(2 * (i & 3) + 1)); }
                    if constexpr (!KDL && k == 12 && i >= 4) gld<(i - 4) * 1024>(kd[i - 4], lane * 16, mk_rsrc(ssmk, 4 << 20), ch * 4096);
                    if constexpr (k == 11 && i == 4) c_setup();
                    if constexpr (k == 10 && i >= 4) { cad[i - 4] = cst + frag(i & 1, (i >> 1) & 1); tie(cad[i - 4]); }
                    sched();
                });
            });
            page<12>();
            TS(1);
            if constexpr (EB == 2) barrier();
            unsigned pf;
            const unsigned fr = lbase + FULL + (lane & 3) * FST;
            wait_v<hv_cnt(KPL, 12) - 1 - (KPL == KFL) + (KDL ? 7 : 11)>();
            dsr32<POLL - 131072>(pf, lbase + 131072 + wid * 256 + lane * 4);
            wait_l<0>();
            tag_chk(pf, (lane & 1) ? ~tag : tag, (lane & 31) * 4, rsf, kfo);
            if constexpr (KDL) static_for<0, 4>([&](auto ic) { gld<CV(ic) * 1024>(kd[CV(ic)], lane * 16, mk_rsrc(ssmk, 4 << 20), ch * 4096); });
            auto b13 = [&]() { if constexpr (NB == 2) { unsigned f = 1; zero_chk<12, 1>(f, lbase + DONE + (lane & 3) * FST); } else if constexpr (NB < 2) barrier(); };
            if constexpr (EB == 1) b13();
            {
                const u32x4 rs = mk_rsrc(ws + WS_KVT + (size_t)(xcd * 32 + idx) * 16384, 16384);
                static_for<0, 4>([&](auto ic) { constexpr int i = CV(ic); dma<U0 + 2048 + i * 1024, true>(lbase + lw * 4096, (lane >> 2) * 64 + (((lane & 3) ^ g4) << 4), rs, lw * 4096 + i * 1024); });
            }
            mark(IC(16));
            auto warm = [&]() {
                const unsigned wo = g4 * 2048 + l15 * 16, dl = lbase + lw * 1024;
                const unsigned qo = (512 * (idx >> 3) + 128 * (idx & 1) + 32 * lw + 4 * ((idx >> 1) & 3)) * 2048 + xcd * 256, xo = (tm2 * 128 + 32 * lw + 8 * (qx & 2)) * 2048 + dt * 256;
                static_for<0, 4>([&](auto ic) { constexpr int i = CV(ic); dma<DUMP, false>(dl, wo, mk_rsrc(qin, 4 << 20), qo + ((i & 1) * 16 + (i >> 1) * 256) * 2048); });
                static_for<0, 4>([&](auto ic) { constexpr int i = CV(ic); dma<DUMP, false>(dl, wo, mk_rsrc(x, 4 << 20), xo + i * 8192); });
            };
            if constexpr (PFQ == 1) warm();
            if constexpr (EB == 0) b13();
            auto c_first = [&]() {
                if constexpr (CL <= 1) c_unit(IC(0));
                if constexpr (CL <= 0) c_unit(IC(1));
                if constexpr (EC == 1) static_for<0, 4>([&](auto ic) { constexpr int i = CV(ic); dma4<DUMP, i>(lbase, vc[i], mk_rsrc(cre + (size_t)dt * 131072, 262144), c_off(lw, 0)); });
                if constexpr (PFQ == 2) warm();
            };
            static_assert(NB != 3 || CL == 2, "");
            if constexpr (!KO) c_first();
            wait_v<5 + 8 * (PFQ == 1) + (KO ? 0 : 4 * (CL <= 1) + 4 * (CL <= 0) + XV)>();
            static_for<0, 4>([&](auto ic) { tie(kd[CV(ic)]); });
#pragma unroll
            for (int n = 0; n < 4; ++n) {
                const unsigned first = n ? __builtin_amdgcn_readlane(kd[n ? n - 1 : 0][3], 63) : 0u;
                const unsigned p = __builtin_amdgcn_update_dpp(first, kd[n][3], 0x138, 0xf, 0xf, false);
                ke[n] = u32x4{__builtin_amdgcn_alignbyte(kd[n][0], p, 2), __builtin_amdgcn_alignbyte(kd[n][1], kd[n][0], 2),
                              __builtin_amdgcn_alignbyte(kd[n][2], kd[n][1], 2), __builtin_amdgcn_alignbyte(kd[n][3], kd[n][2], 2)};
                tie(ke[n]);
            }
            if constexpr (ZM) {
                const unsigned dl = (ch & 1) ? dw >> 16 : dw & 0xffffu, dv = dl << ((~lane & 1) * 16);
                static_for<0, 4>([&](auto ic) { constexpr unsigned long long k = lag0_mask(CV(ic)); ad[CV(ic)] = sel0(dv, k); });
                tie(ad);
            }
            hop<14>();
            static_for<0, 4>([&](auto ic) { constexpr int i = CV(ic); dsw128<64 + i * 1024>(K + lane * 16, kd[i]); dsw128<KCP + 64 + i * 1024>(K + lane * 16, ke[i]); });
            if constexpr (KO) c_first();
            TS(9);
            static_for<0, -CL>([&](auto uc) {
                constexpr int u = CV(uc);
                wait_v<4 + (u < 2 ? XV : 0)>();
                static_for<0, 4>([&](auto ic) { constexpr int i = CV(ic); dsr128<(u & 1) * 4096>(cf[u * 4 + i], cad[i]); });
                wait_l<0>();
                static_for<0, 4>([&](auto ic) { tie(cf[u * 4 + CV(ic)]); });
                c_unit(IC(u + 2));
            });
            pf = 1;
            zero_chk<15 * 4>(pf, fr);
            if constexpr (PRE) { pf = 1; zero_chk<PRE == 1 ? 8 : 4>(pf, lbase + DONE + (lane & 3) * FST); }
            __builtin_amdgcn_s_setprio(0);
        }
        TS(2);
        TL(1);
        unsigned ka[4], ub[4];
#pragma unroll
        for (int i = 0; i < 4; ++i) {
            ka[i] = lbase + KC + lw * KCH + ((l15 & 1) ? 0 : KCP) + 84 + 2 * (l15 & 14) - 16 * g4 + i * 1024;
            const int e = 2 * l15 + g4 + 2;
            ub[i] = lbase + U0 + 64 * e + ((lw ^ (((e >> 2) + 3 - i) & 3)) << 4);
        }
        unsigned gad = lbase + DONE + lw * FST;
        if constexpr (GQ >= 0) tie(gad);
        f32x4 acc[8];
        bf16x4 FA[8][2]; bf16x8 FB[8];
        auto op = [&](auto gc) {
            constexpr Op o = cv_op(CV(gc));
            if constexpr (o.kind == CO_A) { constexpr int s = o.a >> 3, n = o.a & 7, d = (n & 1) * 128 + s * 16 + o.b * 2; dsr2<d, d + 1>(FA[n][o.b], ka[n >> 1]); }
            if constexpr (o.kind == CO_B) { constexpr int s = o.a >> 3, m = o.a & 7; dsr128<2048 * m + 1792 - 256 * s>(FB[m], ub[s & 3]); }
            if constexpr (o.kind == CO_C) dsr128<(o.a & 1) * 4096>(cf[o.a * 4 + o.b], cad[o.b]);
        };
        static_for<-36, 0>(op);
        static_for<0, 288>([&](auto gc) {
            constexpr int G = CV(gc), s = G / 36, q = G % 36, n = cv_n(q), m = cv_m(q);
            if constexpr (cv_wait(G) >= 0) wait_lr<cv_wait(G)>();
            if constexpr (q == 24 && s >= CL && s - CL < 8) static_for<0, 4>([&](auto ic) { tie(cf[(s - CL) * 4 + CV(ic)]); });
            if constexpr (ZM && G < 8) mfma<true>(acc[m], __builtin_bit_cast(bf16x8, ad), FB[m]);
            mfma<s == 0 && n == 0 && !ZM>(acc[n + m], cat(FA[n][0], FA[n][1]), FB[m]);
            sched();
            if constexpr (q == 11) TL(2 + 2 * s);
            if constexpr (q == 11 && s >= CL && s - CL < 8) wait_v<(s - CL < 7 ? 4 : 0) + (s - CL < (CL <= 1) + (CL <= 0) ? XV : 0) + 8 * (PFQ >= 3 && (s == PFQ - 2 || s == PFQ - 1))>();
            if constexpr (q == 11) TL(3 + 2 * s);
            op(gc);
            if constexpr (q == 30 && ((CTM >> (16 + s)) & 1)) ctouch(16 + s);
            if constexpr (G == 16) hop<17>();
            if constexpr (G == 66) hop<19>();
            if constexpr (G == PG1) page<21>();
            if constexpr (G == PG2) page<28>();
            if constexpr (G == 158) hop<23>();
            if constexpr (G == 209) hop<25>();
            if constexpr (G == 254) hop<27>();
            if constexpr (G == GQ) { unsigned f = 1; zero_chk<4, 1>(f, gad); }
            if constexpr (!NB && G == CBQ) barrier();
            if constexpr (NB == 3 && G == 23) { unsigned f = 1; zero_chk<8, 1>(f, lbase + DONE + (lane & 3) * FST); }
            if constexpr ((q == 24 || q == 25 || q == 29 || q == 34) && s + 2 - CL < 8) {
                constexpr int pc = q == 24 ? 0 : q == 25 ? 1 : q == 29 ? 2 : 3, u = s + 2 - CL;
                dma4<(u & 1) * 4096, pc>(cst, vc[pc], rsc, c_off(lw, u));
            }
            sched();
        });
        if constexpr (PG2 >= 288) page<28>();
        TL(18);
        TS(3);
        {
            const __amdgpu_buffer_rsrc_t rz = mk_brs(ws + WS_ZT, 4 << 20);
            if constexpr (ZM) {
                asm volatile("s_nop 7");
#pragma unroll
                for (int jj = 0; jj < 4; ++jj) {
                    tie(acc[2 * jj]); tie(acc[2 * jj + 1]);
                    const f32x4 a = acc[2 * jj], c = acc[2 * jj + 1];
                    __builtin_amdgcn_raw_buffer_store_b128(u32x4{pack2(a[0], a[1]), pack2(a[2], a[3]), pack2(c[0], c[1]), pack2(c[2], c[3])}, rz, lane * 16 + jj * 1024, ch * 4096, 16);
                }
            } else {
            u32x2 kvr[8];
            const unsigned kva = lbase + U0 + 2048 + 128 * l15 + 64 * (g4 >> 1) + ((lw ^ ((l15 >> 1) & 3)) << 4) + 8 * (1 - (g4 & 1));
            static_for<0, 8>([&](auto ic) { dsr64<CV(ic) * 2048>(kvr[CV(ic)], kva); });
            wait_l<0>();
            const float Dd = (ch & 1) ? bfhi(dw) : bflo(dw);
#pragma unroll
            for (int jj = 0; jj < 4; ++jj) {
                u32x4 o;
#pragma unroll
                for (int u = 0; u < 2; ++u) {
                    const int j = 2 * jj + u;
                    tie(kvr[j]); tie(acc[j]);
                    const float kv3 = bflo(kvr[j][0]), kv2 = bfhi(kvr[j][0]), kv1 = bflo(kvr[j][1]), kv0 = bfhi(kvr[j][1]);
                    o[2 * u] = pack2(rbf(acc[j][0]) + rbf(kv0 * Dd), rbf(acc[j][1]) + rbf(kv1 * Dd));
                    o[2 * u + 1] = pack2(rbf(acc[j][2]) + rbf(kv2 * Dd), rbf(acc[j][3]) + rbf(kv3 * Dd));
                }
                __builtin_amdgcn_raw_buffer_store_b128(o, rz, lane * 16 + jj * 1024, ch * 4096, 16);
                if (jj == 1) { sched(); hop<29>(); sched(); }
            }
            }
            TS(8);
            hop<30>();
            if constexpr (CL > 0) {
                wait_v<4>();
                static_for<32 - 4 * CL, 32>([&](auto ic) { constexpr int i = CV(ic); dsr128<((i >> 2) & 1) * 4096>(cf[i], cad[i & 3]); });
                wait_l<0>();
                static_for<32 - 4 * CL, 32>([&](auto ic) { tie(cf[CV(ic)]); });
            }
        }
        TL(19);
        TS(4);
    } else {
        f32x4 acc[8][2];
        cf32 L, cc, M, M2, MG, NG, N4, N8, ag;
        {
            unsigned aad[4], bad[4];
#pragma unroll
            for (int i = 0; i < 4; ++i) { aad[i] = lbase + frag(i & 1, i >> 1); bad[i] = aad[i] + 16384 + wid * 4096; }
            const unsigned fr = lbase + FULL + (lane & 3) * FST;
            sld(tag, ws + WS_F + F_EP);
            bf16x8 fa[4][2], fb[2][4];
            unsigned fl = 1, dz = 0, dw_ = lbase + DONE + wid * FST + lane * 4;
            tie(dz); tie(dw_);
            auto op = [&](auto gc) {
                constexpr int G = CV(gc);
                constexpr Op o = g1_op(G);
                if constexpr (o.kind == OP_A) {
                    constexpr int bo = g1_buf(o.a >> 3), hi = bo >= 65536 ? 65536 : 0, i = o.a & 7;
                    dsr128<bo - hi + (i >> 1) * 4096>(fa[i & 3][o.b], aad[(i & 1) + 2 * o.b] + hi);
                }
                if constexpr (o.kind == OP_B) { constexpr int bo = g1_buf(o.a), hi = bo >= 65536 ? 65536 : 0; dsr128<bo - hi>(fb[o.a & 1][o.b], bad[o.b] + hi); }
                if constexpr (o.kind == OP_FLAG) dsr32<(30 - (G >> 5)) * 4>(fl, fr);
                if constexpr (o.kind == OP_SIG) dsw32<o.a * 4>(dw_, dz);
            };
            auto consts = [&]() {
                float ld, ar, ai;
                asm volatile("s_waitcnt vmcnt(9)\n\tv_lshlrev_b32 %0, 16, %3\n\tv_lshlrev_b32 %1, 16, %4\n\tv_lshlrev_b32 %2, 16, %5"
                             : "=&v"(ld), "=&v"(ar), "=&v"(ai) : "v"(c_dt), "v"(c_re), "v"(c_im) : "memory");
                const float dtv = rbf(expf(ld)), ea = expf(dtv * ar);
                float sn, cs;
                sincosf(dtv * ai, &sn, &cs);
                L = cf32{ea * cs, ea * sn};
                M = L;
#pragma unroll
                for (int e = 0; e < 5; ++e) M = cmul(M, M);
                M2 = cmul(M, M);
                const cf32 N = cmul(M2, M2), N2 = cmul(N, N), one = {1.f, 0.f};
                N4 = cmul(N2, N2); N8 = cmul(N4, N4);
                auto sel = [](bool c, cf32 a, cf32 b) { return cf32{c ? a.r : b.r, c ? a.i : b.i}; };
                MG = cmul(sel(g4 & 1, M, one), sel(g4 & 2, M2, one));
                NG = cmul(sel(g4 & 1, N, one), sel(g4 & 2, N2, one));
                const float nr = L.r - 1.f, den = ar * ar + ai * ai;
                cc = cf32{2.f * ((nr * ar + L.i * ai) / den), 2.f * ((L.i * ar - nr * ai) / den)};
                auto tc = [&](cf32& v) { tie(v.r); tie(v.i); };
                tc(L); tc(cc); tc(M); tc(M2); tc(MG); tc(NG); tc(N4); tc(N8);
                sched();
            };
            if constexpr (CQ < 0) consts();
            __builtin_amdgcn_s_setprio(CP);
            zero_chk<0>(fl, lbase + POLL + (lane & 3) * 256);
            ties(tag);
            tag += 0x01010101u;
            TS(1);
            static_for<-12, 0>(op);
            static_for<0, 512>([&](auto gc) {
                constexpr int G = CV(gc), st = G >> 5, i = (G >> 2) & 7, j = G & 3;
                if constexpr ((G == 0 || j == 2) && G <= 506) { static_assert(g1_wait(G) >= 0, ""); wait_lr<G == 506 ? 0 : g1_wait(G)>(); }
                if constexpr (G == CQ) { asm volatile("s_waitcnt lgkmcnt(0)\n\ts_nop 7" ::: "memory"); consts(); }
                if constexpr (G == HQ) hop_all(wid);
                if constexpr ((G & 31) == 20 && st < 15) zero_chk<(30 - st) * 4>(fl, fr);
                if constexpr (((G & 31) == 30 && st < 13) || (NB < 2 && G == B13)) barrier();
                if constexpr ((G & 31) == 20) TL(st);
                mfma<st == 0 && j < 2>(acc[i][j & 1], fa[i & 3][j >> 1], fb[st & 1][j]);
                sched();
                op(gc);
                if constexpr (G == 31) page<-1>();
                if constexpr (G == 95) hop<2>();
                if constexpr (G == 159) hop<4>();
                if constexpr (G == 207) hop<5>();
                if constexpr (G == 255) page<7>();
                if constexpr (G == 319) hop<8>();
                if constexpr (G == 383) hop<10>();
                if constexpr (G == 431) hop<11>();
                if constexpr (G == 479) page<13>();
                if constexpr (G == 509) hop<15>();
                if constexpr (G == 489) hop<40>();
                if constexpr (G == 494) hop<41>();
                if constexpr (G == 499) hop<42>();
                if constexpr (G == 504) hop<43>();
                if constexpr (G >= 487) {
                    constexpr int k = G - 486;
                    if constexpr (k == 1) { asm volatile("s_nop 3"); ag = cf32{acc[0][0][0], acc[0][1][0]}; }
                    cf32 t;
                    asm volatile("v_fma_f32 %0, -%3, %5, %6\n\tv_fma_f32 %1, %3, %4, %7\n\tv_fmac_f32 %0, %2, %4\n\tv_fmac_f32 %1, %2, %5"
                                 : "=&v"(t.r), "=&v"(t.i) : "v"(L.r), "v"(L.i), "v"(ag.r), "v"(ag.i), "v"(acc[k >> 2][0][k & 3]), "v"(acc[k >> 2][1][k & 3]));
                    ag = t;
                }
                sched();
            });
        }
        __builtin_amdgcn_s_setprio(3);
        TL(16);
        TS(2);
#pragma unroll
        for (int k = 26; k < 32; ++k) ag = cfma(L, ag, cf32{acc[k >> 2][0][k & 3], acc[k >> 2][1][k & 3]});
        tie(ag.r); tie(ag.i); sched(); hop<44>(); sched();
        cf32 ev_, od, p01, p23;
        sw16(ag.r, ev_.r, od.r); sw16(ag.i, ev_.i, od.i);
        const cf32 pp = cfma(M, ev_, od);
        sw32(pp.r, p01.r, p23.r); sw32(pp.i, p01.i, p23.i);
        cf32 E = cfma(M2, p01, p23);

        const __amdgpu_buffer_rsrc_t rse = mk_brs(ws + WS_E + (size_t)(tn * 4 + wid) * 4096, 4096);
        __builtin_amdgcn_raw_buffer_store_b128(u32x4{f2u(E.r), f2u(E.i), tag, ~tag}, rse, g4 ? OOB : l15 * 16, tm * 256, 16);
        sched();
        TL(17);
        TS(3);
        hop<45>();
        sched();
        c_setup();
#pragma unroll
        for (int i = 0; i < 4; ++i) cad[i] = lb + frag(i & 1, i >> 1);
        auto c_read = [&](auto rc) { static_for<0, 8>([&](auto ic) { constexpr int r = CV(rc), i = CV(ic); dsr128<(i >> 2) * 4096>(cf[r * 8 + i], cad[i & 3] + ((r & 1) ? 0 : 2 * BUF)); }); };
        auto c_tie = [&](auto rc) { static_for<0, 8>([&](auto ic) { tie(cf[CV(rc) * 8 + CV(ic)]); }); };
        auto c_unit = [&](auto uc) { static_for<0, 4>([&](auto ic) { constexpr int u = CV(uc), i = CV(ic); dma4<((u & 2) ? 0 : 2 * BUF) + (u & 1) * 4096, i>(lb, vc[i], rsc, c_off(wid, u)); }); };
        auto c_req = [&](auto rc) { c_unit(IC(CV(rc) * 2)); c_unit(IC(CV(rc) * 2 + 1)); };
        const float c2r = cc.r, c2i = cc.i;
        auto scale = [&](int i) {
#pragma unroll
            for (int j = 0; j < 4; ++j) {
                const float br = acc[i][0][j], bi = acc[i][1][j];
                acc[i][0][j] = fma_(c2r, br, -(c2i * bi)); acc[i][1][j] = fma_(-c2r, bi, -(c2i * br));
            }
            tie(acc[i][0]); tie(acc[i][1]);
        };
        auto scales = [&](auto ac, auto bc) {
            static_for<CV(ac), CV(bc)>([&](auto ic) {
                constexpr int i = CV(ic);
                scale(i);
                if constexpr (i & 1) { sched(); hop<i == 1 ? 46 : i == 3 ? 16 : i == 5 ? 47 : 48>(); sched(); }
            });
        };
        constexpr int CA = CVR == 0 ? 0 : CVR == 4 ? 1 : 2;
        if constexpr (NB) {
            unsigned dn = 1;
            scales(IC(0), IC(DP));
            if constexpr (CA) { zero_chk<8>(dn, lbase + DONE + (lane & 3) * FST); static_for<0, CA>(c_unit); }
            if constexpr (SPR != 3) __builtin_amdgcn_s_setprio(SPR);
            sched();
            scales(IC(DP), IC(ELP));
            if constexpr (!CA) zero_chk<8>(dn, lbase + DONE + (lane & 3) * FST);
        } else {
            scales(IC(0), IC(ELP));
            barrier();
            static_for<0, CA>(c_unit);
        }
        TL(18);
        u32x4 ev[4]; unsigned eo[4]; unsigned long long none[4];
#pragma unroll
        for (int i = 0; i < 4; ++i) {
            const int t = tm - 1 - 4 * i - g4;
            eo[i] = t < 0 ? OOB : t * 256 + l15 * 16; none[i] = __builtin_amdgcn_ballot_w64(t < 0);
            asm volatile("buffer_load_dwordx4 %0, %1, %2, 0 offen sc1" : "=v"(ev[i]) : "v"(eo[i]), "s"(mk_rsrc(ws + WS_E + (size_t)(tn * 4 + wid) * 4096, 4096)) : "memory");
        }
        asm volatile("" ::: "memory");
        sched();
        constexpr int CB = CVR == 0 ? 1 : CVR == 4 ? 2 : 4;
        static_for<CA, CB>(c_unit);
        scales(IC(ELP), IC(8));
        cf32 X = cfma(cf32{g4 == 3 ? M.r : g4 == 2 ? 1.f : 0.f, g4 == 3 ? M.i : 0.f}, p01, cf32{(g4 & 1) ? ev_.r : 0.f, (g4 & 1) ? ev_.i : 0.f});
        tie(X.r); tie(X.i);
        sched();
        hop<24>();
        if constexpr (SPR != 3) __builtin_amdgcn_s_setprio(3);
        if constexpr (CVR == 2) { wait_v<12>(); c_read(IC(0)); wait_l<0>(); c_tie(IC(0)); c_req(IC(2)); }
        wait_v<4 * (CB - CA) + 8 * (CVR == 2)>();
        TL(19);
        tie(ev[0]); tie(ev[1]); tie(ev[2]); tie(ev[3]);
        {
            const unsigned long long tag64 = (unsigned long long)tag | ((unsigned long long)~tag << 32);
#pragma unroll 1
            for (unsigned spins = 0; spins < SPIN_CAP; ++spins) {
                unsigned long long ok = ~0ull;
#pragma unroll
                for (int i = 0; i < 4; ++i) ok &= __builtin_amdgcn_ballot_w64(((unsigned long long)ev[i][2] | ((unsigned long long)ev[i][3] << 32)) == tag64) | none[i];
                TSV(11, spins);
                if (ok == ~0ull) break;
                asm volatile("buffer_load_dwordx4 %0, %4, %8, 0 offen sc1\n\tbuffer_load_dwordx4 %1, %5, %8, 0 offen sc1\n\tbuffer_load_dwordx4 %2, %6, %8, 0 offen sc1\n\t"
                             "buffer_load_dwordx4 %3, %7, %8, 0 offen sc1\n\ts_waitcnt vmcnt(0)" : "=&v"(ev[0]), "=&v"(ev[1]), "=&v"(ev[2]), "=&v"(ev[3])
                             : "v"(eo[0]), "v"(eo[1]), "v"(eo[2]), "v"(eo[3]), "s"(mk_rsrc(ws + WS_E + (size_t)(tn * 4 + wid) * 4096, 4096)) : "memory");
            }
        }
        page<18>();
        cf32 h;
        {
            auto e = [&](int i) { return cf32{u2f(ev[i][0]), u2f(ev[i][1])}; };
            cf32 c = cmul(NG, cfma(N8, cfma(N4, e(3), e(2)), cfma(N4, e(1), e(0)))), u, v;
            sw16(c.r, u.r, v.r); sw16(c.i, u.i, v.i);
            sw32(u.r + v.r, u.r, v.r); sw32(u.i + v.i, u.i, v.i);
            c = cfma(MG, cf32{u.r + v.r, u.i + v.i}, X);
            h = cf32{fma_(c2r, c.r, -(c2i * c.i)), fma_(-c2r, c.i, -(c2i * c.r))};
        }
        TS(4);
        {
            const __amdgpu_buffer_rsrc_t rh = mk_brs(ws + WS_H + (size_t)(tm * 16 + tn) * 32768 + wid * 2048, 32768);
            const unsigned ho = g4 * 256 + ((l15 ^ (4 * (g4 & 1))) << 4);
            const cf32 Lc = {L.r, -L.i};
            static_for<0, 4>([&](auto qc) {
                constexpr int q = CV(qc);
                if constexpr (q >= CB) c_unit(qc);
                if constexpr (q == 2) hop<20>();
                u32x4 wr, wi;
#pragma unroll
                for (int u = 0; u < 4; ++u) {
                    const int i = 2 * q + (u >> 1), j = 2 * (u & 1);
                    const cf32 h0 = cfma(Lc, h, cf32{acc[i][0][j], acc[i][1][j]});
                    h = cfma(Lc, h0, cf32{acc[i][0][j + 1], acc[i][1][j + 1]});
                    wr[u] = pack2(h0.r, h.r); wi[u] = pack2(h0.i, h.i);
                }
                if constexpr ((PRE == 2 || GQ >= 0) && q == GE) { tie(wr); tie(wi); dsw32<4>(lbase + DONE + wid * FST + lane * 4, 0u); }
                __builtin_amdgcn_raw_buffer_store_b128(wr, rh, ho, q * 8192, 16);
                __builtin_amdgcn_raw_buffer_store_b128(wi, rh, ho + 1024, q * 8192, 16);
                sched();
            });
        }
        TS(10);
        auto flag = [&]() { __builtin_amdgcn_raw_buffer_store_b64(u32x2{tag, ~tag}, rf, lane ? OOB : 0u, F_H + ((tm * 16 + tn) * 4 + wid) * 8, 16); TS(5); };
        hop<22>();
        if constexpr (CVR == 2) {
            wait_v<16>();
            c_read(IC(1));
            wait_l<0>();
            c_req(IC(3));
            wait_v<8>();
            flag();
        } else {
            if constexpr (CVR == 1) wait_v<8>(); else { wait_v<0>(); flag(); }
            c_read(IC(0)); c_read(IC(1));
            wait_l<8>();
            c_req(IC(2));
            wait_l<0>();
            c_req(IC(3));
            if constexpr (CVR == 1) { wait_v<16>(); flag(); }
        }
        c_tie(IC(0)); c_tie(IC(1));
        static_for<16, 32>([&](auto ic) { undef(cf[CV(ic)]); });
    }
    {
        unsigned ln = lane, dsw_, pf;
        tie(ln);
        const int wid = fresh(wid_), lw = wid & 3, kg = wid >> 2, b = fresh(b_), xcd = b & 7, idx = b >> 3, tq = (idx & 7) + JR(idx >> 3);
        const int tm2 = G2M ? (xcd >> 1) * 4 + (idx >> 3) : (xcd & 2) * 4 + (idx & 7), dt = G2M ? idx & 7 : (xcd & 4) + (idx >> 3), qx = ((~b & 1) * 2) ^ JQ(idx >> 3);
        const int ta = 512 * (idx >> 3) + 128 * (idx & 1) + 16 * wid + 4 * ((idx >> 1) & 3);
        const int tb = tm2 * 128 + 64 * kg;
        dsr32<DSK - 131072>(dsw_, lbase + 131072 + lw * 64 + (ln & 7) * 8 + ((ln >> 1) & 4));
        unsigned wl = lbase + wid * 4096, w2k = lbase + wid * 2048, hs0 = (tm2 * 16 + (b & 1) * 8 + ((tq + (wid >> 1)) & 7)) * 32768 + (wid & 1) * 4096 + qx * 8192;
        unsigned s_x = (tb + qx * 8) * 2048 + dt * 256 + lw * 64, s_q = ta * 2048 + xcd * 256, s_z = WS_ZT + xcd * 524288 + idx * 128 + wid * 65536, zfo = WS_F + F_Z + xcd * 4096;
        unsigned ppo = WS_PP + b * 16384 + wid * 2048, s_a = ta * 8192 + xcd * 512, s_b = (tb + (2 ^ qx) * 8) * 8192 + 4096 + dt * 512 + lw * 128;
        const unsigned tagg = fresh(__builtin_amdgcn_readfirstlane(tag));
        u32x4 rsx = mk_rsrc(x, (NOX & 1) ? 0 : 4 << 20), rsq = mk_rsrc(qin, (NOX & 2) ? 0 : 4 << 20), rso = mk_rsrc(out, 16 << 20), rws = mk_rsrc(ws, 32 << 20);
        ties(wl); ties(w2k); ties(hs0); ties(s_x); ties(s_q); ties(s_z); ties(zfo); ties(ppo); ties(s_a); ties(s_b);
        ties(rsx); ties(rsq); ties(rso); ties(rws);
        const unsigned l31 = ln & 31, l5 = ln >> 5;
        unsigned l16 = ln * 16, ha[8];
        ha[0] = (lbase + kg * 1536 + l5 * 2048 + (ln & 2) * 128 + (ln & 1) * 8) | ((((ln >> 1) & 8) | ((ln >> 2) & 3) | ((ln & 2) << 1)) << 4);
        ha[1] = ha[0] ^ 64; ha[2] = ha[0] ^ 512; ha[3] = ha[0] ^ 576;
        static_for<0, 4>([&](auto ic) { ha[4 + CV(ic)] = ha[CV(ic)] + 65536; tie(ha[CV(ic)]); tie(ha[4 + CV(ic)]); });
        unsigned a_x = ((((ln >> 2) & 7) | (ln & 32)) << 11) | (l16 & 48), a_xr = (((l16 & 240) << 2) | (ln & 48)) + (w2k + ZST);
        unsigned a_q = ((ln & 48) << 7) | (l16 & 240), a_qr = (l5 << 10) + l31 * 8 + (w2k + QST), a_iw = w2k + IMG + l16, a_ir = a_iw ^ 8192;
        unsigned a_t = wid == 0 && ln == 0 ? idx * 128 : OOB, msk = 0xfff0fff0u;
        unsigned a_f = wid == 1 ? (ln >> 1) * 128 + (ln & 1) * 4 : OOB, expect = wid == 1 ? ((ln & 1) ? ~tagg : tagg) : 0u;
        unsigned a_z = (((ln ^ l5 ^ (wid * 4)) & 7) << 4) | ((ln >> 3) << 12), a_z2 = a_z ^ 32, a_zr = (l31 << 9) + (((((wid ^ ln) & 7) << 1) | l5) << 3) + (lbase + ZST);
        unsigned a_oa = (l5 << 21) | (l31 << 4), a_ob = ((((ln >> 2) & 4) | (ln & 32) | ((ln >> 3) & 1)) << 13) | (l16 & 112);
        u32x2 tg2 = {tagg, ~tagg};
        tie(l16); tie(a_x); tie(a_xr); tie(a_q); tie(a_qr); tie(a_iw); tie(a_ir); tie(a_t); tie(msk); tie(a_f); tie(expect); tie(a_z); tie(a_z2); tie(a_zr);
        tie(a_oa); tie(a_ob); tie(tg2);
        wait_l<0>();
        tie(dsw_);
        u32x4 dg[2];
        static_for<0, 4>([&](auto ic) { constexpr unsigned long long k = diag_mask(CV(ic)); dg[0][CV(ic)] = sel0(dsw_ & 0xffffu, k); dg[1][CV(ic)] = sel0(dsw_ & 0xffff0000u, k); });
        tie(dg[0]); tie(dg[1]);
        unsigned fso, xso, ppi = ppo ^ 16384, tn_[3] = {tagg & 0xf000fu, (tagg >> 8) & 0xf000fu, (tagg >> 4) & 0xf000fu}, psel = 0x05040100u;
        unsigned long long lo8 = 0x00ff00ff00ff00ffull, hi8 = ~lo8;
        ties(ppi); ties(tn_[0]); ties(tn_[1]); ties(tn_[2]); ties(psel); ties(lo8); ties(hi8);
        auto fill = [&](auto sc, auto pc) {
            constexpr int s = CV(sc), p = CV(pc);
            if constexpr (p == 0) dma_x<(2 * s % 3) * BUF, (s >> 1) * 8192 + (s & 1) * 131072, true>(fso, wl, l16, rws, hs0);
            else if constexpr (fill_m0(4 * s + p)) dma_m<(2 * s % 3) * BUF, p * 1024>(fso, wl, l16, rws);
            else dma_n<p * 1024>(fso, l16, rws);
        };
        bf16x4 hf[2][8];
        auto ldh = [&](auto nc, auto ic) {
            constexpr int n = CV(nc), i = CV(ic), j = (i >> 1) & 1, bo = (2 * (n >> 2) % 3) * BUF + (g2_unit(n, j) & 3) * 8192 + (i >> 2) * 4096, hi = bo >= 65536 ? 65536 : 0;
            dsr64tr<bo - hi>(hf[n & 1][i], ha[2 * g2_tile(n, j) + (i & 1) + (hi >> 14)]);
        };
        f32x4 yc[2][2][2];
        f32x4 im[2], ao, ob[2]; u32x4 pl[2], po;
        bf16x8 xf[2];
        u32x2 zv[4], qv[2];
        unsigned ck, ex;
        float tq_[2], tz, sm[8], mx[2], cv[8];
        auto finB = [&](auto pc, auto jc) {
            constexpr int p = CV(pc), j = CV(jc);
            if constexpr (j < 8) { yc[p & 1][0][j & 1][j >> 1] += im[j & 1][j >> 1]; tie(yc[p & 1][0][j & 1]); }
            else {
                constexpr int i = (j - 8) >> 2, k = j & 3, dd = k & 1, h = k >> 1;
                ob[i][k] = sel8(yc[p & 1][0][dd][2 * i + 1 - h], yc[p & 1][0][dd][2 * i + h], h ? hi8 : lo8);
                if constexpr (k == 3 && (p == 3 || !(NOST & 4))) gst<0>(ob[i], a_ob, rso, s_b ^ ((p & 1) * 65536 + i * 16384));
                if constexpr (j == 12) keep(ob[0]);
            }
        };
        auto snd = [&](auto pc, auto jc) {
            constexpr int p = CV(pc), j = CV(jc);
            if constexpr (j < 8) { sm[j] = yc[p][0][j >> 2][j & 3] + im[j >> 2][j & 3]; tie(sm[j]); }
            if constexpr (j == 8) mx[0] = amax3(sm[0], sm[1], sm[2]);
            if constexpr (j == 9) mx[1] = amax3(sm[3], sm[4], sm[5]);
            if constexpr (j == 10) mx[0] = amax3(mx[0], sm[6], sm[7]);
            if constexpr (j == 11) mx[0] = amax3(mx[0], mx[1], mx[1]);
            if constexpr (j == 12) { ex = f2u(mx[0]) + 0x4000u; tie(ex); }
            if constexpr (j == 13) { ex &= 0x7f800000u; tie(ex); }
            if constexpr (j == 14) { mx[0] = u2f(ex + 0x04c00008u); tie(mx[0]); }
            if constexpr (j == 15) { ex >>= 23; tie(ex); }
            if constexpr (j >= 16 && j < 24) { sm[j - 16] += mx[0]; tie(sm[j - 16]); }
            if constexpr (j == 24) { ex |= ex << 12; tie(ex); }
            if constexpr (j >= 25 && j < 29) { po[j - 25] = __builtin_amdgcn_perm(f2u(sm[2 * (j - 25) + 1]), f2u(sm[2 * (j - 25)]), psel); tie(po); }
            if constexpr (j == 29) { po[0] = bfi_v(msk, po[0], ex); tie(po); }
            if constexpr (j >= 30 && j < NS) { po[j - 29] = bfi_s(msk, po[j - 29], tn_[j - 30]); if constexpr (j < NS - 1) tie(po); }
            if constexpr (j == NS - 1 && !(NOST & 2)) gst<p * 1024>(po, l16, rws, ppo);
        };
        auto rcv = [&](auto hc, auto jc) {
            constexpr int h = CV(hc), j = CV(jc);
            if constexpr (j < 3) { pl[h][j + 1] ^= tn_[j]; pp_tie<h>(pl[h]); }
            if constexpr (j == 3) { ck = pl[h][1] | pl[h][2] | pl[h][3]; tie(ck); }
            if constexpr (j == 4) { ck &= 0xf000fu; tie(ck); }
            if constexpr (j == 6 && !(NOST & 2)) pp_chk<h>(pl[h], ck, tagg, l16, rws, ppi);
            if constexpr (j == 7) { ex = pl[h][0] >> 12; tie(ex); }
            if constexpr (j == 8) ex = bfi_v(msk, ex, pl[h][0]);
            if constexpr (j == 9) { pl[h][0] &= msk; pp_tie<h>(pl[h]); }
            if constexpr (j == 10) { ex <<= 23; tie(ex); }
            if constexpr (j == 11) { ex &= 0x7f800000u; tie(ex); }
            if constexpr (j == 12) { mx[1] = u2f(ex) * 0x1p-14f; tie(mx[1]); }
            if constexpr (h == 0 && j >= 13) {
                constexpr int k = j - 13, a = (k + 1) / 2, e = k == 15 ? 7 : k / 2 - 1;
                if constexpr (k == 0 || ((k & 1) && k < 15)) cv[a] = cvt16<a & 1>(pl[h][a >> 1]);
                else { yc[0][0][e >> 2][e & 3] = fma_(cv[e], mx[1], yc[0][0][e >> 2][e & 3]); tie(yc[0][0][e >> 2]); }
            }
            if constexpr (h == 1 && j >= 13 && j < 21) cv[j - 13] = cvt16<(j - 13) & 1>(pl[h][(j - 13) >> 1]);
            if constexpr (h == 1 && j >= 21) { constexpr int e = j - 21; im[e >> 2][e & 3] = fma_(cv[e], mx[1], im[e >> 2][e & 3]); tie(im[e >> 2]); }
        };
        auto side = [&](auto pc) {
            constexpr int P = CV(pc), n = P >> 3, m = P & 7, p = n >> 3;
            if constexpr (P == P_X || P == P_X + 2) dma_x<ZST + (P != P_X) * 1024, (P != P_X) * 16384, false>(xso, w2k, a_x, rsx, s_x);
            if constexpr (!NOA) {
            if constexpr (P == P_Q || P == P_Q + 2) dma_x<QST + (P != P_Q) * 1024, (P != P_Q) * 524288, false>(xso, w2k, a_q, rsq, s_q);
            if constexpr (P == P_T) gst<0>(tg2, a_t, rws, zfo);
            if constexpr (P == AT(ZP, 4)) pf_ld(pf, a_f, rws, zfo);
            if constexpr (P == P_ZC) { wait_v<g2_allow(P, AT(ZP, 4))>(); tag_chk(pf, expect, a_f, rws, zfo); }
            if constexpr (P == P_Z || P == P_Z + 2) dma_x<ZST + (P != P_Z) * 1024, (P != P_Z) * 32768, true>(xso, w2k, P == P_Z ? a_z : a_z2, rws, s_z);
            }
            if constexpr (P == AT(7, 5) || P == AT(7, 7)) dsr128<(m - 5) * 512>(xf[(m - 5) / 2], a_xr);
            if constexpr (P == AT(8, 4)) { tie(xf[0]); tie(xf[1]); }
            if constexpr (P == AT(8, 5) || P == AT(9, 3)) static_for<0, 2>([&](auto dc) { mfma<false>(yc[n & 1][0][CV(dc)], xf[n & 1], __builtin_bit_cast(bf16x8, dg[CV(dc)])); });
            if constexpr ((n & 7) == 6 && m == XW && !(DBG & 2)) dsw128<0>(a_iw, yc[p & 1][1][0]);
            if constexpr ((n & 7) == 6 && m == XW2 && !(DBG & 2)) dsw128<1024>(a_iw, yc[p & 1][1][1]);
            if constexpr ((n & 7) == 7 && (m & 5) == (n < 31 ? 4 : 0)) dsr128<(m & 2) * 512>(im[(m >> 1) & 1], a_ir);
            if constexpr (n >= 8 && (n & 7) == 0 && m == 4) { tie(im[0]); tie(im[1]); }
            if constexpr (P >= P_PP0 && P <= E_PP0) snd(IC(0), IC(P - P_PP0));
            if constexpr (P >= P_PP1 && P <= E_PP1) static_for<0, D1>([&](auto ic) { snd(IC(1), IC((P - P_PP1) * D1 + CV(ic))); });
            if constexpr (P == P_L0 || P == P_L1) pp_ld<P == P_L1>(pl[P == P_L1], l16, rws, ppi);
            if constexpr (P == P_R0 || P == P_R1) { wait_v<g2_allow(P, P == P_R0 ? imax(P_L0, XL ? P_X + 2 : 0) : P_L1)>(); pp_tie<P == P_R1>(pl[P == P_R1]); }
            if constexpr (P >= P_R0 && P <= E_R0) static_for<0, D0>([&](auto ic) { if constexpr ((P - P_R0) * D0 + CV(ic) < NR) rcv(IC(0), IC((P - P_R0) * D0 + CV(ic))); });
            if constexpr (P >= P_R1 && P < P_R1 + 21) rcv(IC(1), IC(P - P_R1));
            if constexpr (!NOA) {
            if constexpr (n == ZW && m >= 4) dsr64<(m - 4) * 128>(zv[m - 4], a_zr);
            static_for<0, 4>([&](auto rc) {
                constexpr int r = CV(rc);
                if constexpr (P == q_tie(r)) tie(qv[r & 1]);
                if constexpr (P == q_rd(r)) dsr64<r * 256>(qv[r & 1], a_qr);
            });
            if constexpr (P == AT(ZW + 1, 4)) { tie(zv[0]); tie(zv[1]); tie(zv[2]); tie(zv[3]); }
            }
            if constexpr (a_step(P) >= 0) {
                constexpr int s = a_step(P), e = s == 0 ? 0 : s % 3 == 2 ? (s + 1) / 3 : s % 3 == 1 ? s / 3 : s / 3 - 1, r = e >> 2, c = e & 3;
                if constexpr (s == 0 || (s % 3 == 2 && e < 16)) { tq_[e & 1] = (c & 1) ? bfhi(qv[r & 1][c >> 1]) : bflo(qv[r & 1][c >> 1]); tie(tq_[e & 1]); }
                else if constexpr (s % 3 == 1) { tz = (r & 1) ? bfhi(zv[c][r >> 1]) : bflo(zv[c][r >> 1]); tie(tz); }
                else if constexpr (s % 3 == 0) {
                    ao[c] = tq_[e & 1] * tz;
                    if constexpr (c == 3 && !(NOST & 1)) gst<0>(ao, a_oa, rso, s_a + r * 8192);
                    else tie(ao);
                }
            }
            if constexpr (P >= P_F2 && P < P_F2 + 16) finB(IC(2), IC(P - P_F2));
            if constexpr (a_step(P - 1) > 0 && a_step(P - 1) % 12 == 0) keep(ao);
            if constexpr (P == E_PP0 + 1 || P == E_PP1 + 1) keep(po);
            if constexpr (P == P_F2 + 16) keep(ob[1]);
        };
        {
            unsigned e_h = wid == 3 ? ((ln & 1) ? ~tagg : tagg) : 0u, hsl = lbase + lw * 8192, hso = (tm2 * 16 + (b & 1) * 8 + ((tq + wid) & 7)) * 32768 + qx * 8192;
            unsigned a_h = wid == 3 ? ln * 4 : OOB, s_h = WS_F + F_H + (tm2 * 16 + (b & 1) * 8) * 32, ca2[4], a_g = lbase + HGO + ln * 4;
            static_for<0, 4>([&](auto ic) { ca2[CV(ic)] = cad[CV(ic)] + 2 * BUF; tie(ca2[CV(ic)]); });
            tie(e_h); tie(a_h); tie(a_g); ties(hsl); ties(hso); ties(s_h);
            sched();
            if (wid < 4) {
                if constexpr (PW == 0) { pf_ld(pf, a_h, rws, s_h); wait_v<1>(); }
                if constexpr (PW == 1) { wait_v<0>(); pf_ld(pf, a_h, rws, s_h); }
                if constexpr (PW == 2) { wait_v<8>(); pf_ld(pf, a_h, rws, s_h); wait_v<1>(); }
                static_for<16, 32>([&](auto ic) { constexpr int i = CV(ic); dsr128u<((i >> 2) & 1) * 4096>(cf[i], (i & 8) ? cad[i & 3] : ca2[i & 3]); });
                wait_l<0>();
                static_for<16, 32>([&](auto ic) { tie(cf[CV(ic)]); });
                wait_v<0>();
                tag_chk<1>(pf, e_h, a_h, rws, s_h);
                if (wid == 3) flag_set(a_g, 1u); else gate<1>(a_g, 1);
                TS(6);
                hop<26>();
                static_for<0, 4>([&](auto ic) {
                    constexpr int i = CV(ic);
                    dma_x<(i >> 1) * 2 * BUF + (i & 1) * 4096, (i >> 1) * 131072 + (i & 1) * 4096, true>(fso, hsl, l16, rws, hso);
                    dma_n<1024>(fso, l16, rws); dma_n<2048>(fso, l16, rws); dma_n<3072>(fso, l16, rws);
                });
            }
        }
        wait_v<8>();
        if (wid < 4) TS(7);
        TG_DECL
        barrier();
        static_for<0, 8>([&](auto ic) { ldh(IC(0), ic); });
        static_for<0, 32>([&](auto nc) {
            constexpr int n = CV(nc), p = n >> 3;
            if constexpr (n == PG3) page<33>();
            if constexpr (n == PG4) page<37>();
            wait_lr<(n & 3) == 3 ? 0 : 4 + g2_lx(n - 1)>();
            if constexpr (n == 31) barrier();
            if constexpr ((n & 3) == 3 && n < 31) {
                constexpr int need = g2_need(n);
                TL(21 + n / 4 * 3);
                wait_v<g2_allow(8 * n, need)>();
                TL(22 + n / 4 * 3);
                barrier();
                TL(23 + n / 4 * 3);
            }
            if constexpr (n == 1) hop<31>();
            if constexpr (n == 4) hop<32>();
            if constexpr (n == 11) hop<34>();
            if constexpr (n == 15) hop<35>();
            if constexpr (n == 19) hop<36>();
            if constexpr (n == 27) hop<38>();
            if constexpr (n == 31) hop<39>();
            if constexpr (n == 0) TL(20);
            TG(20 + n);
            if constexpr (n == 31) TL(42);
            if constexpr (PRG) __builtin_amdgcn_s_setprio(n == 31 ? 0 : 3 - ((n + 1) & 3));
            sched();
            static_for<0, 8>([&](auto mc) {
                constexpr int m = CV(mc), ks = m >> 2, tt = (m >> 1) & 1, dd = m & 1, P = n * 8 + m;
                if constexpr (m == 4 && (n & 3) != 3) wait_lr<(rd1(n) ? 8 : 4) + ((n & 7) == 6 ? (XW < 4) + (XW2 < 4) : 0)>();
                mfma<(n & 7) == 0 && ks == 0>(yc[p & 1][(DBG & 1) ? tt : g2_tile(n, tt)][dd], cat(hf[n & 1][4 * ks + 2 * tt], hf[n & 1][4 * ks + 2 * tt + 1]), cf[g2_unit(n, tt) * 4 + 2 * ks + dd]);
                sched();
                if constexpr (n < 31 && rd1(n) && m < 4) { ldh(IC(n + 1), IC(2 * m)); ldh(IC(n + 1), IC(2 * m + 1)); }
                if constexpr (n < 31 && !rd1(n)) ldh(IC(n + 1), mc);
                if constexpr (g2_fill(P) >= 0) fill(IC(g2_fill(P) >> 2), IC(g2_fill(P) & 3));
                side(IC(P));
                sched();
            });
        });
        TG(52);
        if (wid >= 4) TS(5); else TS(8);
        TG(53);
        static_for<256 - P_R1, 21>([&](auto jc) { rcv(IC(1), jc); });
        wait_l<0>();
        tie(im[0]); tie(im[1]);
        static_for<21, NR>([&](auto jc) { rcv(IC(1), jc); });
        asm volatile("s_nop 3");
        sched();
        static_for<0, 16>([&](auto jc) { finB(IC(3), jc); });
        if (ppo == WS_PP + 255 * 16384) gst<0>(u32x2{tagg, tagg}, l16 ? OOB : 0u, rws, WS_F + F_EP);
    }
}

}

#if defined(__HIP_DEVICE_COMPILE__)
#pragma clang attribute pop
#endif

extern "C" void kernel_launch(void* const* d_in, const int* in_sizes, int n_in, void* d_out, int out_size, void* d_ws, size_t ws_size,
                              hipStream_t stream) {
    (void)in_sizes; (void)n_in; (void)out_size; (void)ws_size;
    using namespace fk;
    const uint16_t* q = (const uint16_t*)d_in[0];
    const uint16_t* kin = (const uint16_t*)d_in[1];
    const uint16_t* vin = (const uint16_t*)d_in[2];
    const uint16_t* sk = (const uint16_t*)d_in[3];
    const uint16_t* ssmk = (const uint16_t*)d_in[4];
    const uint16_t* ssmD = (const uint16_t*)d_in[5];
    const uint16_t* x = (const uint16_t*)d_in[6];
    const uint16_t* lre = (const uint16_t*)d_in[7];
    const uint16_t* lim = (const uint16_t*)d_in[8];
    const uint16_t* bre = (const uint16_t*)d_in[9];
    const uint16_t* bim = (const uint16_t*)d_in[10];
    const uint16_t* cre = (const uint16_t*)d_in[11];
    const uint16_t* cim = (const uint16_t*)d_in[12];
    const uint16_t* logdt = (const uint16_t*)d_in[13];
    const uint16_t* dskip = (const uint16_t*)d_in[14];
    char* ws = (char*)d_ws;
    hipLaunchKernelGGL(k1, dim3(256), dim3(512), 0, stream, x, bre, bim, lre, lim, logdt, cre, cim, dskip, ssmk, ssmD, q, kin, vin, sk, ws, (float*)d_out TS_PASS(0));
}
```

```cpp
#include <hip/hip_runtime.h>
#include <stdint.h>
#include <type_traits>

#if defined(__HIP_DEVICE_COMPILE__)
#pragma clang attribute push(__attribute__((target("no-packed-fp32-ops"))), apply_to = function)
#endif

namespace fk {

typedef __attribute__((ext_vector_type(8))) short bf16x8;
typedef __attribute__((ext_vector_type(4))) short bf16x4;
typedef __attribute__((ext_vector_type(4))) float f32x4;
typedef __attribute__((ext_vector_type(4))) unsigned u32x4;
typedef __attribute__((ext_vector_type(2))) unsigned u32x2;
typedef __attribute__((ext_vector_type(2))) __bf16 bf16x2_t;
typedef __attribute__((address_space(1))) unsigned gu32;
typedef __attribute__((address_space(1))) unsigned long long gu64;
#define RLX_AGENT __ATOMIC_RELAXED, __HIP_MEMORY_SCOPE_AGENT

__device__ __forceinline__ float u2f(unsigned v) { return __builtin_bit_cast(float, v); }
__device__ __forceinline__ unsigned f2u(float v) { return __builtin_bit_cast(unsigned, v); }
__device__ __forceinline__ bool wall(bool p) { return __builtin_amdgcn_ballot_w64(p) == ~0ull; }
__device__ __forceinline__ float bflo(unsigned w) { return u2f(w << 16); }
__device__ __forceinline__ float bfhi(unsigned w) { return u2f(w & 0xffff0000u); }
__device__ __forceinline__ unsigned pack2(float lo, float hi) {
    bf16x2_t v; v[0] = (__bf16)lo; v[1] = (__bf16)hi; return __builtin_bit_cast(unsigned, v);
}
__device__ __forceinline__ float rbf(float f) { float r; asm("v_cvt_pk_bf16_f32 %0, 0, %1" : "=v"(r) : "v"(f)); return r; }
__device__ __forceinline__ float fma_(float a, float b, float c) { return __builtin_fmaf(a, b, c); }

template <int I, int N, class F>
__device__ __forceinline__ void static_for(F&& f) {
    if constexpr (I + 1 == N) f(std::integral_constant<int, I>{});
    else if constexpr (I < N) { static_for<I, I + (N - I) / 2>(f); static_for<I + (N - I) / 2, N>(f); }
}
#define IC(v) std::integral_constant<int, (v)>{}
#define CV(c) decltype(c)::value

#define TS_ARG
#define TS_PASS(off)
#define TS_DECL
#define TS(i)
#define TSV(i, v)
#define TL_DECL
#define TL(i)
#define TS_FLUSH(slot)

#define TG_DECL
#define TG(i)
template <int N> __device__ __forceinline__ void wait_v() { asm volatile("s_waitcnt vmcnt(%0)" ::"n"(N > 63 ? 63 : N) : "memory"); }
template <int N> __device__ __forceinline__ void wait_l() { asm volatile("s_waitcnt lgkmcnt(%0)" ::"n"(N > 14 ? 14 : N) : "memory"); }
template <int N> __device__ __forceinline__ void wait_lr() { __builtin_amdgcn_s_waitcnt(0xc07f | ((N > 14 ? 14 : N) << 8)); }
template <class T> __device__ __forceinline__ void tie(T& x) { asm volatile("" : "+v"(x)); }
template <class T> __device__ __forceinline__ void undef(T& x) { asm volatile("" : "=v"(x)); }
template <int OFF, class T> __device__ __forceinline__ void dsr128u(T& d, unsigned a) { asm volatile("ds_read_b128 %0, %1 offset:%2" : "+v"(d) : "v"(a), "n"(OFF)); }
template <class T> __device__ __forceinline__ void keep(const T& x) { asm volatile("" :: "v"(x)); }
template <int OFF, class T> __device__ __forceinline__ void dsr128(T& d, unsigned a) { asm volatile("ds_read_b128 %0, %1 offset:%2" : "=v"(d) : "v"(a), "n"(OFF)); }
template <int OFF, class T> __device__ __forceinline__ void dsr64(T& d, unsigned a) { asm volatile("ds_read_b64 %0, %1 offset:%2" : "=v"(d) : "v"(a), "n"(OFF)); }
template <int OFF> __device__ __forceinline__ void dsr32(unsigned& d, unsigned a) { asm volatile("ds_read_b32 %0, %1 offset:%2" : "=v"(d) : "v"(a), "n"(OFF)); }
template <int O0, int O1, class T> __device__ __forceinline__ void dsr2(T& d, unsigned a) { asm volatile("ds_read2_b32 %0, %1 offset0:%2 offset1:%3" : "=v"(d) : "v"(a), "n"(O0), "n"(O1)); }
template <int OFF> __device__ __forceinline__ void dsr64tr(bf16x4& d, unsigned a) { asm volatile("ds_read_b64_tr_b16 %0, %1 offset:%2" : "=v"(d) : "v"(a), "n"(OFF)); }
template <int OFF, class T> __device__ __forceinline__ void dsw128(unsigned a, const T& v) { asm volatile("ds_write_b128 %0, %1 offset:%2" :: "v"(a), "v"(v), "n"(OFF) : "memory"); }
template <int OFF> __device__ __forceinline__ void dsw32(unsigned a, unsigned v) { asm volatile("ds_write_b32 %0, %1 offset:%2" :: "v"(a), "v"(v), "n"(OFF) : "memory"); }
__device__ __forceinline__ bf16x8 cat(const bf16x4& lo, const bf16x4& hi) { return __builtin_shufflevector(lo, hi, 0, 1, 2, 3, 4, 5, 6, 7); }
__device__ __forceinline__ void barrier() { asm volatile("" ::: "memory"); __builtin_amdgcn_s_barrier(); asm volatile("" ::: "memory"); }
__device__ __forceinline__ int fresh(int v) { asm volatile("" : "+s"(v)); return v; }
__device__ __forceinline__ void sched() { __builtin_amdgcn_sched_barrier(0); }
template <bool FIRST> __device__ __forceinline__ void mfma(f32x4& c, const bf16x8& a, const bf16x8& b) {
    if constexpr (FIRST) asm volatile("v_mfma_f32_16x16x32_bf16 %0, %1, %2, 0" : "=v"(c) : "v"(a), "v"(b));
    else asm volatile("v_mfma_f32_16x16x32_bf16 %0, %1, %2, %0" : "+v"(c) : "v"(a), "v"(b));
}
struct cf32 { float r, i; };
__device__ __forceinline__ cf32 cmul(cf32 a, cf32 b) { return cf32{fma_(a.r, b.r, -(a.i * b.i)), fma_(a.r, b.i, a.i * b.r)}; }
__device__ __forceinline__ cf32 cfma(cf32 a, cf32 b, cf32 c) { return cf32{fma_(a.r, b.r, fma_(-a.i, b.i, c.r)), fma_(a.r, b.i, fma_(a.i, b.r, c.i))}; }
__device__ __forceinline__ void sw16(float v, float& e, float& o) { const auto r = __builtin_amdgcn_permlane16_swap(f2u(v), f2u(v), false, false); e = u2f(r[0]); o = u2f(r[1]); }
__device__ __forceinline__ void sw32(float v, float& e, float& o) { const auto r = __builtin_amdgcn_permlane32_swap(f2u(v), f2u(v), false, false); e = u2f(r[0]); o = u2f(r[1]); }
constexpr unsigned SPIN_CAP = 1u << 16;
constexpr unsigned OOB = 0x40000000u;

constexpr size_t WS_H = 0;
constexpr size_t WS_KVT = (size_t)8 << 20;
constexpr size_t WS_ZT = (size_t)12 << 20;
constexpr size_t WS_PP = (size_t)16 << 20;
constexpr size_t WS_E = (size_t)24 << 20;
constexpr size_t WS_F = WS_E + (256 << 10);
constexpr int F_H = 0;
constexpr int F_K = 4096;
constexpr int F_Z = 16384;
constexpr int F_EP = F_Z + 32768;
constexpr int F_C = F_EP + 256;
constexpr unsigned WS_P = 25 << 20;
#define KCQ 2
#define KCC 4
#define KVP 0
#define RSL 1
#define KVS 1
#define GCR 273
constexpr int LDS_TOTAL = 163840, BUF = 32768;
constexpr int KC = 98304, KCH = 8320, KCP = 4160;
constexpr int U0 = KC + 4 * KCH;
constexpr int FULL = U0 + 18432 + 8192, FST = 388;
constexpr int DONE = U0 + 18432, DUMP = DONE + 4096;
constexpr int HGO = FULL + 1792, DSK = HGO + 256, POLL = DSK + 256;
static_assert(POLL + 8 * 256 <= LDS_TOTAL && 4 * FST <= 1792, "lds");
constexpr int g1_buf(int s) { return ((0x0213 >> ((s & 3) * 4)) & 3) * BUF; }
constexpr int IMG = KC, QST = KC + 16384, ZST = U0, PLD = 2 * BUF;
#define CODE_KB 47
#define EXACT 0
#define HP 1
#define CP 2
#define FLQ 11
#define R01 3
#define ZP 7
#define SLP 8
#define STR_(x) #x
#define STR(x) STR_(x)
#define NOST 0
#define G2M 0
#define RD 2
#define PRG 1
#define OSC 16
#define NOA 0
#define PPH 0
#define HS 0
#define JQ(j) (HS == 0 ? 0 : HS == 1 ? (j) & 1 : HS == 2 ? (j) : (j) & 2)
#define JR(j) (HS == 0 ? 2 * (j) : HS == 1 ? 2 * ((j) & 2) : HS == 2 ? 0 : 4 * ((j) & 1))
#define NOX 0

__device__ __forceinline__ u32x4 mk_rsrc(const void* p, unsigned bytes) {
    const unsigned long long a = (unsigned long long)p;
    return u32x4{(unsigned)a, (unsigned)(a >> 32) & 0xffffu, bytes, 0x00020000u};
}
__device__ __forceinline__ __amdgpu_buffer_rsrc_t mk_brs(const void* p, unsigned bytes) { return __builtin_amdgcn_make_buffer_rsrc((void*)p, (short)0, bytes, 0x00020000); }
__device__ __forceinline__ void st16(const u32x4& d, __amdgpu_buffer_rsrc_t r, unsigned voff, unsigned soff) {
    __builtin_amdgcn_raw_buffer_store_b128(d, r, voff, soff, 16);
    asm volatile("s_nop 1" :: "v"(d) : "memory");
}
template <int IMM, bool SC1> __device__ __forceinline__ void dma(unsigned lds, unsigned voff, const u32x4& rs, unsigned soff) {
    if constexpr (SC1) asm volatile("s_add_u32 m0, %0, %1\n\ts_nop 0\n\tbuffer_load_dwordx4 %2, %3, %4 offen sc1 lds" :: "s"(lds), "n"(IMM), "v"(voff), "s"(rs), "s"(soff) : "memory", "scc");
    else asm volatile("s_add_u32 m0, %0, %1\n\ts_nop 0\n\tbuffer_load_dwordx4 %2, %3, %4 offen lds" :: "s"(lds), "n"(IMM), "v"(voff), "s"(rs), "s"(soff) : "memory", "scc");
}
template <int IMM, int I> __device__ __forceinline__ void dma4(unsigned lds, unsigned voff, const u32x4& rs, unsigned soff) {
    if constexpr (I == 0) dma<IMM, false>(lds, voff, rs, soff);
    else asm volatile("buffer_load_dwordx4 %0, %1, %2 offen offset:%3 lds" :: "v"(voff), "s"(rs), "s"(soff), "n"(I * 1024) : "memory");
}
template <int IMM> __device__ __forceinline__ void dma1(unsigned lds, unsigned voff, const u32x4& rs, unsigned soff) {
    asm volatile("s_add_u32 m0, %0, %1\n\ts_nop 0\n\tbuffer_load_dword %2, %3, %4 offen sc1 lds" :: "s"(lds), "n"(IMM), "v"(voff), "s"(rs), "s"(soff) : "memory", "scc");
}
__device__ __forceinline__ void flag_set(unsigned waddr, unsigned v) { asm volatile("ds_write_b32 %0, %1" :: "v"(waddr), "v"(v) : "memory"); }
__device__ __forceinline__ void flag_chk(unsigned& f, unsigned raddr, unsigned need) {
    unsigned cnt;
    asm volatile("v_cmp_le_u32 vcc, %3, %0\n\t"
                 "s_cmp_eq_u64 vcc, -1\n\t"
                 "s_cbranch_scc1 1f\n\t"
                 "s_mov_b32 %1, 0x100000\n"
                 "0:\n\t"
                 "ds_read_b32 %0, %2\n\t"
                 "s_sub_u32 %1, %1, 1\n\t"
                 "s_waitcnt lgkmcnt(0)\n\t"
                 "s_cbranch_scc1 1f\n\t"
                 "v_cmp_le_u32 vcc, %3, %0\n\t"
                 "s_cmp_eq_u64 vcc, -1\n\t"
                 "s_cbranch_scc0 0b\n"
                 "1:" : "+v"(f), "=&s"(cnt) : "v"(raddr), "s"(need) : "vcc", "scc", "memory");
}
template <int OFF, int SL = 0> __device__ __forceinline__ void zero_chk(unsigned& f, unsigned raddr) {
    unsigned cnt;
    asm volatile("v_cmp_ne_u32 vcc, 0, %0\n\t"
                 "s_cbranch_vccz 1f\n\t"
                 "s_mov_b32 %1, 0x100000\n"
                 "0:\n\t"
                 ".if %4\n\ts_sleep %4\n\t.endif\n\t"
                 "ds_read_b32 %0, %2 offset:%3\n\t"
                 "s_sub_u32 %1, %1, 1\n\t"
                 "s_waitcnt lgkmcnt(0)\n\t"
                 "s_cbranch_scc1 1f\n\t"
                 "v_cmp_ne_u32 vcc, 0, %0\n\t"
                 "s_cbranch_vccnz 0b\n"
                 "1:" : "+v"(f), "=&s"(cnt) : "v"(raddr), "n"(OFF), "n"(SL) : "vcc", "scc", "memory");
}
template <int IMM, class T> __device__ __forceinline__ void gld(T& d, unsigned voff, const u32x4& rs, unsigned soff) {
    if constexpr (sizeof(T) == 16) asm volatile("buffer_load_dwordx4 %0, %1, %2, %3 offen offset:%4" : "=v"(d) : "v"(voff), "s"(rs), "s"(soff), "n"(IMM) : "memory");
    else if constexpr (sizeof(T) == 8) asm volatile("buffer_load_dwordx2 %0, %1, %2, %3 offen offset:%4" : "=v"(d) : "v"(voff), "s"(rs), "s"(soff), "n"(IMM) : "memory");
    else asm volatile("buffer_load_ushort %0, %1, %2, %3 offen offset:%4" : "=v"(d) : "v"(voff), "s"(rs), "s"(soff), "n"(IMM) : "memory");
}
__device__ __forceinline__ void gld32(unsigned& d, unsigned voff, const u32x4& rs, unsigned soff) { asm volatile("buffer_load_dword %0, %1, %2, %3 offen" : "=v"(d) : "v"(voff), "s"(rs), "s"(soff) : "memory"); }
__device__ __forceinline__ void sld(unsigned& d, const void* p) { asm volatile("s_load_dword %0, %1, 0x0" : "=s"(d) : "s"(p) : "memory"); }
__device__ __forceinline__ void stouch(unsigned long long p) { asm volatile("s_load_dword s101, %0, 0x0\n\ts_load_dword s101, %0, 0x80\n\ts_load_dword s101, %0, 0x100" :: "s"(p) : "s101", "memory"); }
template <class T> __device__ __forceinline__ void ties(T& x) { asm volatile("" : "+s"(x)); }
template <int IMM, class T> __device__ __forceinline__ void gst(const T& d, unsigned voff, const u32x4& rs, unsigned soff) {
    if constexpr (sizeof(T) == 16) asm volatile("buffer_store_dwordx4 %0, %1, %2, %3 offen offset:%4 sc1" :: "v"(d), "v"(voff), "s"(rs), "s"(soff), "n"(IMM) : "memory");
    else asm volatile("buffer_store_dwordx2 %0, %1, %2, %3 offen offset:%4 sc1" :: "v"(d), "v"(voff), "s"(rs), "s"(soff), "n"(IMM) : "memory");
}
template <int SLQ> __device__ __forceinline__ void gate(unsigned raddr, unsigned need) {
    unsigned cnt, f;
    asm volatile("s_mov_b32 %1, 0x40000\n"
                 "0:\n\t"
                 "ds_read_b32 %0, %2\n\t"
                 "s_sub_u32 %1, %1, 1\n\t"
                 "s_waitcnt lgkmcnt(0)\n\t"
                 "s_cbranch_scc1 1f\n\t"
                 "v_cmp_le_u32 vcc, %3, %0\n\t"
                 "s_cmp_eq_u64 vcc, -1\n\t"
                 "s_cbranch_scc1 1f\n\t"
                 "s_sleep %4\n\t"
                 "s_branch 0b\n"
                 "1:" : "=&v"(f), "=&s"(cnt) : "v"(raddr), "s"(need), "n"(SLQ) : "vcc", "scc", "memory");
}
template <int SL = SLP> __device__ __forceinline__ void tag_chk(unsigned& f, unsigned expect, unsigned voff, const u32x4& rs, unsigned soff) {
    unsigned cnt;
    asm volatile("v_cmp_eq_u32 vcc, %2, %0\n\t"
                 "s_cmp_eq_u64 vcc, -1\n\t"
                 "s_cbranch_scc1 1f\n\t"
                 "s_mov_b32 %1, 0x10000\n"
                 "0:\n\t"
                 "s_sleep %6\n\t"
                 "buffer_load_dword %0, %3, %4, %5 offen sc1\n\t"
                 "s_sub_u32 %1, %1, 1\n\t"
                 "s_waitcnt vmcnt(0)\n\t"
                 "s_cbranch_scc1 1f\n\t"
                 "v_cmp_eq_u32 vcc, %2, %0\n\t"
                 "s_cmp_eq_u64 vcc, -1\n\t"
                 "s_cbranch_scc0 0b\n"
                 "1:" : "+v"(f), "=&s"(cnt) : "v"(expect), "v"(voff), "s"(rs), "s"(soff), "n"(SL) : "vcc", "scc", "memory");
}

__device__ __forceinline__ unsigned race(unsigned w, const void* ws, unsigned long long rnd) {
    unsigned v;
    asm volatile("s_cmp_lt_u32 %1, 32\n\t"
                 "s_cbranch_scc0 1f\n\t"
                 "s_mov_b64 s[98:99], %3\n\t"
                 "s_lshl_b32 s96, s98, 7\n\t"
                 "s_lshl_b32 s97, %1, 13\n\t"
                 "s_andn2_b32 s96, s96, 0x2000\n\t"
                 "s_xor_b32 s96, s96, s97\n\t"
                 "s_and_b32 s96, s96, 0x7ff80\n\t"
                 "s_add_u32 s96, s96, %4\n\t"
                 "s_xor_b32 s97, s96, 0x2000\n\t"
                 "s_mov_b32 %0, s98\n\t"
                 "s_load_dword s99, %2, s96\n\t"
                 "s_load_dword s98, %2, s97\n\t"
                 "s_lshr_b32 s96, %1, 4\n\t"
                 "s_xor_b32 s96, s96, %1\n\t"
                 "s_waitcnt lgkmcnt(1)\n\t"
                 "s_sleep %5\n\t"
                 "s_cmp_lg_u32 %0, s98\n\t"
                 "s_addc_u32 %0, s96, 0\n\t"
                 "s_and_b32 %0, %0, 1\n"
                 "1:" : "=&s"(v) : "s"(w), "s"(ws), "s"(rnd), "n"(WS_P), "n"(RSL) : "s96", "s97", "s98", "s99", "scc", "memory");
    return v;
}
__device__ __forceinline__ void vote_chk(unsigned& f, unsigned expect, unsigned odd, unsigned raddr, unsigned lds, unsigned voff, const u32x4& rs, unsigned soff, unsigned& cnt) {
    unsigned t;
    asm volatile("s_mov_b32 %1, 0x10000\n\t"
                 "v_xor_b32 %2, %0, %3\n\t"
                 "v_cmp_gt_u32 vcc, %2, %4\n\t"
                 "s_cbranch_vccz 1f\n\t"
                 "s_mov_b32 m0, %6\n"
                 "0:\n\t"
                 "s_sleep 4\n\t"
                 "buffer_load_dword %7, %8, %9 offen sc1 lds\n\t"
                 "s_sub_u32 %1, %1, 1\n\t"
                 "s_waitcnt vmcnt(0)\n\t"
                 "ds_read_b32 %0, %5\n\t"
                 "s_waitcnt lgkmcnt(0)\n\t"
                 "s_cbranch_scc1 1f\n\t"
                 "v_xor_b32 %2, %0, %3\n\t"
                 "v_cmp_gt_u32 vcc, %2, %4\n\t"
                 "s_cbranch_vccnz 0b\n"
                 "1:" : "+v"(f), "=&s"(cnt), "=&v"(t) : "v"(expect), "v"(odd), "v"(raddr), "s"(lds), "v"(voff), "s"(rs), "s"(soff) : "vcc", "scc", "memory");
}
__device__ __forceinline__ unsigned vote_cnt(unsigned f, unsigned tag, unsigned up, unsigned& n) {
    unsigned r;
    asm volatile("s_xnor_b32 %0, %3, 1\n\tv_cmp_eq_u32 vcc, %0, %2\n\ts_bcnt1_i32_b64 %1, vcc\n\ts_cmp_gt_u32 %1, 16\n\ts_cselect_b32 %0, 0x2000, 0\n\ts_xor_b32 %0, %0, %4"
                 : "=&s"(r), "=&s"(n) : "v"(f), "s"(tag), "s"(up) : "vcc", "scc");
    return r;
}

template <int H> __device__ __forceinline__ void pp_ld(u32x4& d, unsigned voff, const u32x4& rs, unsigned soff) {
    if constexpr (H) asm volatile("buffer_load_dwordx4 v[244:247], %1, %2, %3 offen offset:1024 sc1" : "={v[244:247]}"(d) : "v"(voff), "s"(rs), "s"(soff) : "memory");
    else asm volatile("buffer_load_dwordx4 v[240:243], %1, %2, %3 offen sc1" : "={v[240:243]}"(d) : "v"(voff), "s"(rs), "s"(soff) : "memory");
}
__device__ __forceinline__ void pf_ld(unsigned& d, unsigned voff, const u32x4& rs, unsigned soff) { asm volatile("buffer_load_dword v248, %1, %2, %3 offen sc1" : "={v248}"(d) : "v"(voff), "s"(rs), "s"(soff) : "memory"); }
#define PP_CHK(R, R1, R2, R3, OFF) \
    asm volatile("v_cmp_ne_u32 vcc, 0, %2\n\t" \
                 "s_cbranch_vccz 1f\n\t" \
                 "s_mov_b32 %1, 0x10000\n" \
                 "0:\n\t" \
                 "buffer_load_dwordx4 " R ", %5, %6, %7 offen offset:" OFF " sc1\n\t" \
                 "s_waitcnt vmcnt(0)\n\t" \
                 "s_and_b32 %3, %4, 0xf000f\n\t" \
                 "v_xor_b32 " R1 ", %3, " R1 "\n\t" \
                 "s_lshr_b32 %3, %4, 8\n\t" \
                 "s_and_b32 %3, %3, 0xf000f\n\t" \
                 "v_xor_b32 " R2 ", %3, " R2 "\n\t" \
                 "s_lshr_b32 %3, %4, 4\n\t" \
                 "s_and_b32 %3, %3, 0xf000f\n\t" \
                 "v_xor_b32 " R3 ", %3, " R3 "\n\t" \
                 "v_or3_b32 %2, " R1 ", " R2 ", " R3 "\n\t" \
                 "v_and_b32 %2, 0xf000f, %2\n\t" \
                 "s_sub_u32 %1, %1, 1\n\t" \
                 "s_cbranch_scc1 1f\n\t" \
                 "v_cmp_ne_u32 vcc, 0, %2\n\t" \
                 "s_cbranch_vccnz 0b\n" \
                 "1:" : "+{" R "}"(p), "=&s"(cnt), "+v"(bad), "=&s"(t) : "s"(tag), "v"(voff), "s"(rs), "s"(soff) : "vcc", "scc", "memory")
template <int H> __device__ __forceinline__ void pp_chk(u32x4& p, unsigned bad, unsigned tag, unsigned voff, const u32x4& rs, unsigned soff) {
    unsigned cnt, t;
    if constexpr (H) PP_CHK("v[244:247]", "v245", "v246", "v247", "1024"); else PP_CHK("v[240:243]", "v241", "v242", "v243", "0");
}
template <int H> __device__ __forceinline__ void pp_tie(u32x4& p) { if constexpr (H) asm volatile("" : "+{v[244:247]}"(p)); else asm volatile("" : "+{v[240:243]}"(p)); }
__device__ __forceinline__ float amax3(float a, float b, float c) { float r; asm volatile("v_max3_f32 %0, |%1|, |%2|, |%3|" : "=v"(r) : "v"(a), "v"(b), "v"(c)); return r; }
__device__ __forceinline__ unsigned bfi_v(unsigned m, unsigned a, unsigned b) { unsigned r; asm volatile("v_bfi_b32 %0, %1, %2, %3" : "=v"(r) : "v"(m), "v"(a), "v"(b)); return r; }
__device__ __forceinline__ unsigned bfi_s(unsigned m, unsigned a, unsigned b) { unsigned r; asm volatile("v_bfi_b32 %0, %1, %2, %3" : "=v"(r) : "v"(m), "v"(a), "s"(b)); return r; }
template <int H> __device__ __forceinline__ float cvt16(unsigned w) {
    float r;
    if constexpr (H) asm volatile("v_cvt_f32_i32_sdwa %0, sext(%1) dst_sel:DWORD dst_unused:UNUSED_PAD src0_sel:WORD_1" : "=v"(r) : "v"(w));
    else asm volatile("v_cvt_f32_i32_sdwa %0, sext(%1) dst_sel:DWORD dst_unused:UNUSED_PAD src0_sel:WORD_0" : "=v"(r) : "v"(w));
    return r;
}
__device__ __forceinline__ float sel8(float a, float b, unsigned long long mask) {
    float r;
    asm volatile("s_mov_b64 vcc, %3\n\tv_cndmask_b32_dpp %0, %1, %2, vcc row_ror:8 row_mask:0xf bank_mask:0xf" : "=v"(r) : "v"(a), "v"(b), "s"(mask) : "vcc");
    return r;
}
__device__ __forceinline__ unsigned sel0(unsigned a, unsigned long long mask) { unsigned r; asm volatile("v_cndmask_b32_e64 %0, 0, %1, %2" : "=v"(r) : "v"(a), "s"(mask)); return r; }
constexpr unsigned long long diag_mask(int R) {
    unsigned long long m = 0;
    for (int l = 0; l < 64; ++l) if ((l >> 4) == ((l & 7) >> 1) && 2 * (l & 1) + ((l >> 3) & 1) == R) m |= 1ull << l;
    return m;
}
template <int IMM, int X, bool SC1> __device__ __forceinline__ void dma_x(unsigned& so, unsigned lds, unsigned voff, const u32x4& rs, unsigned base) {
    if constexpr (SC1) asm volatile("s_add_u32 m0, %1, %2\n\ts_xor_b32 %0, %5, %6\n\tbuffer_load_dwordx4 %3, %4, %0 offen sc1 lds" : "=&s"(so) : "s"(lds), "n"(IMM), "v"(voff), "s"(rs), "s"(base), "n"(X) : "memory", "scc");
    else asm volatile("s_add_u32 m0, %1, %2\n\ts_xor_b32 %0, %5, %6\n\tbuffer_load_dwordx4 %3, %4, %0 offen lds" : "=&s"(so) : "s"(lds), "n"(IMM), "v"(voff), "s"(rs), "s"(base), "n"(X) : "memory", "scc");
}
template <int OFF> __device__ __forceinline__ void dma_n(unsigned so, unsigned voff, const u32x4& rs) {
    asm volatile("buffer_load_dwordx4 %0, %1, %2 offen offset:%3 sc1 lds" :: "v"(voff), "s"(rs), "s"(so), "n"(OFF) : "memory");
}
template <int IMM, int OFF> __device__ __forceinline__ void dma_m(unsigned so, unsigned lds, unsigned voff, const u32x4& rs) {
    asm volatile("s_add_u32 m0, %4, %5\n\ts_nop 0\n\tbuffer_load_dwordx4 %0, %1, %2 offen offset:%3 sc1 lds" :: "v"(voff), "s"(rs), "s"(so), "n"(OFF), "s"(lds), "n"(IMM) : "memory", "scc");
}
__device__ __forceinline__ unsigned and_or(unsigned a, unsigned m, unsigned o) { unsigned r; asm("v_and_or_b32 %0, %1, %2, %3" : "=v"(r) : "v"(a), "v"(m), "s"(o)); return r; }

#define HOPS 1
#define HM 0x1ff221125B082ull
#define PG1 95
#define PG2 288
#define PG3 8
#define PG4 24
#define HQ 52
#define HW 4
constexpr int hop_nth(int n) { for (int k = 0; k < 62; ++k) if (((HM >> k) & 1) && n-- == 0) return k; return 63; }
constexpr int hop_next(int K) { int n = 0; for (int k = 0; k < K; ++k) n += (HM >> k) & 1; return hop_nth(n + HW); }
template <int K> __device__ __forceinline__ void hop() {
    if constexpr (HOPS && ((HM >> K) & 1)) asm volatile(".Lfkh%0:\n\ts_cbranch_execz .Lfkh%1" :: "n"(K), "n"(hop_next(K)));
}
template <int K> __device__ __forceinline__ void page() {
    if constexpr (HOPS && K >= 0 && ((HM >> (K & 63)) & 1)) asm volatile("s_branch 1f\n\t.p2align 12\n.Lfkh%0:\n\ts_cbranch_execz .Lfkh%1\n1:" :: "n"(K), "n"(hop_next(K)));
    else if constexpr (HOPS) asm volatile("s_branch 1f\n\t.p2align 12\n1:");
}
__device__ __forceinline__ void hop_all(int w) {
    if constexpr (HOPS) asm volatile("s_mov_b64 exec, 0\n\ts_cmp_eq_u32 %0, 0\n\ts_cbranch_scc1 .Lfkh%1\n\ts_cmp_eq_u32 %0, 1\n\ts_cbranch_scc1 .Lfkh%2\n\t"
                                     "s_cmp_eq_u32 %0, 2\n\ts_cbranch_scc1 .Lfkh%3\n\ts_branch .Lfkh%4\n.Lfkh63:\n\ts_mov_b64 exec, -1"
                                     :: "s"(w), "n"(hop_nth(0)), "n"(hop_nth(1)), "n"(hop_nth(2)), "n"(hop_nth(3)) : "scc");
}
#define NB 1
#define PFQ 0
#define CVR 3
#define SPR 3
#define ELP 6
#define PW 1
#define CPG 95
#define GQ -1
#define GE 3
#define KDL 0
#define GP 1
#define SG 507
#define KO 0
#define DP 0
#define B13 460
#define PRE 0
enum { OP_NONE, OP_A, OP_B, OP_FLAG, OP_SIG, OP_VOTE };
struct Op { int kind, a, b; };
constexpr Op g1_op(int G) {
    const int st = (G + 32) / 32 - 1, q = G - 32 * st, i = q >> 2, j = q & 3;
    if (i < 5) {
        if (st < 0) return {OP_NONE, 0, 0};
        if ((j & 1) == 0) return {OP_A, st * 8 + i + 3, j >> 1};
        if (q == FLQ && st < 15) return {OP_FLAG, 0, 0};
        if (NB && G == SG) return {OP_SIG, 2, 0};
        if (G == GCR) return {OP_VOTE, 0, 0};
        if (NB == 2 && G == 435) return {OP_SIG, 3, 0};
        return {OP_NONE, 0, 0};
    }
    if (st == 15) return NB && G == SG ? Op{OP_SIG, 2, 0} : Op{OP_NONE, 0, 0};
    const int n = (st + 1) * 8;
    switch (q) {
        case 20: return {OP_B, st + 1, 0}; case 21: return {OP_B, st + 1, 1}; case 22: return {OP_A, n, 0}; case 23: return {OP_B, st + 1, 2};
        case 24: return {OP_B, st + 1, 3}; case 25: return {OP_A, n, 1}; case 26: return {OP_A, n + 1, 0}; case 27: return {OP_A, n + 1, 1};
        case 28: return {OP_A, n + 2, 0}; case 30: return {OP_A, n + 2, 1};
    }
    return {OP_NONE, 0, 0};
}
constexpr int g1_need(int G) {
    const int st = G >> 5, j = G & 3;
    for (int h = G - 1; h >= -12; --h) {
        const Op o = g1_op(h);
        if (o.kind == OP_A && o.a == (G >> 2) && o.b == (j >> 1)) return h;
        if (o.kind == OP_B && o.a == st && o.b == j) return h;
        if (o.kind == OP_FLAG && (G & 31) == 20 && (h >> 5) == st) return h;
    }
    return -100;
}
constexpr int g1_wait(int G) {
    int n = -100, c = 0;
    for (int h = G; h < (G ? G + 4 : 2) && h < 512; ++h) if (g1_need(h) > n) n = g1_need(h);
    for (int h = n + 1; h < G; ++h) c += g1_op(h).kind != OP_NONE;
    return n < G ? c : -1;
}
#define KV0 0
#define KVN 0x11111112
constexpr int kv_first(int k) { int n = 0; for (int i = KV0; i < k && i < KV0 + 8; ++i) n += (KVN >> (4 * (i - KV0))) & 15; return n > 8 ? 8 : n; }
constexpr int kv_last() { int k = KV0; while (kv_first(k + 1) < 8) ++k; return k; }
#define CQ -1
#define CTM 0
#define EC 2
#define ED 2
#define EB 0
#define KST 0
#define KFD 3
#define KPL 11
#define KPI 0
#define KSL SLP
#define CBQ 20
constexpr int KVL = kv_last(), KFL = KVL + KFD, KSD = KVS ? KCC : -1;
static_assert(KFL <= KPL && KPL <= 12 && KPL > KVL && (KPI & 3) == 0 && KVP >= 0 && KVP < KCQ && KCQ < KCC && KSD <= KVL && KCC <= KPL && GCR > 32 * KCC + 62, "");
constexpr int hv_cnt(int k0, int k1) {
    int c = 0;
    for (int k = k0; k < k1; ++k) c += 9 + (KST ? 4 * (k == KVL) : k < KSD ? 0 : kv_first(k + 1) / 2 - (k == KSD ? 0 : kv_first(k) / 2)) + (k == KFL) + (k == KPL) + (k == KCQ) + (k == KVP);
    return c;
}
#define ZM 1
#define CL 1
#define EP 0
constexpr unsigned long long lag0_mask(int w) {
    unsigned long long m = 0;
    for (int l = 0; l < 64; ++l) if ((l >> 4) == 2 + ((l >> 3) & 1) && 3 - ((l & 7) >> 1) == w) m |= 1ull << l;
    return m;
}
constexpr int cv_start(int n) { return n * 8 - n * (n - 1) / 2; }
constexpr int cv_n(int q) { int n = 0; while (cv_start(n + 1) <= q) ++n; return n; }
constexpr int cv_m(int q) { return 7 - cv_n(q) - (q - cv_start(cv_n(q))); }
enum { CO_NONE, CO_A, CO_B, CO_C };
constexpr Op cv_op(int G) {
    const int s = (G + 36) / 36 - 1, q = G - 36 * s;
    if (q >= 1 && q <= 6) return s >= 0 ? Op{CO_A, s * 8 + 5 + (q - 1) / 2, (q - 1) & 1} : Op{CO_NONE, 0, 0};
    if (q >= 11 && q <= 14) return s >= 0 && s >= CL && s - CL < 8 ? Op{CO_C, s - CL, q - 11} : Op{CO_NONE, 0, 0};
    if (s == 7) return {CO_NONE, 0, 0};
    for (int m = 0; m < 8; ++m) if (q == cv_start(7 - m)) return {CO_B, (s + 1) * 8 + m, 0};
    for (int n = 0; n < 5; ++n) if (q == cv_start(n + 1) + 1 || q == cv_start(n + 1) + 2) return {CO_A, (s + 1) * 8 + n, q - cv_start(n + 1) - 1};
    return {CO_NONE, 0, 0};
}
constexpr int cv_need(int G) {
    const int s = G / 36, q = G % 36;
    for (int h = G - 1; h >= -36; --h) {
        const Op o = cv_op(h);
        if (o.kind == CO_A && o.a == s * 8 + cv_n(q)) return h;
        if (o.kind == CO_B && o.a == s * 8 + cv_m(q)) return h;
        if (o.kind == CO_C && q == 24 && o.a == s - CL) return h;
    }
    return -100;
}
constexpr int cv_wait(int G) {
    const int n = cv_need(G);
    for (int h = G - 1; h >= 0 && h >= G - 40; --h) if (cv_need(h) >= n) return -1;
    int c = 0;
    for (int h = n + 1; h < G; ++h) c += cv_op(h).kind != CO_NONE;
    return c;
}
#define AT(n, m) ((n) * 8 + (m))
#define FM 0x326260
constexpr int fill_pos(int s, int i) {
    const int f = (FM >> (4 * (s - 2))) & 15, n = s == 2 ? 0 : 4 * s - 9;
    switch (f) {
        case 1: return AT(n + i, 5) - (s == 2 && i == 3) * 6;
        case 2: return AT(n + 1 + i / 2, 5 + 2 * (i & 1));
        case 3: return i < 2 ? AT(n, 5 + 2 * i) : AT(n + i - 1, 5);
        case 4: return AT(n + 2 + i / 2, 5 + 2 * (i & 1));
        case 5: return AT(n, 1 + 2 * i) + (s == 2 ? 4 : 0);
        case 6: return AT(n + 3 + i / 2, 5 + 2 * (i & 1));
        case 7: return AT(n + 3, 5 + 2 * i);
        case 8: return i < 2 ? AT(n + 1, 5 + 2 * i) : AT(n + i, 5);
        case 9: return AT(n + 1 + i, 1);
        case 10: return i < 2 ? AT(n, 1 + 2 * i) : AT(n + 3, 1 + 2 * i);
    }
    return AT(n + i / 2, 5 + 2 * (i & 1));
}
constexpr int g2_fill(int p) {
    for (int k = 8; k < 32; ++k) if (fill_pos(k >> 2, k & 3) == p) return k;
    return -1;
}
#define QX AT(2, 5)
#define QQ AT(3, 2)
#define QZ AT(11, 2)
#define QT AT(3, 4)
constexpr int P_X = QX, P_Q = QQ, P_Z = QZ, P_T = QT;
constexpr bool XL = false;
constexpr bool fill_m0(int k) {
    const int a = fill_pos(k >> 2, (k & 3) - 1), b = fill_pos(k >> 2, k & 3);
    return (P_Q + 2 >= a && P_Q < b) || (P_Z + 2 >= a && P_Z < b) || (P_X + 2 >= a && P_X < b);
}
#define DBG 0
#define XW 4
#define XW2 4
#define QPP0 AT(9, 4)
#define QL0 AT(21, 1)
#define QR0 AT(24, 6)
#define QF2 AT(28, 3)
#define QL1 AT(26, 1)
#define QR1 AT(29, 3)
#define QA AT(16, 4)
#define AD 0
#define D1 2
#define D0 1
constexpr int NS = 33, NR = 29;
constexpr int P_PP0 = QPP0, P_PP1 = AT(16, 4), P_L0 = QL0, P_R0 = QR0, P_F2 = QF2, P_L1 = QL1, P_R1 = QR1, P_ZC = AT(10, 6), P_A = QA;
constexpr int E_PP0 = P_PP0 + NS - 1, E_PP1 = P_PP1 + (NS - 1) / D1, E_R0 = P_R0 + (NR - 1) / D0;
constexpr int ZW = P_A >= AT(20, 4) ? 19 : 15;
static_assert(P_R0 + 14 / D0 >= AT(24, 3) && E_R0 < P_F2 && P_F2 >= AT(24, 4) && P_F2 + 8 <= AT(31, 0) && E_R0 < P_R1, "");
static_assert(P_PP0 >= AT(9, 2) && P_PP0 + 8 <= AT(15, 4) && E_PP0 < P_PP1 && P_A >= AT(16, 4), "");
constexpr bool a_free(int p) {
    return p >= P_A && (AD || !((p >= P_PP1 && p <= E_PP1) || (p >= P_R0 && p <= E_R0) || (p >= P_F2 && p < P_F2 + 16)));
}
constexpr int a_step(int p) {
    if (NOA || !a_free(p)) return -1;
    int c = 0;
    for (int h = P_A; h < p; ++h) c += a_free(h);
    return c <= 48 ? c : -1;
}
constexpr int a_pos(int s) { int p = P_A; while (a_step(p) != s) ++p; return p; }
constexpr int q_rd(int r) { int p = r < 2 ? AT(ZW, 6) + r : a_pos(12 * r - 16) + 1; while ((p & 7) < 4) ++p; return p; }
constexpr int q_tie(int r) { const int n = (q_rd(r) >> 3) + 1; return AT(n, (n & 3) == 3 ? 0 : 4); }
static_assert(NOA || (a_pos(48) < 256 && q_tie(2) <= a_pos(23) && q_tie(3) <= a_pos(35)), "");
constexpr int g2_vm(int p) {
    int c = g2_fill(p) >= 0;
    c += a_step(p) > 0 && a_step(p) % 12 == 0 && !(NOST & 1);
    if (!NOA) c += (p == P_T) + (p == P_Q || p == P_Q + 2) + (p == AT(ZP, 4)) + (p == P_Z || p == P_Z + 2);
    c += p == P_X || p == P_X + 2;
    c += !(NOST & 2) && (p == E_PP0 || p == E_PP1);
    c += p == P_L0 || p == P_L1;
    c += !(NOST & 4) && (p == P_F2 + 11 || p == P_F2 + 15);
    return c;
}
constexpr int g2_lx(int n) {
    int c = 2 * (n == 7);
    if (n >= 0 && (n & 7) >= 6) c += 2;
    if (!NOA) { c += 4 * (n == ZW); for (int r = 0; r < 4; ++r) c += (q_rd(r) >> 3) == n; }
    return c;
}
constexpr int g2_tile(int n, int j) { return (n & 4) ? (n & 2) == 0 : j; }
constexpr int g2_unit(int n, int j) { return (n & 4) ? 4 + 2 * (n & 1) + j : n & 7; }
constexpr int imax(int a, int b) { return a > b ? a : b; }
constexpr int g2_need(int n) {
    int r = n == 3 ? -1 : fill_pos((n + 1) / 4, 3);
    if (n == 7 && !XL) r = imax(r, P_X + 2);
    if (n == ZW && !NOA) r = imax(r, imax(P_Q, P_Z) + 2);
    return r;
}
static_assert(P_Z > AT(11, 0) && P_T > AT(3, 0) && P_T < AT(ZP, 4) && (!XL || (P_X + 2 < P_R0 && P_R0 + 2 >= AT(24, 2) && P_R0 < AT(27, 0) && D0 == 1)), "");
constexpr bool rd1(int n) { return RD == 1 || (RD == 3 && (n & 3) == 2); }
constexpr int g2_allow(int e, int p) { int c = 0; for (int h = p + 1; h < e; ++h) c += g2_vm(h); return c; }

__global__ __launch_bounds__(512, 2) __attribute__((aligned(4096))) void k1(const uint16_t* __restrict__ x, const uint16_t* __restrict__ bre, const uint16_t* __restrict__ bim,
                                             const uint16_t* __restrict__ lre, const uint16_t* __restrict__ lim, const uint16_t* __restrict__ logdt,
                                             const uint16_t* __restrict__ cre, const uint16_t* __restrict__ cim, const uint16_t* __restrict__ dskip,
                                             const uint16_t* __restrict__ ssmk, const uint16_t* __restrict__ ssmD, const uint16_t* __restrict__ qin,
                                             const uint16_t* __restrict__ kin, const uint16_t* __restrict__ vin, const uint16_t* __restrict__ sk,
                                             char* __restrict__ ws, float* __restrict__ out TS_ARG) {
    unsigned long long pc0, rnd;
    asm volatile("s_getpc_b64 %0\n\ts_memtime %1" : "=s"(pc0), "=s"(rnd));
    TS_DECL TS(0);
    TL_DECL
    __shared__ __attribute__((aligned(1024))) char shm[LDS_TOTAL];
    const int tid = threadIdx.x, lane = tid & 63, wid = __builtin_amdgcn_readfirstlane(tid >> 6);
    const int b = blockIdx.x, xcd = b & 7, idx = b >> 3, hb = xcd & 1, tq = idx & 7;
    const int tm = (xcd >> 1) * 4 + (idx >> 3), tn = hb * 8 + tq;
    unsigned nr;
    const int tm2 = (xcd & 4) * 2 + tq, dt = (xcd & 2) * 2 + (idx >> 3), rot2 = tq + JR(idx >> 3);
    const unsigned lbase = (unsigned)(uintptr_t)shm;
    const int l15 = lane & 15, g4 = lane >> 4, lw = wid & 3, kg = wid >> 2;
    dma<U0 + 4096, false>(lbase + wid * 1024, lane * 16, mk_rsrc((const void*)(pc0 & ~255ull), CODE_KB * 1024), ((idx * 8 + wid) & 63) * 1024);
    const unsigned mk0 = lbase + POLL + lw * 256 + kg * (FULL - POLL + lw * (FST - 256));
    flag_set(mk0 + lane * 4, 1u);
    flag_set(lbase + HGO + lane * 4, 0u);
    if constexpr (NB) flag_set(lbase + DONE + lw * FST + lane * 4, 1u);
    unsigned vo[4];
    const bool xw = lw < 2;
#pragma unroll
    for (int i = 0; i < 4; ++i) {
        const int v = l15 ^ (i * 4 + g4);
        vo[i] = ((xw ? 64 * (i & 1) + 4 * (i >> 1) + (lane & 32) + 2 * (g4 & 1) : 8 * (i & 1) + 2 * g4) + (v >> 3)) * 2048 + (v & 7) * 16 - (i & 1) * 1024;
    }
    const int bw = lw >> 1;
    unsigned r0 = (tm * 128 + lw * 16 + bw * (tn * 64 + lw * 16 - 64 - tm * 128)) * 2048, rsz = 229376 - bw * 163840;
    unsigned rot = 8 * ((tq + (idx >> 3)) & 1) + 2 * (xcd >> 1) + 4 * hb, rst = 16384 << bw, lb = lbase + lw * 8192, oob = OOB;
    unsigned lb0 = lb + g1_buf(0) + kg * (g1_buf(1) - g1_buf(0)), so0 = ((kg + rot) & 15) * 128;
    unsigned co = l15 * 2 + (tn * 128 + lw * 32 + kg * 2048), vw = b * 4 + lw + 32 - 32 * kg;
    tie(vo[0]); tie(vo[1]); tie(vo[2]); tie(vo[3]); tie(oob); tie(co);
    ties(r0); ties(rsz); ties(rot); ties(rst); ties(lb); ties(lb0); ties(so0); ties(vw);
    sched();
    asm volatile("" :: "s"(x), "s"(bre), "s"(bim), "s"(lre), "s"(lim), "s"(logdt), "s"(cre), "s"(cim), "s"(dskip), "s"(ssmk), "s"(ssmD), "s"(qin),
                       "s"(kin), "s"(vin), "s"(sk), "s"(ws), "s"(out));
    const bool xv = fresh(bw) == 0;
    const u32x4 rs0 = mk_rsrc((const char*)(xv ? x : bre) + r0, rsz), rs1 = mk_rsrc((const char*)(xv ? x : bim) + r0 - 2048, rsz + 2048);
    unsigned c_dt, c_re, c_im;
    gld<0>(c_dt, co, mk_rsrc(logdt, 2048), 0);
    gld<0>(c_re, co, mk_rsrc(lre, 2048), 0);
    gld<0>(c_im, co, mk_rsrc(lim, 2048), 0);
    const unsigned vb = race(vw, ws, rnd);
    static_for<0, 8>([&](auto ic) { constexpr int i = CV(ic); dma4<(i & 4) * 1024, i & 3>(lb0, vo[i & 3], (i & 2) ? rs1 : rs0, so0 + (i >> 2) * rst); });
    wait_l<0>();
    barrier();
    dma1<0>(mk0 + kg * 120, oob, rs0, 0);
    sched();
    const unsigned a00 = (l15 >> 1) * 256 + ((((l15 & 1) * 8 + g4) ^ (l15 >> 1)) << 4);
    auto frag = [&](int par, int ks) { return (a00 ^ (ks * 64) ^ (par * 128)) + par * 2048; };
    bf16x8 cf[32];
    unsigned vc[4], cad[4];
    auto c_setup = [&]() {
#pragma unroll
        for (int i = 0; i < 4; ++i) { const int v = l15 ^ (i * 4 + g4); vc[i] = (4 * (2 * g4 + (v >> 3)) + 2 * (i & 1) + (i >> 1)) * 2048 + (v & 7) * 16 - i * 1024; }
    };
    const u32x4 rsc = mk_rsrc((kg ? cim : cre) + (size_t)dt * 131072, 262144);
    auto c_off = [&](int dg, int j) { return (unsigned)(dg * 65536 + hb * 1024 + ((rot2 + j) & 7) * 128); };
    const unsigned cst = lbase + (kg ? BUF : 2 * BUF) + lw * 8192;
    unsigned tag;
    const u32x4 rsf = mk_rsrc(ws + WS_F, 65536);
    const __amdgpu_buffer_rsrc_t rf = mk_brs(ws + WS_F, 65536);
    const int qx = (hb ? 0 : 2) ^ JQ(idx >> 3), wid_ = wid, b_ = b;

    if (wid >= 4) {
        page<-1>();
        const int ch = xcd * 128 + (idx >> 2) * 16 + lw * 4 + (idx & 3);
        unsigned dw, nx; u32x4 ad;
        auto c_unit = [&](auto uc) { static_for<0, 4>([&](auto ic) { constexpr int u = CV(uc), i = CV(ic); dma4<(u & 1) * 4096, i>(cst, vc[i], rsc, c_off(lw, u)); }); };
        constexpr int XV = 4 * (EC == 1) + 8 * (PFQ == 2);
        auto ctouch = [&](int r) { const unsigned j = (fresh(idx * 4 + lw) + r * 37) & 127; stouch((pc0 & ~255ull) + (j < CODE_KB * 8 / 3 ? j : j - 64) * 384); };
        {
            const unsigned fb = lbase + FULL + lw * FST;
            auto piece = [&](auto sc, auto ic) {
                constexpr int s = CV(sc), i = CV(ic);
                dma4<g1_buf(s) + (i & 4) * 1024, i & 3>(lb, vo[i & 3], (i & 2) ? rs1 : rs0, ((s + rot) & 15) * 128 + (i >> 2) * rst);
            };
            auto mark = [&](auto sc) { dma1<(31 - CV(sc)) * 4>(fb, oob, rs0, 0); };
            auto feed = [&](auto kc, auto ic) {
                constexpr int k = CV(kc), i = CV(ic);
                if constexpr (i < 4) piece(IC(k + 3), IC(i + 4));
                if constexpr (i == 3) mark(IC(k + 3));
                if constexpr (i >= 4 && k < 12) piece(IC(k + 4), IC(i - 4));
            };
            __builtin_amdgcn_s_setprio(HP);
            TS(7);
            sld(tag, ws + WS_F + F_EP);
            sld(dw, ssmD + (ch & ~1));
            const int sb = idx * 2 + (lw >> 1), kh = lw & 1;
            u32x2 kk[11], vv[8]; u32x4 w4[2], kd[4], ke[4], kvo[4];
            const unsigned ko = g4 * 16384 + l15 * 8, so = sb * 65536 + xcd * 256 + kh * 128;
            {
                const u32x4 rs = mk_rsrc(kin, 4 << 20);
                gld<0>(w4[0], l15 * 32, mk_rsrc(sk, 8192), xcd * 1024 + kh * 512);
                gld<16>(w4[1], l15 * 32, mk_rsrc(sk, 8192), xcd * 1024 + kh * 512);
                static_for<0, 3>([&](auto ic) { gld<0>(kk[CV(ic)], ko + so - 6144 + CV(ic) * 2048, rs, 0); });
                static_for<0, 8>([&](auto ic) { constexpr int i = CV(ic); gld<(i & 1) * 2048>(kk[3 + i], ko, rs, so + (i >> 1) * 4096); });
                static_for<0, 8>([&](auto ic) { constexpr int i = CV(ic); gld<(i & 1) * 2048>(vv[i], ko, mk_rsrc(vin, 4 << 20), so + (i >> 1) * 4096); });
                dma1<DSK>(lbase, lane * 4, mk_rsrc(dskip, 2048), dt * 256);
                dma<U0, false>(lbase + kh * 1024, oob, rs0, 0);
            }
            static_for<4, 8>([&](auto ic) { feed(IC(-2), ic); });
            const __amdgpu_buffer_rsrc_t rk = mk_brs(ws + WS_KVT + (size_t)((xcd & 3) * 32 + kh * 16) * 32768, 524288);
            const unsigned K = lbase + KC + lw * KCH;
            unsigned kfo;
            const unsigned n0 = (xcd & 4) << 11;
            auto st = [&](int c) { st16(kvo[c], rk, (l15 >> 2) * 131072 + g4 * 64 + (l15 & 3) * 16, c * 32768 + (sb >> 5) * 16384 + (KVS ? nx : n0 ^ ((sb & 32) << 8)) + (sb & 31) * 256); };
            float kf[7], pa[4], pb[4];
            auto kv_step = [&](auto uc, auto jc) {
                constexpr int u = CV(uc), j = CV(jc), c = u >> 1, h = u & 1;
                auto el = [&](const u32x2& q) -> float { return (c & 1) ? bfhi(q[c >> 1]) : bflo(q[c >> 1]); };
                auto tap = [&](int t) { const unsigned w = w4[c >> 1][(c & 1) * 2 + (t >> 1)]; return (t & 1) ? bfhi(w) : bflo(w); };
                auto all = [&](auto f) {
#pragma unroll
                    for (int i = 0; i < 4; ++i) f(i);
                };
                if constexpr (j == 0) {
#pragma unroll
                    for (int i = 0; i < 7; ++i) { kf[i] = (h && i < 3) ? kf[i + 4] : el(kk[4 * h + i]); tie(kf[i]); }
                }
                if constexpr (EXACT) {
                    if constexpr (j == 1) { const float t = tap(3); all([&](int i) { pa[i] = kf[i] * t; }); all([&](int i) { pa[i] = rbf(pa[i]); tie(pa[i]); }); }
                    if constexpr (j == 2 || j == 4 || j == 6) { const float t = tap(3 - j / 2); all([&](int i) { pb[i] = kf[i + j / 2] * t; }); all([&](int i) { pb[i] = rbf(pb[i]); tie(pb[i]); }); }
                    if constexpr (j == 3 || j == 5 || j == 7) { all([&](int i) { pa[i] += pb[i]; }); all([&](int i) { pa[i] = rbf(pa[i]); tie(pa[i]); }); }
                } else {
                    if constexpr (j == 1) { const float t = tap(3); all([&](int i) { pa[i] = kf[i] * t; tie(pa[i]); }); }
                    if constexpr (j >= 2 && j <= 4) { const float t = tap(4 - j); all([&](int i) { pa[i] = fma_(kf[i + j - 1], t, pa[i]); tie(pa[i]); }); }
                    if constexpr (j == 5) all([&](int i) { pa[i] = rbf(pa[i]); tie(pa[i]); });
                }
                if constexpr (j == (EXACT ? 7 : 6)) all([&](int i) { pb[i] = el(vv[4 * h + i]); tie(pb[i]); });
                if constexpr (j == 7) {
                    all([&](int i) { pa[i] *= pb[i]; });
                    kvo[c][3 - 2 * h] = pack2(pa[1], pa[0]); kvo[c][2 - 2 * h] = pack2(pa[3], pa[2]);
                    if constexpr (h && !KST && u >= kv_first(KSD)) st(c);
                    else if constexpr (u == 7) all(st);
                    else tie(kvo[c]);
                }
            };
            static_for<-1, 13>([&](auto kc) {
                constexpr int k = CV(kc);
                if constexpr (k == 3) page<1>();
                if constexpr (k == 2) hop<0>();
                if constexpr (k == 6) hop<6>();
                if constexpr (k == 8) hop<9>();
                if constexpr (k >= 0) barrier();
                if constexpr (k >= 0 && ((CTM >> k) & 1)) ctouch(k);
                if constexpr (k == KV0) {
                    wait_v<4 + hv_cnt(-1, k)>();
                    static_for<0, 11>([&](auto ic) { tie(kk[CV(ic)]); });
                    static_for<0, 8>([&](auto ic) { tie(vv[CV(ic)]); });
                    tie(w4[0]); tie(w4[1]); keep(c_dt); keep(c_re); keep(c_im);
                    TS(8);
                }
                if constexpr (k == KVP) {
                    wait_l<0>();
                    ties(tag); ties(dw);
                    tag += 0x01010101u;
                    __builtin_amdgcn_raw_buffer_store_b64(u32x2{tag, ~tag ^ vb}, rf, lane || vw >= 32 ? OOB : 0u, F_C + vw * 8, 16);
                }
                if constexpr (k == KCQ) dma1<POLL>(lbase + lw * 256, lane * 4, rsf, F_C);
                if constexpr (k == KCC) {
                    unsigned f, n, sp;
                    wait_v<hv_cnt(KCQ, k) - 1>();
                    dsr32<POLL - 131072>(f, lbase + 131072 + lw * 256 + lane * 4);
                    wait_l<0>();
                    vote_chk(f, (lane & 1) ? ~tag : tag, lane & 1, lbase + POLL + lw * 256 + lane * 4, lbase + POLL + lw * 256, lane * 4, rsf, F_C, sp);
                    nr = vote_cnt(f, tag, (xcd & 4) << 11, n);
                    nx = nr ^ ((xcd & 2) << 12);
                    kfo = F_K + (KVS ? nr : n0) + (((xcd & 3) * 2 + (idx >> 4)) * 64 + lw * 16) * 8;
                    TSV(11, n + 100 * (0x10000 - sp));
                }
                if constexpr (k == KFL) {
                    wait_v<hv_cnt(KVL + 1, k)>();
                    __builtin_amdgcn_raw_buffer_store_b64(u32x2{tag, ~tag}, rf, lane ? OOB : 0u, F_K + (KVS ? nr : n0) + (((xcd & 3) * 2 + kh) * 64 + sb) * 8, 16);
                    TS(10);
                }
                if constexpr (k == 12) {
                    dma1<0>(K, oob, rs0, 0);
                    dma1<KCP>(K, oob, rs0, 0);
                }
                static_for<0, 8>([&](auto ic) {
                    constexpr int i = CV(ic), u0 = kv_first(k), nu = kv_first(k + 1) - u0;
                    if constexpr (k == KPL && i == KPI) dma1<POLL>(lbase + wid * 256, (lane & 31) * 4, rsf, kfo);
                    feed(kc, ic);
                    if constexpr (k == 4 && i == 4) hop<3>();
                    if constexpr (!KST && k == KSD && (i & 1) && i / 2 < kv_first(KSD) / 2) st(i / 2);
                    if constexpr (nu == 1) kv_step(IC(u0), ic);
                    if constexpr (nu == 2) { kv_step(IC(u0 + i / 4), IC(2 * (i & 3))); kv_step(IC(u0 + i / 4), IC(2 * (i & 3) + 1)); }
                    if constexpr (!KDL && k == 12 && i >= 4) gld<(i - 4) * 1024>(kd[i - 4], lane * 16, mk_rsrc(ssmk, 4 << 20), ch * 4096);
                    if constexpr (k == 11 && i == 4) c_setup();
                    if constexpr (k == 10 && i >= 4) { cad[i - 4] = cst + frag(i & 1, (i >> 1) & 1); tie(cad[i - 4]); }
                    sched();
                });
            });
            page<12>();
            TS(1);
            if constexpr (EB == 2) barrier();
            unsigned pf;
            const unsigned fr = lbase + FULL + (lane & 3) * FST;
            wait_v<hv_cnt(KPL, 12) - (KPL < 12 ? 1 : 2) - (KPL == KFL) - KPI - (KPI > 3) + (KDL ? 7 : 11)>();
            dsr32<POLL - 131072>(pf, lbase + 131072 + wid * 256 + lane * 4);
            wait_l<0>();
            tag_chk<KSL>(pf, (lane & 1) ? ~tag : tag, (lane & 31) * 4, rsf, kfo);
            if constexpr (KDL) static_for<0, 4>([&](auto ic) { gld<CV(ic) * 1024>(kd[CV(ic)], lane * 16, mk_rsrc(ssmk, 4 << 20), ch * 4096); });
            auto b13 = [&]() { if constexpr (NB == 2) { unsigned f = 1; zero_chk<12, 1>(f, lbase + DONE + (lane & 3) * FST); } else if constexpr (NB < 2) barrier(); };
            if constexpr (EB == 1) b13();
            {
                const u32x4 rs = mk_rsrc(ws + WS_KVT + (size_t)((xcd & 3) * 32 + idx) * 32768, 32768);
                static_for<0, 4>([&](auto ic) { constexpr int i = CV(ic); dma<U0 + 2048 + i * 1024, true>(lbase + lw * 4096, (lane >> 2) * 64 + (((lane & 3) ^ g4) << 4), rs, (lw >> 1) * 16384 + (KVS ? nx : n0 ^ ((lw & 2) << 12)) + (lw & 1) * 4096 + i * 1024); });
            }
            mark(IC(16));
            auto warm = [&]() {
                const unsigned wo = g4 * 2048 + l15 * 16, dl = lbase + lw * 1024;
                const unsigned qo = (512 * (idx >> 3) + 128 * (idx & 1) + 32 * lw + 4 * ((idx >> 1) & 3)) * 2048 + xcd * 256, xo = (tm2 * 128 + 32 * lw + 8 * (qx & 2)) * 2048 + dt * 256;
                static_for<0, 4>([&](auto ic) { constexpr int i = CV(ic); dma<DUMP, false>(dl, wo, mk_rsrc(qin, 4 << 20), qo + ((i & 1) * 16 + (i >> 1) * 256) * 2048); });
                static_for<0, 4>([&](auto ic) { constexpr int i = CV(ic); dma<DUMP, false>(dl, wo, mk_rsrc(x, 4 << 20), xo + i * 8192); });
            };
            if constexpr (PFQ == 1) warm();
            if constexpr (EB == 0) b13();
            auto c_first = [&]() {
                if constexpr (CL <= 1) c_unit(IC(0));
                if constexpr (CL <= 0) c_unit(IC(1));
                if constexpr (EC == 1) static_for<0, 4>([&](auto ic) { constexpr int i = CV(ic); dma4<DUMP, i>(lbase, vc[i], mk_rsrc(cre + (size_t)dt * 131072, 262144), c_off(lw, 0)); });
                if constexpr (PFQ == 2) warm();
            };
            static_assert(NB != 3 || CL == 2, "");
            if constexpr (!KO) c_first();
            wait_v<5 + 8 * (PFQ == 1) + (KO ? 0 : 4 * (CL <= 1) + 4 * (CL <= 0) + XV)>();
            static_for<0, 4>([&](auto ic) { tie(kd[CV(ic)]); });
#pragma unroll
            for (int n = 0; n < 4; ++n) {
                const unsigned first = n ? __builtin_amdgcn_readlane(kd[n ? n - 1 : 0][3], 63) : 0u;
                const unsigned p = __builtin_amdgcn_update_dpp(first, kd[n][3], 0x138, 0xf, 0xf, false);
                ke[n] = u32x4{__builtin_amdgcn_alignbyte(kd[n][0], p, 2), __builtin_amdgcn_alignbyte(kd[n][1], kd[n][0], 2),
                              __builtin_amdgcn_alignbyte(kd[n][2], kd[n][1], 2), __builtin_amdgcn_alignbyte(kd[n][3], kd[n][2], 2)};
                tie(ke[n]);
            }
            if constexpr (ZM) {
                const unsigned dl = (ch & 1) ? dw >> 16 : dw & 0xffffu, dv = dl << ((~lane & 1) * 16);
                static_for<0, 4>([&](auto ic) { constexpr unsigned long long k = lag0_mask(CV(ic)); ad[CV(ic)] = sel0(dv, k); });
                tie(ad);
            }
            hop<14>();
            static_for<0, 4>([&](auto ic) { constexpr int i = CV(ic); dsw128<64 + i * 1024>(K + lane * 16, kd[i]); dsw128<KCP + 64 + i * 1024>(K + lane * 16, ke[i]); });
            if constexpr (KO) c_first();
            TS(9);
            static_for<0, -CL>([&](auto uc) {
                constexpr int u = CV(uc);
                wait_v<4 + (u < 2 ? XV : 0)>();
                static_for<0, 4>([&](auto ic) { constexpr int i = CV(ic); dsr128<(u & 1) * 4096>(cf[u * 4 + i], cad[i]); });
                wait_l<0>();
                static_for<0, 4>([&](auto ic) { tie(cf[u * 4 + CV(ic)]); });
                c_unit(IC(u + 2));
            });
            pf = 1;
            zero_chk<15 * 4>(pf, fr);
            if constexpr (PRE) { pf = 1; zero_chk<PRE == 1 ? 8 : 4>(pf, lbase + DONE + (lane & 3) * FST); }
            __builtin_amdgcn_s_setprio(0);
        }
        TS(2);
        TL(1);
        unsigned ka[4], ub[4];
#pragma unroll
        for (int i = 0; i < 4; ++i) {
            ka[i] = lbase + KC + lw * KCH + ((l15 & 1) ? 0 : KCP) + 84 + 2 * (l15 & 14) - 16 * g4 + i * 1024;
            const int e = 2 * l15 + g4 + 2;
            ub[i] = lbase + U0 + 64 * e + ((lw ^ (((e >> 2) + 3 - i) & 3)) << 4);
        }
        unsigned gad = lbase + DONE + lw * FST;
        if constexpr (GQ >= 0) tie(gad);
        f32x4 acc[8];
        bf16x4 FA[8][2]; bf16x8 FB[8];
        auto op = [&](auto gc) {
            constexpr Op o = cv_op(CV(gc));
            if constexpr (o.kind == CO_A) { constexpr int s = o.a >> 3, n = o.a & 7, d = (n & 1) * 128 + s * 16 + o.b * 2; dsr2<d, d + 1>(FA[n][o.b], ka[n >> 1]); }
            if constexpr (o.kind == CO_B) { constexpr int s = o.a >> 3, m = o.a & 7; dsr128<2048 * m + 1792 - 256 * s>(FB[m], ub[s & 3]); }
            if constexpr (o.kind == CO_C) dsr128<(o.a & 1) * 4096>(cf[o.a * 4 + o.b], cad[o.b]);
        };
        static_for<-36, 0>(op);
        static_for<0, 288>([&](auto gc) {
            constexpr int G = CV(gc), s = G / 36, q = G % 36, n = cv_n(q), m = cv_m(q);
            if constexpr (cv_wait(G) >= 0) wait_lr<cv_wait(G)>();
            if constexpr (q == 24 && s >= CL && s - CL < 8) static_for<0, 4>([&](auto ic) { tie(cf[(s - CL) * 4 + CV(ic)]); });
            if constexpr (ZM && G < 8) mfma<true>(acc[m], __builtin_bit_cast(bf16x8, ad), FB[m]);
            mfma<s == 0 && n == 0 && !ZM>(acc[n + m], cat(FA[n][0], FA[n][1]), FB[m]);
            sched();
            if constexpr (q == 11) TL(2 + 2 * s);
            if constexpr (q == 11 && s >= CL && s - CL < 8) wait_v<(s - CL < 7 ? 4 : 0) + (s - CL < (CL <= 1) + (CL <= 0) ? XV : 0) + 8 * (PFQ >= 3 && (s == PFQ - 2 || s == PFQ - 1))>();
            if constexpr (q == 11) TL(3 + 2 * s);
            op(gc);
            if constexpr (q == 30 && ((CTM >> (16 + s)) & 1)) ctouch(16 + s);
            if constexpr (G == 16) hop<17>();
            if constexpr (G == 66) hop<19>();
            if constexpr (G == PG1) page<21>();
            if constexpr (G == PG2) page<28>();
            if constexpr (G == 158) hop<23>();
            if constexpr (G == 209) hop<25>();
            if constexpr (G == 254) hop<27>();
            if constexpr (G == GQ) { unsigned f = 1; zero_chk<4, 1>(f, gad); }
            if constexpr (!NB && G == CBQ) barrier();
            if constexpr (NB == 3 && G == 23) { unsigned f = 1; zero_chk<8, 1>(f, lbase + DONE + (lane & 3) * FST); }
            if constexpr ((q == 24 || q == 25 || q == 29 || q == 34) && s + 2 - CL < 8) {
                constexpr int pc = q == 24 ? 0 : q == 25 ? 1 : q == 29 ? 2 : 3, u = s + 2 - CL;
                dma4<(u & 1) * 4096, pc>(cst, vc[pc], rsc, c_off(lw, u));
            }
            sched();
        });
        if constexpr (PG2 >= 288) page<28>();
        TL(18);
        TS(3);
        {
            const __amdgpu_buffer_rsrc_t rz = mk_brs(ws + WS_ZT, 4 << 20);
            if constexpr (ZM) {
                asm volatile("s_nop 7");
#pragma unroll
                for (int jj = 0; jj < 4; ++jj) {
                    tie(acc[2 * jj]); tie(acc[2 * jj + 1]);
                    const f32x4 a = acc[2 * jj], c = acc[2 * jj + 1];
                    st16(u32x4{pack2(a[0], a[1]), pack2(a[2], a[3]), pack2(c[0], c[1]), pack2(c[2], c[3])}, rz, lane * 16 + jj * 1024, (ch & 510) * 8192 + nx + (ch & 1) * 4096);
                }
            } else {
            u32x2 kvr[8];
            const unsigned kva = lbase + U0 + 2048 + 128 * l15 + 64 * (g4 >> 1) + ((lw ^ ((l15 >> 1) & 3)) << 4) + 8 * (1 - (g4 & 1));
            static_for<0, 8>([&](auto ic) { dsr64<CV(ic) * 2048>(kvr[CV(ic)], kva); });
            wait_l<0>();
            const float Dd = (ch & 1) ? bfhi(dw) : bflo(dw);
#pragma unroll
            for (int jj = 0; jj < 4; ++jj) {
                u32x4 o;
#pragma unroll
                for (int u = 0; u < 2; ++u) {
                    const int j = 2 * jj + u;
                    tie(kvr[j]); tie(acc[j]);
                    const float kv3 = bflo(kvr[j][0]), kv2 = bfhi(kvr[j][0]), kv1 = bflo(kvr[j][1]), kv0 = bfhi(kvr[j][1]);
                    o[2 * u] = pack2(rbf(acc[j][0]) + rbf(kv0 * Dd), rbf(acc[j][1]) + rbf(kv1 * Dd));
                    o[2 * u + 1] = pack2(rbf(acc[j][2]) + rbf(kv2 * Dd), rbf(acc[j][3]) + rbf(kv3 * Dd));
                }
                st16(o, rz, lane * 16 + jj * 1024, (ch & 510) * 8192 + nx + (ch & 1) * 4096);
                if (jj == 1) { sched(); hop<29>(); sched(); }
            }
            }
            TS(8);
            hop<30>();
            if constexpr (CL > 0) {
                wait_v<4>();
                static_for<32 - 4 * CL, 32>([&](auto ic) { constexpr int i = CV(ic); dsr128<((i >> 2) & 1) * 4096>(cf[i], cad[i & 3]); });
                wait_l<0>();
                static_for<32 - 4 * CL, 32>([&](auto ic) { tie(cf[CV(ic)]); });
            }
        }
        TL(19);
        TS(4);
    } else {
        f32x4 acc[8][2];
        cf32 L, cc, M, M2, MG, NG, N4, N8, ag;
        {
            unsigned aad[4], bad[4];
#pragma unroll
            for (int i = 0; i < 4; ++i) { aad[i] = lbase + frag(i & 1, i >> 1); bad[i] = aad[i] + 16384 + wid * 4096; }
            const unsigned fr = lbase + FULL + (lane & 3) * FST;
            sld(tag, ws + WS_F + F_EP);
            bf16x8 fa[4][2], fb[2][4];
            unsigned fl = 1, dz = 0, dw_ = lbase + DONE + wid * FST + lane * 4, vt = lbase + POLL + wid * 256 + lane * 4;
            tie(dz); tie(dw_); tie(vt);
            auto op = [&](auto gc) {
                constexpr int G = CV(gc);
                constexpr Op o = g1_op(G);
                if constexpr (o.kind == OP_A) {
                    constexpr int bo = g1_buf(o.a >> 3), hi = bo >= 65536 ? 65536 : 0, i = o.a & 7;
                    dsr128<bo - hi + (i >> 1) * 4096>(fa[i & 3][o.b], aad[(i & 1) + 2 * o.b] + hi);
                }
                if constexpr (o.kind == OP_B) { constexpr int bo = g1_buf(o.a), hi = bo >= 65536 ? 65536 : 0; dsr128<bo - hi>(fb[o.a & 1][o.b], bad[o.b] + hi); }
                if constexpr (o.kind == OP_FLAG) dsr32<(30 - (G >> 5)) * 4>(fl, fr);
                if constexpr (o.kind == OP_SIG) dsw32<o.a * 4>(dw_, dz);
                if constexpr (o.kind == OP_VOTE) dsr32<0>(vt, vt);
            };
            auto consts = [&]() {
                float ld, ar, ai;
                asm volatile("s_waitcnt vmcnt(9)\n\tv_lshlrev_b32 %0, 16, %3\n\tv_lshlrev_b32 %1, 16, %4\n\tv_lshlrev_b32 %2, 16, %5"
                             : "=&v"(ld), "=&v"(ar), "=&v"(ai) : "v"(c_dt), "v"(c_re), "v"(c_im) : "memory");
                const float dtv = rbf(expf(ld)), ea = expf(dtv * ar);
                float sn, cs;
                sincosf(dtv * ai, &sn, &cs);
                L = cf32{ea * cs, ea * sn};
                M = L;
#pragma unroll
                for (int e = 0; e < 5; ++e) M = cmul(M, M);
                M2 = cmul(M, M);
                const cf32 N = cmul(M2, M2), N2 = cmul(N, N), one = {1.f, 0.f};
                N4 = cmul(N2, N2); N8 = cmul(N4, N4);
                auto sel = [](bool c, cf32 a, cf32 b) { return cf32{c ? a.r : b.r, c ? a.i : b.i}; };
                MG = cmul(sel(g4 & 1, M, one), sel(g4 & 2, M2, one));
                NG = cmul(sel(g4 & 1, N, one), sel(g4 & 2, N2, one));
                const float nm = L.r - 1.f, den = ar * ar + ai * ai;
                cc = cf32{2.f * ((nm * ar + L.i * ai) / den), 2.f * ((L.i * ar - nm * ai) / den)};
                auto tc = [&](cf32& v) { tie(v.r); tie(v.i); };
                tc(L); tc(cc); tc(M); tc(M2); tc(MG); tc(NG); tc(N4); tc(N8);
                sched();
            };
            if constexpr (CQ < 0) consts();
            __builtin_amdgcn_s_setprio(CP);
            zero_chk<0>(fl, lbase + POLL + (lane & 3) * 256);
            ties(tag);
            tag += 0x01010101u;
            TS(1);
            static_for<-12, 0>(op);
            static_for<0, 512>([&](auto gc) {
                constexpr int G = CV(gc), st = G >> 5, i = (G >> 2) & 7, j = G & 3;
                if constexpr ((G == 0 || j == 2) && G <= 506) { static_assert(g1_wait(G) >= 0, ""); wait_lr<G == 506 ? 0 : g1_wait(G)>(); }
                if constexpr (G == CQ) { asm volatile("s_waitcnt lgkmcnt(0)\n\ts_nop 7" ::: "memory"); consts(); }
                if constexpr (G == HQ) hop_all(wid);
                if constexpr ((G & 31) == 20 && st < 15) zero_chk<(30 - st) * 4>(fl, fr);
                if constexpr (((G & 31) == 30 && st < 13) || (NB < 2 && G == B13)) barrier();
                if constexpr ((G & 31) == 20) TL(st);
                mfma<st == 0 && j < 2>(acc[i][j & 1], fa[i & 3][j >> 1], fb[st & 1][j]);
                sched();
                op(gc);
                if constexpr (G == GCR + 16) { unsigned n; nr = vote_cnt(vt, tag, (xcd & 4) << 11, n); }
                if constexpr (G == 31) page<-1>();
                if constexpr (G == 95) hop<2>();
                if constexpr (G == 159) hop<4>();
                if constexpr (G == 207) hop<5>();
                if constexpr (G == 255) page<7>();
                if constexpr (G == 319) hop<8>();
                if constexpr (G == 383) hop<10>();
                if constexpr (G == 431) hop<11>();
                if constexpr (G == 479) page<13>();
                if constexpr (G == 509) hop<15>();
                if constexpr (G == 489) hop<40>();
                if constexpr (G == 494) hop<41>();
                if constexpr (G == 499) hop<42>();
                if constexpr (G == 504) hop<43>();
                if constexpr (G >= 487) {
                    constexpr int k = G - 486;
                    if constexpr (k == 1) { asm volatile("s_nop 3"); ag = cf32{acc[0][0][0], acc[0][1][0]}; }
                    cf32 t;
                    asm volatile("v_fma_f32 %0, -%3, %5, %6\n\tv_fma_f32 %1, %3, %4, %7\n\tv_fmac_f32 %0, %2, %4\n\tv_fmac_f32 %1, %2, %5"
                                 : "=&v"(t.r), "=&v"(t.i) : "v"(L.r), "v"(L.i), "v"(ag.r), "v"(ag.i), "v"(acc[k >> 2][0][k & 3]), "v"(acc[k >> 2][1][k & 3]));
                    ag = t;
                }
                sched();
            });
        }
        __builtin_amdgcn_s_setprio(3);
        TL(16);
        TS(2);
#pragma unroll
        for (int k = 26; k < 32; ++k) ag = cfma(L, ag, cf32{acc[k >> 2][0][k & 3], acc[k >> 2][1][k & 3]});
        tie(ag.r); tie(ag.i); sched(); hop<44>(); sched();
        cf32 ev_, od, p01, p23;
        sw16(ag.r, ev_.r, od.r); sw16(ag.i, ev_.i, od.i);
        const cf32 pp = cfma(M, ev_, od);
        sw32(pp.r, p01.r, p23.r); sw32(pp.i, p01.i, p23.i);
        cf32 E = cfma(M2, p01, p23);

        const __amdgpu_buffer_rsrc_t rse = mk_brs(ws + WS_E + tn * 16384 + wid * 2048, 16384);
        st16(u32x4{f2u(E.r), f2u(E.i), tag, ~tag}, rse, g4 ? OOB : l15 * 16, nr + (tm & 7) * 256);
        sched();
        TL(17);
        TS(3);
        hop<45>();
        sched();
        c_setup();
#pragma unroll
        for (int i = 0; i < 4; ++i) cad[i] = lb + frag(i & 1, i >> 1);
        auto c_read = [&](auto rc) { static_for<0, 8>([&](auto ic) { constexpr int r = CV(rc), i = CV(ic); dsr128<(i >> 2) * 4096>(cf[r * 8 + i], cad[i & 3] + ((r & 1) ? 0 : 2 * BUF)); }); };
        auto c_tie = [&](auto rc) { static_for<0, 8>([&](auto ic) { tie(cf[CV(rc) * 8 + CV(ic)]); }); };
        auto c_unit = [&](auto uc) { static_for<0, 4>([&](auto ic) { constexpr int u = CV(uc), i = CV(ic); dma4<((u & 2) ? 0 : 2 * BUF) + (u & 1) * 4096, i>(lb, vc[i], rsc, c_off(wid, u)); }); };
        auto c_req = [&](auto rc) { c_unit(IC(CV(rc) * 2)); c_unit(IC(CV(rc) * 2 + 1)); };
        const float c2r = cc.r, c2i = cc.i;
        auto scale = [&](int i) {
#pragma unroll
            for (int j = 0; j < 4; ++j) {
                const float br = acc[i][0][j], bi = acc[i][1][j];
                acc[i][0][j] = fma_(c2r, br, -(c2i * bi)); acc[i][1][j] = fma_(-c2r, bi, -(c2i * br));
            }
            tie(acc[i][0]); tie(acc[i][1]);
        };
        auto scales = [&](auto ac, auto bc) {
            static_for<CV(ac), CV(bc)>([&](auto ic) {
                constexpr int i = CV(ic);
                scale(i);
                if constexpr (i & 1) { sched(); hop<i == 1 ? 46 : i == 3 ? 16 : i == 5 ? 47 : 48>(); sched(); }
            });
        };
        constexpr int CA = CVR == 0 ? 0 : CVR == 4 ? 1 : 2;
        if constexpr (NB) {
            unsigned dn = 1;
            scales(IC(0), IC(DP));
            if constexpr (CA) { zero_chk<8>(dn, lbase + DONE + (lane & 3) * FST); static_for<0, CA>(c_unit); }
            if constexpr (SPR != 3) __builtin_amdgcn_s_setprio(SPR);
            sched();
            scales(IC(DP), IC(ELP));
            if constexpr (!CA) zero_chk<8>(dn, lbase + DONE + (lane & 3) * FST);
        } else {
            scales(IC(0), IC(ELP));
            barrier();
            static_for<0, CA>(c_unit);
        }
        TL(18);
        u32x4 ev[4]; unsigned eo[4]; unsigned long long none[4];
#pragma unroll
        for (int i = 0; i < 4; ++i) {
            const int t = tm - 1 - 4 * i - g4;
            eo[i] = t < 0 ? OOB : ((((t ^ tm) & 8) << 10) ^ nr) + (t & 7) * 256 + l15 * 16; none[i] = __builtin_amdgcn_ballot_w64(t < 0);
            asm volatile("buffer_load_dwordx4 %0, %1, %2, 0 offen sc1" : "=v"(ev[i]) : "v"(eo[i]), "s"(mk_rsrc(ws + WS_E + tn * 16384 + wid * 2048, 16384)) : "memory");
        }
        asm volatile("" ::: "memory");
        sched();
        constexpr int CB = CVR == 0 ? 1 : CVR == 4 ? 2 : 4;
        static_for<CA, CB>(c_unit);
        scales(IC(ELP), IC(8));
        cf32 X = cfma(cf32{g4 == 3 ? M.r : g4 == 2 ? 1.f : 0.f, g4 == 3 ? M.i : 0.f}, p01, cf32{(g4 & 1) ? ev_.r : 0.f, (g4 & 1) ? ev_.i : 0.f});
        tie(X.r); tie(X.i);
        sched();
        hop<24>();
        if constexpr (SPR != 3) __builtin_amdgcn_s_setprio(3);
        if constexpr (CVR == 2) { wait_v<12>(); c_read(IC(0)); wait_l<0>(); c_tie(IC(0)); c_req(IC(2)); }
        wait_v<4 * (CB - CA) + 8 * (CVR == 2)>();
        TL(19);
        tie(ev[0]); tie(ev[1]); tie(ev[2]); tie(ev[3]);
        {
            const unsigned long long tag64 = (unsigned long long)tag | ((unsigned long long)~tag << 32);
#pragma unroll 1
            for (unsigned spins = 0; spins < SPIN_CAP; ++spins) {
                unsigned long long ok = ~0ull;
#pragma unroll
                for (int i = 0; i < 4; ++i) ok &= __builtin_amdgcn_ballot_w64(((unsigned long long)ev[i][2] | ((unsigned long long)ev[i][3] << 32)) == tag64) | none[i];
                TSV(11, spins);
                if (ok == ~0ull) break;
                asm volatile("buffer_load_dwordx4 %0, %4, %8, 0 offen sc1\n\tbuffer_load_dwordx4 %1, %5, %8, 0 offen sc1\n\tbuffer_load_dwordx4 %2, %6, %8, 0 offen sc1\n\t"
                             "buffer_load_dwordx4 %3, %7, %8, 0 offen sc1\n\ts_waitcnt vmcnt(0)" : "=&v"(ev[0]), "=&v"(ev[1]), "=&v"(ev[2]), "=&v"(ev[3])
                             : "v"(eo[0]), "v"(eo[1]), "v"(eo[2]), "v"(eo[3]), "s"(mk_rsrc(ws + WS_E + tn * 16384 + wid * 2048, 16384)) : "memory");
            }
        }
        page<18>();
        cf32 h;
        {
            auto e = [&](int i) { return cf32{u2f(ev[i][0]), u2f(ev[i][1])}; };
            cf32 c = cmul(NG, cfma(N8, cfma(N4, e(3), e(2)), cfma(N4, e(1), e(0)))), u, v;
            sw16(c.r, u.r, v.r); sw16(c.i, u.i, v.i);
            sw32(u.r + v.r, u.r, v.r); sw32(u.i + v.i, u.i, v.i);
            c = cfma(MG, cf32{u.r + v.r, u.i + v.i}, X);
            h = cf32{fma_(c2r, c.r, -(c2i * c.i)), fma_(-c2r, c.i, -(c2i * c.r))};
        }
        TS(4);
        {
            const __amdgpu_buffer_rsrc_t rh = mk_brs(ws + WS_H + (size_t)((tm & 7) * 16 + tn) * 65536 + (nr ^ ((tm & 2) << 12)) + wid * 2048, 65536);
            const unsigned ho = g4 * 256 + ((l15 ^ (4 * (g4 & 1))) << 4);
            const cf32 Lc = {L.r, -L.i};
            static_for<0, 4>([&](auto qc) {
                constexpr int q = CV(qc);
                if constexpr (q >= CB) c_unit(qc);
                if constexpr (q == 2) hop<20>();
                u32x4 wr, wi;
#pragma unroll
                for (int u = 0; u < 4; ++u) {
                    const int i = 2 * q + (u >> 1), j = 2 * (u & 1);
                    const cf32 h0 = cfma(Lc, h, cf32{acc[i][0][j], acc[i][1][j]});
                    h = cfma(Lc, h0, cf32{acc[i][0][j + 1], acc[i][1][j + 1]});
                    wr[u] = pack2(h0.r, h.r); wi[u] = pack2(h0.i, h.i);
                }
                if constexpr ((PRE == 2 || GQ >= 0) && q == GE) { tie(wr); tie(wi); dsw32<4>(lbase + DONE + wid * FST + lane * 4, 0u); }
                __builtin_amdgcn_raw_buffer_store_b128(wr, rh, ho, q * 16384, 16);
                st16(wi, rh, ho + 1024, q * 16384); keep(wr);
                sched();
            });
        }
        TS(10);
        auto flag = [&]() { __builtin_amdgcn_raw_buffer_store_b64(u32x2{tag, ~tag}, rf, lane ? OOB : 0u, F_H + nr + (((tm & 7) * 16 + tn) * 4 + wid) * 8, 16); TS(5); };
        hop<22>();
        if constexpr (CVR == 2) {
            wait_v<16>();
            c_read(IC(1));
            wait_l<0>();
            c_req(IC(3));
            wait_v<8>();
            flag();
        } else {
            if constexpr (CVR == 1) wait_v<8>(); else { wait_v<0>(); flag(); }
            c_read(IC(0)); c_read(IC(1));
            wait_l<8>();
            c_req(IC(2));
            wait_l<0>();
            c_req(IC(3));
            if constexpr (CVR == 1) { wait_v<16>(); flag(); }
        }
        c_tie(IC(0)); c_tie(IC(1));
        static_for<16, 32>([&](auto ic) { undef(cf[CV(ic)]); });
    }
    {
        unsigned ln = lane, dsw_, pf;
        tie(ln);
        const int wid = fresh(wid_), lw = wid & 3, kg = wid >> 2, b = fresh(b_), xcd = b & 7, idx = b >> 3, tq = (idx & 7) + JR(idx >> 3);
        nr = fresh(__builtin_amdgcn_readfirstlane(nr));
        const unsigned nx = nr ^ ((xcd & 2) << 12), nh = nr ^ ((idx & 2) << 12), hp = (idx & 7) * 16 + (b & 1) * 8;
        const int tm2 = (xcd & 4) * 2 + (idx & 7), dt = (xcd & 2) * 2 + (idx >> 3), qx = ((~b & 1) * 2) ^ JQ(idx >> 3);
        const int ta = 512 * (idx >> 3) + 128 * (idx & 1) + 16 * wid + 4 * ((idx >> 1) & 3);
        const int tb = tm2 * 128 + 64 * kg;
        dsr32<DSK - 131072>(dsw_, lbase + 131072 + lw * 64 + (ln & 7) * 8 + ((ln >> 1) & 4));
        unsigned wl = lbase + wid * 4096, w2k = lbase + wid * 2048, hs0 = (hp + ((tq + (wid >> 1)) & 7)) * 65536 + nh + (wid & 1) * 4096 + qx * 16384;
        unsigned s_x = (tb + qx * 8) * 2048 + dt * 256 + lw * 64, s_q = ta * 2048 + xcd * 256, s_z = WS_ZT + (xcd & 3) * 1048576 + nx + idx * 128 + wid * 131072, zfo = WS_F + F_Z + (xcd & 2) * 8192 + nr + (xcd & 1) * 4096;
        unsigned ppo = WS_PP + (xcd & 2) * 1048576 + idx * 65536 + (xcd & 1) * 32768 + kg * 16384 + nx + lw * 2048, s_a = ta * 8192 + xcd * 512, s_b = (tb + (2 ^ qx) * 8) * 8192 + 4096 + dt * 512 + lw * 128;
        const unsigned tagg = fresh(__builtin_amdgcn_readfirstlane(tag));
        u32x4 rsx = mk_rsrc(x, (NOX & 1) ? 0 : 4 << 20), rsq = mk_rsrc(qin, (NOX & 2) ? 0 : 4 << 20), rso = mk_rsrc(out, 16 << 20), rws = mk_rsrc(ws, 32 << 20);
        ties(wl); ties(w2k); ties(hs0); ties(s_x); ties(s_q); ties(s_z); ties(zfo); ties(ppo); ties(s_a); ties(s_b);
        ties(rsx); ties(rsq); ties(rso); ties(rws);
        const unsigned l31 = ln & 31, l5 = ln >> 5;
        unsigned l16 = ln * 16, ha[8];
        ha[0] = (lbase + kg * 1536 + l5 * 2048 + (ln & 2) * 128 + (ln & 1) * 8) | ((((ln >> 1) & 8) | ((ln >> 2) & 3) | ((ln & 2) << 1)) << 4);
        ha[1] = ha[0] ^ 64; ha[2] = ha[0] ^ 512; ha[3] = ha[0] ^ 576;
        static_for<0, 4>([&](auto ic) { ha[4 + CV(ic)] = ha[CV(ic)] + 65536; tie(ha[CV(ic)]); tie(ha[4 + CV(ic)]); });
        unsigned a_x = ((((ln >> 2) & 7) | (ln & 32)) << 11) | (l16 & 48), a_xr = (((l16 & 240) << 2) | (ln & 48)) + (w2k + ZST);
        unsigned a_q = ((ln & 48) << 7) | (l16 & 240), a_qr = (l5 << 10) + l31 * 8 + (w2k + QST), a_iw = w2k + IMG + l16, a_ir = a_iw ^ 8192;
        unsigned a_t = wid == 0 && ln == 0 ? idx * 128 : OOB, msk = 0xfff0fff0u;
        unsigned a_f = wid == 1 ? (ln >> 1) * 128 + (ln & 1) * 4 : OOB, expect = wid == 1 ? ((ln & 1) ? ~tagg : tagg) : 0u;
        unsigned a_z = (((ln ^ l5 ^ (wid * 4)) & 7) << 4) | ((ln & 48) << 10) | ((ln & 8) << 9), a_z2 = a_z ^ 32, a_zr = (l31 << 9) + (((((wid ^ ln) & 7) << 1) | l5) << 3) + (lbase + ZST);
        unsigned a_oa = (l5 << 21) | (l31 << 4), a_ob = ((((ln >> 2) & 4) | (ln & 32) | ((ln >> 3) & 1)) << 13) | (l16 & 112);
        u32x2 tg2 = {tagg, ~tagg};
        tie(l16); tie(a_x); tie(a_xr); tie(a_q); tie(a_qr); tie(a_iw); tie(a_ir); tie(a_t); tie(msk); tie(a_f); tie(expect); tie(a_z); tie(a_z2); tie(a_zr);
        tie(a_oa); tie(a_ob); tie(tg2);
        wait_l<0>();
        tie(dsw_);
        u32x4 dg[2];
        static_for<0, 4>([&](auto ic) { constexpr unsigned long long k = diag_mask(CV(ic)); dg[0][CV(ic)] = sel0(dsw_ & 0xffffu, k); dg[1][CV(ic)] = sel0(dsw_ & 0xffff0000u, k); });
        tie(dg[0]); tie(dg[1]);
        unsigned fso, xso, ppi = ppo ^ 32768, tn_[3] = {tagg & 0xf000fu, (tagg >> 8) & 0xf000fu, (tagg >> 4) & 0xf000fu}, psel = 0x05040100u;
        unsigned long long lo8 = 0x00ff00ff00ff00ffull, hi8 = ~lo8;
        ties(ppi); ties(tn_[0]); ties(tn_[1]); ties(tn_[2]); ties(psel); ties(lo8); ties(hi8);
        auto fill = [&](auto sc, auto pc) {
            constexpr int s = CV(sc), p = CV(pc);
            if constexpr (p == 0) dma_x<(2 * s % 3) * BUF, (s >> 1) * 16384 + (s & 1) * 262144, true>(fso, wl, l16, rws, hs0);
            else if constexpr (fill_m0(4 * s + p)) dma_m<(2 * s % 3) * BUF, p * 1024>(fso, wl, l16, rws);
            else dma_n<p * 1024>(fso, l16, rws);
        };
        bf16x4 hf[2][8];
        auto ldh = [&](auto nc, auto ic) {
            constexpr int n = CV(nc), i = CV(ic), j = (i >> 1) & 1, bo = (2 * (n >> 2) % 3) * BUF + (g2_unit(n, j) & 3) * 8192 + (i >> 2) * 4096, hi = bo >= 65536 ? 65536 : 0;
            dsr64tr<bo - hi>(hf[n & 1][i], ha[2 * g2_tile(n, j) + (i & 1) + (hi >> 14)]);
        };
        f32x4 yc[2][2][2];
        f32x4 im[2], ao, ob[2]; u32x4 pl[2], po;
        bf16x8 xf[2];
        u32x2 zv[4], qv[2];
        unsigned ck, ex;
        float tq_[2], tz, sm[8], mx[2], cv[8];
        auto finB = [&](auto pc, auto jc) {
            constexpr int p = CV(pc), j = CV(jc);
            if constexpr (j < 8) { yc[p & 1][0][j & 1][j >> 1] += im[j & 1][j >> 1]; tie(yc[p & 1][0][j & 1]); }
            else {
                constexpr int i = (j - 8) >> 2, k = j & 3, dd = k & 1, h = k >> 1;
                ob[i][k] = sel8(yc[p & 1][0][dd][2 * i + 1 - h], yc[p & 1][0][dd][2 * i + h], h ? hi8 : lo8);
                if constexpr (k == 3 && (p == 3 || !(NOST & 4))) gst<0>(ob[i], a_ob, rso, s_b ^ ((p & 1) * 65536 + i * 16384));
                if constexpr (j == 12) keep(ob[0]);
            }
        };
        auto snd = [&](auto pc, auto jc) {
            constexpr int p = CV(pc), j = CV(jc);
            if constexpr (j < 8) { sm[j] = yc[p][0][j >> 2][j & 3] + im[j >> 2][j & 3]; tie(sm[j]); }
            if constexpr (j == 8) mx[0] = amax3(sm[0], sm[1], sm[2]);
            if constexpr (j == 9) mx[1] = amax3(sm[3], sm[4], sm[5]);
            if constexpr (j == 10) mx[0] = amax3(mx[0], sm[6], sm[7]);
            if constexpr (j == 11) mx[0] = amax3(mx[0], mx[1], mx[1]);
            if constexpr (j == 12) { ex = f2u(mx[0]) + 0x4000u; tie(ex); }
            if constexpr (j == 13) { ex &= 0x7f800000u; tie(ex); }
            if constexpr (j == 14) { mx[0] = u2f(ex + 0x04c00008u); tie(mx[0]); }
            if constexpr (j == 15) { ex >>= 23; tie(ex); }
            if constexpr (j >= 16 && j < 24) { sm[j - 16] += mx[0]; tie(sm[j - 16]); }
            if constexpr (j == 24) { ex |= ex << 12; tie(ex); }
            if constexpr (j >= 25 && j < 29) { po[j - 25] = __builtin_amdgcn_perm(f2u(sm[2 * (j - 25) + 1]), f2u(sm[2 * (j - 25)]), psel); tie(po); }
            if constexpr (j == 29) { po[0] = bfi_v(msk, po[0], ex); tie(po); }
            if constexpr (j >= 30 && j < NS) { po[j - 29] = bfi_s(msk, po[j - 29], tn_[j - 30]); if constexpr (j < NS - 1) tie(po); }
            if constexpr (j == NS - 1 && !(NOST & 2)) gst<p * 1024>(po, l16, rws, ppo);
        };
        auto rcv = [&](auto hc, auto jc) {
            constexpr int h = CV(hc), j = CV(jc);
            if constexpr (j < 3) { pl[h][j + 1] ^= tn_[j]; pp_tie<h>(pl[h]); }
            if constexpr (j == 3) { ck = pl[h][1] | pl[h][2] | pl[h][3]; tie(ck); }
            if constexpr (j == 4) { ck &= 0xf000fu; tie(ck); }
            if constexpr (j == 6 && !(NOST & 2)) pp_chk<h>(pl[h], ck, tagg, l16, rws, ppi);
            if constexpr (j == 7) { ex = pl[h][0] >> 12; tie(ex); }
            if constexpr (j == 8) ex = bfi_v(msk, ex, pl[h][0]);
            if constexpr (j == 9) { pl[h][0] &= msk; pp_tie<h>(pl[h]); }
            if constexpr (j == 10) { ex <<= 23; tie(ex); }
            if constexpr (j == 11) { ex &= 0x7f800000u; tie(ex); }
            if constexpr (j == 12) { mx[1] = u2f(ex) * 0x1p-14f; tie(mx[1]); }
            if constexpr (h == 0 && j >= 13) {
                constexpr int k = j - 13, a = (k + 1) / 2, e = k == 15 ? 7 : k / 2 - 1;
                if constexpr (k == 0 || ((k & 1) && k < 15)) cv[a] = cvt16<a & 1>(pl[h][a >> 1]);
                else { yc[0][0][e >> 2][e & 3] = fma_(cv[e], mx[1], yc[0][0][e >> 2][e & 3]); tie(yc[0][0][e >> 2]); }
            }
            if constexpr (h == 1 && j >= 13 && j < 21) cv[j - 13] = cvt16<(j - 13) & 1>(pl[h][(j - 13) >> 1]);
            if constexpr (h == 1 && j >= 21) { constexpr int e = j - 21; im[e >> 2][e & 3] = fma_(cv[e], mx[1], im[e >> 2][e & 3]); tie(im[e >> 2]); }
        };
        auto side = [&](auto pc) {
            constexpr int P = CV(pc), n = P >> 3, m = P & 7, p = n >> 3;
            if constexpr (P == P_X || P == P_X + 2) dma_x<ZST + (P != P_X) * 1024, (P != P_X) * 16384, false>(xso, w2k, a_x, rsx, s_x);
            if constexpr (!NOA) {
            if constexpr (P == P_Q || P == P_Q + 2) dma_x<QST + (P != P_Q) * 1024, (P != P_Q) * 524288, false>(xso, w2k, a_q, rsq, s_q);
            if constexpr (P == P_T) gst<0>(tg2, a_t, rws, zfo);
            if constexpr (P == AT(ZP, 4)) pf_ld(pf, a_f, rws, zfo);
            if constexpr (P == P_ZC) { wait_v<g2_allow(P, AT(ZP, 4))>(); tag_chk(pf, expect, a_f, rws, zfo); }
            if constexpr (P == P_Z || P == P_Z + 2) dma_x<ZST + (P != P_Z) * 1024, (P != P_Z) * 65536, true>(xso, w2k, P == P_Z ? a_z : a_z2, rws, s_z);
            }
            if constexpr (P == AT(7, 5) || P == AT(7, 7)) dsr128<(m - 5) * 512>(xf[(m - 5) / 2], a_xr);
            if constexpr (P == AT(8, 4)) { tie(xf[0]); tie(xf[1]); }
            if constexpr (P == AT(8, 5) || P == AT(9, 3)) static_for<0, 2>([&](auto dc) { mfma<false>(yc[n & 1][0][CV(dc)], xf[n & 1], __builtin_bit_cast(bf16x8, dg[CV(dc)])); });
            if constexpr ((n & 7) == 6 && m == XW && !(DBG & 2)) dsw128<0>(a_iw, yc[p & 1][1][0]);
            if constexpr ((n & 7) == 6 && m == XW2 && !(DBG & 2)) dsw128<1024>(a_iw, yc[p & 1][1][1]);
            if constexpr ((n & 7) == 7 && (m & 5) == (n < 31 ? 4 : 0)) dsr128<(m & 2) * 512>(im[(m >> 1) & 1], a_ir);
            if constexpr (n >= 8 && (n & 7) == 0 && m == 4) { tie(im[0]); tie(im[1]); }
            if constexpr (P >= P_PP0 && P <= E_PP0) snd(IC(0), IC(P - P_PP0));
            if constexpr (P >= P_PP1 && P <= E_PP1) static_for<0, D1>([&](auto ic) { snd(IC(1), IC((P - P_PP1) * D1 + CV(ic))); });
            if constexpr (P == P_L0 || P == P_L1) pp_ld<P == P_L1>(pl[P == P_L1], l16, rws, ppi);
            if constexpr (P == P_R0 || P == P_R1) { wait_v<g2_allow(P, P == P_R0 ? imax(P_L0, XL ? P_X + 2 : 0) : P_L1)>(); pp_tie<P == P_R1>(pl[P == P_R1]); }
            if constexpr (P >= P_R0 && P <= E_R0) static_for<0, D0>([&](auto ic) { if constexpr ((P - P_R0) * D0 + CV(ic) < NR) rcv(IC(0), IC((P - P_R0) * D0 + CV(ic))); });
            if constexpr (P >= P_R1 && P < P_R1 + 21) rcv(IC(1), IC(P - P_R1));
            if constexpr (!NOA) {
            if constexpr (n == ZW && m >= 4) dsr64<(m - 4) * 128>(zv[m - 4], a_zr);
            static_for<0, 4>([&](auto rc) {
                constexpr int r = CV(rc);
                if constexpr (P == q_tie(r)) tie(qv[r & 1]);
                if constexpr (P == q_rd(r)) dsr64<r * 256>(qv[r & 1], a_qr);
            });
            if constexpr (P == AT(ZW + 1, 4)) { tie(zv[0]); tie(zv[1]); tie(zv[2]); tie(zv[3]); }
            }
            if constexpr (a_step(P) >= 0) {
                constexpr int s = a_step(P), e = s == 0 ? 0 : s % 3 == 2 ? (s + 1) / 3 : s % 3 == 1 ? s / 3 : s / 3 - 1, r = e >> 2, c = e & 3;
                if constexpr (s == 0 || (s % 3 == 2 && e < 16)) { tq_[e & 1] = (c & 1) ? bfhi(qv[r & 1][c >> 1]) : bflo(qv[r & 1][c >> 1]); tie(tq_[e & 1]); }
                else if constexpr (s % 3 == 1) { tz = (r & 1) ? bfhi(zv[c][r >> 1]) : bflo(zv[c][r >> 1]); tie(tz); }
                else if constexpr (s % 3 == 0) {
                    ao[c] = tq_[e & 1] * tz;
                    if constexpr (c == 3 && !(NOST & 1)) gst<0>(ao, a_oa, rso, s_a + r * 8192);
                    else tie(ao);
                }
            }
            if constexpr (P >= P_F2 && P < P_F2 + 16) finB(IC(2), IC(P - P_F2));
            if constexpr (a_step(P - 1) > 0 && a_step(P - 1) % 12 == 0) keep(ao);
            if constexpr (P == E_PP0 + 1 || P == E_PP1 + 1) keep(po);
            if constexpr (P == P_F2 + 16) keep(ob[1]);
        };
        {
            unsigned e_h = wid == 3 ? ((ln & 1) ? ~tagg : tagg) : 0u, hsl = lbase + lw * 8192, hso = (hp + ((tq + wid) & 7)) * 65536 + nh + qx * 16384;
            unsigned a_h = wid == 3 ? ln * 4 : OOB, s_h = WS_F + F_H + nr + hp * 32, ca2[4], a_g = lbase + HGO + ln * 4;
            static_for<0, 4>([&](auto ic) { ca2[CV(ic)] = cad[CV(ic)] + 2 * BUF; tie(ca2[CV(ic)]); });
            tie(e_h); tie(a_h); tie(a_g); ties(hsl); ties(hso); ties(s_h);
            sched();
            if (wid < 4) {
                if constexpr (PW == 0) { pf_ld(pf, a_h, rws, s_h); wait_v<1>(); }
                if constexpr (PW == 1) { wait_v<0>(); pf_ld(pf, a_h, rws, s_h); }
                if constexpr (PW == 2) { wait_v<8>(); pf_ld(pf, a_h, rws, s_h); wait_v<1>(); }
                static_for<16, 32>([&](auto ic) { constexpr int i = CV(ic); dsr128u<((i >> 2) & 1) * 4096>(cf[i], (i & 8) ? cad[i & 3] : ca2[i & 3]); });
                wait_l<0>();
                static_for<16, 32>([&](auto ic) { tie(cf[CV(ic)]); });
                wait_v<0>();
                tag_chk<1>(pf, e_h, a_h, rws, s_h);
                if (wid == 3) flag_set(a_g, 1u); else gate<1>(a_g, 1);
                TS(6);
                hop<26>();
                static_for<0, 4>([&](auto ic) {
                    constexpr int i = CV(ic);
                    dma_x<(i >> 1) * 2 * BUF + (i & 1) * 4096, (i >> 1) * 262144 + (i & 1) * 4096, true>(fso, hsl, l16, rws, hso);
                    dma_n<1024>(fso, l16, rws); dma_n<2048>(fso, l16, rws); dma_n<3072>(fso, l16, rws);
                });
            }
        }
        wait_v<8>();
        if (wid < 4) TS(7);
        TG_DECL
        barrier();
        static_for<0, 8>([&](auto ic) { ldh(IC(0), ic); });
        static_for<0, 32>([&](auto nc) {
            constexpr int n = CV(nc), p = n >> 3;
            if constexpr (n == PG3) page<33>();
            if constexpr (n == PG4) page<37>();
            wait_lr<(n & 3) == 3 ? 0 : 4 + g2_lx(n - 1)>();
            if constexpr (n == 31) barrier();
            if constexpr ((n & 3) == 3 && n < 31) {
                constexpr int need = g2_need(n);
                TL(21 + n / 4 * 3);
                wait_v<g2_allow(8 * n, need)>();
                TL(22 + n / 4 * 3);
                barrier();
                TL(23 + n / 4 * 3);
            }
            if constexpr (n == 1) hop<31>();
            if constexpr (n == 4) hop<32>();
            if constexpr (n == 11) hop<34>();
            if constexpr (n == 15) hop<35>();
            if constexpr (n == 19) hop<36>();
            if constexpr (n == 27) hop<38>();
            if constexpr (n == 31) hop<39>();
            if constexpr (n == 0) TL(20);
            TG(20 + n);
            if constexpr (n == 31) TL(42);
            if constexpr (PRG) __builtin_amdgcn_s_setprio(n == 31 ? 0 : 3 - ((n + 1) & 3));
            sched();
            static_for<0, 8>([&](auto mc) {
                constexpr int m = CV(mc), ks = m >> 2, tt = (m >> 1) & 1, dd = m & 1, P = n * 8 + m;
                if constexpr (m == 4 && (n & 3) != 3) wait_lr<(rd1(n) ? 8 : 4) + ((n & 7) == 6 ? (XW < 4) + (XW2 < 4) : 0)>();
                mfma<(n & 7) == 0 && ks == 0>(yc[p & 1][(DBG & 1) ? tt : g2_tile(n, tt)][dd], cat(hf[n & 1][4 * ks + 2 * tt], hf[n & 1][4 * ks + 2 * tt + 1]), cf[g2_unit(n, tt) * 4 + 2 * ks + dd]);
                sched();
                if constexpr (n < 31 && rd1(n) && m < 4) { ldh(IC(n + 1), IC(2 * m)); ldh(IC(n + 1), IC(2 * m + 1)); }
                if constexpr (n < 31 && !rd1(n)) ldh(IC(n + 1), mc);
                if constexpr (g2_fill(P) >= 0) fill(IC(g2_fill(P) >> 2), IC(g2_fill(P) & 3));
                side(IC(P));
                sched();
            });
        });
        TG(52);
        if (wid >= 4) TS(5); else TS(8);
        TG(53);
        static_for<256 - P_R1, 21>([&](auto jc) { rcv(IC(1), jc); });
        wait_l<0>();
        tie(im[0]); tie(im[1]);
        static_for<21, NR>([&](auto jc) { rcv(IC(1), jc); });
        asm volatile("s_nop 3");
        sched();
        static_for<0, 16>([&](auto jc) { finB(IC(3), jc); });
        if (b == 255 && wid == 0) gst<0>(u32x2{tagg, tagg}, l16 ? OOB : 0u, rws, WS_F + F_EP);
    }
}

}

#if defined(__HIP_DEVICE_COMPILE__)
#pragma clang attribute pop
#endif

extern "C" void kernel_launch(void* const* d_in, const int* in_sizes, int n_in, void* d_out, int out_size, void* d_ws, size_t ws_size,
                              hipStream_t stream) {
    (void)in_sizes; (void)n_in; (void)out_size; (void)ws_size;
    using namespace fk;
    const uint16_t* q = (const uint16_t*)d_in[0];
    const uint16_t* kin = (const uint16_t*)d_in[1];
    const uint16_t* vin = (const uint16_t*)d_in[2];
    const uint16_t* sk = (const uint16_t*)d_in[3];
    const uint16_t* ssmk = (const uint16_t*)d_in[4];
    const uint16_t* ssmD = (const uint16_t*)d_in[5];
    const uint16_t* x = (const uint16_t*)d_in[6];
    const uint16_t* lre = (const uint16_t*)d_in[7];
    const uint16_t* lim = (const uint16_t*)d_in[8];
    const uint16_t* bre = (const uint16_t*)d_in[9];
    const uint16_t* bim = (const uint16_t*)d_in[10];
    const uint16_t* cre = (const uint16_t*)d_in[11];
    const uint16_t* cim = (const uint16_t*)d_in[12];
    const uint16_t* logdt = (const uint16_t*)d_in[13];
    const uint16_t* dskip = (const uint16_t*)d_in[14];
    char* ws = (char*)(((uintptr_t)d_ws + 0x1fffff) & ~(uintptr_t)0x1fffff);
    hipLaunchKernelGGL(k1, dim3(256), dim3(512), 0, stream, x, bre, bim, lre, lim, logdt, cre, cim, dskip, ssmk, ssmD, q, kin, vin, sk, ws, (float*)d_out TS_PASS(0));
}
```

```cpp
#include <hip/hip_runtime.h>
#include <stdint.h>
#include <type_traits>

#if defined(__HIP_DEVICE_COMPILE__)
#pragma clang attribute push(__attribute__((target("no-packed-fp32-ops"))), apply_to = function)
#endif

namespace fk {

typedef __attribute__((ext_vector_type(8))) short bf16x8;
typedef __attribute__((ext_vector_type(4))) short bf16x4;
typedef __attribute__((ext_vector_type(4))) float f32x4;
typedef __attribute__((ext_vector_type(4))) unsigned u32x4;
typedef __attribute__((ext_vector_type(2))) unsigned u32x2;
typedef __attribute__((ext_vector_type(2))) __bf16 bf16x2_t;
typedef __attribute__((address_space(1))) unsigned gu32;
typedef __attribute__((address_space(1))) unsigned long long gu64;
#define RLX_AGENT __ATOMIC_RELAXED, __HIP_MEMORY_SCOPE_AGENT

__device__ __forceinline__ float u2f(unsigned v) { return __builtin_bit_cast(float, v); }
__device__ __forceinline__ unsigned f2u(float v) { return __builtin_bit_cast(unsigned, v); }
__device__ __forceinline__ bool wall(bool p) { return __builtin_amdgcn_ballot_w64(p) == ~0ull; }
__device__ __forceinline__ float bflo(unsigned w) { return u2f(w << 16); }
__device__ __forceinline__ float bfhi(unsigned w) { return u2f(w & 0xffff0000u); }
__device__ __forceinline__ unsigned pack2(float lo, float hi) {
    bf16x2_t v; v[0] = (__bf16)lo; v[1] = (__bf16)hi; return __builtin_bit_cast(unsigned, v);
}
__device__ __forceinline__ float rbf(float f) { float r; asm("v_cvt_pk_bf16_f32 %0, 0, %1" : "=v"(r) : "v"(f)); return r; }
__device__ __forceinline__ float fma_(float a, float b, float c) { return __builtin_fmaf(a, b, c); }

template <int I, int N, class F>
__device__ __forceinline__ void static_for(F&& f) {
    if constexpr (I + 1 == N) f(std::integral_constant<int, I>{});
    else if constexpr (I < N) { static_for<I, I + (N - I) / 2>(f); static_for<I + (N - I) / 2, N>(f); }
}
#define IC(v) std::integral_constant<int, (v)>{}
#define CV(c) decltype(c)::value

#define TS_ARG
#define TS_PASS(off)
#define TS_DECL
#define TS(i)
#define TSV(i, v)
#define TL_DECL
#define TL(i)
#define TS_FLUSH(slot)

#define TG_DECL
#define TG(i)
template <int N> __device__ __forceinline__ void wait_v() { asm volatile("s_waitcnt vmcnt(%0)" ::"n"(N > 63 ? 63 : N) : "memory"); }
template <int N> __device__ __forceinline__ void wait_l() { asm volatile("s_waitcnt lgkmcnt(%0)" ::"n"(N > 14 ? 14 : N) : "memory"); }
template <int N> __device__ __forceinline__ void wait_lr() { __builtin_amdgcn_s_waitcnt(0xc07f | ((N > 14 ? 14 : N) << 8)); }
template <class T> __device__ __forceinline__ void tie(T& x) { asm volatile("" : "+v"(x)); }
template <class T> __device__ __forceinline__ void undef(T& x) { asm volatile("" : "=v"(x)); }
template <int OFF, class T> __device__ __forceinline__ void dsr128u(T& d, unsigned a) { asm volatile("ds_read_b128 %0, %1 offset:%2" : "+v"(d) : "v"(a), "n"(OFF)); }
template <class T> __device__ __forceinline__ void keep(const T& x) { asm volatile("" :: "v"(x)); }
template <int OFF, class T> __device__ __forceinline__ void dsr128(T& d, unsigned a) { asm volatile("ds_read_b128 %0, %1 offset:%2" : "=v"(d) : "v"(a), "n"(OFF)); }
template <int OFF, class T> __device__ __forceinline__ void dsr64(T& d, unsigned a) { asm volatile("ds_read_b64 %0, %1 offset:%2" : "=v"(d) : "v"(a), "n"(OFF)); }
template <int OFF> __device__ __forceinline__ void dsr32(unsigned& d, unsigned a) { asm volatile("ds_read_b32 %0, %1 offset:%2" : "=v"(d) : "v"(a), "n"(OFF)); }
template <int O0, int O1, class T> __device__ __forceinline__ void dsr2(T& d, unsigned a) { asm volatile("ds_read2_b32 %0, %1 offset0:%2 offset1:%3" : "=v"(d) : "v"(a), "n"(O0), "n"(O1)); }
template <int OFF> __device__ __forceinline__ void dsr64tr(bf16x4& d, unsigned a) { asm volatile("ds_read_b64_tr_b16 %0, %1 offset:%2" : "=v"(d) : "v"(a), "n"(OFF)); }
template <int OFF, class T> __device__ __forceinline__ void dsw128(unsigned a, const T& v) { asm volatile("ds_write_b128 %0, %1 offset:%2" :: "v"(a), "v"(v), "n"(OFF) : "memory"); }
template <int OFF> __device__ __forceinline__ void dsw32(unsigned a, unsigned v) { asm volatile("ds_write_b32 %0, %1 offset:%2" :: "v"(a), "v"(v), "n"(OFF) : "memory"); }
__device__ __forceinline__ bf16x8 cat(const bf16x4& lo, const bf16x4& hi) { return __builtin_shufflevector(lo, hi, 0, 1, 2, 3, 4, 5, 6, 7); }
__device__ __forceinline__ void barrier() { asm volatile("" ::: "memory"); __builtin_amdgcn_s_barrier(); asm volatile("" ::: "memory"); }
__device__ __forceinline__ int fresh(int v) { asm volatile("" : "+s"(v)); return v; }
__device__ __forceinline__ void sched() { __builtin_amdgcn_sched_barrier(0); }
template <bool FIRST> __device__ __forceinline__ void mfma(f32x4& c, const bf16x8& a, const bf16x8& b) {
    if constexpr (FIRST) asm volatile("v_mfma_f32_16x16x32_bf16 %0, %1, %2, 0" : "=v"(c) : "v"(a), "v"(b));
    else asm volatile("v_mfma_f32_16x16x32_bf16 %0, %1, %2, %0" : "+v"(c) : "v"(a), "v"(b));
}
struct cf32 { float r, i; };
__device__ __forceinline__ cf32 cmul(cf32 a, cf32 b) { return cf32{fma_(a.r, b.r, -(a.i * b.i)), fma_(a.r, b.i, a.i * b.r)}; }
__device__ __forceinline__ cf32 cfma(cf32 a, cf32 b, cf32 c) { return cf32{fma_(a.r, b.r, fma_(-a.i, b.i, c.r)), fma_(a.r, b.i, fma_(a.i, b.r, c.i))}; }
__device__ __forceinline__ void sw16(float v, float& e, float& o) { const auto r = __builtin_amdgcn_permlane16_swap(f2u(v), f2u(v), false, false); e = u2f(r[0]); o = u2f(r[1]); }
__device__ __forceinline__ void sw32(float v, float& e, float& o) { const auto r = __builtin_amdgcn_permlane32_swap(f2u(v), f2u(v), false, false); e = u2f(r[0]); o = u2f(r[1]); }
constexpr unsigned SPIN_CAP = 1u << 16;
constexpr unsigned OOB = 0x40000000u;

constexpr size_t WS_H = 0;
constexpr size_t WS_KVT = (size_t)8 << 20;
constexpr size_t WS_ZT = (size_t)12 << 20;
constexpr size_t WS_PP = (size_t)16 << 20;
constexpr size_t WS_E = (size_t)24 << 20;
constexpr size_t WS_F = WS_E + (256 << 10);
constexpr int F_H = 0;
constexpr int F_K = 4096;
constexpr int F_Z = 16384;
constexpr int F_EP = F_Z + 32768;
constexpr int F_C = F_EP + 256;
constexpr unsigned WS_P = 25 << 20;
#define KCQ 4
#define KCC 6
#define KVP 0
#define RSL 1
#define KVS 0
#define GCR 273
constexpr int LDS_TOTAL = 163840, BUF = 32768;
constexpr int KC = 98304, KCH = 8320, KCP = 4160;
constexpr int U0 = KC + 4 * KCH;
constexpr int FULL = U0 + 18432 + 8192, FST = 388;
constexpr int DONE = U0 + 18432, DUMP = DONE + 4096;
constexpr int HGO = FULL + 1792, DSK = HGO + 256, POLL = DSK + 256;
static_assert(POLL + 8 * 256 <= LDS_TOTAL && 4 * FST <= 1792, "lds");
constexpr int g1_buf(int s) { return ((0x0213 >> ((s & 3) * 4)) & 3) * BUF; }
constexpr int IMG = KC, QST = KC + 16384, ZST = U0, PLD = 2 * BUF;
#define CODE_KB 47
#define EXACT 0
#define HP 1
#define CP 2
#define FLQ 11
#define R01 3
#define ZP 7
#define SLP 8
#define STR_(x) #x
#define STR(x) STR_(x)
#define NOST 0
#define G2M 0
#define RD 2
#define PRG 1
#define OSC 16
#define NOA 0
#define PPH 0
#define HS 0
#define JQ(j) (HS == 0 ? 0 : HS == 1 ? (j) & 1 : HS == 2 ? (j) : (j) & 2)
#define JR(j) (HS == 0 ? 2 * (j) : HS == 1 ? 2 * ((j) & 2) : HS == 2 ? 0 : 4 * ((j) & 1))
#define NOX 0

__device__ __forceinline__ u32x4 mk_rsrc(const void* p, unsigned bytes) {
    const unsigned long long a = (unsigned long long)p;
    return u32x4{(unsigned)a, (unsigned)(a >> 32) & 0xffffu, bytes, 0x00020000u};
}
__device__ __forceinline__ __amdgpu_buffer_rsrc_t mk_brs(const void* p, unsigned bytes) { return __builtin_amdgcn_make_buffer_rsrc((void*)p, (short)0, bytes, 0x00020000); }
__device__ __forceinline__ void st16(const u32x4& d, __amdgpu_buffer_rsrc_t r, unsigned voff, unsigned soff) {
    __builtin_amdgcn_raw_buffer_store_b128(d, r, voff, soff, 16);
    asm volatile("s_nop 1" :: "v"(d) : "memory");
}
template <int IMM, bool SC1> __device__ __forceinline__ void dma(unsigned lds, unsigned voff, const u32x4& rs, unsigned soff) {
    if constexpr (SC1) asm volatile("s_add_u32 m0, %0, %1\n\ts_nop 0\n\tbuffer_load_dwordx4 %2, %3, %4 offen sc1 lds" :: "s"(lds), "n"(IMM), "v"(voff), "s"(rs), "s"(soff) : "memory", "scc");
    else asm volatile("s_add_u32 m0, %0, %1\n\ts_nop 0\n\tbuffer_load_dwordx4 %2, %3, %4 offen lds" :: "s"(lds), "n"(IMM), "v"(voff), "s"(rs), "s"(soff) : "memory", "scc");
}
template <int IMM, int I> __device__ __forceinline__ void dma4(unsigned lds, unsigned voff, const u32x4& rs, unsigned soff) {
    if constexpr (I == 0) dma<IMM, false>(lds, voff, rs, soff);
    else asm volatile("buffer_load_dwordx4 %0, %1, %2 offen offset:%3 lds" :: "v"(voff), "s"(rs), "s"(soff), "n"(I * 1024) : "memory");
}
template <int IMM> __device__ __forceinline__ void dma1(unsigned lds, unsigned voff, const u32x4& rs, unsigned soff) {
    asm volatile("s_add_u32 m0, %0, %1\n\ts_nop 0\n\tbuffer_load_dword %2, %3, %4 offen sc1 lds" :: "s"(lds), "n"(IMM), "v"(voff), "s"(rs), "s"(soff) : "memory", "scc");
}
__device__ __forceinline__ void flag_set(unsigned waddr, unsigned v) { asm volatile("ds_write_b32 %0, %1" :: "v"(waddr), "v"(v) : "memory"); }
__device__ __forceinline__ void flag_chk(unsigned& f, unsigned raddr, unsigned need) {
    unsigned cnt;
    asm volatile("v_cmp_le_u32 vcc, %3, %0\n\t"
                 "s_cmp_eq_u64 vcc, -1\n\t"
                 "s_cbranch_scc1 1f\n\t"
                 "s_mov_b32 %1, 0x100000\n"
                 "0:\n\t"
                 "ds_read_b32 %0, %2\n\t"
                 "s_sub_u32 %1, %1, 1\n\t"
                 "s_waitcnt lgkmcnt(0)\n\t"
                 "s_cbranch_scc1 1f\n\t"
                 "v_cmp_le_u32 vcc, %3, %0\n\t"
                 "s_cmp_eq_u64 vcc, -1\n\t"
                 "s_cbranch_scc0 0b\n"
                 "1:" : "+v"(f), "=&s"(cnt) : "v"(raddr), "s"(need) : "vcc", "scc", "memory");
}
template <int OFF, int SL = 0> __device__ __forceinline__ void zero_chk(unsigned& f, unsigned raddr) {
    unsigned cnt;
    asm volatile("v_cmp_ne_u32 vcc, 0, %0\n\t"
                 "s_cbranch_vccz 1f\n\t"
                 "s_mov_b32 %1, 0x100000\n"
                 "0:\n\t"
                 ".if %4\n\ts_sleep %4\n\t.endif\n\t"
                 "ds_read_b32 %0, %2 offset:%3\n\t"
                 "s_sub_u32 %1, %1, 1\n\t"
                 "s_waitcnt lgkmcnt(0)\n\t"
                 "s_cbranch_scc1 1f\n\t"
                 "v_cmp_ne_u32 vcc, 0, %0\n\t"
                 "s_cbranch_vccnz 0b\n"
                 "1:" : "+v"(f), "=&s"(cnt) : "v"(raddr), "n"(OFF), "n"(SL) : "vcc", "scc", "memory");
}
template <int IMM, class T> __device__ __forceinline__ void gld(T& d, unsigned voff, const u32x4& rs, unsigned soff) {
    if constexpr (sizeof(T) == 16) asm volatile("buffer_load_dwordx4 %0, %1, %2, %3 offen offset:%4" : "=v"(d) : "v"(voff), "s"(rs), "s"(soff), "n"(IMM) : "memory");
    else if constexpr (sizeof(T) == 8) asm volatile("buffer_load_dwordx2 %0, %1, %2, %3 offen offset:%4" : "=v"(d) : "v"(voff), "s"(rs), "s"(soff), "n"(IMM) : "memory");
    else asm volatile("buffer_load_ushort %0, %1, %2, %3 offen offset:%4" : "=v"(d) : "v"(voff), "s"(rs), "s"(soff), "n"(IMM) : "memory");
}
__device__ __forceinline__ void gld32(unsigned& d, unsigned voff, const u32x4& rs, unsigned soff) { asm volatile("buffer_load_dword %0, %1, %2, %3 offen" : "=v"(d) : "v"(voff), "s"(rs), "s"(soff) : "memory"); }
__device__ __forceinline__ void sld(unsigned& d, const void* p) { asm volatile("s_load_dword %0, %1, 0x0" : "=s"(d) : "s"(p) : "memory"); }
__device__ __forceinline__ void stouch(unsigned long long p) { asm volatile("s_load_dword s101, %0, 0x0\n\ts_load_dword s101, %0, 0x80\n\ts_load_dword s101, %0, 0x100" :: "s"(p) : "s101", "memory"); }
template <class T> __device__ __forceinline__ void ties(T& x) { asm volatile("" : "+s"(x)); }
template <int IMM, class T> __device__ __forceinline__ void gst(const T& d, unsigned voff, const u32x4& rs, unsigned soff) {
    if constexpr (sizeof(T) == 16) asm volatile("buffer_store_dwordx4 %0, %1, %2, %3 offen offset:%4 sc1" :: "v"(d), "v"(voff), "s"(rs), "s"(soff), "n"(IMM) : "memory");
    else asm volatile("buffer_store_dwordx2 %0, %1, %2, %3 offen offset:%4 sc1" :: "v"(d), "v"(voff), "s"(rs), "s"(soff), "n"(IMM) : "memory");
}
template <int SLQ> __device__ __forceinline__ void gate(unsigned raddr, unsigned need) {
    unsigned cnt, f;
    asm volatile("s_mov_b32 %1, 0x40000\n"
                 "0:\n\t"
                 "ds_read_b32 %0, %2\n\t"
                 "s_sub_u32 %1, %1, 1\n\t"
                 "s_waitcnt lgkmcnt(0)\n\t"
                 "s_cbranch_scc1 1f\n\t"
                 "v_cmp_le_u32 vcc, %3, %0\n\t"
                 "s_cmp_eq_u64 vcc, -1\n\t"
                 "s_cbranch_scc1 1f\n\t"
                 "s_sleep %4\n\t"
                 "s_branch 0b\n"
                 "1:" : "=&v"(f), "=&s"(cnt) : "v"(raddr), "s"(need), "n"(SLQ) : "vcc", "scc", "memory");
}
template <int SL = SLP> __device__ __forceinline__ void tag_chk(unsigned& f, unsigned expect, unsigned voff, const u32x4& rs, unsigned soff) {
    unsigned cnt;
    asm volatile("v_cmp_eq_u32 vcc, %2, %0\n\t"
                 "s_cmp_eq_u64 vcc, -1\n\t"
                 "s_cbranch_scc1 1f\n\t"
                 "s_mov_b32 %1, 0x10000\n"
                 "0:\n\t"
                 "s_sleep %6\n\t"
                 "buffer_load_dword %0, %3, %4, %5 offen sc1\n\t"
                 "s_sub_u32 %1, %1, 1\n\t"
                 "s_waitcnt vmcnt(0)\n\t"
                 "s_cbranch_scc1 1f\n\t"
                 "v_cmp_eq_u32 vcc, %2, %0\n\t"
                 "s_cmp_eq_u64 vcc, -1\n\t"
                 "s_cbranch_scc0 0b\n"
                 "1:" : "+v"(f), "=&s"(cnt) : "v"(expect), "v"(voff), "s"(rs), "s"(soff), "n"(SL) : "vcc", "scc", "memory");
}

__device__ __forceinline__ unsigned race(unsigned w, const void* ws, unsigned long long rnd) {
    unsigned v;
    asm volatile("s_cmp_lt_u32 %1, 32\n\t"
                 "s_cbranch_scc0 1f\n\t"
                 "s_mov_b64 s[98:99], %3\n\t"
                 "s_lshl_b32 s96, s98, 7\n\t"
                 "s_lshl_b32 s97, %1, 13\n\t"
                 "s_andn2_b32 s96, s96, 0x2000\n\t"
                 "s_xor_b32 s96, s96, s97\n\t"
                 "s_and_b32 s96, s96, 0x7ff80\n\t"
                 "s_add_u32 s96, s96, %4\n\t"
                 "s_xor_b32 s97, s96, 0x2000\n\t"
                 "s_mov_b32 %0, s98\n\t"
                 "s_load_dword s99, %2, s96\n\t"
                 "s_load_dword s98, %2, s97\n\t"
                 "s_lshr_b32 s96, %1, 4\n\t"
                 "s_xor_b32 s96, s96, %1\n\t"
                 "s_waitcnt lgkmcnt(1)\n\t"
                 "s_sleep %5\n\t"
                 "s_cmp_lg_u32 %0, s98\n\t"
                 "s_addc_u32 %0, s96, 0\n\t"
                 "s_and_b32 %0, %0, 1\n"
                 "1:" : "=&s"(v) : "s"(w), "s"(ws), "s"(rnd), "n"(WS_P), "n"(RSL) : "s96", "s97", "s98", "s99", "scc", "memory");
    return v;
}
__device__ __forceinline__ void vote_chk(unsigned& f, unsigned expect, unsigned odd, unsigned raddr, unsigned lds, unsigned voff, const u32x4& rs, unsigned soff, unsigned& cnt) {
    unsigned t;
    asm volatile("s_mov_b32 %1, 0x10000\n\t"
                 "v_xor_b32 %2, %0, %3\n\t"
                 "v_cmp_gt_u32 vcc, %2, %4\n\t"
                 "s_cbranch_vccz 1f\n\t"
                 "s_mov_b32 m0, %6\n"
                 "0:\n\t"
                 "s_sleep 4\n\t"
                 "buffer_load_dword %7, %8, %9 offen sc1 lds\n\t"
                 "s_sub_u32 %1, %1, 1\n\t"
                 "s_waitcnt vmcnt(0)\n\t"
                 "ds_read_b32 %0, %5\n\t"
                 "s_waitcnt lgkmcnt(0)\n\t"
                 "s_cbranch_scc1 1f\n\t"
                 "v_xor_b32 %2, %0, %3\n\t"
                 "v_cmp_gt_u32 vcc, %2, %4\n\t"
                 "s_cbranch_vccnz 0b\n"
                 "1:" : "+v"(f), "=&s"(cnt), "=&v"(t) : "v"(expect), "v"(odd), "v"(raddr), "s"(lds), "v"(voff), "s"(rs), "s"(soff) : "vcc", "scc", "memory");
}
__device__ __forceinline__ unsigned vote_cnt(unsigned f, unsigned tag, unsigned up, unsigned& n) {
    unsigned r;
    asm volatile("s_xnor_b32 %0, %3, 1\n\tv_cmp_eq_u32 vcc, %0, %2\n\ts_bcnt1_i32_b64 %1, vcc\n\ts_cmp_gt_u32 %1, 16\n\ts_cselect_b32 %0, 0x2000, 0\n\ts_xor_b32 %0, %0, %4"
                 : "=&s"(r), "=&s"(n) : "v"(f), "s"(tag), "s"(up) : "vcc", "scc");
    return r;
}

template <int H> __device__ __forceinline__ void pp_ld(u32x4& d, unsigned voff, const u32x4& rs, unsigned soff) {
    if constexpr (H) asm volatile("buffer_load_dwordx4 v[244:247], %1, %2, %3 offen offset:1024 sc1" : "={v[244:247]}"(d) : "v"(voff), "s"(rs), "s"(soff) : "memory");
    else asm volatile("buffer_load_dwordx4 v[240:243], %1, %2, %3 offen sc1" : "={v[240:243]}"(d) : "v"(voff), "s"(rs), "s"(soff) : "memory");
}
__device__ __forceinline__ void pf_ld(unsigned& d, unsigned voff, const u32x4& rs, unsigned soff) { asm volatile("buffer_load_dword v248, %1, %2, %3 offen sc1" : "={v248}"(d) : "v"(voff), "s"(rs), "s"(soff) : "memory"); }
#define PP_CHK(R, R1, R2, R3, OFF) \
    asm volatile("v_cmp_ne_u32 vcc, 0, %2\n\t" \
                 "s_cbranch_vccz 1f\n\t" \
                 "s_mov_b32 %1, 0x10000\n" \
                 "0:\n\t" \
                 "buffer_load_dwordx4 " R ", %5, %6, %7 offen offset:" OFF " sc1\n\t" \
                 "s_waitcnt vmcnt(0)\n\t" \
                 "s_and_b32 %3, %4, 0xf000f\n\t" \
                 "v_xor_b32 " R1 ", %3, " R1 "\n\t" \
                 "s_lshr_b32 %3, %4, 8\n\t" \
                 "s_and_b32 %3, %3, 0xf000f\n\t" \
                 "v_xor_b32 " R2 ", %3, " R2 "\n\t" \
                 "s_lshr_b32 %3, %4, 4\n\t" \
                 "s_and_b32 %3, %3, 0xf000f\n\t" \
                 "v_xor_b32 " R3 ", %3, " R3 "\n\t" \
                 "v_or3_b32 %2, " R1 ", " R2 ", " R3 "\n\t" \
                 "v_and_b32 %2, 0xf000f, %2\n\t" \
                 "s_sub_u32 %1, %1, 1\n\t" \
                 "s_cbranch_scc1 1f\n\t" \
                 "v_cmp_ne_u32 vcc, 0, %2\n\t" \
                 "s_cbranch_vccnz 0b\n" \
                 "1:" : "+{" R "}"(p), "=&s"(cnt), "+v"(bad), "=&s"(t) : "s"(tag), "v"(voff), "s"(rs), "s"(soff) : "vcc", "scc", "memory")
template <int H> __device__ __forceinline__ void pp_chk(u32x4& p, unsigned bad, unsigned tag, unsigned voff, const u32x4& rs, unsigned soff) {
    unsigned cnt, t;
    if constexpr (H) PP_CHK("v[244:247]", "v245", "v246", "v247", "1024"); else PP_CHK("v[240:243]", "v241", "v242", "v243", "0");
}
template <int H> __device__ __forceinline__ void pp_tie(u32x4& p) { if constexpr (H) asm volatile("" : "+{v[244:247]}"(p)); else asm volatile("" : "+{v[240:243]}"(p)); }
__device__ __forceinline__ float amax3(float a, float b, float c) { float r; asm volatile("v_max3_f32 %0, |%1|, |%2|, |%3|" : "=v"(r) : "v"(a), "v"(b), "v"(c)); return r; }
__device__ __forceinline__ unsigned bfi_v(unsigned m, unsigned a, unsigned b) { unsigned r; asm volatile("v_bfi_b32 %0, %1, %2, %3" : "=v"(r) : "v"(m), "v"(a), "v"(b)); return r; }
__device__ __forceinline__ unsigned bfi_s(unsigned m, unsigned a, unsigned b) { unsigned r; asm volatile("v_bfi_b32 %0, %1, %2, %3" : "=v"(r) : "v"(m), "v"(a), "s"(b)); return r; }
template <int H> __device__ __forceinline__ float cvt16(unsigned w) {
    float r;
    if constexpr (H) asm volatile("v_cvt_f32_i32_sdwa %0, sext(%1) dst_sel:DWORD dst_unused:UNUSED_PAD src0_sel:WORD_1" : "=v"(r) : "v"(w));
    else asm volatile("v_cvt_f32_i32_sdwa %0, sext(%1) dst_sel:DWORD dst_unused:UNUSED_PAD src0_sel:WORD_0" : "=v"(r) : "v"(w));
    return r;
}
__device__ __forceinline__ float sel8(float a, float b, unsigned long long mask) {
    float r;
    asm volatile("s_mov_b64 vcc, %3\n\tv_cndmask_b32_dpp %0, %1, %2, vcc row_ror:8 row_mask:0xf bank_mask:0xf" : "=v"(r) : "v"(a), "v"(b), "s"(mask) : "vcc");
    return r;
}
__device__ __forceinline__ unsigned sel0(unsigned a, unsigned long long mask) { unsigned r; asm volatile("v_cndmask_b32_e64 %0, 0, %1, %2" : "=v"(r) : "v"(a), "s"(mask)); return r; }
constexpr unsigned long long diag_mask(int R) {
    unsigned long long m = 0;
    for (int l = 0; l < 64; ++l) if ((l >> 4) == ((l & 7) >> 1) && 2 * (l & 1) + ((l >> 3) & 1) == R) m |= 1ull << l;
    return m;
}
template <int IMM, int X, bool SC1> __device__ __forceinline__ void dma_x(unsigned& so, unsigned lds, unsigned voff, const u32x4& rs, unsigned base) {
    if constexpr (SC1) asm volatile("s_add_u32 m0, %1, %2\n\ts_xor_b32 %0, %5, %6\n\tbuffer_load_dwordx4 %3, %4, %0 offen sc1 lds" : "=&s"(so) : "s"(lds), "n"(IMM), "v"(voff), "s"(rs), "s"(base), "n"(X) : "memory", "scc");
    else asm volatile("s_add_u32 m0, %1, %2\n\ts_xor_b32 %0, %5, %6\n\tbuffer_load_dwordx4 %3, %4, %0 offen lds" : "=&s"(so) : "s"(lds), "n"(IMM), "v"(voff), "s"(rs), "s"(base), "n"(X) : "memory", "scc");
}
template <int OFF> __device__ __forceinline__ void dma_n(unsigned so, unsigned voff, const u32x4& rs) {
    asm volatile("buffer_load_dwordx4 %0, %1, %2 offen offset:%3 sc1 lds" :: "v"(voff), "s"(rs), "s"(so), "n"(OFF) : "memory");
}
template <int IMM, int OFF> __device__ __forceinline__ void dma_m(unsigned so, unsigned lds, unsigned voff, const u32x4& rs) {
    asm volatile("s_add_u32 m0, %4, %5\n\ts_nop 0\n\tbuffer_load_dwordx4 %0, %1, %2 offen offset:%3 sc1 lds" :: "v"(voff), "s"(rs), "s"(so), "n"(OFF), "s"(lds), "n"(IMM) : "memory", "scc");
}
__device__ __forceinline__ unsigned and_or(unsigned a, unsigned m, unsigned o) { unsigned r; asm("v_and_or_b32 %0, %1, %2, %3" : "=v"(r) : "v"(a), "v"(m), "s"(o)); return r; }

#define HOPS 1
#define HM 0x1ff221125B082ull
#define PG1 95
#define PG2 288
#define PG3 8
#define PG4 24
#define HQ 52
#define HW 4
constexpr int hop_nth(int n) { for (int k = 0; k < 62; ++k) if (((HM >> k) & 1) && n-- == 0) return k; return 63; }
constexpr int hop_next(int K) { int n = 0; for (int k = 0; k < K; ++k) n += (HM >> k) & 1; return hop_nth(n + HW); }
template <int K> __device__ __forceinline__ void hop() {
    if constexpr (HOPS && ((HM >> K) & 1)) asm volatile(".Lfkh%0:\n\ts_cbranch_execz .Lfkh%1" :: "n"(K), "n"(hop_next(K)));
}
template <int K> __device__ __forceinline__ void page() {
    if constexpr (HOPS && K >= 0 && ((HM >> (K & 63)) & 1)) asm volatile("s_branch 1f\n\t.p2align 12\n.Lfkh%0:\n\ts_cbranch_execz .Lfkh%1\n1:" :: "n"(K), "n"(hop_next(K)));
    else if constexpr (HOPS) asm volatile("s_branch 1f\n\t.p2align 12\n1:");
}
__device__ __forceinline__ void hop_all(int w) {
    if constexpr (HOPS) asm volatile("s_mov_b64 exec, 0\n\ts_cmp_eq_u32 %0, 0\n\ts_cbranch_scc1 .Lfkh%1\n\ts_cmp_eq_u32 %0, 1\n\ts_cbranch_scc1 .Lfkh%2\n\t"
                                     "s_cmp_eq_u32 %0, 2\n\ts_cbranch_scc1 .Lfkh%3\n\ts_branch .Lfkh%4\n.Lfkh63:\n\ts_mov_b64 exec, -1"
                                     :: "s"(w), "n"(hop_nth(0)), "n"(hop_nth(1)), "n"(hop_nth(2)), "n"(hop_nth(3)) : "scc");
}
#define NB 1
#define PFQ 0
#define CVR 3
#define SPR 3
#define ELP 6
#define PW 1
#define CPG 95
#define GQ -1
#define GE 3
#define KDL 0
#define GP 1
#define SG 507
#define KO 0
#define DP 0
#define B13 460
#define PRE 0
enum { OP_NONE, OP_A, OP_B, OP_FLAG, OP_SIG, OP_VOTE };
struct Op { int kind, a, b; };
constexpr Op g1_op(int G) {
    const int st = (G + 32) / 32 - 1, q = G - 32 * st, i = q >> 2, j = q & 3;
    if (i < 5) {
        if (st < 0) return {OP_NONE, 0, 0};
        if ((j & 1) == 0) return {OP_A, st * 8 + i + 3, j >> 1};
        if (q == FLQ && st < 15) return {OP_FLAG, 0, 0};
        if (NB && G == SG) return {OP_SIG, 2, 0};
        if (G == GCR) return {OP_VOTE, 0, 0};
        if (NB == 2 && G == 435) return {OP_SIG, 3, 0};
        return {OP_NONE, 0, 0};
    }
    if (st == 15) return NB && G == SG ? Op{OP_SIG, 2, 0} : Op{OP_NONE, 0, 0};
    const int n = (st + 1) * 8;
    switch (q) {
        case 20: return {OP_B, st + 1, 0}; case 21: return {OP_B, st + 1, 1}; case 22: return {OP_A, n, 0}; case 23: return {OP_B, st + 1, 2};
        case 24: return {OP_B, st + 1, 3}; case 25: return {OP_A, n, 1}; case 26: return {OP_A, n + 1, 0}; case 27: return {OP_A, n + 1, 1};
        case 28: return {OP_A, n + 2, 0}; case 30: return {OP_A, n + 2, 1};
    }
    return {OP_NONE, 0, 0};
}
constexpr int g1_need(int G) {
    const int st = G >> 5, j = G & 3;
    for (int h = G - 1; h >= -12; --h) {
        const Op o = g1_op(h);
        if (o.kind == OP_A && o.a == (G >> 2) && o.b == (j >> 1)) return h;
        if (o.kind == OP_B && o.a == st && o.b == j) return h;
        if (o.kind == OP_FLAG && (G & 31) == 20 && (h >> 5) == st) return h;
    }
    return -100;
}
constexpr int g1_wait(int G) {
    int n = -100, c = 0;
    for (int h = G; h < (G ? G + 4 : 2) && h < 512; ++h) if (g1_need(h) > n) n = g1_need(h);
    for (int h = n + 1; h < G; ++h) c += g1_op(h).kind != OP_NONE;
    return n < G ? c : -1;
}
#define KV0 0
#define KVN 0x11111112
constexpr int kv_first(int k) { int n = 0; for (int i = KV0; i < k && i < KV0 + 8; ++i) n += (KVN >> (4 * (i - KV0))) & 15; return n > 8 ? 8 : n; }
constexpr int kv_last() { int k = KV0; while (kv_first(k + 1) < 8) ++k; return k; }
#define CQ -1
#define CTM 0
#define EC 2
#define ED 2
#define EB 0
#define KST 0
#define KFD 3
#define KPL 11
#define KPI 0
#define KSL SLP
#define CBQ 20
constexpr int KVL = kv_last(), KFL = KVL + KFD, KSD = KVS ? KCC : -1;
static_assert(KFL <= KPL && KPL <= 12 && KPL > KVL && (KPI & 3) == 0 && KVP >= 0 && KVP < KCQ && KCQ < KCC && KSD <= KVL && KCC <= KPL && GCR > 32 * KCC + 62, "");
constexpr int hv_cnt(int k0, int k1) {
    int c = 0;
    for (int k = k0; k < k1; ++k) c += 9 + (KST ? 4 * (k == KVL) : k < KSD ? 0 : kv_first(k + 1) / 2 - (k == KSD ? 0 : kv_first(k) / 2)) + (k == KFL) + (k == KPL) + (k == KCQ) + (k == KVP);
    return c;
}
#define ZM 1
#define CL 1
#define EP 0
constexpr unsigned long long lag0_mask(int w) {
    unsigned long long m = 0;
    for (int l = 0; l < 64; ++l) if ((l >> 4) == 2 + ((l >> 3) & 1) && 3 - ((l & 7) >> 1) == w) m |= 1ull << l;
    return m;
}
constexpr int cv_start(int n) { return n * 8 - n * (n - 1) / 2; }
constexpr int cv_n(int q) { int n = 0; while (cv_start(n + 1) <= q) ++n; return n; }
constexpr int cv_m(int q) { return 7 - cv_n(q) - (q - cv_start(cv_n(q))); }
enum { CO_NONE, CO_A, CO_B, CO_C };
constexpr Op cv_op(int G) {
    const int s = (G + 36) / 36 - 1, q = G - 36 * s;
    if (q >= 1 && q <= 6) return s >= 0 ? Op{CO_A, s * 8 + 5 + (q - 1) / 2, (q - 1) & 1} : Op{CO_NONE, 0, 0};
    if (q >= 11 && q <= 14) return s >= 0 && s >= CL && s - CL < 8 ? Op{CO_C, s - CL, q - 11} : Op{CO_NONE, 0, 0};
    if (s == 7) return {CO_NONE, 0, 0};
    for (int m = 0; m < 8; ++m) if (q == cv_start(7 - m)) return {CO_B, (s + 1) * 8 + m, 0};
    for (int n = 0; n < 5; ++n) if (q == cv_start(n + 1) + 1 || q == cv_start(n + 1) + 2) return {CO_A, (s + 1) * 8 + n, q - cv_start(n + 1) - 1};
    return {CO_NONE, 0, 0};
}
constexpr int cv_need(int G) {
    const int s = G / 36, q = G % 36;
    for (int h = G - 1; h >= -36; --h) {
        const Op o = cv_op(h);
        if (o.kind == CO_A && o.a == s * 8 + cv_n(q)) return h;
        if (o.kind == CO_B && o.a == s * 8 + cv_m(q)) return h;
        if (o.kind == CO_C && q == 24 && o.a == s - CL) return h;
    }
    return -100;
}
constexpr int cv_wait(int G) {
    const int n = cv_need(G);
    for (int h = G - 1; h >= 0 && h >= G - 40; --h) if (cv_need(h) >= n) return -1;
    int c = 0;
    for (int h = n + 1; h < G; ++h) c += cv_op(h).kind != CO_NONE;
    return c;
}
#define AT(n, m) ((n) * 8 + (m))
#define FM 0x326260
constexpr int fill_pos(int s, int i) {
    const int f = (FM >> (4 * (s - 2))) & 15, n = s == 2 ? 0 : 4 * s - 9;
    switch (f) {
        case 1: return AT(n + i, 5) - (s == 2 && i == 3) * 6;
        case 2: return AT(n + 1 + i / 2, 5 + 2 * (i & 1));
        case 3: return i < 2 ? AT(n, 5 + 2 * i) : AT(n + i - 1, 5);
        case 4: return AT(n + 2 + i / 2, 5 + 2 * (i & 1));
        case 5: return AT(n, 1 + 2 * i) + (s == 2 ? 4 : 0);
        case 6: return AT(n + 3 + i / 2, 5 + 2 * (i & 1));
        case 7: return AT(n + 3, 5 + 2 * i);
        case 8: return i < 2 ? AT(n + 1, 5 + 2 * i) : AT(n + i, 5);
        case 9: return AT(n + 1 + i, 1);
        case 10: return i < 2 ? AT(n, 1 + 2 * i) : AT(n + 3, 1 + 2 * i);
    }
    return AT(n + i / 2, 5 + 2 * (i & 1));
}
constexpr int g2_fill(int p) {
    for (int k = 8; k < 32; ++k) if (fill_pos(k >> 2, k & 3) == p) return k;
    return -1;
}
#define QX AT(2, 5)
#define QQ AT(3, 2)
#define QZ AT(11, 2)
#define QT AT(3, 4)
constexpr int P_X = QX, P_Q = QQ, P_Z = QZ, P_T = QT;
constexpr bool XL = false;
constexpr bool fill_m0(int k) {
    const int a = fill_pos(k >> 2, (k & 3) - 1), b = fill_pos(k >> 2, k & 3);
    return (P_Q + 2 >= a && P_Q < b) || (P_Z + 2 >= a && P_Z < b) || (P_X + 2 >= a && P_X < b);
}
#define DBG 0
#define XW 4
#define XW2 4
#define QPP0 AT(9, 4)
#define QL0 AT(21, 1)
#define QR0 AT(24, 6)
#define QF2 AT(28, 3)
#define QL1 AT(26, 1)
#define QR1 AT(29, 3)
#define QA AT(16, 4)
#define AD 0
#define D1 2
#define D0 1
constexpr int NS = 33, NR = 29;
constexpr int P_PP0 = QPP0, P_PP1 = AT(16, 4), P_L0 = QL0, P_R0 = QR0, P_F2 = QF2, P_L1 = QL1, P_R1 = QR1, P_ZC = AT(10, 6), P_A = QA;
constexpr int E_PP0 = P_PP0 + NS - 1, E_PP1 = P_PP1 + (NS - 1) / D1, E_R0 = P_R0 + (NR - 1) / D0;
constexpr int ZW = P_A >= AT(20, 4) ? 19 : 15;
static_assert(P_R0 + 14 / D0 >= AT(24, 3) && E_R0 < P_F2 && P_F2 >= AT(24, 4) && P_F2 + 8 <= AT(31, 0) && E_R0 < P_R1, "");
static_assert(P_PP0 >= AT(9, 2) && P_PP0 + 8 <= AT(15, 4) && E_PP0 < P_PP1 && P_A >= AT(16, 4), "");
constexpr bool a_free(int p) {
    return p >= P_A && (AD || !((p >= P_PP1 && p <= E_PP1) || (p >= P_R0 && p <= E_R0) || (p >= P_F2 && p < P_F2 + 16)));
}
constexpr int a_step(int p) {
    if (NOA || !a_free(p)) return -1;
    int c = 0;
    for (int h = P_A; h < p; ++h) c += a_free(h);
    return c <= 48 ? c : -1;
}
constexpr int a_pos(int s) { int p = P_A; while (a_step(p) != s) ++p; return p; }
constexpr int q_rd(int r) { int p = r < 2 ? AT(ZW, 6) + r : a_pos(12 * r - 16) + 1; while ((p & 7) < 4) ++p; return p; }
constexpr int q_tie(int r) { const int n = (q_rd(r) >> 3) + 1; return AT(n, (n & 3) == 3 ? 0 : 4); }
static_assert(NOA || (a_pos(48) < 256 && q_tie(2) <= a_pos(23) && q_tie(3) <= a_pos(35)), "");
constexpr int g2_vm(int p) {
    int c = g2_fill(p) >= 0;
    c += a_step(p) > 0 && a_step(p) % 12 == 0 && !(NOST & 1);
    if (!NOA) c += (p == P_T) + (p == P_Q || p == P_Q + 2) + (p == AT(ZP, 4)) + (p == P_Z || p == P_Z + 2);
    c += p == P_X || p == P_X + 2;
    c += !(NOST & 2) && (p == E_PP0 || p == E_PP1);
    c += p == P_L0 || p == P_L1;
    c += !(NOST & 4) && (p == P_F2 + 11 || p == P_F2 + 15);
    return c;
}
constexpr int g2_lx(int n) {
    int c = 2 * (n == 7);
    if (n >= 0 && (n & 7) >= 6) c += 2;
    if (!NOA) { c += 4 * (n == ZW); for (int r = 0; r < 4; ++r) c += (q_rd(r) >> 3) == n; }
    return c;
}
constexpr int g2_tile(int n, int j) { return (n & 4) ? (n & 2) == 0 : j; }
constexpr int g2_unit(int n, int j) { return (n & 4) ? 4 + 2 * (n & 1) + j : n & 7; }
constexpr int imax(int a, int b) { return a > b ? a : b; }
constexpr int g2_need(int n) {
    int r = n == 3 ? -1 : fill_pos((n + 1) / 4, 3);
    if (n == 7 && !XL) r = imax(r, P_X + 2);
    if (n == ZW && !NOA) r = imax(r, imax(P_Q, P_Z) + 2);
    return r;
}
static_assert(P_Z > AT(11, 0) && P_T > AT(3, 0) && P_T < AT(ZP, 4) && (!XL || (P_X + 2 < P_R0 && P_R0 + 2 >= AT(24, 2) && P_R0 < AT(27, 0) && D0 == 1)), "");
constexpr bool rd1(int n) { return RD == 1 || (RD == 3 && (n & 3) == 2); }
constexpr int g2_allow(int e, int p) { int c = 0; for (int h = p + 1; h < e; ++h) c += g2_vm(h); return c; }

__global__ __launch_bounds__(512, 2) __attribute__((aligned(4096))) void k1(const uint16_t* __restrict__ x, const uint16_t* __restrict__ bre, const uint16_t* __restrict__ bim,
                                             const uint16_t* __restrict__ lre, const uint16_t* __restrict__ lim, const uint16_t* __restrict__ logdt,
                                             const uint16_t* __restrict__ cre, const uint16_t* __restrict__ cim, const uint16_t* __restrict__ dskip,
                                             const uint16_t* __restrict__ ssmk, const uint16_t* __restrict__ ssmD, const uint16_t* __restrict__ qin,
                                             const uint16_t* __restrict__ kin, const uint16_t* __restrict__ vin, const uint16_t* __restrict__ sk,
                                             char* __restrict__ ws, float* __restrict__ out TS_ARG) {
    unsigned long long pc0, rnd;
    asm volatile("s_getpc_b64 %0\n\ts_memtime %1" : "=s"(pc0), "=s"(rnd));
    TS_DECL TS(0);
    TL_DECL
    __shared__ __attribute__((aligned(1024))) char shm[LDS_TOTAL];
    const int tid = threadIdx.x, lane = tid & 63, wid = __builtin_amdgcn_readfirstlane(tid >> 6);
    const int b = blockIdx.x, xcd = b & 7, idx = b >> 3, hb = xcd & 1, tq = idx & 7;
    const int tm = (xcd >> 1) * 4 + (idx >> 3), tn = hb * 8 + tq;
    unsigned nr;
    const int tm2 = (xcd & 4) * 2 + tq, dt = (xcd & 2) * 2 + (idx >> 3), rot2 = tq + JR(idx >> 3);
    const unsigned lbase = (unsigned)(uintptr_t)shm;
    const int l15 = lane & 15, g4 = lane >> 4, lw = wid & 3, kg = wid >> 2;
    dma<U0 + 4096, false>(lbase + wid * 1024, lane * 16, mk_rsrc((const void*)(pc0 & ~255ull), CODE_KB * 1024), ((idx * 8 + wid) & 63) * 1024);
    const unsigned mk0 = lbase + POLL + lw * 256 + kg * (FULL - POLL + lw * (FST - 256));
    flag_set(mk0 + lane * 4, 1u);
    flag_set(lbase + HGO + lane * 4, 0u);
    if constexpr (NB) flag_set(lbase + DONE + lw * FST + lane * 4, 1u);
    unsigned vo[4];
    const bool xw = lw < 2;
#pragma unroll
    for (int i = 0; i < 4; ++i) {
        const int v = l15 ^ (i * 4 + g4);
        vo[i] = ((xw ? 64 * (i & 1) + 4 * (i >> 1) + (lane & 32) + 2 * (g4 & 1) : 8 * (i & 1) + 2 * g4) + (v >> 3)) * 2048 + (v & 7) * 16 - (i & 1) * 1024;
    }
    const int bw = lw >> 1;
    unsigned r0 = (tm * 128 + lw * 16 + bw * (tn * 64 + lw * 16 - 64 - tm * 128)) * 2048, rsz = 229376 - bw * 163840;
    unsigned rot = 8 * ((tq + (idx >> 3)) & 1) + 2 * (xcd >> 1) + 4 * hb, rst = 16384 << bw, lb = lbase + lw * 8192, oob = OOB;
    unsigned lb0 = lb + g1_buf(0) + kg * (g1_buf(1) - g1_buf(0)), so0 = ((kg + rot) & 15) * 128;
    unsigned co = l15 * 2 + (tn * 128 + lw * 32 + kg * 2048), vw = b * 4 + lw + 32 - 32 * kg;
    tie(vo[0]); tie(vo[1]); tie(vo[2]); tie(vo[3]); tie(oob); tie(co);
    ties(r0); ties(rsz); ties(rot); ties(rst); ties(lb); ties(lb0); ties(so0); ties(vw);
    sched();
    asm volatile("" :: "s"(x), "s"(bre), "s"(bim), "s"(lre), "s"(lim), "s"(logdt), "s"(cre), "s"(cim), "s"(dskip), "s"(ssmk), "s"(ssmD), "s"(qin),
                       "s"(kin), "s"(vin), "s"(sk), "s"(ws), "s"(out));
    const bool xv = fresh(bw) == 0;
    const u32x4 rs0 = mk_rsrc((const char*)(xv ? x : bre) + r0, rsz), rs1 = mk_rsrc((const char*)(xv ? x : bim) + r0 - 2048, rsz + 2048);
    unsigned c_dt, c_re, c_im;
    gld<0>(c_dt, co, mk_rsrc(logdt, 2048), 0);
    gld<0>(c_re, co, mk_rsrc(lre, 2048), 0);
    gld<0>(c_im, co, mk_rsrc(lim, 2048), 0);
    const unsigned vb = race(vw, ws, rnd);
    static_for<0, 8>([&](auto ic) { constexpr int i = CV(ic); dma4<(i & 4) * 1024, i & 3>(lb0, vo[i & 3], (i & 2) ? rs1 : rs0, so0 + (i >> 2) * rst); });
    wait_l<0>();
    barrier();
    dma1<0>(mk0 + kg * 120, oob, rs0, 0);
    sched();
    const unsigned a00 = (l15 >> 1) * 256 + ((((l15 & 1) * 8 + g4) ^ (l15 >> 1)) << 4);
    auto frag = [&](int par, int ks) { return (a00 ^ (ks * 64) ^ (par * 128)) + par * 2048; };
    bf16x8 cf[32];
    unsigned vc[4], cad[4];
    auto c_setup = [&]() {
#pragma unroll
        for (int i = 0; i < 4; ++i) { const int v = l15 ^ (i * 4 + g4); vc[i] = (4 * (2 * g4 + (v >> 3)) + 2 * (i & 1) + (i >> 1)) * 2048 + (v & 7) * 16 - i * 1024; }
    };
    const u32x4 rsc = mk_rsrc((kg ? cim : cre) + (size_t)dt * 131072, 262144);
    auto c_off = [&](int dg, int j) { return (unsigned)(dg * 65536 + hb * 1024 + ((rot2 + j) & 7) * 128); };
    const unsigned cst = lbase + (kg ? BUF : 2 * BUF) + lw * 8192;
    unsigned tag;
    const u32x4 rsf = mk_rsrc(ws + WS_F, 65536);
    const __amdgpu_buffer_rsrc_t rf = mk_brs(ws + WS_F, 65536);
    const int qx = (hb ? 0 : 2) ^ JQ(idx >> 3), wid_ = wid, b_ = b;

    if (wid >= 4) {
        page<-1>();
        const int ch = xcd * 128 + (idx >> 2) * 16 + lw * 4 + (idx & 3);
        unsigned dw, nx; u32x4 ad;
        auto c_unit = [&](auto uc) { static_for<0, 4>([&](auto ic) { constexpr int u = CV(uc), i = CV(ic); dma4<(u & 1) * 4096, i>(cst, vc[i], rsc, c_off(lw, u)); }); };
        constexpr int XV = 4 * (EC == 1) + 8 * (PFQ == 2);
        auto ctouch = [&](int r) { const unsigned j = (fresh(idx * 4 + lw) + r * 37) & 127; stouch((pc0 & ~255ull) + (j < CODE_KB * 8 / 3 ? j : j - 64) * 384); };
        {
            const unsigned fb = lbase + FULL + lw * FST;
            auto piece = [&](auto sc, auto ic) {
                constexpr int s = CV(sc), i = CV(ic);
                dma4<g1_buf(s) + (i & 4) * 1024, i & 3>(lb, vo[i & 3], (i & 2) ? rs1 : rs0, ((s + rot) & 15) * 128 + (i >> 2) * rst);
            };
            auto mark = [&](auto sc) { dma1<(31 - CV(sc)) * 4>(fb, oob, rs0, 0); };
            auto feed = [&](auto kc, auto ic) {
                constexpr int k = CV(kc), i = CV(ic);
                if constexpr (i < 4) piece(IC(k + 3), IC(i + 4));
                if constexpr (i == 3) mark(IC(k + 3));
                if constexpr (i >= 4 && k < 12) piece(IC(k + 4), IC(i - 4));
            };
            __builtin_amdgcn_s_setprio(HP);
            TS(7);
            sld(tag, ws + WS_F + F_EP);
            sld(dw, ssmD + (ch & ~1));
            const int sb = idx * 2 + (lw >> 1), kh = lw & 1;
            u32x2 kk[11], vv[8]; u32x4 w4[2], kd[4], ke[4], kvo[4];
            const unsigned ko = g4 * 16384 + l15 * 8, so = sb * 65536 + xcd * 256 + kh * 128;
            {
                const u32x4 rs = mk_rsrc(kin, 4 << 20);
                gld<0>(w4[0], l15 * 32, mk_rsrc(sk, 8192), xcd * 1024 + kh * 512);
                gld<16>(w4[1], l15 * 32, mk_rsrc(sk, 8192), xcd * 1024 + kh * 512);
                static_for<0, 3>([&](auto ic) { gld<0>(kk[CV(ic)], ko + so - 6144 + CV(ic) * 2048, rs, 0); });
                static_for<0, 8>([&](auto ic) { constexpr int i = CV(ic); gld<(i & 1) * 2048>(kk[3 + i], ko, rs, so + (i >> 1) * 4096); });
                static_for<0, 8>([&](auto ic) { constexpr int i = CV(ic); gld<(i & 1) * 2048>(vv[i], ko, mk_rsrc(vin, 4 << 20), so + (i >> 1) * 4096); });
                dma1<DSK>(lbase, lane * 4, mk_rsrc(dskip, 2048), dt * 256);
                dma<U0, false>(lbase + kh * 1024, oob, rs0, 0);
            }
            static_for<4, 8>([&](auto ic) { feed(IC(-2), ic); });
            const __amdgpu_buffer_rsrc_t rk = mk_brs(ws + WS_KVT + (size_t)((xcd & 3) * 32 + kh * 16) * 32768, 524288);
            const unsigned K = lbase + KC + lw * KCH;
            unsigned kfo;
            const unsigned n0 = (xcd & 4) << 11;
            auto st = [&](int c) { st16(kvo[c], rk, (l15 >> 2) * 131072 + g4 * 64 + (l15 & 3) * 16, c * 32768 + (sb >> 5) * 16384 + (KVS ? nx : n0 ^ ((sb & 32) << 8)) + (sb & 31) * 256); };
            float kf[7], pa[4], pb[4];
            auto kv_step = [&](auto uc, auto jc) {
                constexpr int u = CV(uc), j = CV(jc), c = u >> 1, h = u & 1;
                auto el = [&](const u32x2& q) -> float { return (c & 1) ? bfhi(q[c >> 1]) : bflo(q[c >> 1]); };
                auto tap = [&](int t) { const unsigned w = w4[c >> 1][(c & 1) * 2 + (t >> 1)]; return (t & 1) ? bfhi(w) : bflo(w); };
                auto all = [&](auto f) {
#pragma unroll
                    for (int i = 0; i < 4; ++i) f(i);
                };
                if constexpr (j == 0) {
#pragma unroll
                    for (int i = 0; i < 7; ++i) { kf[i] = (h && i < 3) ? kf[i + 4] : el(kk[4 * h + i]); tie(kf[i]); }
                }
                if constexpr (EXACT) {
                    if constexpr (j == 1) { const float t = tap(3); all([&](int i) { pa[i] = kf[i] * t; }); all([&](int i) { pa[i] = rbf(pa[i]); tie(pa[i]); }); }
                    if constexpr (j == 2 || j == 4 || j == 6) { const float t = tap(3 - j / 2); all([&](int i) { pb[i] = kf[i + j / 2] * t; }); all([&](int i) { pb[i] = rbf(pb[i]); tie(pb[i]); }); }
                    if constexpr (j == 3 || j == 5 || j == 7) { all([&](int i) { pa[i] += pb[i]; }); all([&](int i) { pa[i] = rbf(pa[i]); tie(pa[i]); }); }
                } else {
                    if constexpr (j == 1) { const float t = tap(3); all([&](int i) { pa[i] = kf[i] * t; tie(pa[i]); }); }
                    if constexpr (j >= 2 && j <= 4) { const float t = tap(4 - j); all([&](int i) { pa[i] = fma_(kf[i + j - 1], t, pa[i]); tie(pa[i]); }); }
                    if constexpr (j == 5) all([&](int i) { pa[i] = rbf(pa[i]); tie(pa[i]); });
                }
                if constexpr (j == (EXACT ? 7 : 6)) all([&](int i) { pb[i] = el(vv[4 * h + i]); tie(pb[i]); });
                if constexpr (j == 7) {
                    all([&](int i) { pa[i] *= pb[i]; });
                    kvo[c][3 - 2 * h] = pack2(pa[1], pa[0]); kvo[c][2 - 2 * h] = pack2(pa[3], pa[2]);
                    if constexpr (h && !KST && u >= kv_first(KSD)) st(c);
                    else if constexpr (u == 7) all(st);
                    else tie(kvo[c]);
                }
            };
            static_for<-1, 13>([&](auto kc) {
                constexpr int k = CV(kc);
                if constexpr (k == 3) page<1>();
                if constexpr (k == 2) hop<0>();
                if constexpr (k == 6) hop<6>();
                if constexpr (k == 8) hop<9>();
                if constexpr (k >= 0) barrier();
                if constexpr (k >= 0 && ((CTM >> k) & 1)) ctouch(k);
                if constexpr (k == KV0) {
                    wait_v<4 + hv_cnt(-1, k)>();
                    static_for<0, 11>([&](auto ic) { tie(kk[CV(ic)]); });
                    static_for<0, 8>([&](auto ic) { tie(vv[CV(ic)]); });
                    tie(w4[0]); tie(w4[1]); keep(c_dt); keep(c_re); keep(c_im);
                    TS(8);
                }
                if constexpr (k == KVP) {
                    wait_l<0>();
                    ties(tag); ties(dw);
                    tag += 0x01010101u;
                    __builtin_amdgcn_raw_buffer_store_b64(u32x2{tag, ~tag ^ vb}, rf, lane || vw >= 32 ? OOB : 0u, F_C + vw * 8, 16);
                }
                if constexpr (k == KCQ) dma1<POLL>(lbase + lw * 256, lane * 4, rsf, F_C);
                if constexpr (k == KCC) {
                    unsigned f, n, sp;
                    wait_v<hv_cnt(KCQ, k) - 1>();
                    dsr32<POLL - 131072>(f, lbase + 131072 + lw * 256 + lane * 4);
                    wait_l<0>();
                    vote_chk(f, (lane & 1) ? ~tag : tag, lane & 1, lbase + POLL + lw * 256 + lane * 4, lbase + POLL + lw * 256, lane * 4, rsf, F_C, sp);
                    nr = vote_cnt(f, tag, (xcd & 4) << 11, n);
                    nx = nr ^ ((xcd & 2) << 12);
                    kfo = F_K + (KVS ? nr : n0) + (((xcd & 3) * 2 + (idx >> 4)) * 64 + lw * 16) * 8;
                    TSV(11, n + 100 * (0x10000 - sp));
                }
                if constexpr (k == KFL) {
                    wait_v<hv_cnt(KVL + 1, k)>();
                    __builtin_amdgcn_raw_buffer_store_b64(u32x2{tag, ~tag}, rf, lane ? OOB : 0u, F_K + (KVS ? nr : n0) + (((xcd & 3) * 2 + kh) * 64 + sb) * 8, 16);
                    TS(10);
                }
                if constexpr (k == 12) {
                    dma1<0>(K, oob, rs0, 0);
                    dma1<KCP>(K, oob, rs0, 0);
                }
                static_for<0, 8>([&](auto ic) {
                    constexpr int i = CV(ic), u0 = kv_first(k), nu = kv_first(k + 1) - u0;
                    if constexpr (k == KPL && i == KPI) dma1<POLL>(lbase + wid * 256, (lane & 31) * 4, rsf, kfo);
                    feed(kc, ic);
                    if constexpr (k == 4 && i == 4) hop<3>();
                    if constexpr (!KST && k == KSD && (i & 1) && i / 2 < kv_first(KSD) / 2) st(i / 2);
                    if constexpr (nu == 1) kv_step(IC(u0), ic);
                    if constexpr (nu == 2) { kv_step(IC(u0 + i / 4), IC(2 * (i & 3))); kv_step(IC(u0 + i / 4), IC(2 * (i & 3) + 1)); }
                    if constexpr (!KDL && k == 12 && i >= 4) gld<(i - 4) * 1024>(kd[i - 4], lane * 16, mk_rsrc(ssmk, 4 << 20), ch * 4096);
                    if constexpr (k == 11 && i == 4) c_setup();
                    if constexpr (k == 10 && i >= 4) { cad[i - 4] = cst + frag(i & 1, (i >> 1) & 1); tie(cad[i - 4]); }
                    sched();
                });
            });
            page<12>();
            TS(1);
            if constexpr (EB == 2) barrier();
            unsigned pf;
            const unsigned fr = lbase + FULL + (lane & 3) * FST;
            wait_v<hv_cnt(KPL, 12) - (KPL < 12 ? 1 : 2) - (KPL == KFL) - KPI - (KPI > 3) + (KDL ? 7 : 11)>();
            dsr32<POLL - 131072>(pf, lbase + 131072 + wid * 256 + lane * 4);
            wait_l<0>();
            tag_chk<KSL>(pf, (lane & 1) ? ~tag : tag, (lane & 31) * 4, rsf, kfo);
            if constexpr (KDL) static_for<0, 4>([&](auto ic) { gld<CV(ic) * 1024>(kd[CV(ic)], lane * 16, mk_rsrc(ssmk, 4 << 20), ch * 4096); });
            auto b13 = [&]() { if constexpr (NB == 2) { unsigned f = 1; zero_chk<12, 1>(f, lbase + DONE + (lane & 3) * FST); } else if constexpr (NB < 2) barrier(); };
            if constexpr (EB == 1) b13();
            {
                const u32x4 rs = mk_rsrc(ws + WS_KVT + (size_t)((xcd & 3) * 32 + idx) * 32768, 32768);
                static_for<0, 4>([&](auto ic) { constexpr int i = CV(ic); dma<U0 + 2048 + i * 1024, true>(lbase + lw * 4096, (lane >> 2) * 64 + (((lane & 3) ^ g4) << 4), rs, (lw >> 1) * 16384 + (KVS ? nx : n0 ^ ((lw & 2) << 12)) + (lw & 1) * 4096 + i * 1024); });
            }
            mark(IC(16));
            auto warm = [&]() {
                const unsigned wo = g4 * 2048 + l15 * 16, dl = lbase + lw * 1024;
                const unsigned qo = (512 * (idx >> 3) + 128 * (idx & 1) + 32 * lw + 4 * ((idx >> 1) & 3)) * 2048 + xcd * 256, xo = (tm2 * 128 + 32 * lw + 8 * (qx & 2)) * 2048 + dt * 256;
                static_for<0, 4>([&](auto ic) { constexpr int i = CV(ic); dma<DUMP, false>(dl, wo, mk_rsrc(qin, 4 << 20), qo + ((i & 1) * 16 + (i >> 1) * 256) * 2048); });
                static_for<0, 4>([&](auto ic) { constexpr int i = CV(ic); dma<DUMP, false>(dl, wo, mk_rsrc(x, 4 << 20), xo + i * 8192); });
            };
            if constexpr (PFQ == 1) warm();
            if constexpr (EB == 0) b13();
            auto c_first = [&]() {
                if constexpr (CL <= 1) c_unit(IC(0));
                if constexpr (CL <= 0) c_unit(IC(1));
                if constexpr (EC == 1) static_for<0, 4>([&](auto ic) { constexpr int i = CV(ic); dma4<DUMP, i>(lbase, vc[i], mk_rsrc(cre + (size_t)dt * 131072, 262144), c_off(lw, 0)); });
                if constexpr (PFQ == 2) warm();
            };
            static_assert(NB != 3 || CL == 2, "");
            if constexpr (!KO) c_first();
            wait_v<5 + 8 * (PFQ == 1) + (KO ? 0 : 4 * (CL <= 1) + 4 * (CL <= 0) + XV)>();
            static_for<0, 4>([&](auto ic) { tie(kd[CV(ic)]); });
#pragma unroll
            for (int n = 0; n < 4; ++n) {
                const unsigned first = n ? __builtin_amdgcn_readlane(kd[n ? n - 1 : 0][3], 63) : 0u;
                const unsigned p = __builtin_amdgcn_update_dpp(first, kd[n][3], 0x138, 0xf, 0xf, false);
                ke[n] = u32x4{__builtin_amdgcn_alignbyte(kd[n][0], p, 2), __builtin_amdgcn_alignbyte(kd[n][1], kd[n][0], 2),
                              __builtin_amdgcn_alignbyte(kd[n][2], kd[n][1], 2), __builtin_amdgcn_alignbyte(kd[n][3], kd[n][2], 2)};
                tie(ke[n]);
            }
            if constexpr (ZM) {
                const unsigned dl = (ch & 1) ? dw >> 16 : dw & 0xffffu, dv = dl << ((~lane & 1) * 16);
                static_for<0, 4>([&](auto ic) { constexpr unsigned long long k = lag0_mask(CV(ic)); ad[CV(ic)] = sel0(dv, k); });
                tie(ad);
            }
            hop<14>();
            static_for<0, 4>([&](auto ic) { constexpr int i = CV(ic); dsw128<64 + i * 1024>(K + lane * 16, kd[i]); dsw128<KCP + 64 + i * 1024>(K + lane * 16, ke[i]); });
            if constexpr (KO) c_first();
            TS(9);
            static_for<0, -CL>([&](auto uc) {
                constexpr int u = CV(uc);
                wait_v<4 + (u < 2 ? XV : 0)>();
                static_for<0, 4>([&](auto ic) { constexpr int i = CV(ic); dsr128<(u & 1) * 4096>(cf[u * 4 + i], cad[i]); });
                wait_l<0>();
                static_for<0, 4>([&](auto ic) { tie(cf[u * 4 + CV(ic)]); });
                c_unit(IC(u + 2));
            });
            pf = 1;
            zero_chk<15 * 4>(pf, fr);
            if constexpr (PRE) { pf = 1; zero_chk<PRE == 1 ? 8 : 4>(pf, lbase + DONE + (lane & 3) * FST); }
            __builtin_amdgcn_s_setprio(0);
        }
        TS(2);
        TL(1);
        unsigned ka[4], ub[4];
#pragma unroll
        for (int i = 0; i < 4; ++i) {
            ka[i] = lbase + KC + lw * KCH + ((l15 & 1) ? 0 : KCP) + 84 + 2 * (l15 & 14) - 16 * g4 + i * 1024;
            const int e = 2 * l15 + g4 + 2;
            ub[i] = lbase + U0 + 64 * e + ((lw ^ (((e >> 2) + 3 - i) & 3)) << 4);
        }
        unsigned gad = lbase + DONE + lw * FST;
        if constexpr (GQ >= 0) tie(gad);
        f32x4 acc[8];
        bf16x4 FA[8][2]; bf16x8 FB[8];
        auto op = [&](auto gc) {
            constexpr Op o = cv_op(CV(gc));
            if constexpr (o.kind == CO_A) { constexpr int s = o.a >> 3, n = o.a & 7, d = (n & 1) * 128 + s * 16 + o.b * 2; dsr2<d, d + 1>(FA[n][o.b], ka[n >> 1]); }
            if constexpr (o.kind == CO_B) { constexpr int s = o.a >> 3, m = o.a & 7; dsr128<2048 * m + 1792 - 256 * s>(FB[m], ub[s & 3]); }
            if constexpr (o.kind == CO_C) dsr128<(o.a & 1) * 4096>(cf[o.a * 4 + o.b], cad[o.b]);
        };
        static_for<-36, 0>(op);
        static_for<0, 288>([&](auto gc) {
            constexpr int G = CV(gc), s = G / 36, q = G % 36, n = cv_n(q), m = cv_m(q);
            if constexpr (cv_wait(G) >= 0) wait_lr<cv_wait(G)>();
            if constexpr (q == 24 && s >= CL && s - CL < 8) static_for<0, 4>([&](auto ic) { tie(cf[(s - CL) * 4 + CV(ic)]); });
            if constexpr (ZM && G < 8) mfma<true>(acc[m], __builtin_bit_cast(bf16x8, ad), FB[m]);
            mfma<s == 0 && n == 0 && !ZM>(acc[n + m], cat(FA[n][0], FA[n][1]), FB[m]);
            sched();
            if constexpr (q == 11) TL(2 + 2 * s);
            if constexpr (q == 11 && s >= CL && s - CL < 8) wait_v<(s - CL < 7 ? 4 : 0) + (s - CL < (CL <= 1) + (CL <= 0) ? XV : 0) + 8 * (PFQ >= 3 && (s == PFQ - 2 || s == PFQ - 1))>();
            if constexpr (q == 11) TL(3 + 2 * s);
            op(gc);
            if constexpr (q == 30 && ((CTM >> (16 + s)) & 1)) ctouch(16 + s);
            if constexpr (G == 16) hop<17>();
            if constexpr (G == 66) hop<19>();
            if constexpr (G == PG1) page<21>();
            if constexpr (G == PG2) page<28>();
            if constexpr (G == 158) hop<23>();
            if constexpr (G == 209) hop<25>();
            if constexpr (G == 254) hop<27>();
            if constexpr (G == GQ) { unsigned f = 1; zero_chk<4, 1>(f, gad); }
            if constexpr (!NB && G == CBQ) barrier();
            if constexpr (NB == 3 && G == 23) { unsigned f = 1; zero_chk<8, 1>(f, lbase + DONE + (lane & 3) * FST); }
            if constexpr ((q == 24 || q == 25 || q == 29 || q == 34) && s + 2 - CL < 8) {
                constexpr int pc = q == 24 ? 0 : q == 25 ? 1 : q == 29 ? 2 : 3, u = s + 2 - CL;
                dma4<(u & 1) * 4096, pc>(cst, vc[pc], rsc, c_off(lw, u));
            }
            sched();
        });
        if constexpr (PG2 >= 288) page<28>();
        TL(18);
        TS(3);
        {
            const __amdgpu_buffer_rsrc_t rz = mk_brs(ws + WS_ZT, 4 << 20);
            if constexpr (ZM) {
                asm volatile("s_nop 7");
#pragma unroll
                for (int jj = 0; jj < 4; ++jj) {
                    tie(acc[2 * jj]); tie(acc[2 * jj + 1]);
                    const f32x4 a = acc[2 * jj], c = acc[2 * jj + 1];
                    st16(u32x4{pack2(a[0], a[1]), pack2(a[2], a[3]), pack2(c[0], c[1]), pack2(c[2], c[3])}, rz, lane * 16 + jj * 1024, (ch & 510) * 8192 + nx + (ch & 1) * 4096);
                }
            } else {
            u32x2 kvr[8];
            const unsigned kva = lbase + U0 + 2048 + 128 * l15 + 64 * (g4 >> 1) + ((lw ^ ((l15 >> 1) & 3)) << 4) + 8 * (1 - (g4 & 1));
            static_for<0, 8>([&](auto ic) { dsr64<CV(ic) * 2048>(kvr[CV(ic)], kva); });
            wait_l<0>();
            const float Dd = (ch & 1) ? bfhi(dw) : bflo(dw);
#pragma unroll
            for (int jj = 0; jj < 4; ++jj) {
                u32x4 o;
#pragma unroll
                for (int u = 0; u < 2; ++u) {
                    const int j = 2 * jj + u;
                    tie(kvr[j]); tie(acc[j]);
                    const float kv3 = bflo(kvr[j][0]), kv2 = bfhi(kvr[j][0]), kv1 = bflo(kvr[j][1]), kv0 = bfhi(kvr[j][1]);
                    o[2 * u] = pack2(rbf(acc[j][0]) + rbf(kv0 * Dd), rbf(acc[j][1]) + rbf(kv1 * Dd));
                    o[2 * u + 1] = pack2(rbf(acc[j][2]) + rbf(kv2 * Dd), rbf(acc[j][3]) + rbf(kv3 * Dd));
                }
                st16(o, rz, lane * 16 + jj * 1024, (ch & 510) * 8192 + nx + (ch & 1) * 4096);
                if (jj == 1) { sched(); hop<29>(); sched(); }
            }
            }
            TS(8);
            hop<30>();
            if constexpr (CL > 0) {
                wait_v<4>();
                static_for<32 - 4 * CL, 32>([&](auto ic) { constexpr int i = CV(ic); dsr128<((i >> 2) & 1) * 4096>(cf[i], cad[i & 3]); });
                wait_l<0>();
                static_for<32 - 4 * CL, 32>([&](auto ic) { tie(cf[CV(ic)]); });
            }
        }
        TL(19);
        TS(4);
    } else {
        f32x4 acc[8][2];
        cf32 L, cc, M, M2, MG, NG, N4, N8, ag;
        {
            unsigned aad[4], bad[4];
#pragma unroll
            for (int i = 0; i < 4; ++i) { aad[i] = lbase + frag(i & 1, i >> 1); bad[i] = aad[i] + 16384 + wid * 4096; }
            const unsigned fr = lbase + FULL + (lane & 3) * FST;
            sld(tag, ws + WS_F + F_EP);
            bf16x8 fa[4][2], fb[2][4];
            unsigned fl = 1, dz = 0, dw_ = lbase + DONE + wid * FST + lane * 4, vt = lbase + POLL + wid * 256 + lane * 4;
            tie(dz); tie(dw_); tie(vt);
            auto op = [&](auto gc) {
                constexpr int G = CV(gc);
                constexpr Op o = g1_op(G);
                if constexpr (o.kind == OP_A) {
                    constexpr int bo = g1_buf(o.a >> 3), hi = bo >= 65536 ? 65536 : 0, i = o.a & 7;
                    dsr128<bo - hi + (i >> 1) * 4096>(fa[i & 3][o.b], aad[(i & 1) + 2 * o.b] + hi);
                }
                if constexpr (o.kind == OP_B) { constexpr int bo = g1_buf(o.a), hi = bo >= 65536 ? 65536 : 0; dsr128<bo - hi>(fb[o.a & 1][o.b], bad[o.b] + hi); }
                if constexpr (o.kind == OP_FLAG) dsr32<(30 - (G >> 5)) * 4>(fl, fr);
                if constexpr (o.kind == OP_SIG) dsw32<o.a * 4>(dw_, dz);
                if constexpr (o.kind == OP_VOTE) dsr32<0>(vt, vt);
            };
            auto consts = [&]() {
                float ld, ar, ai;
                asm volatile("s_waitcnt vmcnt(9)\n\tv_lshlrev_b32 %0, 16, %3\n\tv_lshlrev_b32 %1, 16, %4\n\tv_lshlrev_b32 %2, 16, %5"
                             : "=&v"(ld), "=&v"(ar), "=&v"(ai) : "v"(c_dt), "v"(c_re), "v"(c_im) : "memory");
                const float dtv = rbf(expf(ld)), ea = expf(dtv * ar);
                float sn, cs;
                sincosf(dtv * ai, &sn, &cs);
                L = cf32{ea * cs, ea * sn};
                M = L;
#pragma unroll
                for (int e = 0; e < 5; ++e) M = cmul(M, M);
                M2 = cmul(M, M);
                const cf32 N = cmul(M2, M2), N2 = cmul(N, N), one = {1.f, 0.f};
                N4 = cmul(N2, N2); N8 = cmul(N4, N4);
                auto sel = [](bool c, cf32 a, cf32 b) { return cf32{c ? a.r : b.r, c ? a.i : b.i}; };
                MG = cmul(sel(g4 & 1, M, one), sel(g4 & 2, M2, one));
                NG = cmul(sel(g4 & 1, N, one), sel(g4 & 2, N2, one));
                const float nm = L.r - 1.f, den = ar * ar + ai * ai;
                cc = cf32{2.f * ((nm * ar + L.i * ai) / den), 2.f * ((L.i * ar - nm * ai) / den)};
                auto tc = [&](cf32& v) { tie(v.r); tie(v.i); };
                tc(L); tc(cc); tc(M); tc(M2); tc(MG); tc(NG); tc(N4); tc(N8);
                sched();
            };
            if constexpr (CQ < 0) consts();
            __builtin_amdgcn_s_setprio(CP);
            zero_chk<0>(fl, lbase + POLL + (lane & 3) * 256);
            ties(tag);
            tag += 0x01010101u;
            TS(1);
            static_for<-12, 0>(op);
            static_for<0, 512>([&](auto gc) {
                constexpr int G = CV(gc), st = G >> 5, i = (G >> 2) & 7, j = G & 3;
                if constexpr ((G == 0 || j == 2) && G <= 506) { static_assert(g1_wait(G) >= 0, ""); wait_lr<G == 506 ? 0 : g1_wait(G)>(); }
                if constexpr (G == CQ) { asm volatile("s_waitcnt lgkmcnt(0)\n\ts_nop 7" ::: "memory"); consts(); }
                if constexpr (G == HQ) hop_all(wid);
                if constexpr ((G & 31) == 20 && st < 15) zero_chk<(30 - st) * 4>(fl, fr);
                if constexpr (((G & 31) == 30 && st < 13) || (NB < 2 && G == B13)) barrier();
                if constexpr ((G & 31) == 20) TL(st);
                mfma<st == 0 && j < 2>(acc[i][j & 1], fa[i & 3][j >> 1], fb[st & 1][j]);
                sched();
                op(gc);
                if constexpr (G == GCR + 16) { unsigned n; nr = vote_cnt(vt, tag, (xcd & 4) << 11, n); }
                if constexpr (G == 31) page<-1>();
                if constexpr (G == 95) hop<2>();
                if constexpr (G == 159) hop<4>();
                if constexpr (G == 207) hop<5>();
                if constexpr (G == 255) page<7>();
                if constexpr (G == 319) hop<8>();
                if constexpr (G == 383) hop<10>();
                if constexpr (G == 431) hop<11>();
                if constexpr (G == 479) page<13>();
                if constexpr (G == 509) hop<15>();
                if constexpr (G == 489) hop<40>();
                if constexpr (G == 494) hop<41>();
                if constexpr (G == 499) hop<42>();
                if constexpr (G == 504) hop<43>();
                if constexpr (G >= 487) {
                    constexpr int k = G - 486;
                    if constexpr (k == 1) { asm volatile("s_nop 3"); ag = cf32{acc[0][0][0], acc[0][1][0]}; }
                    cf32 t;
                    asm volatile("v_fma_f32 %0, -%3, %5, %6\n\tv_fma_f32 %1, %3, %4, %7\n\tv_fmac_f32 %0, %2, %4\n\tv_fmac_f32 %1, %2, %5"
                                 : "=&v"(t.r), "=&v"(t.i) : "v"(L.r), "v"(L.i), "v"(ag.r), "v"(ag.i), "v"(acc[k >> 2][0][k & 3]), "v"(acc[k >> 2][1][k & 3]));
                    ag = t;
                }
                sched();
            });
        }
        __builtin_amdgcn_s_setprio(3);
        TL(16);
        TS(2);
#pragma unroll
        for (int k = 26; k < 32; ++k) ag = cfma(L, ag, cf32{acc[k >> 2][0][k & 3], acc[k >> 2][1][k & 3]});
        tie(ag.r); tie(ag.i); sched(); hop<44>(); sched();
        cf32 ev_, od, p01, p23;
        sw16(ag.r, ev_.r, od.r); sw16(ag.i, ev_.i, od.i);
        const cf32 pp = cfma(M, ev_, od);
        sw32(pp.r, p01.r, p23.r); sw32(pp.i, p01.i, p23.i);
        cf32 E = cfma(M2, p01, p23);

        const __amdgpu_buffer_rsrc_t rse = mk_brs(ws + WS_E + tn * 16384 + wid * 2048, 16384);
        st16(u32x4{f2u(E.r), f2u(E.i), tag, ~tag}, rse, g4 ? OOB : l15 * 16, nr + (tm & 7) * 256);
        sched();
        TL(17);
        TS(3);
        hop<45>();
        sched();
        c_setup();
#pragma unroll
        for (int i = 0; i < 4; ++i) cad[i] = lb + frag(i & 1, i >> 1);
        auto c_read = [&](auto rc) { static_for<0, 8>([&](auto ic) { constexpr int r = CV(rc), i = CV(ic); dsr128<(i >> 2) * 4096>(cf[r * 8 + i], cad[i & 3] + ((r & 1) ? 0 : 2 * BUF)); }); };
        auto c_tie = [&](auto rc) { static_for<0, 8>([&](auto ic) { tie(cf[CV(rc) * 8 + CV(ic)]); }); };
        auto c_unit = [&](auto uc) { static_for<0, 4>([&](auto ic) { constexpr int u = CV(uc), i = CV(ic); dma4<((u & 2) ? 0 : 2 * BUF) + (u & 1) * 4096, i>(lb, vc[i], rsc, c_off(wid, u)); }); };
        auto c_req = [&](auto rc) { c_unit(IC(CV(rc) * 2)); c_unit(IC(CV(rc) * 2 + 1)); };
        const float c2r = cc.r, c2i = cc.i;
        auto scale = [&](int i) {
#pragma unroll
            for (int j = 0; j < 4; ++j) {
                const float br = acc[i][0][j], bi = acc[i][1][j];
                acc[i][0][j] = fma_(c2r, br, -(c2i * bi)); acc[i][1][j] = fma_(-c2r, bi, -(c2i * br));
            }
            tie(acc[i][0]); tie(acc[i][1]);
        };
        auto scales = [&](auto ac, auto bc) {
            static_for<CV(ac), CV(bc)>([&](auto ic) {
                constexpr int i = CV(ic);
                scale(i);
                if constexpr (i & 1) { sched(); hop<i == 1 ? 46 : i == 3 ? 16 : i == 5 ? 47 : 48>(); sched(); }
            });
        };
        constexpr int CA = CVR == 0 ? 0 : CVR == 4 ? 1 : 2;
        if constexpr (NB) {
            unsigned dn = 1;
            scales(IC(0), IC(DP));
            if constexpr (CA) { zero_chk<8>(dn, lbase + DONE + (lane & 3) * FST); static_for<0, CA>(c_unit); }
            if constexpr (SPR != 3) __builtin_amdgcn_s_setprio(SPR);
            sched();
            scales(IC(DP), IC(ELP));
            if constexpr (!CA) zero_chk<8>(dn, lbase + DONE + (lane & 3) * FST);
        } else {
            scales(IC(0), IC(ELP));
            barrier();
            static_for<0, CA>(c_unit);
        }
        TL(18);
        u32x4 ev[4]; unsigned eo[4]; unsigned long long none[4];
#pragma unroll
        for (int i = 0; i < 4; ++i) {
            const int t = tm - 1 - 4 * i - g4;
            eo[i] = t < 0 ? OOB : ((((t ^ tm) & 8) << 10) ^ nr) + (t & 7) * 256 + l15 * 16; none[i] = __builtin_amdgcn_ballot_w64(t < 0);
            asm volatile("buffer_load_dwordx4 %0, %1, %2, 0 offen sc1" : "=v"(ev[i]) : "v"(eo[i]), "s"(mk_rsrc(ws + WS_E + tn * 16384 + wid * 2048, 16384)) : "memory");
        }
        asm volatile("" ::: "memory");
        sched();
        constexpr int CB = CVR == 0 ? 1 : CVR == 4 ? 2 : 4;
        static_for<CA, CB>(c_unit);
        scales(IC(ELP), IC(8));
        cf32 X = cfma(cf32{g4 == 3 ? M.r : g4 == 2 ? 1.f : 0.f, g4 == 3 ? M.i : 0.f}, p01, cf32{(g4 & 1) ? ev_.r : 0.f, (g4 & 1) ? ev_.i : 0.f});
        tie(X.r); tie(X.i);
        sched();
        hop<24>();
        if constexpr (SPR != 3) __builtin_amdgcn_s_setprio(3);
        if constexpr (CVR == 2) { wait_v<12>(); c_read(IC(0)); wait_l<0>(); c_tie(IC(0)); c_req(IC(2)); }
        wait_v<4 * (CB - CA) + 8 * (CVR == 2)>();
        TL(19);
        tie(ev[0]); tie(ev[1]); tie(ev[2]); tie(ev[3]);
        {
            const unsigned long long tag64 = (unsigned long long)tag | ((unsigned long long)~tag << 32);
#pragma unroll 1
            for (unsigned spins = 0; spins < SPIN_CAP; ++spins) {
                unsigned long long ok = ~0ull;
#pragma unroll
                for (int i = 0; i < 4; ++i) ok &= __builtin_amdgcn_ballot_w64(((unsigned long long)ev[i][2] | ((unsigned long long)ev[i][3] << 32)) == tag64) | none[i];
                TSV(11, spins);
                if (ok == ~0ull) break;
                asm volatile("buffer_load_dwordx4 %0, %4, %8, 0 offen sc1\n\tbuffer_load_dwordx4 %1, %5, %8, 0 offen sc1\n\tbuffer_load_dwordx4 %2, %6, %8, 0 offen sc1\n\t"
                             "buffer_load_dwordx4 %3, %7, %8, 0 offen sc1\n\ts_waitcnt vmcnt(0)" : "=&v"(ev[0]), "=&v"(ev[1]), "=&v"(ev[2]), "=&v"(ev[3])
                             : "v"(eo[0]), "v"(eo[1]), "v"(eo[2]), "v"(eo[3]), "s"(mk_rsrc(ws + WS_E + tn * 16384 + wid * 2048, 16384)) : "memory");
            }
        }
        page<18>();
        cf32 h;
        {
            auto e = [&](int i) { return cf32{u2f(ev[i][0]), u2f(ev[i][1])}; };
            cf32 c = cmul(NG, cfma(N8, cfma(N4, e(3), e(2)), cfma(N4, e(1), e(0)))), u, v;
            sw16(c.r, u.r, v.r); sw16(c.i, u.i, v.i);
            sw32(u.r + v.r, u.r, v.r); sw32(u.i + v.i, u.i, v.i);
            c = cfma(MG, cf32{u.r + v.r, u.i + v.i}, X);
            h = cf32{fma_(c2r, c.r, -(c2i * c.i)), fma_(-c2r, c.i, -(c2i * c.r))};
        }
        TS(4);
        {
            const __amdgpu_buffer_rsrc_t rh = mk_brs(ws + WS_H + (size_t)((tm & 7) * 16 + tn) * 65536 + (nr ^ ((tm & 2) << 12)) + wid * 2048, 65536);
            const unsigned ho = g4 * 256 + ((l15 ^ (4 * (g4 & 1))) << 4);
            const cf32 Lc = {L.r, -L.i};
            static_for<0, 4>([&](auto qc) {
                constexpr int q = CV(qc);
                if constexpr (q >= CB) c_unit(qc);
                if constexpr (q == 2) hop<20>();
                u32x4 wr, wi;
#pragma unroll
                for (int u = 0; u < 4; ++u) {
                    const int i = 2 * q + (u >> 1), j = 2 * (u & 1);
                    const cf32 h0 = cfma(Lc, h, cf32{acc[i][0][j], acc[i][1][j]});
                    h = cfma(Lc, h0, cf32{acc[i][0][j + 1], acc[i][1][j + 1]});
                    wr[u] = pack2(h0.r, h.r); wi[u] = pack2(h0.i, h.i);
                }
                if constexpr ((PRE == 2 || GQ >= 0) && q == GE) { tie(wr); tie(wi); dsw32<4>(lbase + DONE + wid * FST + lane * 4, 0u); }
                __builtin_amdgcn_raw_buffer_store_b128(wr, rh, ho, q * 16384, 16);
                st16(wi, rh, ho + 1024, q * 16384); keep(wr);
                sched();
            });
        }
        TS(10);
        auto flag = [&]() { __builtin_amdgcn_raw_buffer_store_b64(u32x2{tag, ~tag}, rf, lane ? OOB : 0u, F_H + nr + (((tm & 7) * 16 + tn) * 4 + wid) * 8, 16); TS(5); };
        hop<22>();
        if constexpr (CVR == 2) {
            wait_v<16>();
            c_read(IC(1));
            wait_l<0>();
            c_req(IC(3));
            wait_v<8>();
            flag();
        } else {
            if constexpr (CVR == 1) wait_v<8>(); else { wait_v<0>(); flag(); }
            c_read(IC(0)); c_read(IC(1));
            wait_l<8>();
            c_req(IC(2));
            wait_l<0>();
            c_req(IC(3));
            if constexpr (CVR == 1) { wait_v<16>(); flag(); }
        }
        c_tie(IC(0)); c_tie(IC(1));
        static_for<16, 32>([&](auto ic) { undef(cf[CV(ic)]); });
    }
    {
        unsigned ln = lane, dsw_, pf;
        tie(ln);
        const int wid = fresh(wid_), lw = wid & 3, kg = wid >> 2, b = fresh(b_), xcd = b & 7, idx = b >> 3, tq = (idx & 7) + JR(idx >> 3);
        nr = fresh(__builtin_amdgcn_readfirstlane(nr));
        const unsigned nx = nr ^ ((xcd & 2) << 12), nh = nr ^ ((idx & 2) << 12), hp = (idx & 7) * 16 + (b & 1) * 8;
        const int tm2 = (xcd & 4) * 2 + (idx & 7), dt = (xcd & 2) * 2 + (idx >> 3), qx = ((~b & 1) * 2) ^ JQ(idx >> 3);
        const int ta = 512 * (idx >> 3) + 128 * (idx & 1) + 16 * wid + 4 * ((idx >> 1) & 3);
        const int tb = tm2 * 128 + 64 * kg;
        dsr32<DSK - 131072>(dsw_, lbase + 131072 + lw * 64 + (ln & 7) * 8 + ((ln >> 1) & 4));
        unsigned wl = lbase + wid * 4096, w2k = lbase + wid * 2048, hs0 = (hp + ((tq + (wid >> 1)) & 7)) * 65536 + nh + (wid & 1) * 4096 + qx * 16384;
        unsigned s_x = (tb + qx * 8) * 2048 + dt * 256 + lw * 64, s_q = ta * 2048 + xcd * 256, s_z = WS_ZT + (xcd & 3) * 1048576 + nx + idx * 128 + wid * 131072, zfo = WS_F + F_Z + (xcd & 2) * 8192 + nr + (xcd & 1) * 4096;
        unsigned ppo = WS_PP + (xcd & 2) * 1048576 + idx * 65536 + (xcd & 1) * 32768 + kg * 16384 + nx + lw * 2048, s_a = ta * 8192 + xcd * 512, s_b = (tb + (2 ^ qx) * 8) * 8192 + 4096 + dt * 512 + lw * 128;
        const unsigned tagg = fresh(__builtin_amdgcn_readfirstlane(tag));
        u32x4 rsx = mk_rsrc(x, (NOX & 1) ? 0 : 4 << 20), rsq = mk_rsrc(qin, (NOX & 2) ? 0 : 4 << 20), rso = mk_rsrc(out, 16 << 20), rws = mk_rsrc(ws, 32 << 20);
        ties(wl); ties(w2k); ties(hs0); ties(s_x); ties(s_q); ties(s_z); ties(zfo); ties(ppo); ties(s_a); ties(s_b);
        ties(rsx); ties(rsq); ties(rso); ties(rws);
        const unsigned l31 = ln & 31, l5 = ln >> 5;
        unsigned l16 = ln * 16, ha[8];
        ha[0] = (lbase + kg * 1536 + l5 * 2048 + (ln & 2) * 128 + (ln & 1) * 8) | ((((ln >> 1) & 8) | ((ln >> 2) & 3) | ((ln & 2) << 1)) << 4);
        ha[1] = ha[0] ^ 64; ha[2] = ha[0] ^ 512; ha[3] = ha[0] ^ 576;
        static_for<0, 4>([&](auto ic) { ha[4 + CV(ic)] = ha[CV(ic)] + 65536; tie(ha[CV(ic)]); tie(ha[4 + CV(ic)]); });
        unsigned a_x = ((((ln >> 2) & 7) | (ln & 32)) << 11) | (l16 & 48), a_xr = (((l16 & 240) << 2) | (ln & 48)) + (w2k + ZST);
        unsigned a_q = ((ln & 48) << 7) | (l16 & 240), a_qr = (l5 << 10) + l31 * 8 + (w2k + QST), a_iw = w2k + IMG + l16, a_ir = a_iw ^ 8192;
        unsigned a_t = wid == 0 && ln == 0 ? idx * 128 : OOB, msk = 0xfff0fff0u;
        unsigned a_f = wid == 1 ? (ln >> 1) * 128 + (ln & 1) * 4 : OOB, expect = wid == 1 ? ((ln & 1) ? ~tagg : tagg) : 0u;
        unsigned a_z = (((ln ^ l5 ^ (wid * 4)) & 7) << 4) | ((ln & 48) << 10) | ((ln & 8) << 9), a_z2 = a_z ^ 32, a_zr = (l31 << 9) + (((((wid ^ ln) & 7) << 1) | l5) << 3) + (lbase + ZST);
        unsigned a_oa = (l5 << 21) | (l31 << 4), a_ob = ((((ln >> 2) & 4) | (ln & 32) | ((ln >> 3) & 1)) << 13) | (l16 & 112);
        u32x2 tg2 = {tagg, ~tagg};
        tie(l16); tie(a_x); tie(a_xr); tie(a_q); tie(a_qr); tie(a_iw); tie(a_ir); tie(a_t); tie(msk); tie(a_f); tie(expect); tie(a_z); tie(a_z2); tie(a_zr);
        tie(a_oa); tie(a_ob); tie(tg2);
        wait_l<0>();
        tie(dsw_);
        u32x4 dg[2];
        static_for<0, 4>([&](auto ic) { constexpr unsigned long long k = diag_mask(CV(ic)); dg[0][CV(ic)] = sel0(dsw_ & 0xffffu, k); dg[1][CV(ic)] = sel0(dsw_ & 0xffff0000u, k); });
        tie(dg[0]); tie(dg[1]);
        unsigned fso, xso, ppi = ppo ^ 32768, tn_[3] = {tagg & 0xf000fu, (tagg >> 8) & 0xf000fu, (tagg >> 4) & 0xf000fu}, psel = 0x05040100u;
        unsigned long long lo8 = 0x00ff00ff00ff00ffull, hi8 = ~lo8;
        ties(ppi); ties(tn_[0]); ties(tn_[1]); ties(tn_[2]); ties(psel); ties(lo8); ties(hi8);
        auto fill = [&](auto sc, auto pc) {
            constexpr int s = CV(sc), p = CV(pc);
            if constexpr (p == 0) dma_x<(2 * s % 3) * BUF, (s >> 1) * 16384 + (s & 1) * 262144, true>(fso, wl, l16, rws, hs0);
            else if constexpr (fill_m0(4 * s + p)) dma_m<(2 * s % 3) * BUF, p * 1024>(fso, wl, l16, rws);
            else dma_n<p * 1024>(fso, l16, rws);
        };
        bf16x4 hf[2][8];
        auto ldh = [&](auto nc, auto ic) {
            constexpr int n = CV(nc), i = CV(ic), j = (i >> 1) & 1, bo = (2 * (n >> 2) % 3) * BUF + (g2_unit(n, j) & 3) * 8192 + (i >> 2) * 4096, hi = bo >= 65536 ? 65536 : 0;
            dsr64tr<bo - hi>(hf[n & 1][i], ha[2 * g2_tile(n, j) + (i & 1) + (hi >> 14)]);
        };
        f32x4 yc[2][2][2];
        f32x4 im[2], ao, ob[2]; u32x4 pl[2], po;
        bf16x8 xf[2];
        u32x2 zv[4], qv[2];
        unsigned ck, ex;
        float tq_[2], tz, sm[8], mx[2], cv[8];
        auto finB = [&](auto pc, auto jc) {
            constexpr int p = CV(pc), j = CV(jc);
            if constexpr (j < 8) { yc[p & 1][0][j & 1][j >> 1] += im[j & 1][j >> 1]; tie(yc[p & 1][0][j & 1]); }
            else {
                constexpr int i = (j - 8) >> 2, k = j & 3, dd = k & 1, h = k >> 1;
                ob[i][k] = sel8(yc[p & 1][0][dd][2 * i + 1 - h], yc[p & 1][0][dd][2 * i + h], h ? hi8 : lo8);
                if constexpr (k == 3 && (p == 3 || !(NOST & 4))) gst<0>(ob[i], a_ob, rso, s_b ^ ((p & 1) * 65536 + i * 16384));
                if constexpr (j == 12) keep(ob[0]);
            }
        };
        auto snd = [&](auto pc, auto jc) {
            constexpr int p = CV(pc), j = CV(jc);
            if constexpr (j < 8) { sm[j] = yc[p][0][j >> 2][j & 3] + im[j >> 2][j & 3]; tie(sm[j]); }
            if constexpr (j == 8) mx[0] = amax3(sm[0], sm[1], sm[2]);
            if constexpr (j == 9) mx[1] = amax3(sm[3], sm[4], sm[5]);
            if constexpr (j == 10) mx[0] = amax3(mx[0], sm[6], sm[7]);
            if constexpr (j == 11) mx[0] = amax3(mx[0], mx[1], mx[1]);
            if constexpr (j == 12) { ex = f2u(mx[0]) + 0x4000u; tie(ex); }
            if constexpr (j == 13) { ex &= 0x7f800000u; tie(ex); }
            if constexpr (j == 14) { mx[0] = u2f(ex + 0x04c00008u); tie(mx[0]); }
            if constexpr (j == 15) { ex >>= 23; tie(ex); }
            if constexpr (j >= 16 && j < 24) { sm[j - 16] += mx[0]; tie(sm[j - 16]); }
            if constexpr (j == 24) { ex |= ex << 12; tie(ex); }
            if constexpr (j >= 25 && j < 29) { po[j - 25] = __builtin_amdgcn_perm(f2u(sm[2 * (j - 25) + 1]), f2u(sm[2 * (j - 25)]), psel); tie(po); }
            if constexpr (j == 29) { po[0] = bfi_v(msk, po[0], ex); tie(po); }
            if constexpr (j >= 30 && j < NS) { po[j - 29] = bfi_s(msk, po[j - 29], tn_[j - 30]); if constexpr (j < NS - 1) tie(po); }
            if constexpr (j == NS - 1 && !(NOST & 2)) gst<p * 1024>(po, l16, rws, ppo);
        };
        auto rcv = [&](auto hc, auto jc) {
            constexpr int h = CV(hc), j = CV(jc);
            if constexpr (j < 3) { pl[h][j + 1] ^= tn_[j]; pp_tie<h>(pl[h]); }
            if constexpr (j == 3) { ck = pl[h][1] | pl[h][2] | pl[h][3]; tie(ck); }
            if constexpr (j == 4) { ck &= 0xf000fu; tie(ck); }
            if constexpr (j == 6 && !(NOST & 2)) pp_chk<h>(pl[h], ck, tagg, l16, rws, ppi);
            if constexpr (j == 7) { ex = pl[h][0] >> 12; tie(ex); }
            if constexpr (j == 8) ex = bfi_v(msk, ex, pl[h][0]);
            if constexpr (j == 9) { pl[h][0] &= msk; pp_tie<h>(pl[h]); }
            if constexpr (j == 10) { ex <<= 23; tie(ex); }
            if constexpr (j == 11) { ex &= 0x7f800000u; tie(ex); }
            if constexpr (j == 12) { mx[1] = u2f(ex) * 0x1p-14f; tie(mx[1]); }
            if constexpr (h == 0 && j >= 13) {
                constexpr int k = j - 13, a = (k + 1) / 2, e = k == 15 ? 7 : k / 2 - 1;
                if constexpr (k == 0 || ((k & 1) && k < 15)) cv[a] = cvt16<a & 1>(pl[h][a >> 1]);
                else { yc[0][0][e >> 2][e & 3] = fma_(cv[e], mx[1], yc[0][0][e >> 2][e & 3]); tie(yc[0][0][e >> 2]); }
            }
            if constexpr (h == 1 && j >= 13 && j < 21) cv[j - 13] = cvt16<(j - 13) & 1>(pl[h][(j - 13) >> 1]);
            if constexpr (h == 1 && j >= 21) { constexpr int e = j - 21; im[e >> 2][e & 3] = fma_(cv[e], mx[1], im[e >> 2][e & 3]); tie(im[e >> 2]); }
        };
        auto side = [&](auto pc) {
            constexpr int P = CV(pc), n = P >> 3, m = P & 7, p = n >> 3;
            if constexpr (P == P_X || P == P_X + 2) dma_x<ZST + (P != P_X) * 1024, (P != P_X) * 16384, false>(xso, w2k, a_x, rsx, s_x);
            if constexpr (!NOA) {
            if constexpr (P == P_Q || P == P_Q + 2) dma_x<QST + (P != P_Q) * 1024, (P != P_Q) * 524288, false>(xso, w2k, a_q, rsq, s_q);
            if constexpr (P == P_T) gst<0>(tg2, a_t, rws, zfo);
            if constexpr (P == AT(ZP, 4)) pf_ld(pf, a_f, rws, zfo);
            if constexpr (P == P_ZC) { wait_v<g2_allow(P, AT(ZP, 4))>(); tag_chk(pf, expect, a_f, rws, zfo); }
            if constexpr (P == P_Z || P == P_Z + 2) dma_x<ZST + (P != P_Z) * 1024, (P != P_Z) * 65536, true>(xso, w2k, P == P_Z ? a_z : a_z2, rws, s_z);
            }
            if constexpr (P == AT(7, 5) || P == AT(7, 7)) dsr128<(m - 5) * 512>(xf[(m - 5) / 2], a_xr);
            if constexpr (P == AT(8, 4)) { tie(xf[0]); tie(xf[1]); }
            if constexpr (P == AT(8, 5) || P == AT(9, 3)) static_for<0, 2>([&](auto dc) { mfma<false>(yc[n & 1][0][CV(dc)], xf[n & 1], __builtin_bit_cast(bf16x8, dg[CV(dc)])); });
            if constexpr ((n & 7) == 6 && m == XW && !(DBG & 2)) dsw128<0>(a_iw, yc[p & 1][1][0]);
            if constexpr ((n & 7) == 6 && m == XW2 && !(DBG & 2)) dsw128<1024>(a_iw, yc[p & 1][1][1]);
            if constexpr ((n & 7) == 7 && (m & 5) == (n < 31 ? 4 : 0)) dsr128<(m & 2) * 512>(im[(m >> 1) & 1], a_ir);
            if constexpr (n >= 8 && (n & 7) == 0 && m == 4) { tie(im[0]); tie(im[1]); }
            if constexpr (P >= P_PP0 && P <= E_PP0) snd(IC(0), IC(P - P_PP0));
            if constexpr (P >= P_PP1 && P <= E_PP1) static_for<0, D1>([&](auto ic) { snd(IC(1), IC((P - P_PP1) * D1 + CV(ic))); });
            if constexpr (P == P_L0 || P == P_L1) pp_ld<P == P_L1>(pl[P == P_L1], l16, rws, ppi);
            if constexpr (P == P_R0 || P == P_R1) { wait_v<g2_allow(P, P == P_R0 ? imax(P_L0, XL ? P_X + 2 : 0) : P_L1)>(); pp_tie<P == P_R1>(pl[P == P_R1]); }
            if constexpr (P >= P_R0 && P <= E_R0) static_for<0, D0>([&](auto ic) { if constexpr ((P - P_R0) * D0 + CV(ic) < NR) rcv(IC(0), IC((P - P_R0) * D0 + CV(ic))); });
            if constexpr (P >= P_R1 && P < P_R1 + 21) rcv(IC(1), IC(P - P_R1));
            if constexpr (!NOA) {
            if constexpr (n == ZW && m >= 4) dsr64<(m - 4) * 128>(zv[m - 4], a_zr);
            static_for<0, 4>([&](auto rc) {
                constexpr int r = CV(rc);
                if constexpr (P == q_tie(r)) tie(qv[r & 1]);
                if constexpr (P == q_rd(r)) dsr64<r * 256>(qv[r & 1], a_qr);
            });
            if constexpr (P == AT(ZW + 1, 4)) { tie(zv[0]); tie(zv[1]); tie(zv[2]); tie(zv[3]); }
            }
            if constexpr (a_step(P) >= 0) {
                constexpr int s = a_step(P), e = s == 0 ? 0 : s % 3 == 2 ? (s + 1) / 3 : s % 3 == 1 ? s / 3 : s / 3 - 1, r = e >> 2, c = e & 3;
                if constexpr (s == 0 || (s % 3 == 2 && e < 16)) { tq_[e & 1] = (c & 1) ? bfhi(qv[r & 1][c >> 1]) : bflo(qv[r & 1][c >> 1]); tie(tq_[e & 1]); }
                else if constexpr (s % 3 == 1) { tz = (r & 1) ? bfhi(zv[c][r >> 1]) : bflo(zv[c][r >> 1]); tie(tz); }
                else if constexpr (s % 3 == 0) {
                    ao[c] = tq_[e & 1] * tz;
                    if constexpr (c == 3 && !(NOST & 1)) gst<0>(ao, a_oa, rso, s_a + r * 8192);
                    else tie(ao);
                }
            }
            if constexpr (P >= P_F2 && P < P_F2 + 16) finB(IC(2), IC(P - P_F2));
            if constexpr (a_step(P - 1) > 0 && a_step(P - 1) % 12 == 0) keep(ao);
            if constexpr (P == E_PP0 + 1 || P == E_PP1 + 1) keep(po);
            if constexpr (P == P_F2 + 16) keep(ob[1]);
        };
        {
            unsigned e_h = wid == 3 ? ((ln & 1) ? ~tagg : tagg) : 0u, hsl = lbase + lw * 8192, hso = (hp + ((tq + wid) & 7)) * 65536 + nh + qx * 16384;
            unsigned a_h = wid == 3 ? ln * 4 : OOB, s_h = WS_F + F_H + nr + hp * 32, ca2[4], a_g = lbase + HGO + ln * 4;
            static_for<0, 4>([&](auto ic) { ca2[CV(ic)] = cad[CV(ic)] + 2 * BUF; tie(ca2[CV(ic)]); });
            tie(e_h); tie(a_h); tie(a_g); ties(hsl); ties(hso); ties(s_h);
            sched();
            if (wid < 4) {
                if constexpr (PW == 0) { pf_ld(pf, a_h, rws, s_h); wait_v<1>(); }
                if constexpr (PW == 1) { wait_v<0>(); pf_ld(pf, a_h, rws, s_h); }
                if constexpr (PW == 2) { wait_v<8>(); pf_ld(pf, a_h, rws, s_h); wait_v<1>(); }
                static_for<16, 32>([&](auto ic) { constexpr int i = CV(ic); dsr128u<((i >> 2) & 1) * 4096>(cf[i], (i & 8) ? cad[i & 3] : ca2[i & 3]); });
                wait_l<0>();
                static_for<16, 32>([&](auto ic) { tie(cf[CV(ic)]); });
                wait_v<0>();
                tag_chk<1>(pf, e_h, a_h, rws, s_h);
                if (wid == 3) flag_set(a_g, 1u); else gate<1>(a_g, 1);
                TS(6);
                hop<26>();
                static_for<0, 4>([&](auto ic) {
                    constexpr int i = CV(ic);
                    dma_x<(i >> 1) * 2 * BUF + (i & 1) * 4096, (i >> 1) * 262144 + (i & 1) * 4096, true>(fso, hsl, l16, rws, hso);
                    dma_n<1024>(fso, l16, rws); dma_n<2048>(fso, l16, rws); dma_n<3072>(fso, l16, rws);
                });
            }
        }
        wait_v<8>();
        if (wid < 4) TS(7);
        TG_DECL
        barrier();
        static_for<0, 8>([&](auto ic) { ldh(IC(0), ic); });
        static_for<0, 32>([&](auto nc) {
            constexpr int n = CV(nc), p = n >> 3;
            if constexpr (n == PG3) page<33>();
            if constexpr (n == PG4) page<37>();
            wait_lr<(n & 3) == 3 ? 0 : 4 + g2_lx(n - 1)>();
            if constexpr (n == 31) barrier();
            if constexpr ((n & 3) == 3 && n < 31) {
                constexpr int need = g2_need(n);
                TL(21 + n / 4 * 3);
                wait_v<g2_allow(8 * n, need)>();
                TL(22 + n / 4 * 3);
                barrier();
                TL(23 + n / 4 * 3);
            }
            if constexpr (n == 1) hop<31>();
            if constexpr (n == 4) hop<32>();
            if constexpr (n == 11) hop<34>();
            if constexpr (n == 15) hop<35>();
            if constexpr (n == 19) hop<36>();
            if constexpr (n == 27) hop<38>();
            if constexpr (n == 31) hop<39>();
            if constexpr (n == 0) TL(20);
            TG(20 + n);
            if constexpr (n == 31) TL(42);
            if constexpr (PRG) __builtin_amdgcn_s_setprio(n == 31 ? 0 : 3 - ((n + 1) & 3));
            sched();
            static_for<0, 8>([&](auto mc) {
                constexpr int m = CV(mc), ks = m >> 2, tt = (m >> 1) & 1, dd = m & 1, P = n * 8 + m;
                if constexpr (m == 4 && (n & 3) != 3) wait_lr<(rd1(n) ? 8 : 4) + ((n & 7) == 6 ? (XW < 4) + (XW2 < 4) : 0)>();
                mfma<(n & 7) == 0 && ks == 0>(yc[p & 1][(DBG & 1) ? tt : g2_tile(n, tt)][dd], cat(hf[n & 1][4 * ks + 2 * tt], hf[n & 1][4 * ks + 2 * tt + 1]), cf[g2_unit(n, tt) * 4 + 2 * ks + dd]);
                sched();
                if constexpr (n < 31 && rd1(n) && m < 4) { ldh(IC(n + 1), IC(2 * m)); ldh(IC(n + 1), IC(2 * m + 1)); }
                if constexpr (n < 31 && !rd1(n)) ldh(IC(n + 1), mc);
                if constexpr (g2_fill(P) >= 0) fill(IC(g2_fill(P) >> 2), IC(g2_fill(P) & 3));
                side(IC(P));
                sched();
            });
        });
        TG(52);
        if (wid >= 4) TS(5); else TS(8);
        TG(53);
        static_for<256 - P_R1, 21>([&](auto jc) { rcv(IC(1), jc); });
        wait_l<0>();
        tie(im[0]); tie(im[1]);
        static_for<21, NR>([&](auto jc) { rcv(IC(1), jc); });
        asm volatile("s_nop 3");
        sched();
        static_for<0, 16>([&](auto jc) { finB(IC(3), jc); });
        if (b == 255 && wid == 0) gst<0>(u32x2{tagg, tagg}, l16 ? OOB : 0u, rws, WS_F + F_EP);
    }
}

}

#if defined(__HIP_DEVICE_COMPILE__)
#pragma clang attribute pop
#endif

extern "C" void kernel_launch(void* const* d_in, const int* in_sizes, int n_in, void* d_out, int out_size, void* d_ws, size_t ws_size,
                              hipStream_t stream) {
    (void)in_sizes; (void)n_in; (void)out_size; (void)ws_size;
    using namespace fk;
    const uint16_t* q = (const uint16_t*)d_in[0];
    const uint16_t* kin = (const uint16_t*)d_in[1];
    const uint16_t* vin = (const uint16_t*)d_in[2];
    const uint16_t* sk = (const uint16_t*)d_in[3];
    const uint16_t* ssmk = (const uint16_t*)d_in[4];
    const uint16_t* ssmD = (const uint16_t*)d_in[5];
    const uint16_t* x = (const uint16_t*)d_in[6];
    const uint16_t* lre = (const uint16_t*)d_in[7];
    const uint16_t* lim = (const uint16_t*)d_in[8];
    const uint16_t* bre = (const uint16_t*)d_in[9];
    const uint16_t* bim = (const uint16_t*)d_in[10];
    const uint16_t* cre = (const uint16_t*)d_in[11];
    const uint16_t* cim = (const uint16_t*)d_in[12];
    const uint16_t* logdt = (const uint16_t*)d_in[13];
    const uint16_t* dskip = (const uint16_t*)d_in[14];
#define WOFF 112
    char* ws = (char*)(((uintptr_t)d_ws + ((size_t)WOFF << 20) + 0x1fffff) & ~(uintptr_t)0x1fffff);
    hipLaunchKernelGGL(k1, dim3(256), dim3(512), 0, stream, x, bre, bim, lre, lim, logdt, cre, cim, dskip, ssmk, ssmD, q, kin, vin, sk, ws, (float*)d_out TS_PASS(0));
}
```

```cpp
#include <hip/hip_runtime.h>
#include <stdint.h>
#include <type_traits>

#if defined(__HIP_DEVICE_COMPILE__)
#pragma clang attribute push(__attribute__((target("no-packed-fp32-ops"))), apply_to = function)
#endif

namespace fk {

typedef __attribute__((ext_vector_type(8))) short bf16x8;
typedef __attribute__((ext_vector_type(4))) short bf16x4;
typedef __attribute__((ext_vector_type(4))) float f32x4;
typedef __attribute__((ext_vector_type(4))) unsigned u32x4;
typedef __attribute__((ext_vector_type(2))) unsigned u32x2;
typedef __attribute__((ext_vector_type(2))) __bf16 bf16x2_t;
typedef __attribute__((address_space(1))) unsigned gu32;
typedef __attribute__((address_space(1))) unsigned long long gu64;
#define RLX_AGENT __ATOMIC_RELAXED, __HIP_MEMORY_SCOPE_AGENT

__device__ __forceinline__ float u2f(unsigned v) { return __builtin_bit_cast(float, v); }
__device__ __forceinline__ unsigned f2u(float v) { return __builtin_bit_cast(unsigned, v); }
__device__ __forceinline__ bool wall(bool p) { return __builtin_amdgcn_ballot_w64(p) == ~0ull; }
__device__ __forceinline__ float bflo(unsigned w) { return u2f(w << 16); }
__device__ __forceinline__ float bfhi(unsigned w) { return u2f(w & 0xffff0000u); }
__device__ __forceinline__ unsigned pack2(float lo, float hi) {
    bf16x2_t v; v[0] = (__bf16)lo; v[1] = (__bf16)hi; return __builtin_bit_cast(unsigned, v);
}
__device__ __forceinline__ float rbf(float f) { float r; asm("v_cvt_pk_bf16_f32 %0, 0, %1" : "=v"(r) : "v"(f)); return r; }
__device__ __forceinline__ float fma_(float a, float b, float c) { return __builtin_fmaf(a, b, c); }

template <int I, int N, class F>
__device__ __forceinline__ void static_for(F&& f) {
    if constexpr (I + 1 == N) f(std::integral_constant<int, I>{});
    else if constexpr (I < N) { static_for<I, I + (N - I) / 2>(f); static_for<I + (N - I) / 2, N>(f); }
}
#define IC(v) std::integral_constant<int, (v)>{}
#define CV(c) decltype(c)::value

#define TS_ARG
#define TS_PASS(off)
#define TS_DECL
#define TS(i)
#define TSV(i, v)
#define TL_DECL
#define TL(i)
#define TS_FLUSH(slot)

#define TG_DECL
#define TG(i)
template <int N> __device__ __forceinline__ void wait_v() { asm volatile("s_waitcnt vmcnt(%0)" ::"n"(N > 63 ? 63 : N) : "memory"); }
template <int N> __device__ __forceinline__ void wait_l() { asm volatile("s_waitcnt lgkmcnt(%0)" ::"n"(N > 14 ? 14 : N) : "memory"); }
template <int N> __device__ __forceinline__ void wait_lr() { __builtin_amdgcn_s_waitcnt(0xc07f | ((N > 14 ? 14 : N) << 8)); }
template <class T> __device__ __forceinline__ void tie(T& x) { asm volatile("" : "+v"(x)); }
template <class T> __device__ __forceinline__ void undef(T& x) { asm volatile("" : "=v"(x)); }
template <int OFF, class T> __device__ __forceinline__ void dsr128u(T& d, unsigned a) { asm volatile("ds_read_b128 %0, %1 offset:%2" : "+v"(d) : "v"(a), "n"(OFF)); }
template <class T> __device__ __forceinline__ void keep(const T& x) { asm volatile("" :: "v"(x)); }
template <int OFF, class T> __device__ __forceinline__ void dsr128(T& d, unsigned a) { asm volatile("ds_read_b128 %0, %1 offset:%2" : "=v"(d) : "v"(a), "n"(OFF)); }
template <int OFF, class T> __device__ __forceinline__ void dsr64(T& d, unsigned a) { asm volatile("ds_read_b64 %0, %1 offset:%2" : "=v"(d) : "v"(a), "n"(OFF)); }
template <int OFF> __device__ __forceinline__ void dsr32(unsigned& d, unsigned a) { asm volatile("ds_read_b32 %0, %1 offset:%2" : "=v"(d) : "v"(a), "n"(OFF)); }
template <int O0, int O1, class T> __device__ __forceinline__ void dsr2(T& d, unsigned a) { asm volatile("ds_read2_b32 %0, %1 offset0:%2 offset1:%3" : "=v"(d) : "v"(a), "n"(O0), "n"(O1)); }
template <int OFF> __device__ __forceinline__ void dsr64tr(bf16x4& d, unsigned a) { asm volatile("ds_read_b64_tr_b16 %0, %1 offset:%2" : "=v"(d) : "v"(a), "n"(OFF)); }
template <int OFF, class T> __device__ __forceinline__ void dsw128(unsigned a, const T& v) { asm volatile("ds_write_b128 %0, %1 offset:%2" :: "v"(a), "v"(v), "n"(OFF) : "memory"); }
template <int OFF> __device__ __forceinline__ void dsw32(unsigned a, unsigned v) { asm volatile("ds_write_b32 %0, %1 offset:%2" :: "v"(a), "v"(v), "n"(OFF) : "memory"); }
__device__ __forceinline__ bf16x8 cat(const bf16x4& lo, const bf16x4& hi) { return __builtin_shufflevector(lo, hi, 0, 1, 2, 3, 4, 5, 6, 7); }
__device__ __forceinline__ void barrier() { asm volatile("" ::: "memory"); __builtin_amdgcn_s_barrier(); asm volatile("" ::: "memory"); }
__device__ __forceinline__ int fresh(int v) { asm volatile("" : "+s"(v)); return v; }
__device__ __forceinline__ void sched() { __builtin_amdgcn_sched_barrier(0); }
template <bool FIRST> __device__ __forceinline__ void mfma(f32x4& c, const bf16x8& a, const bf16x8& b) {
    if constexpr (FIRST) asm volatile("v_mfma_f32_16x16x32_bf16 %0, %1, %2, 0" : "=v"(c) : "v"(a), "v"(b));
    else asm volatile("v_mfma_f32_16x16x32_bf16 %0, %1, %2, %0" : "+v"(c) : "v"(a), "v"(b));
}
struct cf32 { float r, i; };
__device__ __forceinline__ cf32 cmul(cf32 a, cf32 b) { return cf32{fma_(a.r, b.r, -(a.i * b.i)), fma_(a.r, b.i, a.i * b.r)}; }
__device__ __forceinline__ cf32 cfma(cf32 a, cf32 b, cf32 c) { return cf32{fma_(a.r, b.r, fma_(-a.i, b.i, c.r)), fma_(a.r, b.i, fma_(a.i, b.r, c.i))}; }
__device__ __forceinline__ void sw16(float v, float& e, float& o) { const auto r = __builtin_amdgcn_permlane16_swap(f2u(v), f2u(v), false, false); e = u2f(r[0]); o = u2f(r[1]); }
__device__ __forceinline__ void sw32(float v, float& e, float& o) { const auto r = __builtin_amdgcn_permlane32_swap(f2u(v), f2u(v), false, false); e = u2f(r[0]); o = u2f(r[1]); }
constexpr unsigned SPIN_CAP = 1u << 16;
constexpr unsigned OOB = 0x40000000u;

constexpr size_t WS_H = 0;
constexpr size_t WS_KVT = (size_t)8 << 20;
constexpr size_t WS_ZT = (size_t)12 << 20;
constexpr size_t WS_PP = (size_t)16 << 20;
constexpr size_t WS_E = (size_t)24 << 20;
constexpr size_t WS_F = WS_E + (256 << 10);
constexpr int F_H = 0;
constexpr int F_K = 4096;
constexpr int F_Z = 16384;
constexpr int F_EP = F_Z + 32768;
constexpr int F_C = F_EP + 256;
constexpr unsigned WS_P = 25 << 20;
#define KCQ 2
#define KCC 4
#define KVP 0
#define RSL 1
#define PB 0
#define KTI 1
#define TM 0
#define TX 0
#define PK 1
#define HA 0
#define KVS 1
#define GCR 273
constexpr int LDS_TOTAL = 163840, BUF = 32768;
constexpr int KC = 98304, KCH = 8320, KCP = 4160;
constexpr int U0 = KC + 4 * KCH;
constexpr int FULL = U0 + 18432 + 8192, FST = 388;
constexpr int DONE = U0 + 18432, DUMP = DONE + 4096;
constexpr int HGO = FULL + 1792, DSK = HGO + 256, POLL = DSK + 256;
static_assert(POLL + 8 * 256 <= LDS_TOTAL && 4 * FST <= 1792, "lds");
constexpr int g1_buf(int s) { return ((0x0213 >> ((s & 3) * 4)) & 3) * BUF; }
constexpr int IMG = KC, QST = KC + 16384, ZST = U0, PLD = 2 * BUF;
#define CODE_KB 47
#define EXACT 0
#define HP 1
#define CP 2
#define FLQ 11
#define R01 3
#define ZP 7
#define SLP 8
#define STR_(x) #x
#define STR(x) STR_(x)
#define NOST 0
#define G2M 0
#define RD 2
#define PRG 1
#define OSC 16
#define NOA 0
#define PPH 0
#define HS 0
#define JQ(j) (HS == 0 ? 0 : HS == 1 ? (j) & 1 : HS == 2 ? (j) : (j) & 2)
#define JR(j) (HS == 0 ? 2 * (j) : HS == 1 ? 2 * ((j) & 2) : HS == 2 ? 0 : 4 * ((j) & 1))
#define NOX 0

__device__ __forceinline__ u32x4 mk_rsrc(const void* p, unsigned bytes) {
    const unsigned long long a = (unsigned long long)p;
    return u32x4{(unsigned)a, (unsigned)(a >> 32) & 0xffffu, bytes, 0x00020000u};
}
__device__ __forceinline__ __amdgpu_buffer_rsrc_t mk_brs(const void* p, unsigned bytes) { return __builtin_amdgcn_make_buffer_rsrc((void*)p, (short)0, bytes, 0x00020000); }
__device__ __forceinline__ void st16(const u32x4& d, __amdgpu_buffer_rsrc_t r, unsigned voff, unsigned soff) {
    __builtin_amdgcn_raw_buffer_store_b128(d, r, voff, soff, 16);
    asm volatile("s_nop 1" :: "v"(d) : "memory");
}
template <int IMM, bool SC1> __device__ __forceinline__ void dma(unsigned lds, unsigned voff, const u32x4& rs, unsigned soff) {
    if constexpr (SC1) asm volatile("s_add_u32 m0, %0, %1\n\ts_nop 0\n\tbuffer_load_dwordx4 %2, %3, %4 offen sc1 lds" :: "s"(lds), "n"(IMM), "v"(voff), "s"(rs), "s"(soff) : "memory", "scc");
    else asm volatile("s_add_u32 m0, %0, %1\n\ts_nop 0\n\tbuffer_load_dwordx4 %2, %3, %4 offen lds" :: "s"(lds), "n"(IMM), "v"(voff), "s"(rs), "s"(soff) : "memory", "scc");
}
template <int IMM, int I> __device__ __forceinline__ void dma4(unsigned lds, unsigned voff, const u32x4& rs, unsigned soff) {
    if constexpr (I == 0) dma<IMM, false>(lds, voff, rs, soff);
    else asm volatile("buffer_load_dwordx4 %0, %1, %2 offen offset:%3 lds" :: "v"(voff), "s"(rs), "s"(soff), "n"(I * 1024) : "memory");
}
template <int IMM> __device__ __forceinline__ void dma1(unsigned lds, unsigned voff, const u32x4& rs, unsigned soff) {
    asm volatile("s_add_u32 m0, %0, %1\n\ts_nop 0\n\tbuffer_load_dword %2, %3, %4 offen sc1 lds" :: "s"(lds), "n"(IMM), "v"(voff), "s"(rs), "s"(soff) : "memory", "scc");
}
__device__ __forceinline__ void flag_set(unsigned waddr, unsigned v) { asm volatile("ds_write_b32 %0, %1" :: "v"(waddr), "v"(v) : "memory"); }
__device__ __forceinline__ void flag_chk(unsigned& f, unsigned raddr, unsigned need) {
    unsigned cnt;
    asm volatile("v_cmp_le_u32 vcc, %3, %0\n\t"
                 "s_cmp_eq_u64 vcc, -1\n\t"
                 "s_cbranch_scc1 1f\n\t"
                 "s_mov_b32 %1, 0x100000\n"
                 "0:\n\t"
                 "ds_read_b32 %0, %2\n\t"
                 "s_sub_u32 %1, %1, 1\n\t"
                 "s_waitcnt lgkmcnt(0)\n\t"
                 "s_cbranch_scc1 1f\n\t"
                 "v_cmp_le_u32 vcc, %3, %0\n\t"
                 "s_cmp_eq_u64 vcc, -1\n\t"
                 "s_cbranch_scc0 0b\n"
                 "1:" : "+v"(f), "=&s"(cnt) : "v"(raddr), "s"(need) : "vcc", "scc", "memory");
}
template <int OFF, int SL = 0> __device__ __forceinline__ void zero_chk(unsigned& f, unsigned raddr) {
    unsigned cnt;
    asm volatile("v_cmp_ne_u32 vcc, 0, %0\n\t"
                 "s_cbranch_vccz 1f\n\t"
                 "s_mov_b32 %1, 0x100000\n"
                 "0:\n\t"
                 ".if %4\n\ts_sleep %4\n\t.endif\n\t"
                 "ds_read_b32 %0, %2 offset:%3\n\t"
                 "s_sub_u32 %1, %1, 1\n\t"
                 "s_waitcnt lgkmcnt(0)\n\t"
                 "s_cbranch_scc1 1f\n\t"
                 "v_cmp_ne_u32 vcc, 0, %0\n\t"
                 "s_cbranch_vccnz 0b\n"
                 "1:" : "+v"(f), "=&s"(cnt) : "v"(raddr), "n"(OFF), "n"(SL) : "vcc", "scc", "memory");
}
template <int IMM, class T> __device__ __forceinline__ void gld(T& d, unsigned voff, const u32x4& rs, unsigned soff) {
    if constexpr (sizeof(T) == 16) asm volatile("buffer_load_dwordx4 %0, %1, %2, %3 offen offset:%4" : "=v"(d) : "v"(voff), "s"(rs), "s"(soff), "n"(IMM) : "memory");
    else if constexpr (sizeof(T) == 8) asm volatile("buffer_load_dwordx2 %0, %1, %2, %3 offen offset:%4" : "=v"(d) : "v"(voff), "s"(rs), "s"(soff), "n"(IMM) : "memory");
    else asm volatile("buffer_load_ushort %0, %1, %2, %3 offen offset:%4" : "=v"(d) : "v"(voff), "s"(rs), "s"(soff), "n"(IMM) : "memory");
}
__device__ __forceinline__ void gld32(unsigned& d, unsigned voff, const u32x4& rs, unsigned soff) { asm volatile("buffer_load_dword %0, %1, %2, %3 offen" : "=v"(d) : "v"(voff), "s"(rs), "s"(soff) : "memory"); }
__device__ __forceinline__ void sld(unsigned& d, const void* p) { asm volatile("s_load_dword %0, %1, 0x0" : "=s"(d) : "s"(p) : "memory"); }
__device__ __forceinline__ void stouch(unsigned long long p) { asm volatile("s_load_dword s101, %0, 0x0\n\ts_load_dword s101, %0, 0x80\n\ts_load_dword s101, %0, 0x100" :: "s"(p) : "s101", "memory"); }
template <class T> __device__ __forceinline__ void ties(T& x) { asm volatile("" : "+s"(x)); }
template <int IMM, class T> __device__ __forceinline__ void gst(const T& d, unsigned voff, const u32x4& rs, unsigned soff) {
    if constexpr (sizeof(T) == 16) asm volatile("buffer_store_dwordx4 %0, %1, %2, %3 offen offset:%4 sc1" :: "v"(d), "v"(voff), "s"(rs), "s"(soff), "n"(IMM) : "memory");
    else asm volatile("buffer_store_dwordx2 %0, %1, %2, %3 offen offset:%4 sc1" :: "v"(d), "v"(voff), "s"(rs), "s"(soff), "n"(IMM) : "memory");
}
template <int SLQ> __device__ __forceinline__ void gate(unsigned raddr, unsigned need) {
    unsigned cnt, f;
    asm volatile("s_mov_b32 %1, 0x40000\n"
                 "0:\n\t"
                 "ds_read_b32 %0, %2\n\t"
                 "s_sub_u32 %1, %1, 1\n\t"
                 "s_waitcnt lgkmcnt(0)\n\t"
                 "s_cbranch_scc1 1f\n\t"
                 "v_cmp_le_u32 vcc, %3, %0\n\t"
                 "s_cmp_eq_u64 vcc, -1\n\t"
                 "s_cbranch_scc1 1f\n\t"
                 "s_sleep %4\n\t"
                 "s_branch 0b\n"
                 "1:" : "=&v"(f), "=&s"(cnt) : "v"(raddr), "s"(need), "n"(SLQ) : "vcc", "scc", "memory");
}
template <int SL = SLP, int AT_ = 0> __device__ __forceinline__ void tag_chk(unsigned& f, unsigned expect, unsigned voff, const u32x4& rs, unsigned soff) {
    unsigned cnt;
    asm volatile("v_cmp_eq_u32 vcc, %2, %0\n\t"
                 "s_cmp_eq_u64 vcc, -1\n\t"
                 "s_cbranch_scc1 1f\n\t"
                 "s_mov_b32 %1, 0x10000\n"
                 "0:\n\t"
                 ".if %7\n\tv_mov_b32 %0, 0\n\t.endif\n\t"
                 "s_sleep %6\n\t"
                 ".if %7\n\tbuffer_atomic_or %0, %3, %4, %5 offen sc0 sc1\n\t.else\n\tbuffer_load_dword %0, %3, %4, %5 offen sc1\n\t.endif\n\t"
                 "s_sub_u32 %1, %1, 1\n\t"
                 "s_waitcnt vmcnt(0)\n\t"
                 "s_cbranch_scc1 1f\n\t"
                 "v_cmp_eq_u32 vcc, %2, %0\n\t"
                 "s_cmp_eq_u64 vcc, -1\n\t"
                 "s_cbranch_scc0 0b\n"
                 "1:" : "+v"(f), "=&s"(cnt) : "v"(expect), "v"(voff), "s"(rs), "s"(soff), "n"(SL), "n"(AT_) : "vcc", "scc", "memory");
}

__device__ __forceinline__ unsigned race(unsigned w, const void* ws, unsigned long long rnd) {
    unsigned v;
    asm volatile("s_cmp_lt_u32 %1, 32\n\t"
                 "s_cbranch_scc0 1f\n\t"
                 "s_mov_b64 s[98:99], %3\n\t"
                 "s_lshl_b32 s96, s98, 7\n\t"
                 "s_lshl_b32 s97, %1, 13\n\t"
                 "s_andn2_b32 s96, s96, 0x2000\n\t"
                 "s_xor_b32 s96, s96, s97\n\t"
                 "s_and_b32 s96, s96, 0x7ff80\n\t"
                 "s_add_u32 s96, s96, %4\n\t"
                 "s_xor_b32 s97, s96, 0x2000\n\t"
                 "s_mov_b32 %0, s98\n\t"
                 "s_load_dword s99, %2, s96\n\t"
                 "s_load_dword s98, %2, s97\n\t"
                 "s_lshr_b32 s96, %1, 4\n\t"
                 "s_xor_b32 s96, s96, %1\n\t"
                 "s_waitcnt lgkmcnt(1)\n\t"
                 "s_sleep %5\n\t"
                 "s_cmp_lg_u32 %0, s98\n\t"
                 "s_addc_u32 %0, s96, 0\n\t"
                 "s_and_b32 %0, %0, 1\n"
                 "1:" : "=&s"(v) : "s"(w), "s"(ws), "s"(rnd), "n"(WS_P), "n"(RSL) : "s96", "s97", "s98", "s99", "scc", "memory");
    return v;
}
__device__ __forceinline__ void vote_chk(unsigned& f, unsigned expect, unsigned odd, unsigned raddr, unsigned lds, unsigned voff, const u32x4& rs, unsigned soff, unsigned& cnt) {
    unsigned t;
    asm volatile("s_mov_b32 %1, 0x10000\n\t"
                 "v_xor_b32 %2, %0, %3\n\t"
                 "v_cmp_gt_u32 vcc, %2, %4\n\t"
                 "s_cbranch_vccz 1f\n\t"
                 "s_mov_b32 m0, %6\n"
                 "0:\n\t"
                 "s_sleep 4\n\t"
                 "buffer_load_dword %7, %8, %9 offen sc1 lds\n\t"
                 "s_sub_u32 %1, %1, 1\n\t"
                 "s_waitcnt vmcnt(0)\n\t"
                 "ds_read_b32 %0, %5\n\t"
                 "s_waitcnt lgkmcnt(0)\n\t"
                 "s_cbranch_scc1 1f\n\t"
                 "v_xor_b32 %2, %0, %3\n\t"
                 "v_cmp_gt_u32 vcc, %2, %4\n\t"
                 "s_cbranch_vccnz 0b\n"
                 "1:" : "+v"(f), "=&s"(cnt), "=&v"(t) : "v"(expect), "v"(odd), "v"(raddr), "s"(lds), "v"(voff), "s"(rs), "s"(soff) : "vcc", "scc", "memory");
}
__device__ __forceinline__ unsigned vote_cnt(unsigned f, unsigned tag, unsigned up, unsigned& n) {
    unsigned r;
    asm volatile("s_xnor_b32 %0, %3, 1\n\tv_cmp_eq_u32 vcc, %0, %2\n\ts_bcnt1_i32_b64 %1, vcc\n\ts_cmp_gt_u32 %1, 16\n\ts_cselect_b32 %0, 0x2000, 0\n\ts_xor_b32 %0, %0, %4"
                 : "=&s"(r), "=&s"(n) : "v"(f), "s"(tag), "s"(up) : "vcc", "scc");
    return r;
}

template <int H> __device__ __forceinline__ void pp_ld(u32x4& d, unsigned voff, const u32x4& rs, unsigned soff) {
    if constexpr (H) asm volatile("buffer_load_dwordx4 v[244:247], %1, %2, %3 offen offset:1024 sc1" : "={v[244:247]}"(d) : "v"(voff), "s"(rs), "s"(soff) : "memory");
    else asm volatile("buffer_load_dwordx4 v[240:243], %1, %2, %3 offen sc1" : "={v[240:243]}"(d) : "v"(voff), "s"(rs), "s"(soff) : "memory");
}
__device__ __forceinline__ void pf_ld(unsigned& d, unsigned voff, const u32x4& rs, unsigned soff) { asm volatile("buffer_load_dword v248, %1, %2, %3 offen sc1" : "={v248}"(d) : "v"(voff), "s"(rs), "s"(soff) : "memory"); }
__device__ __forceinline__ void pf_keep(unsigned d) { asm volatile("" :: "{v248}"(d)); }
__device__ __forceinline__ void pf_zero(unsigned& d) { asm volatile("v_mov_b32 v248, 0" : "={v248}"(d)); }
template <int OFF> __device__ __forceinline__ void pf_at(unsigned& d, unsigned voff, const u32x4& rs, unsigned soff) {
    asm volatile("buffer_atomic_or v248, %1, %2, %3 offen offset:%4 sc0 sc1" : "+{v248}"(d) : "v"(voff), "s"(rs), "s"(soff), "n"(OFF) : "memory");
}
__device__ __forceinline__ void pp_sel(unsigned& so, unsigned& fail, unsigned d, unsigned tn, unsigned yes, unsigned no) {
    asm volatile("v_readlane_b32 %1, v248, 63\n\ts_and_b32 %1, %1, 0xf000f\n\ts_cmp_eq_u32 %1, %3\n\ts_cselect_b32 %0, %4, %5\n\ts_cselect_b32 %1, 0, -1"
                 : "=&s"(so), "=&s"(fail) : "{v248}"(d), "s"(tn), "s"(yes), "s"(no) : "scc");
}
#define PP_CHK(R, R1, R2, R3, OFF) \
    asm volatile("v_cmp_ne_u32 vcc, 0, %2\n\t" \
                 "s_cbranch_vccz 1f\n\t" \
                 "s_mov_b32 %1, 0x10000\n" \
                 "0:\n\t" \
                 ".if %9\n\t" \
                 "v_mov_b32 %2, 0\n\t" \
                 "s_sub_u32 %1, %1, 1\n\t" \
                 "s_cbranch_scc1 1f\n\t" \
                 "buffer_atomic_or %2, %8, %6, %7 offen offset:" OFF " sc0 sc1\n\t" \
                 "s_lshr_b32 vcc_lo, %4, 4\n\t" \
                 "s_waitcnt vmcnt(0)\n\t" \
                 "v_readlane_b32 %3, %2, 63\n\t" \
                 "s_xor_b32 %3, %3, vcc_lo\n\t" \
                 "s_and_b32 %3, %3, 0xf000f\n\t" \
                 "s_cbranch_scc1 0b\n\t" \
                 ".endif\n\t" \
                 "buffer_load_dwordx4 " R ", %5, %6, %7 offen offset:" OFF " sc1\n\t" \
                 "s_waitcnt vmcnt(0)\n\t" \
                 "s_and_b32 %3, %4, 0xf000f\n\t" \
                 "v_xor_b32 " R1 ", %3, " R1 "\n\t" \
                 "s_lshr_b32 %3, %4, 8\n\t" \
                 "s_and_b32 %3, %3, 0xf000f\n\t" \
                 "v_xor_b32 " R2 ", %3, " R2 "\n\t" \
                 "s_lshr_b32 %3, %4, 4\n\t" \
                 "s_and_b32 %3, %3, 0xf000f\n\t" \
                 "v_xor_b32 " R3 ", %3, " R3 "\n\t" \
                 "v_or3_b32 %2, " R1 ", " R2 ", " R3 "\n\t" \
                 "v_and_b32 %2, 0xf000f, %2\n\t" \
                 "s_sub_u32 %1, %1, 1\n\t" \
                 "s_cbranch_scc1 1f\n\t" \
                 "v_cmp_ne_u32 vcc, 0, %2\n\t" \
                 "s_cbranch_vccnz 0b\n" \
                 "1:" : "+{" R "}"(p), "=&s"(cnt), "+v"(bad), "=&s"(t) : "s"(tag), "v"(voff), "s"(rs), "s"(soff), "v"(vp), "n"(PB) : "vcc", "scc", "memory")
template <int H> __device__ __forceinline__ void pp_chk(u32x4& p, unsigned bad, unsigned tag, unsigned voff, const u32x4& rs, unsigned soff, unsigned vp) {
    unsigned cnt, t;
    if constexpr (H) PP_CHK("v[244:247]", "v245", "v246", "v247", "1024"); else PP_CHK("v[240:243]", "v241", "v242", "v243", "0");
}
template <int H> __device__ __forceinline__ void pp_tie(u32x4& p) { if constexpr (H) asm volatile("" : "+{v[244:247]}"(p)); else asm volatile("" : "+{v[240:243]}"(p)); }
__device__ __forceinline__ float amax3(float a, float b, float c) { float r; asm volatile("v_max3_f32 %0, |%1|, |%2|, |%3|" : "=v"(r) : "v"(a), "v"(b), "v"(c)); return r; }
__device__ __forceinline__ unsigned bfi_v(unsigned m, unsigned a, unsigned b) { unsigned r; asm volatile("v_bfi_b32 %0, %1, %2, %3" : "=v"(r) : "v"(m), "v"(a), "v"(b)); return r; }
__device__ __forceinline__ unsigned bfi_s(unsigned m, unsigned a, unsigned b) { unsigned r; asm volatile("v_bfi_b32 %0, %1, %2, %3" : "=v"(r) : "v"(m), "v"(a), "s"(b)); return r; }
__device__ __forceinline__ unsigned bfi_f(unsigned m, unsigned a, unsigned b) { unsigned r; asm volatile("v_bfi_b32 %0, %1, %2, %3" : "=v"(r) : "v"(m), "s"(a), "v"(b)); return r; }
template <int H> __device__ __forceinline__ float cvt16(unsigned w) {
    float r;
    if constexpr (H) asm volatile("v_cvt_f32_i32_sdwa %0, sext(%1) dst_sel:DWORD dst_unused:UNUSED_PAD src0_sel:WORD_1" : "=v"(r) : "v"(w));
    else asm volatile("v_cvt_f32_i32_sdwa %0, sext(%1) dst_sel:DWORD dst_unused:UNUSED_PAD src0_sel:WORD_0" : "=v"(r) : "v"(w));
    return r;
}
__device__ __forceinline__ float sel8(float a, float b, unsigned long long mask) {
    float r;
    asm volatile("s_mov_b64 vcc, %3\n\tv_cndmask_b32_dpp %0, %1, %2, vcc row_ror:8 row_mask:0xf bank_mask:0xf" : "=v"(r) : "v"(a), "v"(b), "s"(mask) : "vcc");
    return r;
}
__device__ __forceinline__ unsigned sel0(unsigned a, unsigned long long mask) { unsigned r; asm volatile("v_cndmask_b32_e64 %0, 0, %1, %2" : "=v"(r) : "v"(a), "s"(mask)); return r; }
constexpr unsigned long long diag_mask(int R) {
    unsigned long long m = 0;
    for (int l = 0; l < 64; ++l) if ((l >> 4) == ((l & 7) >> 1) && 2 * (l & 1) + ((l >> 3) & 1) == R) m |= 1ull << l;
    return m;
}
template <int IMM, int X, bool SC1> __device__ __forceinline__ void dma_x(unsigned& so, unsigned lds, unsigned voff, const u32x4& rs, unsigned base) {
    if constexpr (SC1) asm volatile("s_add_u32 m0, %1, %2\n\ts_xor_b32 %0, %5, %6\n\tbuffer_load_dwordx4 %3, %4, %0 offen sc1 lds" : "=&s"(so) : "s"(lds), "n"(IMM), "v"(voff), "s"(rs), "s"(base), "n"(X) : "memory", "scc");
    else asm volatile("s_add_u32 m0, %1, %2\n\ts_xor_b32 %0, %5, %6\n\tbuffer_load_dwordx4 %3, %4, %0 offen lds" : "=&s"(so) : "s"(lds), "n"(IMM), "v"(voff), "s"(rs), "s"(base), "n"(X) : "memory", "scc");
}
template <int OFF> __device__ __forceinline__ void dma_n(unsigned so, unsigned voff, const u32x4& rs) {
    asm volatile("buffer_load_dwordx4 %0, %1, %2 offen offset:%3 sc1 lds" :: "v"(voff), "s"(rs), "s"(so), "n"(OFF) : "memory");
}
template <int IMM, int OFF> __device__ __forceinline__ void dma_m(unsigned so, unsigned lds, unsigned voff, const u32x4& rs) {
    asm volatile("s_add_u32 m0, %4, %5\n\ts_nop 0\n\tbuffer_load_dwordx4 %0, %1, %2 offen offset:%3 sc1 lds" :: "v"(voff), "s"(rs), "s"(so), "n"(OFF), "s"(lds), "n"(IMM) : "memory", "scc");
}
__device__ __forceinline__ unsigned and_or(unsigned a, unsigned m, unsigned o) { unsigned r; asm("v_and_or_b32 %0, %1, %2, %3" : "=v"(r) : "v"(a), "v"(m), "s"(o)); return r; }

#define HOPS 1
#define HM 0x1ff221125B082ull
#define PG1 95
#define PG2 288
#define PG3 8
#define PG4 24
#define HQ 52
#define HW 4
constexpr int hop_nth(int n) { for (int k = 0; k < 62; ++k) if (((HM >> k) & 1) && n-- == 0) return k; return 63; }
constexpr int hop_next(int K) { int n = 0; for (int k = 0; k < K; ++k) n += (HM >> k) & 1; return hop_nth(n + HW); }
template <int K> __device__ __forceinline__ void hop() {
    if constexpr (HOPS && ((HM >> K) & 1)) asm volatile(".Lfkh%0:\n\ts_cbranch_execz .Lfkh%1" :: "n"(K), "n"(hop_next(K)));
}
template <int K> __device__ __forceinline__ void page() {
    if constexpr (HOPS && K >= 0 && ((HM >> (K & 63)) & 1)) asm volatile("s_branch 1f\n\t.p2align 12\n.Lfkh%0:\n\ts_cbranch_execz .Lfkh%1\n1:" :: "n"(K), "n"(hop_next(K)));
    else if constexpr (HOPS) asm volatile("s_branch 1f\n\t.p2align 12\n1:");
}
__device__ __forceinline__ void hop_all(int w) {
    if constexpr (HOPS) asm volatile("s_mov_b64 exec, 0\n\ts_cmp_eq_u32 %0, 0\n\ts_cbranch_scc1 .Lfkh%1\n\ts_cmp_eq_u32 %0, 1\n\ts_cbranch_scc1 .Lfkh%2\n\t"
                                     "s_cmp_eq_u32 %0, 2\n\ts_cbranch_scc1 .Lfkh%3\n\ts_branch .Lfkh%4\n.Lfkh63:\n\ts_mov_b64 exec, -1"
                                     :: "s"(w), "n"(hop_nth(0)), "n"(hop_nth(1)), "n"(hop_nth(2)), "n"(hop_nth(3)) : "scc");
}
#define NB 1
#define PFQ 0
#define CVR 3
#define SPR 3
#define ELP 6
#define PW 1
#define CPG 95
#define GQ -1
#define GE 3
#define KDL 0
#define GP 1
#define SG 507
#define KO 0
#define DP 0
#define B13 460
#define PRE 0
enum { OP_NONE, OP_A, OP_B, OP_FLAG, OP_SIG, OP_VOTE };
struct Op { int kind, a, b; };
constexpr Op g1_op(int G) {
    const int st = (G + 32) / 32 - 1, q = G - 32 * st, i = q >> 2, j = q & 3;
    if (i < 5) {
        if (st < 0) return {OP_NONE, 0, 0};
        if ((j & 1) == 0) return {OP_A, st * 8 + i + 3, j >> 1};
        if (q == FLQ && st < 15) return {OP_FLAG, 0, 0};
        if (NB && G == SG) return {OP_SIG, 2, 0};
        if (G == GCR) return {OP_VOTE, 0, 0};
        if (NB == 2 && G == 435) return {OP_SIG, 3, 0};
        return {OP_NONE, 0, 0};
    }
    if (st == 15) return NB && G == SG ? Op{OP_SIG, 2, 0} : Op{OP_NONE, 0, 0};
    const int n = (st + 1) * 8;
    switch (q) {
        case 20: return {OP_B, st + 1, 0}; case 21: return {OP_B, st + 1, 1}; case 22: return {OP_A, n, 0}; case 23: return {OP_B, st + 1, 2};
        case 24: return {OP_B, st + 1, 3}; case 25: return {OP_A, n, 1}; case 26: return {OP_A, n + 1, 0}; case 27: return {OP_A, n + 1, 1};
        case 28: return {OP_A, n + 2, 0}; case 30: return {OP_A, n + 2, 1};
    }
    return {OP_NONE, 0, 0};
}
constexpr int g1_need(int G) {
    const int st = G >> 5, j = G & 3;
    for (int h = G - 1; h >= -12; --h) {
        const Op o = g1_op(h);
        if (o.kind == OP_A && o.a == (G >> 2) && o.b == (j >> 1)) return h;
        if (o.kind == OP_B && o.a == st && o.b == j) return h;
        if (o.kind == OP_FLAG && (G & 31) == 20 && (h >> 5) == st) return h;
    }
    return -100;
}
constexpr int g1_wait(int G) {
    int n = -100, c = 0;
    for (int h = G; h < (G ? G + 4 : 2) && h < 512; ++h) if (g1_need(h) > n) n = g1_need(h);
    for (int h = n + 1; h < G; ++h) c += g1_op(h).kind != OP_NONE;
    return n < G ? c : -1;
}
#define KV0 0
#define KVN 0x11111112
constexpr int kv_first(int k) { int n = 0; for (int i = KV0; i < k && i < KV0 + 8; ++i) n += (KVN >> (4 * (i - KV0))) & 15; return n > 8 ? 8 : n; }
constexpr int kv_last() { int k = KV0; while (kv_first(k + 1) < 8) ++k; return k; }
#define CQ -1
#define CTM 0
#define EC 2
#define ED 2
#define EB 0
#define KST 0
#define KFD 3
#define KPL 11
#define KPI 0
#define KSL SLP
#define CBQ 20
constexpr int KVL = kv_last(), KFL = KVL + KFD, KSD = KVS ? KCC : -1;
static_assert(KFL <= KPL && KPL <= 12 && KPL > KVL && (KPI & 3) == 0 && KTI != KCC && KTI != KFL && KTI < KPL && KVP >= 0 && KVP < KCQ && KCQ < KCC && KSD <= KVL && KCC <= KPL && GCR > 32 * KCC + 62, "");
constexpr int hv_cnt(int k0, int k1) {
    int c = 0;
    for (int k = k0; k < k1; ++k) c += 9 + (KST ? 4 * (k == KVL) : k < KSD ? 0 : kv_first(k + 1) / 2 - (k == KSD ? 0 : kv_first(k) / 2)) + (k == KFL) + (k == KPL) + (k == KCQ) + (k == KVP) + (TM && TM < 16 && k == KTI);
    return c;
}
#define ZM 1
#define CL 1
#define EP 0
constexpr unsigned long long lag0_mask(int w) {
    unsigned long long m = 0;
    for (int l = 0; l < 64; ++l) if ((l >> 4) == 2 + ((l >> 3) & 1) && 3 - ((l & 7) >> 1) == w) m |= 1ull << l;
    return m;
}
constexpr int cv_start(int n) { return n * 8 - n * (n - 1) / 2; }
constexpr int cv_n(int q) { int n = 0; while (cv_start(n + 1) <= q) ++n; return n; }
constexpr int cv_m(int q) { return 7 - cv_n(q) - (q - cv_start(cv_n(q))); }
enum { CO_NONE, CO_A, CO_B, CO_C };
constexpr Op cv_op(int G) {
    const int s = (G + 36) / 36 - 1, q = G - 36 * s;
    if (q >= 1 && q <= 6) return s >= 0 ? Op{CO_A, s * 8 + 5 + (q - 1) / 2, (q - 1) & 1} : Op{CO_NONE, 0, 0};
    if (q >= 11 && q <= 14) return s >= 0 && s >= CL && s - CL < 8 ? Op{CO_C, s - CL, q - 11} : Op{CO_NONE, 0, 0};
    if (s == 7) return {CO_NONE, 0, 0};
    for (int m = 0; m < 8; ++m) if (q == cv_start(7 - m)) return {CO_B, (s + 1) * 8 + m, 0};
    for (int n = 0; n < 5; ++n) if (q == cv_start(n + 1) + 1 || q == cv_start(n + 1) + 2) return {CO_A, (s + 1) * 8 + n, q - cv_start(n + 1) - 1};
    return {CO_NONE, 0, 0};
}
constexpr int cv_need(int G) {
    const int s = G / 36, q = G % 36;
    for (int h = G - 1; h >= -36; --h) {
        const Op o = cv_op(h);
        if (o.kind == CO_A && o.a == s * 8 + cv_n(q)) return h;
        if (o.kind == CO_B && o.a == s * 8 + cv_m(q)) return h;
        if (o.kind == CO_C && q == 24 && o.a == s - CL) return h;
    }
    return -100;
}
constexpr int cv_wait(int G) {
    const int n = cv_need(G);
    for (int h = G - 1; h >= 0 && h >= G - 40; --h) if (cv_need(h) >= n) return -1;
    int c = 0;
    for (int h = n + 1; h < G; ++h) c += cv_op(h).kind != CO_NONE;
    return c;
}
#define AT(n, m) ((n) * 8 + (m))
#define FM 0x326260
constexpr int fill_pos(int s, int i) {
    const int f = (FM >> (4 * (s - 2))) & 15, n = s == 2 ? 0 : 4 * s - 9;
    switch (f) {
        case 1: return AT(n + i, 5) - (s == 2 && i == 3) * 6;
        case 2: return AT(n + 1 + i / 2, 5 + 2 * (i & 1));
        case 3: return i < 2 ? AT(n, 5 + 2 * i) : AT(n + i - 1, 5);
        case 4: return AT(n + 2 + i / 2, 5 + 2 * (i & 1));
        case 5: return AT(n, 1 + 2 * i) + (s == 2 ? 4 : 0);
        case 6: return AT(n + 3 + i / 2, 5 + 2 * (i & 1));
        case 7: return AT(n + 3, 5 + 2 * i);
        case 8: return i < 2 ? AT(n + 1, 5 + 2 * i) : AT(n + i, 5);
        case 9: return AT(n + 1 + i, 1);
        case 10: return i < 2 ? AT(n, 1 + 2 * i) : AT(n + 3, 1 + 2 * i);
    }
    return AT(n + i / 2, 5 + 2 * (i & 1));
}
constexpr int g2_fill(int p) {
    for (int k = 8; k < 32; ++k) if (fill_pos(k >> 2, k & 3) == p) return k;
    return -1;
}
#define QX AT(2, 5)
#define QQ AT(3, 2)
#define QZ AT(11, 2)
#define QT AT(3, 4)
constexpr int P_X = QX, P_Q = QQ, P_Z = QZ, P_T = QT;
constexpr bool XL = false;
constexpr bool fill_m0(int k) {
    const int a = fill_pos(k >> 2, (k & 3) - 1), b = fill_pos(k >> 2, k & 3);
    return (P_Q + 2 >= a && P_Q < b) || (P_Z + 2 >= a && P_Z < b) || (P_X + 2 >= a && P_X < b);
}
#define DBG 0
#define XW 4
#define XW2 4
#define QPP0 AT(9, 4)
#define QL0 AT(21, 1)
#define QR0 AT(24, 6)
#define QF2 AT(28, 3)
#define QL1 AT(26, 1)
#define QR1 AT(29, 3)
#define QA AT(16, 4)
#define AD 0
#define D1 2
#define D0 1
constexpr int NS = 33, NR = 29;
constexpr int P_PP0 = QPP0, P_PP1 = AT(16, 4), P_L0 = QL0, P_R0 = QR0, P_F2 = QF2, P_L1 = QL1, P_R1 = QR1, P_ZC = AT(10, 6), P_A = QA;
#define QB 24
#define QW -2
constexpr int P_W = QW;
static_assert(P_W < AT(ZP, 4), "");
constexpr int P_B0 = P_L0 - QB, P_B1 = P_L1 - QB;
static_assert(P_B0 > P_ZC + 1 && P_B1 > P_L0 + 1, "");
constexpr int E_PP0 = P_PP0 + NS - 1, E_PP1 = P_PP1 + (NS - 1) / D1, E_R0 = P_R0 + (NR - 1) / D0;
constexpr int ZW = P_A >= AT(20, 4) ? 19 : 15;
static_assert(P_R0 + 14 / D0 >= AT(24, 3) && E_R0 < P_F2 && P_F2 >= AT(24, 4) && P_F2 + 8 <= AT(31, 0) && E_R0 < P_R1, "");
static_assert(P_PP0 >= AT(9, 2) && P_PP0 + 8 <= AT(15, 4) && E_PP0 < P_PP1 && P_A >= AT(16, 4), "");
constexpr bool a_free(int p) {
    return p >= P_A && (AD || !((p >= P_PP1 && p <= E_PP1) || (p >= P_R0 && p <= E_R0) || (p >= P_F2 && p < P_F2 + 16)));
}
constexpr int a_step(int p) {
    if (NOA || !a_free(p)) return -1;
    int c = 0;
    for (int h = P_A; h < p; ++h) c += a_free(h);
    return c <= 48 ? c : -1;
}
constexpr int a_pos(int s) { int p = P_A; while (a_step(p) != s) ++p; return p; }
constexpr int q_rd(int r) { int p = r < 2 ? AT(ZW, 6) + r : a_pos(12 * r - 16) + 1; while ((p & 7) < 4) ++p; return p; }
constexpr int q_tie(int r) { const int n = (q_rd(r) >> 3) + 1; return AT(n, (n & 3) == 3 ? 0 : 4); }
static_assert(NOA || (a_pos(48) < 256 && q_tie(2) <= a_pos(23) && q_tie(3) <= a_pos(35)), "");
constexpr int g2_vm(int p) {
    int c = g2_fill(p) >= 0;
    c += a_step(p) > 0 && a_step(p) % 12 == 0 && !(NOST & 1);
    if (!NOA) c += (p == P_T) + (p == P_Q || p == P_Q + 2) + (p == AT(ZP, 4)) + (p == P_Z || p == P_Z + 2);
    c += (p == P_X || p == P_X + 2) + (p == P_W);
    c += !(NOST & 2) && (p == E_PP0 || p == E_PP1);
    c += (p == P_L0 || p == P_L1) + (PB && (p == P_B0 || p == P_B1));
    c += !(NOST & 4) && (p == P_F2 + 11 || p == P_F2 + 15);
    return c;
}
constexpr int g2_lx(int n) {
    int c = 2 * (n == 7);
    if (n >= 0 && (n & 7) >= 6) c += 2;
    if (!NOA) { c += 4 * (n == ZW); for (int r = 0; r < 4; ++r) c += (q_rd(r) >> 3) == n; }
    return c;
}
constexpr int g2_tile(int n, int j) { return (n & 4) ? (n & 2) == 0 : j; }
constexpr int g2_unit(int n, int j) { return (n & 4) ? 4 + 2 * (n & 1) + j : n & 7; }
constexpr int imax(int a, int b) { return a > b ? a : b; }
constexpr int g2_need(int n) {
    int r = n == 3 ? -1 : fill_pos((n + 1) / 4, 3);
    if (n == 7 && !XL) r = imax(r, P_X + 2);
    if (n == ZW && !NOA) r = imax(r, imax(P_Q, P_Z) + 2);
    return r;
}
static_assert(P_Z > AT(11, 0) && P_T > AT(3, 0) && P_T < AT(ZP, 4) && (!XL || (P_X + 2 < P_R0 && P_R0 + 2 >= AT(24, 2) && P_R0 < AT(27, 0) && D0 == 1)), "");
constexpr bool rd1(int n) { return RD == 1 || (RD == 3 && (n & 3) == 2); }
constexpr int g2_allow(int e, int p) { int c = 0; for (int h = p + 1; h < e; ++h) c += g2_vm(h); return c; }

__global__ __launch_bounds__(512, 2) __attribute__((aligned(4096))) void k1(const uint16_t* __restrict__ x, const uint16_t* __restrict__ bre, const uint16_t* __restrict__ bim,
                                             const uint16_t* __restrict__ lre, const uint16_t* __restrict__ lim, const uint16_t* __restrict__ logdt,
                                             const uint16_t* __restrict__ cre, const uint16_t* __restrict__ cim, const uint16_t* __restrict__ dskip,
                                             const uint16_t* __restrict__ ssmk, const uint16_t* __restrict__ ssmD, const uint16_t* __restrict__ qin,
                                             const uint16_t* __restrict__ kin, const uint16_t* __restrict__ vin, const uint16_t* __restrict__ sk,
                                             char* __restrict__ ws, float* __restrict__ out TS_ARG) {
    unsigned long long pc0, rnd;
    asm volatile("s_getpc_b64 %0\n\ts_memtime %1" : "=s"(pc0), "=s"(rnd));
    TS_DECL TS(0);
    TL_DECL
    __shared__ __attribute__((aligned(1024))) char shm[LDS_TOTAL];
    const int tid = threadIdx.x, lane = tid & 63, wid = __builtin_amdgcn_readfirstlane(tid >> 6);
    const int b = blockIdx.x, xcd = b & 7, idx = b >> 3, hb = xcd & 1, tq = idx & 7;
    const int tm = (xcd >> 1) * 4 + (idx >> 3), tn = hb * 8 + tq;
    unsigned nr;
    const int tm2 = (xcd & 4) * 2 + tq, dt = (xcd & 2) * 2 + (idx >> 3), rot2 = tq + JR(idx >> 3);
    const unsigned lbase = (unsigned)(uintptr_t)shm;
    const int l15 = lane & 15, g4 = lane >> 4, lw = wid & 3, kg = wid >> 2;
    dma<U0 + 4096, false>(lbase + wid * 1024, lane * 16, mk_rsrc((const void*)(pc0 & ~255ull), CODE_KB * 1024), ((idx * 8 + wid) & 63) * 1024);
    const unsigned mk0 = lbase + POLL + lw * 256 + kg * (FULL - POLL + lw * (FST - 256));
    flag_set(mk0 + lane * 4, 1u);
    flag_set(lbase + HGO + lane * 4, 0u);
    if constexpr (NB) flag_set(lbase + DONE + lw * FST + lane * 4, 1u);
    unsigned vo[4];
    const bool xw = lw < 2;
#pragma unroll
    for (int i = 0; i < 4; ++i) {
        const int v = l15 ^ (i * 4 + g4);
        vo[i] = ((xw ? 64 * (i & 1) + 4 * (i >> 1) + (lane & 32) + 2 * (g4 & 1) : 8 * (i & 1) + 2 * g4) + (v >> 3)) * 2048 + (v & 7) * 16 - (i & 1) * 1024;
    }
    const int bw = lw >> 1;
    unsigned r0 = (tm * 128 + lw * 16 + bw * (tn * 64 + lw * 16 - 64 - tm * 128)) * 2048, rsz = 229376 - bw * 163840;
    unsigned rot = 8 * ((tq + (idx >> 3)) & 1) + 2 * (xcd >> 1) + 4 * hb, rst = 16384 << bw, lb = lbase + lw * 8192, oob = OOB;
    unsigned lb0 = lb + g1_buf(0) + kg * (g1_buf(1) - g1_buf(0)), so0 = ((kg + rot) & 15) * 128;
    unsigned co = l15 * 2 + (tn * 128 + lw * 32 + kg * 2048), vw = b * 4 + lw + 32 - 32 * kg;
    tie(vo[0]); tie(vo[1]); tie(vo[2]); tie(vo[3]); tie(oob); tie(co);
    ties(r0); ties(rsz); ties(rot); ties(rst); ties(lb); ties(lb0); ties(so0); ties(vw);
    sched();
    asm volatile("" :: "s"(x), "s"(bre), "s"(bim), "s"(lre), "s"(lim), "s"(logdt), "s"(cre), "s"(cim), "s"(dskip), "s"(ssmk), "s"(ssmD), "s"(qin),
                       "s"(kin), "s"(vin), "s"(sk), "s"(ws), "s"(out));
    const bool xv = fresh(bw) == 0;
    const u32x4 rs0 = mk_rsrc((const char*)(xv ? x : bre) + r0, rsz), rs1 = mk_rsrc((const char*)(xv ? x : bim) + r0 - 2048, rsz + 2048);
    unsigned c_dt, c_re, c_im;
    gld<0>(c_dt, co, mk_rsrc(logdt, 2048), 0);
    gld<0>(c_re, co, mk_rsrc(lre, 2048), 0);
    gld<0>(c_im, co, mk_rsrc(lim, 2048), 0);
    const unsigned vb = race(vw, ws, rnd);
    static_for<0, 8>([&](auto ic) { constexpr int i = CV(ic); dma4<(i & 4) * 1024, i & 3>(lb0, vo[i & 3], (i & 2) ? rs1 : rs0, so0 + (i >> 2) * rst); });
    wait_l<0>();
    barrier();
    dma1<0>(mk0 + kg * 120, oob, rs0, 0);
    sched();
    const unsigned a00 = (l15 >> 1) * 256 + ((((l15 & 1) * 8 + g4) ^ (l15 >> 1)) << 4);
    auto frag = [&](int par, int ks) { return (a00 ^ (ks * 64) ^ (par * 128)) + par * 2048; };
    bf16x8 cf[32];
    unsigned vc[4], cad[4];
    auto c_setup = [&]() {
#pragma unroll
        for (int i = 0; i < 4; ++i) { const int v = l15 ^ (i * 4 + g4); vc[i] = (4 * (2 * g4 + (v >> 3)) + 2 * (i & 1) + (i >> 1)) * 2048 + (v & 7) * 16 - i * 1024; }
    };
    const u32x4 rsc = mk_rsrc((kg ? cim : cre) + (size_t)dt * 131072, 262144);
    auto c_off = [&](int dg, int j) { return (unsigned)(dg * 65536 + hb * 1024 + ((rot2 + j) & 7) * 128); };
    const unsigned cst = lbase + (kg ? BUF : 2 * BUF) + lw * 8192;
    unsigned tag;
    const u32x4 rsf = mk_rsrc(ws + WS_F, 65536);
    const __amdgpu_buffer_rsrc_t rf = mk_brs(ws + WS_F, 65536);
    const int qx = (hb ? 0 : 2) ^ JQ(idx >> 3), wid_ = wid, b_ = b;

    if (wid >= 4) {
        page<-1>();
        const int ch = xcd * 128 + (idx >> 2) * 16 + lw * 4 + (idx & 3);
        unsigned dw, nx; u32x4 ad;
        auto c_unit = [&](auto uc) { static_for<0, 4>([&](auto ic) { constexpr int u = CV(uc), i = CV(ic); dma4<(u & 1) * 4096, i>(cst, vc[i], rsc, c_off(lw, u)); }); };
        constexpr int XV = 4 * (EC == 1) + 8 * (PFQ == 2);
        auto ctouch = [&](int r) { const unsigned j = (fresh(idx * 4 + lw) + r * 37) & 127; stouch((pc0 & ~255ull) + (j < CODE_KB * 8 / 3 ? j : j - 64) * 384); };
        {
            const unsigned fb = lbase + FULL + lw * FST;
            auto piece = [&](auto sc, auto ic) {
                constexpr int s = CV(sc), i = CV(ic);
                dma4<g1_buf(s) + (i & 4) * 1024, i & 3>(lb, vo[i & 3], (i & 2) ? rs1 : rs0, ((s + rot) & 15) * 128 + (i >> 2) * rst);
            };
            auto mark = [&](auto sc) { dma1<(31 - CV(sc)) * 4>(fb, oob, rs0, 0); };
            auto feed = [&](auto kc, auto ic) {
                constexpr int k = CV(kc), i = CV(ic);
                if constexpr (i < 4) piece(IC(k + 3), IC(i + 4));
                if constexpr (i == 3) mark(IC(k + 3));
                if constexpr (i >= 4 && k < 12) piece(IC(k + 4), IC(i - 4));
            };
            __builtin_amdgcn_s_setprio(HP);
            TS(7);
            sld(tag, ws + WS_F + F_EP);
            sld(dw, ssmD + (ch & ~1));
            const int sb = idx * 2 + (lw >> 1), kh = lw & 1;
            u32x2 kk[11], vv[8]; u32x4 w4[2], kd[4], ke[4], kvo[4];
            const unsigned ko = g4 * 16384 + l15 * 8, so = sb * 65536 + xcd * 256 + kh * 128;
            {
                const u32x4 rs = mk_rsrc(kin, 4 << 20);
                gld<0>(w4[0], l15 * 32, mk_rsrc(sk, 8192), xcd * 1024 + kh * 512);
                gld<16>(w4[1], l15 * 32, mk_rsrc(sk, 8192), xcd * 1024 + kh * 512);
                static_for<0, 3>([&](auto ic) { gld<0>(kk[CV(ic)], ko + so - 6144 + CV(ic) * 2048, rs, 0); });
                static_for<0, 8>([&](auto ic) { constexpr int i = CV(ic); gld<(i & 1) * 2048>(kk[3 + i], ko, rs, so + (i >> 1) * 4096); });
                static_for<0, 8>([&](auto ic) { constexpr int i = CV(ic); gld<(i & 1) * 2048>(vv[i], ko, mk_rsrc(vin, 4 << 20), so + (i >> 1) * 4096); });
                dma1<DSK>(lbase, lane * 4, mk_rsrc(dskip, 2048), dt * 256);
                dma<U0, false>(lbase + kh * 1024, oob, rs0, 0);
            }
            static_for<4, 8>([&](auto ic) { feed(IC(-2), ic); });
            const __amdgpu_buffer_rsrc_t rk = mk_brs(ws + WS_KVT + (size_t)((xcd & 3) * 32 + kh * 16) * 32768, 524288);
            const unsigned K = lbase + KC + lw * KCH;
            unsigned kfo;
            const unsigned n0 = (xcd & 4) << 11;
            auto st = [&](int c) { st16(kvo[c], rk, (l15 >> 2) * 131072 + g4 * 64 + (l15 & 3) * 16, c * 32768 + (sb >> 5) * 16384 + (KVS ? nx : n0 ^ ((sb & 32) << 8)) + (sb & 31) * 256); };
            float kf[7], pa[4], pb[4];
            auto kv_step = [&](auto uc, auto jc) {
                constexpr int u = CV(uc), j = CV(jc), c = u >> 1, h = u & 1;
                auto el = [&](const u32x2& q) -> float { return (c & 1) ? bfhi(q[c >> 1]) : bflo(q[c >> 1]); };
                auto tap = [&](int t) { const unsigned w = w4[c >> 1][(c & 1) * 2 + (t >> 1)]; return (t & 1) ? bfhi(w) : bflo(w); };
                auto all = [&](auto f) {
#pragma unroll
                    for (int i = 0; i < 4; ++i) f(i);
                };
                if constexpr (j == 0) {
#pragma unroll
                    for (int i = 0; i < 7; ++i) { kf[i] = (h && i < 3) ? kf[i + 4] : el(kk[4 * h + i]); tie(kf[i]); }
                }
                if constexpr (EXACT) {
                    if constexpr (j == 1) { const float t = tap(3); all([&](int i) { pa[i] = kf[i] * t; }); all([&](int i) { pa[i] = rbf(pa[i]); tie(pa[i]); }); }
                    if constexpr (j == 2 || j == 4 || j == 6) { const float t = tap(3 - j / 2); all([&](int i) { pb[i] = kf[i + j / 2] * t; }); all([&](int i) { pb[i] = rbf(pb[i]); tie(pb[i]); }); }
                    if constexpr (j == 3 || j == 5 || j == 7) { all([&](int i) { pa[i] += pb[i]; }); all([&](int i) { pa[i] = rbf(pa[i]); tie(pa[i]); }); }
                } else {
                    if constexpr (j == 1) { const float t = tap(3); all([&](int i) { pa[i] = kf[i] * t; tie(pa[i]); }); }
                    if constexpr (j >= 2 && j <= 4) { const float t = tap(4 - j); all([&](int i) { pa[i] = fma_(kf[i + j - 1], t, pa[i]); tie(pa[i]); }); }
                    if constexpr (j == 5) all([&](int i) { pa[i] = rbf(pa[i]); tie(pa[i]); });
                }
                if constexpr (j == (EXACT ? 7 : 6)) all([&](int i) { pb[i] = el(vv[4 * h + i]); tie(pb[i]); });
                if constexpr (j == 7) {
                    all([&](int i) { pa[i] *= pb[i]; });
                    kvo[c][3 - 2 * h] = pack2(pa[1], pa[0]); kvo[c][2 - 2 * h] = pack2(pa[3], pa[2]);
                    if constexpr (h && !KST && u >= kv_first(KSD)) st(c);
                    else if constexpr (u == 7) all(st);
                    else tie(kvo[c]);
                }
            };
            static_for<-1, 13>([&](auto kc) {
                constexpr int k = CV(kc);
                if constexpr (k == 3) page<1>();
                if constexpr (k == 2) hop<0>();
                if constexpr (k == 6) hop<6>();
                if constexpr (k == 8) hop<9>();
                if constexpr (k >= 0) barrier();
                if constexpr (k >= 0 && ((CTM >> k) & 1)) ctouch(k);
                if constexpr (k == KV0) {
                    wait_v<4 + hv_cnt(-1, k)>();
                    static_for<0, 11>([&](auto ic) { tie(kk[CV(ic)]); });
                    static_for<0, 8>([&](auto ic) { tie(vv[CV(ic)]); });
                    tie(w4[0]); tie(w4[1]); keep(c_dt); keep(c_re); keep(c_im);
                    TS(8);
                }
                if constexpr (k == KVP) {
                    wait_l<0>();
                    ties(tag); ties(dw);
                    tag += 0x01010101u;
                    __builtin_amdgcn_raw_buffer_store_b64(u32x2{tag, ~tag ^ vb}, rf, lane || vw >= 32 ? OOB : 0u, F_C + vw * 8, 16);
                }
                if constexpr (k == KCQ) dma1<POLL>(lbase + lw * 256, lane * 4, rsf, F_C);
                if constexpr (TM == 16 && k == KTI) stouch((unsigned long long)(ws + WS_E) + (TX ? (xcd * 128 + idx * 4 + lw) : b * 4 + lw) * 384);
                if constexpr (TM && TM < 16 && k == KTI) {
                    const unsigned wv = b * 4 + lw, a = lane < 32 ? ((TM & 1) ? wv * 4096 + lane * 128 : (TM & 4) && lane < 4 ? wv * 4096 + lane * 1024 + 896 : OOB) : lane < 35 && (TM & 2) ? (unsigned)(WS_E - WS_PP) + (wv * 3 + lane - 32) * 128 : OOB;
                    if constexpr (TM & 8) dma1<DUMP>(lbase + lw * 256, a, mk_rsrc(ws + WS_PP, WS_F + F_EP - WS_PP), 0);
                    else asm volatile("buffer_atomic_or %0, %1, %2, 0 offen sc1" :: "v"(0u), "v"(a), "s"(mk_rsrc(ws + WS_PP, WS_F + F_EP - WS_PP)) : "memory");
                }
                if constexpr (k == KCC) {
                    unsigned f, n, sp;
                    wait_v<hv_cnt(KCQ, k) - 1>();
                    dsr32<POLL - 131072>(f, lbase + 131072 + lw * 256 + lane * 4);
                    wait_l<0>();
                    vote_chk(f, (lane & 1) ? ~tag : tag, lane & 1, lbase + POLL + lw * 256 + lane * 4, lbase + POLL + lw * 256, lane * 4, rsf, F_C, sp);
                    nr = vote_cnt(f, tag, (xcd & 4) << 11, n);
                    nx = nr ^ ((xcd & 2) << 12);
                    kfo = F_K + (KVS ? nr : n0) + (((xcd & 3) * 2 + (idx >> 4)) * 64 + lw * 16) * 8;
                    TSV(11, n + 100 * (0x10000 - sp));
                }
                if constexpr (k == KFL) {
                    wait_v<hv_cnt(KVL + 1, k)>();
                    __builtin_amdgcn_raw_buffer_store_b64(u32x2{tag, ~tag}, rf, lane ? OOB : 0u, F_K + (KVS ? nr : n0) + (((xcd & 3) * 2 + kh) * 64 + sb) * 8, 16);
                    TS(10);
                }
                if constexpr (k == 12) {
                    dma1<0>(K, oob, rs0, 0);
                    dma1<KCP>(K, oob, rs0, 0);
                }
                static_for<0, 8>([&](auto ic) {
                    constexpr int i = CV(ic), u0 = kv_first(k), nu = kv_first(k + 1) - u0;
                    if constexpr (k == KPL && i == KPI) dma1<POLL>(lbase + wid * 256, (lane & 31) * 4, rsf, kfo);
                    feed(kc, ic);
                    if constexpr (k == 4 && i == 4) hop<3>();
                    if constexpr (!KST && k == KSD && (i & 1) && i / 2 < kv_first(KSD) / 2) st(i / 2);
                    if constexpr (nu == 1) kv_step(IC(u0), ic);
                    if constexpr (nu == 2) { kv_step(IC(u0 + i / 4), IC(2 * (i & 3))); kv_step(IC(u0 + i / 4), IC(2 * (i & 3) + 1)); }
                    if constexpr (!KDL && k == 12 && i >= 4) gld<(i - 4) * 1024>(kd[i - 4], lane * 16, mk_rsrc(ssmk, 4 << 20), ch * 4096);
                    if constexpr (k == 11 && i == 4) c_setup();
                    if constexpr (k == 10 && i >= 4) { cad[i - 4] = cst + frag(i & 1, (i >> 1) & 1); tie(cad[i - 4]); }
                    sched();
                });
            });
            page<12>();
            TS(1);
            if constexpr (EB == 2) barrier();
            unsigned pf;
            const unsigned fr = lbase + FULL + (lane & 3) * FST;
            wait_v<hv_cnt(KPL, 12) - (KPL < 12 ? 1 : 2) - (KPL == KFL) - KPI - (KPI > 3) + (KDL ? 7 : 11)>();
            dsr32<POLL - 131072>(pf, lbase + 131072 + wid * 256 + lane * 4);
            wait_l<0>();
            tag_chk<KSL>(pf, (lane & 1) ? ~tag : tag, (lane & 31) * 4, rsf, kfo);
            if constexpr (KDL) static_for<0, 4>([&](auto ic) { gld<CV(ic) * 1024>(kd[CV(ic)], lane * 16, mk_rsrc(ssmk, 4 << 20), ch * 4096); });
            auto b13 = [&]() { if constexpr (NB == 2) { unsigned f = 1; zero_chk<12, 1>(f, lbase + DONE + (lane & 3) * FST); } else if constexpr (NB < 2) barrier(); };
            if constexpr (EB == 1) b13();
            {
                const u32x4 rs = mk_rsrc(ws + WS_KVT + (size_t)((xcd & 3) * 32 + idx) * 32768, 32768);
                static_for<0, 4>([&](auto ic) { constexpr int i = CV(ic); dma<U0 + 2048 + i * 1024, true>(lbase + lw * 4096, (lane >> 2) * 64 + (((lane & 3) ^ g4) << 4), rs, (lw >> 1) * 16384 + (KVS ? nx : n0 ^ ((lw & 2) << 12)) + (lw & 1) * 4096 + i * 1024); });
            }
            mark(IC(16));
            auto warm = [&]() {
                const unsigned wo = g4 * 2048 + l15 * 16, dl = lbase + lw * 1024;
                const unsigned qo = (512 * (idx >> 3) + 128 * (idx & 1) + 32 * lw + 4 * ((idx >> 1) & 3)) * 2048 + xcd * 256, xo = (tm2 * 128 + 32 * lw + 8 * (qx & 2)) * 2048 + dt * 256;
                static_for<0, 4>([&](auto ic) { constexpr int i = CV(ic); dma<DUMP, false>(dl, wo, mk_rsrc(qin, 4 << 20), qo + ((i & 1) * 16 + (i >> 1) * 256) * 2048); });
                static_for<0, 4>([&](auto ic) { constexpr int i = CV(ic); dma<DUMP, false>(dl, wo, mk_rsrc(x, 4 << 20), xo + i * 8192); });
            };
            if constexpr (PFQ == 1) warm();
            if constexpr (EB == 0) b13();
            auto c_first = [&]() {
                if constexpr (CL <= 1) c_unit(IC(0));
                if constexpr (CL <= 0) c_unit(IC(1));
                if constexpr (EC == 1) static_for<0, 4>([&](auto ic) { constexpr int i = CV(ic); dma4<DUMP, i>(lbase, vc[i], mk_rsrc(cre + (size_t)dt * 131072, 262144), c_off(lw, 0)); });
                if constexpr (PFQ == 2) warm();
            };
            static_assert(NB != 3 || CL == 2, "");
            if constexpr (!KO) c_first();
            wait_v<5 + 8 * (PFQ == 1) + (KO ? 0 : 4 * (CL <= 1) + 4 * (CL <= 0) + XV)>();
            static_for<0, 4>([&](auto ic) { tie(kd[CV(ic)]); });
#pragma unroll
            for (int n = 0; n < 4; ++n) {
                const unsigned first = n ? __builtin_amdgcn_readlane(kd[n ? n - 1 : 0][3], 63) : 0u;
                const unsigned p = __builtin_amdgcn_update_dpp(first, kd[n][3], 0x138, 0xf, 0xf, false);
                ke[n] = u32x4{__builtin_amdgcn_alignbyte(kd[n][0], p, 2), __builtin_amdgcn_alignbyte(kd[n][1], kd[n][0], 2),
                              __builtin_amdgcn_alignbyte(kd[n][2], kd[n][1], 2), __builtin_amdgcn_alignbyte(kd[n][3], kd[n][2], 2)};
                tie(ke[n]);
            }
            if constexpr (ZM) {
                const unsigned dl = (ch & 1) ? dw >> 16 : dw & 0xffffu, dv = dl << ((~lane & 1) * 16);
                static_for<0, 4>([&](auto ic) { constexpr unsigned long long k = lag0_mask(CV(ic)); ad[CV(ic)] = sel0(dv, k); });
                tie(ad);
            }
            hop<14>();
            static_for<0, 4>([&](auto ic) { constexpr int i = CV(ic); dsw128<64 + i * 1024>(K + lane * 16, kd[i]); dsw128<KCP + 64 + i * 1024>(K + lane * 16, ke[i]); });
            if constexpr (KO) c_first();
            TS(9);
            static_for<0, -CL>([&](auto uc) {
                constexpr int u = CV(uc);
                wait_v<4 + (u < 2 ? XV : 0)>();
                static_for<0, 4>([&](auto ic) { constexpr int i = CV(ic); dsr128<(u & 1) * 4096>(cf[u * 4 + i], cad[i]); });
                wait_l<0>();
                static_for<0, 4>([&](auto ic) { tie(cf[u * 4 + CV(ic)]); });
                c_unit(IC(u + 2));
            });
            pf = 1;
            zero_chk<15 * 4>(pf, fr);
            if constexpr (PRE) { pf = 1; zero_chk<PRE == 1 ? 8 : 4>(pf, lbase + DONE + (lane & 3) * FST); }
            __builtin_amdgcn_s_setprio(0);
        }
        TS(2);
        TL(1);
        unsigned ka[4], ub[4];
#pragma unroll
        for (int i = 0; i < 4; ++i) {
            ka[i] = lbase + KC + lw * KCH + ((l15 & 1) ? 0 : KCP) + 84 + 2 * (l15 & 14) - 16 * g4 + i * 1024;
            const int e = 2 * l15 + g4 + 2;
            ub[i] = lbase + U0 + 64 * e + ((lw ^ (((e >> 2) + 3 - i) & 3)) << 4);
        }
        unsigned gad = lbase + DONE + lw * FST;
        if constexpr (GQ >= 0) tie(gad);
        f32x4 acc[8];
        bf16x4 FA[8][2]; bf16x8 FB[8];
        auto op = [&](auto gc) {
            constexpr Op o = cv_op(CV(gc));
            if constexpr (o.kind == CO_A) { constexpr int s = o.a >> 3, n = o.a & 7, d = (n & 1) * 128 + s * 16 + o.b * 2; dsr2<d, d + 1>(FA[n][o.b], ka[n >> 1]); }
            if constexpr (o.kind == CO_B) { constexpr int s = o.a >> 3, m = o.a & 7; dsr128<2048 * m + 1792 - 256 * s>(FB[m], ub[s & 3]); }
            if constexpr (o.kind == CO_C) dsr128<(o.a & 1) * 4096>(cf[o.a * 4 + o.b], cad[o.b]);
        };
        static_for<-36, 0>(op);
        static_for<0, 288>([&](auto gc) {
            constexpr int G = CV(gc), s = G / 36, q = G % 36, n = cv_n(q), m = cv_m(q);
            if constexpr (cv_wait(G) >= 0) wait_lr<cv_wait(G)>();
            if constexpr (q == 24 && s >= CL && s - CL < 8) static_for<0, 4>([&](auto ic) { tie(cf[(s - CL) * 4 + CV(ic)]); });
            if constexpr (ZM && G < 8) mfma<true>(acc[m], __builtin_bit_cast(bf16x8, ad), FB[m]);
            mfma<s == 0 && n == 0 && !ZM>(acc[n + m], cat(FA[n][0], FA[n][1]), FB[m]);
            sched();
            if constexpr (q == 11) TL(2 + 2 * s);
            if constexpr (q == 11 && s >= CL && s - CL < 8) wait_v<(s - CL < 7 ? 4 : 0) + (s - CL < (CL <= 1) + (CL <= 0) ? XV : 0) + 8 * (PFQ >= 3 && (s == PFQ - 2 || s == PFQ - 1))>();
            if constexpr (q == 11) TL(3 + 2 * s);
            op(gc);
            if constexpr (q == 30 && ((CTM >> (16 + s)) & 1)) ctouch(16 + s);
            if constexpr (G == 16) hop<17>();
            if constexpr (G == 66) hop<19>();
            if constexpr (G == PG1) page<21>();
            if constexpr (G == PG2) page<28>();
            if constexpr (G == 158) hop<23>();
            if constexpr (G == 209) hop<25>();
            if constexpr (G == 254) hop<27>();
            if constexpr (G == GQ) { unsigned f = 1; zero_chk<4, 1>(f, gad); }
            if constexpr (!NB && G == CBQ) barrier();
            if constexpr (NB == 3 && G == 23) { unsigned f = 1; zero_chk<8, 1>(f, lbase + DONE + (lane & 3) * FST); }
            if constexpr ((q == 24 || q == 25 || q == 29 || q == 34) && s + 2 - CL < 8) {
                constexpr int pc = q == 24 ? 0 : q == 25 ? 1 : q == 29 ? 2 : 3, u = s + 2 - CL;
                dma4<(u & 1) * 4096, pc>(cst, vc[pc], rsc, c_off(lw, u));
            }
            sched();
        });
        if constexpr (PG2 >= 288) page<28>();
        TL(18);
        TS(3);
        {
            const __amdgpu_buffer_rsrc_t rz = mk_brs(ws + WS_ZT, 4 << 20);
            if constexpr (ZM) {
                asm volatile("s_nop 7");
#pragma unroll
                for (int jj = 0; jj < 4; ++jj) {
                    tie(acc[2 * jj]); tie(acc[2 * jj + 1]);
                    const f32x4 a = acc[2 * jj], c = acc[2 * jj + 1];
                    st16(u32x4{pack2(a[0], a[1]), pack2(a[2], a[3]), pack2(c[0], c[1]), pack2(c[2], c[3])}, rz, lane * 16 + jj * 1024, (ch & 510) * 8192 + nx + (ch & 1) * 4096);
                }
            } else {
            u32x2 kvr[8];
            const unsigned kva = lbase + U0 + 2048 + 128 * l15 + 64 * (g4 >> 1) + ((lw ^ ((l15 >> 1) & 3)) << 4) + 8 * (1 - (g4 & 1));
            static_for<0, 8>([&](auto ic) { dsr64<CV(ic) * 2048>(kvr[CV(ic)], kva); });
            wait_l<0>();
            const float Dd = (ch & 1) ? bfhi(dw) : bflo(dw);
#pragma unroll
            for (int jj = 0; jj < 4; ++jj) {
                u32x4 o;
#pragma unroll
                for (int u = 0; u < 2; ++u) {
                    const int j = 2 * jj + u;
                    tie(kvr[j]); tie(acc[j]);
                    const float kv3 = bflo(kvr[j][0]), kv2 = bfhi(kvr[j][0]), kv1 = bflo(kvr[j][1]), kv0 = bfhi(kvr[j][1]);
                    o[2 * u] = pack2(rbf(acc[j][0]) + rbf(kv0 * Dd), rbf(acc[j][1]) + rbf(kv1 * Dd));
                    o[2 * u + 1] = pack2(rbf(acc[j][2]) + rbf(kv2 * Dd), rbf(acc[j][3]) + rbf(kv3 * Dd));
                }
                st16(o, rz, lane * 16 + jj * 1024, (ch & 510) * 8192 + nx + (ch & 1) * 4096);
                if (jj == 1) { sched(); hop<29>(); sched(); }
            }
            }
            TS(8);
            hop<30>();
            if constexpr (CL > 0) {
                wait_v<4>();
                static_for<32 - 4 * CL, 32>([&](auto ic) { constexpr int i = CV(ic); dsr128<((i >> 2) & 1) * 4096>(cf[i], cad[i & 3]); });
                wait_l<0>();
                static_for<32 - 4 * CL, 32>([&](auto ic) { tie(cf[CV(ic)]); });
            }
        }
        TL(19);
        TS(4);
    } else {
        f32x4 acc[8][2];
        cf32 L, cc, M, M2, MG, NG, N4, N8, ag;
        {
            unsigned aad[4], bad[4];
#pragma unroll
            for (int i = 0; i < 4; ++i) { aad[i] = lbase + frag(i & 1, i >> 1); bad[i] = aad[i] + 16384 + wid * 4096; }
            const unsigned fr = lbase + FULL + (lane & 3) * FST;
            sld(tag, ws + WS_F + F_EP);
            bf16x8 fa[4][2], fb[2][4];
            unsigned fl = 1, dz = 0, dw_ = lbase + DONE + wid * FST + lane * 4, vt = lbase + POLL + wid * 256 + lane * 4;
            tie(dz); tie(dw_); tie(vt);
            auto op = [&](auto gc) {
                constexpr int G = CV(gc);
                constexpr Op o = g1_op(G);
                if constexpr (o.kind == OP_A) {
                    constexpr int bo = g1_buf(o.a >> 3), hi = bo >= 65536 ? 65536 : 0, i = o.a & 7;
                    dsr128<bo - hi + (i >> 1) * 4096>(fa[i & 3][o.b], aad[(i & 1) + 2 * o.b] + hi);
                }
                if constexpr (o.kind == OP_B) { constexpr int bo = g1_buf(o.a), hi = bo >= 65536 ? 65536 : 0; dsr128<bo - hi>(fb[o.a & 1][o.b], bad[o.b] + hi); }
                if constexpr (o.kind == OP_FLAG) dsr32<(30 - (G >> 5)) * 4>(fl, fr);
                if constexpr (o.kind == OP_SIG) dsw32<o.a * 4>(dw_, dz);
                if constexpr (o.kind == OP_VOTE) dsr32<0>(vt, vt);
            };
            auto consts = [&]() {
                float ld, ar, ai;
                asm volatile("s_waitcnt vmcnt(9)\n\tv_lshlrev_b32 %0, 16, %3\n\tv_lshlrev_b32 %1, 16, %4\n\tv_lshlrev_b32 %2, 16, %5"
                             : "=&v"(ld), "=&v"(ar), "=&v"(ai) : "v"(c_dt), "v"(c_re), "v"(c_im) : "memory");
                const float dtv = rbf(expf(ld)), ea = expf(dtv * ar);
                float sn, cs;
                sincosf(dtv * ai, &sn, &cs);
                L = cf32{ea * cs, ea * sn};
                M = L;
#pragma unroll
                for (int e = 0; e < 5; ++e) M = cmul(M, M);
                M2 = cmul(M, M);
                const cf32 N = cmul(M2, M2), N2 = cmul(N, N), one = {1.f, 0.f};
                N4 = cmul(N2, N2); N8 = cmul(N4, N4);
                auto sel = [](bool c, cf32 a, cf32 b) { return cf32{c ? a.r : b.r, c ? a.i : b.i}; };
                MG = cmul(sel(g4 & 1, M, one), sel(g4 & 2, M2, one));
                NG = cmul(sel(g4 & 1, N, one), sel(g4 & 2, N2, one));
                const float nm = L.r - 1.f, den = ar * ar + ai * ai;
                cc = cf32{2.f * ((nm * ar + L.i * ai) / den), 2.f * ((L.i * ar - nm * ai) / den)};
                auto tc = [&](cf32& v) { tie(v.r); tie(v.i); };
                tc(L); tc(cc); tc(M); tc(M2); tc(MG); tc(NG); tc(N4); tc(N8);
                sched();
            };
            if constexpr (CQ < 0) consts();
            __builtin_amdgcn_s_setprio(CP);
            zero_chk<0>(fl, lbase + POLL + (lane & 3) * 256);
            ties(tag);
            tag += 0x01010101u;
            TS(1);
            static_for<-12, 0>(op);
            static_for<0, 512>([&](auto gc) {
                constexpr int G = CV(gc), st = G >> 5, i = (G >> 2) & 7, j = G & 3;
                if constexpr ((G == 0 || j == 2) && G <= 506) { static_assert(g1_wait(G) >= 0, ""); wait_lr<G == 506 ? 0 : g1_wait(G)>(); }
                if constexpr (G == CQ) { asm volatile("s_waitcnt lgkmcnt(0)\n\ts_nop 7" ::: "memory"); consts(); }
                if constexpr (G == HQ) hop_all(wid);
                if constexpr ((G & 31) == 20 && st < 15) zero_chk<(30 - st) * 4>(fl, fr);
                if constexpr (((G & 31) == 30 && st < 13) || (NB < 2 && G == B13)) barrier();
                if constexpr ((G & 31) == 20) TL(st);
                mfma<st == 0 && j < 2>(acc[i][j & 1], fa[i & 3][j >> 1], fb[st & 1][j]);
                sched();
                op(gc);
                if constexpr (G == GCR + 16) { unsigned n; nr = vote_cnt(vt, tag, (xcd & 4) << 11, n); }
                if constexpr (G == 31) page<-1>();
                if constexpr (G == 95) hop<2>();
                if constexpr (G == 159) hop<4>();
                if constexpr (G == 207) hop<5>();
                if constexpr (G == 255) page<7>();
                if constexpr (G == 319) hop<8>();
                if constexpr (G == 383) hop<10>();
                if constexpr (G == 431) hop<11>();
                if constexpr (G == 479) page<13>();
                if constexpr (G == 509) hop<15>();
                if constexpr (G == 489) hop<40>();
                if constexpr (G == 494) hop<41>();
                if constexpr (G == 499) hop<42>();
                if constexpr (G == 504) hop<43>();
                if constexpr (G >= 487) {
                    constexpr int k = G - 486;
                    if constexpr (k == 1) { asm volatile("s_nop 3"); ag = cf32{acc[0][0][0], acc[0][1][0]}; }
                    cf32 t;
                    asm volatile("v_fma_f32 %0, -%3, %5, %6\n\tv_fma_f32 %1, %3, %4, %7\n\tv_fmac_f32 %0, %2, %4\n\tv_fmac_f32 %1, %2, %5"
                                 : "=&v"(t.r), "=&v"(t.i) : "v"(L.r), "v"(L.i), "v"(ag.r), "v"(ag.i), "v"(acc[k >> 2][0][k & 3]), "v"(acc[k >> 2][1][k & 3]));
                    ag = t;
                }
                sched();
            });
        }
        __builtin_amdgcn_s_setprio(3);
        TL(16);
        TS(2);
#pragma unroll
        for (int k = 26; k < 32; ++k) ag = cfma(L, ag, cf32{acc[k >> 2][0][k & 3], acc[k >> 2][1][k & 3]});
        tie(ag.r); tie(ag.i); sched(); hop<44>(); sched();
        cf32 ev_, od, p01, p23;
        sw16(ag.r, ev_.r, od.r); sw16(ag.i, ev_.i, od.i);
        const cf32 pp = cfma(M, ev_, od);
        sw32(pp.r, p01.r, p23.r); sw32(pp.i, p01.i, p23.i);
        cf32 E = cfma(M2, p01, p23);

        const __amdgpu_buffer_rsrc_t rse = mk_brs(ws + WS_E + tn * 16384 + wid * 2048, 16384);
        st16(u32x4{f2u(E.r), f2u(E.i), tag, ~tag}, rse, g4 ? OOB : l15 * 16, nr + (tm & 7) * 256);
        sched();
        TL(17);
        TS(3);
        hop<45>();
        sched();
        c_setup();
#pragma unroll
        for (int i = 0; i < 4; ++i) cad[i] = lb + frag(i & 1, i >> 1);
        auto c_read = [&](auto rc) { static_for<0, 8>([&](auto ic) { constexpr int r = CV(rc), i = CV(ic); dsr128<(i >> 2) * 4096>(cf[r * 8 + i], cad[i & 3] + ((r & 1) ? 0 : 2 * BUF)); }); };
        auto c_tie = [&](auto rc) { static_for<0, 8>([&](auto ic) { tie(cf[CV(rc) * 8 + CV(ic)]); }); };
        auto c_unit = [&](auto uc) { static_for<0, 4>([&](auto ic) { constexpr int u = CV(uc), i = CV(ic); dma4<((u & 2) ? 0 : 2 * BUF) + (u & 1) * 4096, i>(lb, vc[i], rsc, c_off(wid, u)); }); };
        auto c_req = [&](auto rc) { c_unit(IC(CV(rc) * 2)); c_unit(IC(CV(rc) * 2 + 1)); };
        const float c2r = cc.r, c2i = cc.i;
        auto scale = [&](int i) {
#pragma unroll
            for (int j = 0; j < 4; ++j) {
                const float br = acc[i][0][j], bi = acc[i][1][j];
                acc[i][0][j] = fma_(c2r, br, -(c2i * bi)); acc[i][1][j] = fma_(-c2r, bi, -(c2i * br));
            }
            tie(acc[i][0]); tie(acc[i][1]);
        };
        auto scales = [&](auto ac, auto bc) {
            static_for<CV(ac), CV(bc)>([&](auto ic) {
                constexpr int i = CV(ic);
                scale(i);
                if constexpr (i & 1) { sched(); hop<i == 1 ? 46 : i == 3 ? 16 : i == 5 ? 47 : 48>(); sched(); }
            });
        };
        constexpr int CA = CVR == 0 ? 0 : CVR == 4 ? 1 : 2;
        if constexpr (NB) {
            unsigned dn = 1;
            scales(IC(0), IC(DP));
            if constexpr (CA) { zero_chk<8>(dn, lbase + DONE + (lane & 3) * FST); static_for<0, CA>(c_unit); }
            if constexpr (SPR != 3) __builtin_amdgcn_s_setprio(SPR);
            sched();
            scales(IC(DP), IC(ELP));
            if constexpr (!CA) zero_chk<8>(dn, lbase + DONE + (lane & 3) * FST);
        } else {
            scales(IC(0), IC(ELP));
            barrier();
            static_for<0, CA>(c_unit);
        }
        TL(18);
        u32x4 ev[4]; unsigned eo[4]; unsigned long long none[4];
#pragma unroll
        for (int i = 0; i < 4; ++i) {
            const int t = tm - 1 - 4 * i - g4;
            eo[i] = t < 0 ? OOB : ((((t ^ tm) & 8) << 10) ^ nr) + (t & 7) * 256 + l15 * 16; none[i] = __builtin_amdgcn_ballot_w64(t < 0);
            asm volatile("buffer_load_dwordx4 %0, %1, %2, 0 offen sc1" : "=v"(ev[i]) : "v"(eo[i]), "s"(mk_rsrc(ws + WS_E + tn * 16384 + wid * 2048, 16384)) : "memory");
        }
        asm volatile("" ::: "memory");
        sched();
        constexpr int CB = CVR == 0 ? 1 : CVR == 4 ? 2 : 4;
        static_for<CA, CB>(c_unit);
        scales(IC(ELP), IC(8));
        cf32 X = cfma(cf32{g4 == 3 ? M.r : g4 == 2 ? 1.f : 0.f, g4 == 3 ? M.i : 0.f}, p01, cf32{(g4 & 1) ? ev_.r : 0.f, (g4 & 1) ? ev_.i : 0.f});
        tie(X.r); tie(X.i);
        sched();
        hop<24>();
        if constexpr (SPR != 3) __builtin_amdgcn_s_setprio(3);
        if constexpr (CVR == 2) { wait_v<12>(); c_read(IC(0)); wait_l<0>(); c_tie(IC(0)); c_req(IC(2)); }
        wait_v<4 * (CB - CA) + 8 * (CVR == 2)>();
        TL(19);
        tie(ev[0]); tie(ev[1]); tie(ev[2]); tie(ev[3]);
        {
            const unsigned long long tag64 = (unsigned long long)tag | ((unsigned long long)~tag << 32);
#pragma unroll 1
            for (unsigned spins = 0; spins < SPIN_CAP; ++spins) {
                unsigned long long ok = ~0ull;
#pragma unroll
                for (int i = 0; i < 4; ++i) ok &= __builtin_amdgcn_ballot_w64(((unsigned long long)ev[i][2] | ((unsigned long long)ev[i][3] << 32)) == tag64) | none[i];
                TSV(11, spins);
                if (ok == ~0ull) break;
                asm volatile("buffer_load_dwordx4 %0, %4, %8, 0 offen sc1\n\tbuffer_load_dwordx4 %1, %5, %8, 0 offen sc1\n\tbuffer_load_dwordx4 %2, %6, %8, 0 offen sc1\n\t"
                             "buffer_load_dwordx4 %3, %7, %8, 0 offen sc1\n\ts_waitcnt vmcnt(0)" : "=&v"(ev[0]), "=&v"(ev[1]), "=&v"(ev[2]), "=&v"(ev[3])
                             : "v"(eo[0]), "v"(eo[1]), "v"(eo[2]), "v"(eo[3]), "s"(mk_rsrc(ws + WS_E + tn * 16384 + wid * 2048, 16384)) : "memory");
            }
        }
        page<18>();
        cf32 h;
        {
            auto e = [&](int i) { return cf32{u2f(ev[i][0]), u2f(ev[i][1])}; };
            cf32 c = cmul(NG, cfma(N8, cfma(N4, e(3), e(2)), cfma(N4, e(1), e(0)))), u, v;
            sw16(c.r, u.r, v.r); sw16(c.i, u.i, v.i);
            sw32(u.r + v.r, u.r, v.r); sw32(u.i + v.i, u.i, v.i);
            c = cfma(MG, cf32{u.r + v.r, u.i + v.i}, X);
            h = cf32{fma_(c2r, c.r, -(c2i * c.i)), fma_(-c2r, c.i, -(c2i * c.r))};
        }
        TS(4);
        {
            const __amdgpu_buffer_rsrc_t rh = mk_brs(ws + WS_H + (size_t)((tm & 7) * 16 + tn) * 65536 + (nr ^ ((tm & 2) << 12)) + wid * 2048, 65536);
            const unsigned ho = g4 * 256 + ((l15 ^ (4 * (g4 & 1))) << 4);
            const cf32 Lc = {L.r, -L.i};
            static_for<0, 4>([&](auto qc) {
                constexpr int q = CV(qc);
                if constexpr (q >= CB) c_unit(qc);
                if constexpr (q == 2) hop<20>();
                u32x4 wr, wi;
#pragma unroll
                for (int u = 0; u < 4; ++u) {
                    const int i = 2 * q + (u >> 1), j = 2 * (u & 1);
                    const cf32 h0 = cfma(Lc, h, cf32{acc[i][0][j], acc[i][1][j]});
                    h = cfma(Lc, h0, cf32{acc[i][0][j + 1], acc[i][1][j + 1]});
                    wr[u] = pack2(h0.r, h.r); wi[u] = pack2(h0.i, h.i);
                }
                if constexpr ((PRE == 2 || GQ >= 0) && q == GE) { tie(wr); tie(wi); dsw32<4>(lbase + DONE + wid * FST + lane * 4, 0u); }
                __builtin_amdgcn_raw_buffer_store_b128(wr, rh, ho, q * 16384, 16);
                st16(wi, rh, ho + 1024, q * 16384); keep(wr);
                sched();
            });
        }
        TS(10);
        auto flag = [&]() { __builtin_amdgcn_raw_buffer_store_b64(u32x2{tag, ~tag}, rf, lane ? OOB : 0u, F_H + nr + (((tm & 7) * 16 + tn) * 4 + wid) * 8, 16); TS(5); };
        hop<22>();
        if constexpr (CVR == 2) {
            wait_v<16>();
            c_read(IC(1));
            wait_l<0>();
            c_req(IC(3));
            wait_v<8>();
            flag();
        } else {
            if constexpr (CVR == 1) wait_v<8>(); else { wait_v<0>(); flag(); }
            c_read(IC(0)); c_read(IC(1));
            wait_l<8>();
            c_req(IC(2));
            wait_l<0>();
            c_req(IC(3));
            if constexpr (CVR == 1) { wait_v<16>(); flag(); }
        }
        c_tie(IC(0)); c_tie(IC(1));
        static_for<16, 32>([&](auto ic) { undef(cf[CV(ic)]); });
    }
    {
        unsigned ln = lane, dsw_, pf;
        tie(ln);
        const int wid = fresh(wid_), lw = wid & 3, kg = wid >> 2, b = fresh(b_), xcd = b & 7, idx = b >> 3, tq = (idx & 7) + JR(idx >> 3);
        nr = fresh(__builtin_amdgcn_readfirstlane(nr));
        const unsigned nx = nr ^ ((xcd & 2) << 12), nh = nr ^ ((idx & 2) << 12), hp = (idx & 7) * 16 + (b & 1) * 8;
        const int tm2 = (xcd & 4) * 2 + (idx & 7), dt = (xcd & 2) * 2 + (idx >> 3), qx = ((~b & 1) * 2) ^ JQ(idx >> 3);
        const int ta = 512 * (idx >> 3) + 128 * (idx & 1) + 16 * wid + 4 * ((idx >> 1) & 3);
        const int tb = tm2 * 128 + 64 * kg;
        dsr32<DSK - 131072>(dsw_, lbase + 131072 + lw * 64 + (ln & 7) * 8 + ((ln >> 1) & 4));
        unsigned wl = lbase + wid * 4096, w2k = lbase + wid * 2048, hs0 = (hp + ((tq + (wid >> 1)) & 7)) * 65536 + nh + (wid & 1) * 4096 + qx * 16384;
        unsigned s_x = (tb + qx * 8) * 2048 + dt * 256 + lw * 64, s_q = ta * 2048 + xcd * 256, s_z = WS_ZT + (xcd & 3) * 1048576 + nx + idx * 128 + wid * 131072, zfo = WS_F + F_Z + (xcd & 2) * 8192 + nr + (xcd & 1) * 4096;
        unsigned ppo = PK ? WS_KVT + (xcd & 3) * 1048576 + idx * 32768 + kg * 16384 + (KVS ? nx : ((xcd ^ wid) & 4) << 11) + lw * 2048
                          : WS_PP + (xcd & 2) * 1048576 + idx * 65536 + (xcd & 1) * 32768 + kg * 16384 + nx + lw * 2048, s_a = ta * 8192 + xcd * 512, s_b = (tb + (2 ^ qx) * 8) * 8192 + 4096 + dt * 512 + lw * 128;
        const unsigned tagg = fresh(__builtin_amdgcn_readfirstlane(tag));
        u32x4 rsx = mk_rsrc(x, (NOX & 1) ? 0 : 4 << 20), rsq = mk_rsrc(qin, (NOX & 2) ? 0 : 4 << 20), rso = mk_rsrc(out, 16 << 20), rws = mk_rsrc(ws, 32 << 20);
        ties(wl); ties(w2k); ties(hs0); ties(s_x); ties(s_q); ties(s_z); ties(zfo); ties(ppo); ties(s_a); ties(s_b);
        ties(rsx); ties(rsq); ties(rso); ties(rws);
        const unsigned l31 = ln & 31, l5 = ln >> 5;
        unsigned l16 = ln * 16, ha[8];
        ha[0] = (lbase + kg * 1536 + l5 * 2048 + (ln & 2) * 128 + (ln & 1) * 8) | ((((ln >> 1) & 8) | ((ln >> 2) & 3) | ((ln & 2) << 1)) << 4);
        ha[1] = ha[0] ^ 64; ha[2] = ha[0] ^ 512; ha[3] = ha[0] ^ 576;
        static_for<0, 4>([&](auto ic) { ha[4 + CV(ic)] = ha[CV(ic)] + 65536; tie(ha[CV(ic)]); tie(ha[4 + CV(ic)]); });
        unsigned a_x = ((((ln >> 2) & 7) | (ln & 32)) << 11) | (l16 & 48), a_xr = (((l16 & 240) << 2) | (ln & 48)) + (w2k + ZST);
        unsigned a_q = ((ln & 48) << 7) | (l16 & 240), a_qr = (l5 << 10) + l31 * 8 + (w2k + QST), a_iw = w2k + IMG + l16, a_ir = a_iw ^ 8192;
        unsigned a_t = wid == 0 && ln == 0 ? idx * 128 : OOB, msk = 0xfff0fff0u;
        unsigned a_f = wid == 1 ? (ln >> 1) * 128 + (ln & 1) * 4 : OOB, expect = wid == 1 ? ((ln & 1) ? ~tagg : tagg) : 0u;
        unsigned a_z = (((ln ^ l5 ^ (wid * 4)) & 7) << 4) | ((ln & 48) << 10) | ((ln & 8) << 9), a_z2 = a_z ^ 32, a_zr = (l31 << 9) + (((((wid ^ ln) & 7) << 1) | l5) << 3) + (lbase + ZST);
        unsigned a_oa = (l5 << 21) | (l31 << 4), a_ob = ((((ln >> 2) & 4) | (ln & 32) | ((ln >> 3) & 1)) << 13) | (l16 & 112);
        unsigned a_p = PB ? (ln == 63 ? 1020u : OOB) : l16, a_w = ln < 16 ? ln * 128 : OOB;
        if constexpr (PB) tie(a_p);
        if constexpr (P_W >= -1) tie(a_w);
        if constexpr (P_W == -1) pf_ld(pf, a_w, rws, ppo);
        u32x2 tg2 = {tagg, ~tagg};
        tie(l16); tie(a_x); tie(a_xr); tie(a_q); tie(a_qr); tie(a_iw); tie(a_ir); tie(a_t); tie(msk); tie(a_f); tie(expect); tie(a_z); tie(a_z2); tie(a_zr);
        tie(a_oa); tie(a_ob); tie(tg2);
        wait_l<0>();
        tie(dsw_);
        u32x4 dg[2];
        static_for<0, 4>([&](auto ic) { constexpr unsigned long long k = diag_mask(CV(ic)); dg[0][CV(ic)] = sel0(dsw_ & 0xffffu, k); dg[1][CV(ic)] = sel0(dsw_ & 0xffff0000u, k); });
        tie(dg[0]); tie(dg[1]);
        unsigned fso, xso, ppi = ppo ^ (PK ? 1048576 : 32768), tn_[3] = {tagg & 0xf000fu, (tagg >> 8) & 0xf000fu, (tagg >> 4) & 0xf000fu}, psel = 0x05040100u;
        unsigned long long lo8 = 0x00ff00ff00ff00ffull, hi8 = ~lo8;
        ties(ppi); ties(tn_[0]); ties(tn_[1]); ties(tn_[2]); ties(psel); ties(lo8); ties(hi8);
        auto fill = [&](auto sc, auto pc) {
            constexpr int s = CV(sc), p = CV(pc);
            if constexpr (p == 0) dma_x<(2 * s % 3) * BUF, (s >> 1) * 16384 + (s & 1) * 262144, true>(fso, wl, l16, rws, hs0);
            else if constexpr (fill_m0(4 * s + p)) dma_m<(2 * s % 3) * BUF, p * 1024>(fso, wl, l16, rws);
            else dma_n<p * 1024>(fso, l16, rws);
        };
        bf16x4 hf[2][8];
        auto ldh = [&](auto nc, auto ic) {
            constexpr int n = CV(nc), i = CV(ic), j = (i >> 1) & 1, bo = (2 * (n >> 2) % 3) * BUF + (g2_unit(n, j) & 3) * 8192 + (i >> 2) * 4096, hi = bo >= 65536 ? 65536 : 0;
            dsr64tr<bo - hi>(hf[n & 1][i], ha[2 * g2_tile(n, j) + (i & 1) + (hi >> 14)]);
        };
        f32x4 yc[2][2][2];
        f32x4 im[2], ao, ob[2]; u32x4 pl[2], po;
        bf16x8 xf[2];
        u32x2 zv[4], qv[2];
        unsigned ck, ex, fail[2];
        float tq_[2], tz, sm[8], mx[2], cv[8];
        auto finB = [&](auto pc, auto jc) {
            constexpr int p = CV(pc), j = CV(jc);
            if constexpr (j < 8) { yc[p & 1][0][j & 1][j >> 1] += im[j & 1][j >> 1]; tie(yc[p & 1][0][j & 1]); }
            else {
                constexpr int i = (j - 8) >> 2, k = j & 3, dd = k & 1, h = k >> 1;
                ob[i][k] = sel8(yc[p & 1][0][dd][2 * i + 1 - h], yc[p & 1][0][dd][2 * i + h], h ? hi8 : lo8);
                if constexpr (k == 3 && (p == 3 || !(NOST & 4))) gst<0>(ob[i], a_ob, rso, s_b ^ ((p & 1) * 65536 + i * 16384));
                if constexpr (j == 12) keep(ob[0]);
            }
        };
        auto snd = [&](auto pc, auto jc) {
            constexpr int p = CV(pc), j = CV(jc);
            if constexpr (j < 8) { sm[j] = yc[p][0][j >> 2][j & 3] + im[j >> 2][j & 3]; tie(sm[j]); }
            if constexpr (j == 8) mx[0] = amax3(sm[0], sm[1], sm[2]);
            if constexpr (j == 9) mx[1] = amax3(sm[3], sm[4], sm[5]);
            if constexpr (j == 10) mx[0] = amax3(mx[0], sm[6], sm[7]);
            if constexpr (j == 11) mx[0] = amax3(mx[0], mx[1], mx[1]);
            if constexpr (j == 12) { ex = f2u(mx[0]) + 0x4000u; tie(ex); }
            if constexpr (j == 13) { ex &= 0x7f800000u; tie(ex); }
            if constexpr (j == 14) { mx[0] = u2f(ex + 0x04c00008u); tie(mx[0]); }
            if constexpr (j == 15) { ex >>= 23; tie(ex); }
            if constexpr (j >= 16 && j < 24) { sm[j - 16] += mx[0]; tie(sm[j - 16]); }
            if constexpr (j == 24) { ex |= ex << 12; tie(ex); }
            if constexpr (j >= 25 && j < 29) { po[j - 25] = __builtin_amdgcn_perm(f2u(sm[2 * (j - 25) + 1]), f2u(sm[2 * (j - 25)]), psel); tie(po); }
            if constexpr (j == 29) { po[0] = bfi_v(msk, po[0], ex); tie(po); }
            if constexpr (j >= 30 && j < NS) { po[j - 29] = bfi_s(msk, po[j - 29], tn_[j - 30]); if constexpr (j < NS - 1) tie(po); }
            if constexpr (j == NS - 1 && !(NOST & 2)) gst<p * 1024>(po, l16, rws, ppo);
        };
        auto rcv = [&](auto hc, auto jc) {
            constexpr int h = CV(hc), j = CV(jc);
            if constexpr (j < 3) { pl[h][j + 1] ^= tn_[j]; pp_tie<h>(pl[h]); }
            if constexpr (j == 3) { ck = pl[h][1] | pl[h][2] | pl[h][3]; tie(ck); }
            if constexpr (j == 4) { if constexpr (PB) ck = bfi_f(msk, fail[h], ck); else ck &= 0xf000fu; tie(ck); }
            if constexpr (j == 6 && !(NOST & 2)) pp_chk<h>(pl[h], ck, tagg, l16, rws, ppi, a_p);
            if constexpr (j == 7) { ex = pl[h][0] >> 12; tie(ex); }
            if constexpr (j == 8) ex = bfi_v(msk, ex, pl[h][0]);
            if constexpr (j == 9) { pl[h][0] &= msk; pp_tie<h>(pl[h]); }
            if constexpr (j == 10) { ex <<= 23; tie(ex); }
            if constexpr (j == 11) { ex &= 0x7f800000u; tie(ex); }
            if constexpr (j == 12) { mx[1] = u2f(ex) * 0x1p-14f; tie(mx[1]); }
            if constexpr (h == 0 && j >= 13) {
                constexpr int k = j - 13, a = (k + 1) / 2, e = k == 15 ? 7 : k / 2 - 1;
                if constexpr (k == 0 || ((k & 1) && k < 15)) cv[a] = cvt16<a & 1>(pl[h][a >> 1]);
                else { yc[0][0][e >> 2][e & 3] = fma_(cv[e], mx[1], yc[0][0][e >> 2][e & 3]); tie(yc[0][0][e >> 2]); }
            }
            if constexpr (h == 1 && j >= 13 && j < 21) cv[j - 13] = cvt16<(j - 13) & 1>(pl[h][(j - 13) >> 1]);
            if constexpr (h == 1 && j >= 21) { constexpr int e = j - 21; im[e >> 2][e & 3] = fma_(cv[e], mx[1], im[e >> 2][e & 3]); tie(im[e >> 2]); }
        };
        auto side = [&](auto pc) {
            constexpr int P = CV(pc), n = P >> 3, m = P & 7, p = n >> 3;
            if constexpr (P == P_X || P == P_X + 2) dma_x<ZST + (P != P_X) * 1024, (P != P_X) * 16384, false>(xso, w2k, a_x, rsx, s_x);
            if constexpr (P == P_W) pf_ld(pf, a_w, rws, ppo);
            if constexpr (!NOA) {
            if constexpr (P == P_Q || P == P_Q + 2) dma_x<QST + (P != P_Q) * 1024, (P != P_Q) * 524288, false>(xso, w2k, a_q, rsq, s_q);
            if constexpr (P == P_T) gst<0>(tg2, a_t, rws, zfo);
            if constexpr (P == AT(ZP, 4)) { if constexpr (P_W >= 0) pf_keep(pf); pf_ld(pf, a_f, rws, zfo); }
            if constexpr (P == P_ZC) { wait_v<g2_allow(P, AT(ZP, 4))>(); tag_chk(pf, expect, a_f, rws, zfo); }
            if constexpr (P == P_Z || P == P_Z + 2) dma_x<ZST + (P != P_Z) * 1024, (P != P_Z) * 65536, true>(xso, w2k, P == P_Z ? a_z : a_z2, rws, s_z);
            }
            if constexpr (P == AT(7, 5) || P == AT(7, 7)) dsr128<(m - 5) * 512>(xf[(m - 5) / 2], a_xr);
            if constexpr (P == AT(8, 4)) { tie(xf[0]); tie(xf[1]); }
            if constexpr (P == AT(8, 5) || P == AT(9, 3)) static_for<0, 2>([&](auto dc) { mfma<false>(yc[n & 1][0][CV(dc)], xf[n & 1], __builtin_bit_cast(bf16x8, dg[CV(dc)])); });
            if constexpr ((n & 7) == 6 && m == XW && !(DBG & 2)) dsw128<0>(a_iw, yc[p & 1][1][0]);
            if constexpr ((n & 7) == 6 && m == XW2 && !(DBG & 2)) dsw128<1024>(a_iw, yc[p & 1][1][1]);
            if constexpr ((n & 7) == 7 && (m & 5) == (n < 31 ? 4 : 0)) dsr128<(m & 2) * 512>(im[(m >> 1) & 1], a_ir);
            if constexpr (n >= 8 && (n & 7) == 0 && m == 4) { tie(im[0]); tie(im[1]); }
            if constexpr (P >= P_PP0 && P <= E_PP0) snd(IC(0), IC(P - P_PP0));
            if constexpr (P >= P_PP1 && P <= E_PP1) static_for<0, D1>([&](auto ic) { snd(IC(1), IC((P - P_PP1) * D1 + CV(ic))); });
            if constexpr (PB && (P == P_B0 - 1 || P == P_B1 - 1)) pf_zero(pf);
            if constexpr (PB && (P == P_B0 || P == P_B1)) pf_at<(P == P_B1) * 1024>(pf, a_p, rws, ppi);
            if constexpr (P == P_L0 || P == P_L1) {
                unsigned so = ppi;
                if constexpr (PB) { wait_v<g2_allow(P, P - QB) + (g2_fill(P) >= 0)>(); pp_sel(so, fail[P == P_L1], pf, tn_[2], ppi, ppo); }
                pp_ld<P == P_L1>(pl[P == P_L1], l16, rws, so);
            }
            if constexpr (P == P_R0 || P == P_R1) { wait_v<g2_allow(P, P == P_R0 ? imax(P_L0, XL ? P_X + 2 : 0) : P_L1)>(); pp_tie<P == P_R1>(pl[P == P_R1]); }
            if constexpr (P >= P_R0 && P <= E_R0) static_for<0, D0>([&](auto ic) { if constexpr ((P - P_R0) * D0 + CV(ic) < NR) rcv(IC(0), IC((P - P_R0) * D0 + CV(ic))); });
            if constexpr (P >= P_R1 && P < P_R1 + 21) rcv(IC(1), IC(P - P_R1));
            if constexpr (!NOA) {
            if constexpr (n == ZW && m >= 4) dsr64<(m - 4) * 128>(zv[m - 4], a_zr);
            static_for<0, 4>([&](auto rc) {
                constexpr int r = CV(rc);
                if constexpr (P == q_tie(r)) tie(qv[r & 1]);
                if constexpr (P == q_rd(r)) dsr64<r * 256>(qv[r & 1], a_qr);
            });
            if constexpr (P == AT(ZW + 1, 4)) { tie(zv[0]); tie(zv[1]); tie(zv[2]); tie(zv[3]); }
            }
            if constexpr (a_step(P) >= 0) {
                constexpr int s = a_step(P), e = s == 0 ? 0 : s % 3 == 2 ? (s + 1) / 3 : s % 3 == 1 ? s / 3 : s / 3 - 1, r = e >> 2, c = e & 3;
                if constexpr (s == 0 || (s % 3 == 2 && e < 16)) { tq_[e & 1] = (c & 1) ? bfhi(qv[r & 1][c >> 1]) : bflo(qv[r & 1][c >> 1]); tie(tq_[e & 1]); }
                else if constexpr (s % 3 == 1) { tz = (r & 1) ? bfhi(zv[c][r >> 1]) : bflo(zv[c][r >> 1]); tie(tz); }
                else if constexpr (s % 3 == 0) {
                    ao[c] = tq_[e & 1] * tz;
                    if constexpr (c == 3 && !(NOST & 1)) gst<0>(ao, a_oa, rso, s_a + r * 8192);
                    else tie(ao);
                }
            }
            if constexpr (P >= P_F2 && P < P_F2 + 16) finB(IC(2), IC(P - P_F2));
            if constexpr (a_step(P - 1) > 0 && a_step(P - 1) % 12 == 0) keep(ao);
            if constexpr (P == E_PP0 + 1 || P == E_PP1 + 1) keep(po);
            if constexpr (P == P_F2 + 16) keep(ob[1]);
        };
        {
            unsigned e_h = wid == 3 ? ((ln & 1) ? ~tagg : tagg) : 0u, hsl = lbase + lw * 8192, hso = (hp + ((tq + wid) & 7)) * 65536 + nh + qx * 16384;
            unsigned a_h = wid == 3 ? ln * 4 : OOB, s_h = WS_F + F_H + nr + hp * 32, ca2[4], a_g = lbase + HGO + ln * 4;
            static_for<0, 4>([&](auto ic) { ca2[CV(ic)] = cad[CV(ic)] + 2 * BUF; tie(ca2[CV(ic)]); });
            tie(e_h); tie(a_h); tie(a_g); ties(hsl); ties(hso); ties(s_h);
            sched();
            if (wid < 4) {
                auto look = [&]() { if constexpr (HA) pf_at<0>(pf, a_h, rws, s_h); else pf_ld(pf, a_h, rws, s_h); };
                if constexpr (HA) pf_zero(pf);
                if constexpr (PW == 0) { look(); wait_v<1>(); }
                if constexpr (PW == 1) { wait_v<0>(); look(); }
                if constexpr (PW == 2) { wait_v<8>(); look(); wait_v<1>(); }
                static_for<16, 32>([&](auto ic) { constexpr int i = CV(ic); dsr128u<((i >> 2) & 1) * 4096>(cf[i], (i & 8) ? cad[i & 3] : ca2[i & 3]); });
                wait_l<0>();
                static_for<16, 32>([&](auto ic) { tie(cf[CV(ic)]); });
                wait_v<0>();
                tag_chk<1, HA>(pf, e_h, a_h, rws, s_h);
                if (wid == 3) flag_set(a_g, 1u); else gate<1>(a_g, 1);
                TS(6);
                hop<26>();
                static_for<0, 4>([&](auto ic) {
                    constexpr int i = CV(ic);
                    dma_x<(i >> 1) * 2 * BUF + (i & 1) * 4096, (i >> 1) * 262144 + (i & 1) * 4096, true>(fso, hsl, l16, rws, hso);
                    dma_n<1024>(fso, l16, rws); dma_n<2048>(fso, l16, rws); dma_n<3072>(fso, l16, rws);
                });
            }
        }
        wait_v<8>();
        if (wid < 4) TS(7);
        TG_DECL
        barrier();
        static_for<0, 8>([&](auto ic) { ldh(IC(0), ic); });
        static_for<0, 32>([&](auto nc) {
            constexpr int n = CV(nc), p = n >> 3;
            if constexpr (n == PG3) page<33>();
            if constexpr (n == PG4) page<37>();
            wait_lr<(n & 3) == 3 ? 0 : 4 + g2_lx(n - 1)>();
            if constexpr (n == 31) barrier();
            if constexpr ((n & 3) == 3 && n < 31) {
                constexpr int need = g2_need(n);
                TL(21 + n / 4 * 3);
                wait_v<g2_allow(8 * n, need)>();
                TL(22 + n / 4 * 3);
                barrier();
                TL(23 + n / 4 * 3);
            }
            if constexpr (n == 1) hop<31>();
            if constexpr (n == 4) hop<32>();
            if constexpr (n == 11) hop<34>();
            if constexpr (n == 15) hop<35>();
            if constexpr (n == 19) hop<36>();
            if constexpr (n == 27) hop<38>();
            if constexpr (n == 31) hop<39>();
            if constexpr (n == 0) TL(20);
            TG(20 + n);
            if constexpr (n == 31) TL(42);
            if constexpr (PRG) __builtin_amdgcn_s_setprio(n == 31 ? 0 : 3 - ((n + 1) & 3));
            sched();
            static_for<0, 8>([&](auto mc) {
                constexpr int m = CV(mc), ks = m >> 2, tt = (m >> 1) & 1, dd = m & 1, P = n * 8 + m;
                if constexpr (m == 4 && (n & 3) != 3) wait_lr<(rd1(n) ? 8 : 4) + ((n & 7) == 6 ? (XW < 4) + (XW2 < 4) : 0)>();
                mfma<(n & 7) == 0 && ks == 0>(yc[p & 1][(DBG & 1) ? tt : g2_tile(n, tt)][dd], cat(hf[n & 1][4 * ks + 2 * tt], hf[n & 1][4 * ks + 2 * tt + 1]), cf[g2_unit(n, tt) * 4 + 2 * ks + dd]);
                sched();
                if constexpr (n < 31 && rd1(n) && m < 4) { ldh(IC(n + 1), IC(2 * m)); ldh(IC(n + 1), IC(2 * m + 1)); }
                if constexpr (n < 31 && !rd1(n)) ldh(IC(n + 1), mc);
                if constexpr (g2_fill(P) >= 0) fill(IC(g2_fill(P) >> 2), IC(g2_fill(P) & 3));
                side(IC(P));
                sched();
            });
        });
        TG(52);
        if (wid >= 4) TS(5); else TS(8);
        TG(53);
        static_for<256 - P_R1, 21>([&](auto jc) { rcv(IC(1), jc); });
        wait_l<0>();
        tie(im[0]); tie(im[1]);
        static_for<21, NR>([&](auto jc) { rcv(IC(1), jc); });
        asm volatile("s_nop 3");
        sched();
        static_for<0, 16>([&](auto jc) { finB(IC(3), jc); });
        if (b == 255 && wid == 0) gst<0>(u32x2{tagg, tagg}, l16 ? OOB : 0u, rws, WS_F + F_EP);
    }
}

}

#if defined(__HIP_DEVICE_COMPILE__)
#pragma clang attribute pop
#endif

extern "C" void kernel_launch(void* const* d_in, const int* in_sizes, int n_in, void* d_out, int out_size, void* d_ws, size_t ws_size,
                              hipStream_t stream) {
    (void)in_sizes; (void)n_in; (void)out_size; (void)ws_size;
    using namespace fk;
    const uint16_t* q = (const uint16_t*)d_in[0];
    const uint16_t* kin = (const uint16_t*)d_in[1];
    const uint16_t* vin = (const uint16_t*)d_in[2];
    const uint16_t* sk = (const uint16_t*)d_in[3];
    const uint16_t* ssmk = (const uint16_t*)d_in[4];
    const uint16_t* ssmD = (const uint16_t*)d_in[5];
    const uint16_t* x = (const uint16_t*)d_in[6];
    const uint16_t* lre = (const uint16_t*)d_in[7];
    const uint16_t* lim = (const uint16_t*)d_in[8];
    const uint16_t* bre = (const uint16_t*)d_in[9];
    const uint16_t* bim = (const uint16_t*)d_in[10];
    const uint16_t* cre = (const uint16_t*)d_in[11];
    const uint16_t* cim = (const uint16_t*)d_in[12];
    const uint16_t* logdt = (const uint16_t*)d_in[13];
    const uint16_t* dskip = (const uint16_t*)d_in[14];
#define WOFF 0
    char* ws = (char*)(((uintptr_t)d_ws + ((size_t)WOFF << 20) + 0x1fffff) & ~(uintptr_t)0x1fffff);
    hipLaunchKernelGGL(k1, dim3(256), dim3(512), 0, stream, x, bre, bim, lre, lim, logdt, cre, cim, dskip, ssmk, ssmD, q, kin, vin, sk, ws, (float*)d_out TS_PASS(0));
}
```

```cpp
#define HQ 84
#define KL0 4
#include <hip/hip_runtime.h>
#include <stdint.h>
#include <type_traits>

#if defined(__HIP_DEVICE_COMPILE__)
#pragma clang attribute push(__attribute__((target("no-packed-fp32-ops"))), apply_to = function)
#endif

namespace fk {

typedef __attribute__((ext_vector_type(8))) short bf16x8;
typedef __attribute__((ext_vector_type(4))) short bf16x4;
typedef __attribute__((ext_vector_type(4))) float f32x4;
typedef __attribute__((ext_vector_type(4))) unsigned u32x4;
typedef __attribute__((ext_vector_type(2))) unsigned u32x2;
typedef __attribute__((ext_vector_type(2))) __bf16 bf16x2_t;
typedef __attribute__((address_space(1))) unsigned gu32;
typedef __attribute__((address_space(1))) unsigned long long gu64;
#define RLX_AGENT __ATOMIC_RELAXED, __HIP_MEMORY_SCOPE_AGENT

__device__ __forceinline__ float u2f(unsigned v) { return __builtin_bit_cast(float, v); }
__device__ __forceinline__ unsigned f2u(float v) { return __builtin_bit_cast(unsigned, v); }
__device__ __forceinline__ bool wall(bool p) { return __builtin_amdgcn_ballot_w64(p) == ~0ull; }
__device__ __forceinline__ float bflo(unsigned w) { return u2f(w << 16); }
__device__ __forceinline__ float bfhi(unsigned w) { return u2f(w & 0xffff0000u); }
__device__ __forceinline__ unsigned pack2(float lo, float hi) {
    bf16x2_t v; v[0] = (__bf16)lo; v[1] = (__bf16)hi; return __builtin_bit_cast(unsigned, v);
}
__device__ __forceinline__ float rbf(float f) { float r; asm("v_cvt_pk_bf16_f32 %0, 0, %1" : "=v"(r) : "v"(f)); return r; }
__device__ __forceinline__ float fma_(float a, float b, float c) { return __builtin_fmaf(a, b, c); }

template <int I, int N, class F>
__device__ __forceinline__ void static_for(F&& f) {
    if constexpr (I + 1 == N) f(std::integral_constant<int, I>{});
    else if constexpr (I < N) { static_for<I, I + (N - I) / 2>(f); static_for<I + (N - I) / 2, N>(f); }
}
#define IC(v) std::integral_constant<int, (v)>{}
#define CV(c) decltype(c)::value

#define TS_ARG
#define TS_PASS(off)
#define TS_DECL
#define TS(i)
#define TSV(i, v)
#define TL_DECL
#define TL(i)
#define TS_FLUSH(slot)

#define TG_DECL
#define TG(i)
template <int N> __device__ __forceinline__ void wait_v() { asm volatile("s_waitcnt vmcnt(%0)" ::"n"(N > 63 ? 63 : N) : "memory"); }
template <int N> __device__ __forceinline__ void wait_l() { asm volatile("s_waitcnt lgkmcnt(%0)" ::"n"(N > 14 ? 14 : N) : "memory"); }
template <int N> __device__ __forceinline__ void wait_lr() { __builtin_amdgcn_s_waitcnt(0xc07f | ((N > 14 ? 14 : N) << 8)); }
template <class T> __device__ __forceinline__ void tie(T& x) { asm volatile("" : "+v"(x)); }
template <class T> __device__ __forceinline__ void undef(T& x) { asm volatile("" : "=v"(x)); }
template <int OFF, class T> __device__ __forceinline__ void dsr128u(T& d, unsigned a) { asm volatile("ds_read_b128 %0, %1 offset:%2" : "+v"(d) : "v"(a), "n"(OFF)); }
template <class T> __device__ __forceinline__ void keep(const T& x) { asm volatile("" :: "v"(x)); }
template <int OFF, class T> __device__ __forceinline__ void dsr128(T& d, unsigned a) { asm volatile("ds_read_b128 %0, %1 offset:%2" : "=v"(d) : "v"(a), "n"(OFF)); }
template <int OFF, class T> __device__ __forceinline__ void dsr64(T& d, unsigned a) { asm volatile("ds_read_b64 %0, %1 offset:%2" : "=v"(d) : "v"(a), "n"(OFF)); }
template <int OFF> __device__ __forceinline__ void dsr32(unsigned& d, unsigned a) { asm volatile("ds_read_b32 %0, %1 offset:%2" : "=v"(d) : "v"(a), "n"(OFF)); }
template <int O0, int O1, class T> __device__ __forceinline__ void dsr2(T& d, unsigned a) { asm volatile("ds_read2_b32 %0, %1 offset0:%2 offset1:%3" : "=v"(d) : "v"(a), "n"(O0), "n"(O1)); }
template <int OFF> __device__ __forceinline__ void dsr64tr(bf16x4& d, unsigned a) { asm volatile("ds_read_b64_tr_b16 %0, %1 offset:%2" : "=v"(d) : "v"(a), "n"(OFF)); }
template <int OFF, class T> __device__ __forceinline__ void dsw128(unsigned a, const T& v) { asm volatile("ds_write_b128 %0, %1 offset:%2" :: "v"(a), "v"(v), "n"(OFF) : "memory"); }
template <int OFF> __device__ __forceinline__ void dsw32(unsigned a, unsigned v) { asm volatile("ds_write_b32 %0, %1 offset:%2" :: "v"(a), "v"(v), "n"(OFF) : "memory"); }
__device__ __forceinline__ bf16x8 cat(const bf16x4& lo, const bf16x4& hi) { return __builtin_shufflevector(lo, hi, 0, 1, 2, 3, 4, 5, 6, 7); }
__device__ __forceinline__ void barrier() { asm volatile("" ::: "memory"); __builtin_amdgcn_s_barrier(); asm volatile("" ::: "memory"); }
__device__ __forceinline__ int fresh(int v) { asm volatile("" : "+s"(v)); return v; }
__device__ __forceinline__ void sched() { __builtin_amdgcn_sched_barrier(0); }
template <bool FIRST> __device__ __forceinline__ void mfma(f32x4& c, const bf16x8& a, const bf16x8& b) {
    if constexpr (FIRST) asm volatile("v_mfma_f32_16x16x32_bf16 %0, %1, %2, 0" : "=v"(c) : "v"(a), "v"(b));
    else asm volatile("v_mfma_f32_16x16x32_bf16 %0, %1, %2, %0" : "+v"(c) : "v"(a), "v"(b));
}
struct cf32 { float r, i; };
__device__ __forceinline__ cf32 cmul(cf32 a, cf32 b) { return cf32{fma_(a.r, b.r, -(a.i * b.i)), fma_(a.r, b.i, a.i * b.r)}; }
__device__ __forceinline__ cf32 cfma(cf32 a, cf32 b, cf32 c) { return cf32{fma_(a.r, b.r, fma_(-a.i, b.i, c.r)), fma_(a.r, b.i, fma_(a.i, b.r, c.i))}; }
__device__ __forceinline__ void sw16(float v, float& e, float& o) { const auto r = __builtin_amdgcn_permlane16_swap(f2u(v), f2u(v), false, false); e = u2f(r[0]); o = u2f(r[1]); }
__device__ __forceinline__ void sw32(float v, float& e, float& o) { const auto r = __builtin_amdgcn_permlane32_swap(f2u(v), f2u(v), false, false); e = u2f(r[0]); o = u2f(r[1]); }
constexpr unsigned SPIN_CAP = 1u << 16;
constexpr unsigned OOB = 0x40000000u;

constexpr size_t WS_H = 0;
constexpr size_t WS_KVT = (size_t)8 << 20;
constexpr size_t WS_ZT = (size_t)12 << 20;
constexpr size_t WS_PP = (size_t)16 << 20;
constexpr size_t WS_E = (size_t)24 << 20;
constexpr size_t WS_F = WS_E + (256 << 10);
constexpr int F_H = 0;
constexpr int F_K = 4096;
constexpr int F_Z = 16384;
constexpr int F_EP = F_Z + 32768;
constexpr int F_C = F_EP + 256;
constexpr unsigned WS_P = 25 << 20;
#define KCQ 2
#define KCC 4
#define KVP 0
#define RSL 1
#define PB 0
#define KTI 1
#define TM 0
#define TX 0
#define PK 1
#define HA 0
#define KVS 1
#define GCR 273
constexpr int LDS_TOTAL = 163840, BUF = 32768;
constexpr int KC = 98304, KCH = 8320, KCP = 4160;
constexpr int U0 = KC + 4 * KCH;
constexpr int FULL = U0 + 18432 + 8192, FST = 388;
constexpr int DONE = U0 + 18432, DUMP = DONE + 4096;
constexpr int HGO = FULL + 1792, DSK = HGO + 256, POLL = DSK + 256;
static_assert(POLL + 8 * 256 <= LDS_TOTAL && 4 * FST <= 1792, "lds");
constexpr int g1_buf(int s) { return ((0x0213 >> ((s & 3) * 4)) & 3) * BUF; }
constexpr int IMG = KC, QST = KC + 16384, ZST = U0, PLD = 2 * BUF;
#define CODE_KB 47
#define EXACT 0
#define HP 1
#define CP 2
#define FLQ 11
#define R01 3
#define ZP 7
#define SLP 8
#define STR_(x) #x
#define STR(x) STR_(x)
#define NOST 0
#define G2M 0
#define RD 2
#define PRG 1
#define OSC 16
#define NOA 0
#define PPH 0
#define HS 0
#define JQ(j) (HS == 0 ? 0 : HS == 1 ? (j) & 1 : HS == 2 ? (j) : (j) & 2)
#define JR(j) (HS == 0 ? 2 * (j) : HS == 1 ? 2 * ((j) & 2) : HS == 2 ? 0 : 4 * ((j) & 1))
#define NOX 0

__device__ __forceinline__ u32x4 mk_rsrc(const void* p, unsigned bytes) {
    const unsigned long long a = (unsigned long long)p;
    return u32x4{(unsigned)a, (unsigned)(a >> 32) & 0xffffu, bytes, 0x00020000u};
}
__device__ __forceinline__ __amdgpu_buffer_rsrc_t mk_brs(const void* p, unsigned bytes) { return __builtin_amdgcn_make_buffer_rsrc((void*)p, (short)0, bytes, 0x00020000); }
__device__ __forceinline__ void st16(const u32x4& d, __amdgpu_buffer_rsrc_t r, unsigned voff, unsigned soff) {
    __builtin_amdgcn_raw_buffer_store_b128(d, r, voff, soff, 16);
    asm volatile("s_nop 1" :: "v"(d) : "memory");
}
template <int IMM, bool SC1> __device__ __forceinline__ void dma(unsigned lds, unsigned voff, const u32x4& rs, unsigned soff) {
    if constexpr (SC1) asm volatile("s_add_u32 m0, %0, %1\n\ts_nop 0\n\tbuffer_load_dwordx4 %2, %3, %4 offen sc1 lds" :: "s"(lds), "n"(IMM), "v"(voff), "s"(rs), "s"(soff) : "memory", "scc");
    else asm volatile("s_add_u32 m0, %0, %1\n\ts_nop 0\n\tbuffer_load_dwordx4 %2, %3, %4 offen lds" :: "s"(lds), "n"(IMM), "v"(voff), "s"(rs), "s"(soff) : "memory", "scc");
}
template <int IMM, int I> __device__ __forceinline__ void dma4(unsigned lds, unsigned voff, const u32x4& rs, unsigned soff) {
    if constexpr (I == 0) dma<IMM, false>(lds, voff, rs, soff);
    else asm volatile("buffer_load_dwordx4 %0, %1, %2 offen offset:%3 lds" :: "v"(voff), "s"(rs), "s"(soff), "n"(I * 1024) : "memory");
}
template <int IMM> __device__ __forceinline__ void dma1(unsigned lds, unsigned voff, const u32x4& rs, unsigned soff) {
    asm volatile("s_add_u32 m0, %0, %1\n\ts_nop 0\n\tbuffer_load_dword %2, %3, %4 offen sc1 lds" :: "s"(lds), "n"(IMM), "v"(voff), "s"(rs), "s"(soff) : "memory", "scc");
}
__device__ __forceinline__ void flag_set(unsigned waddr, unsigned v) { asm volatile("ds_write_b32 %0, %1" :: "v"(waddr), "v"(v) : "memory"); }
__device__ __forceinline__ void flag_chk(unsigned& f, unsigned raddr, unsigned need) {
    unsigned cnt;
    asm volatile("v_cmp_le_u32 vcc, %3, %0\n\t"
                 "s_cmp_eq_u64 vcc, -1\n\t"
                 "s_cbranch_scc1 1f\n\t"
                 "s_mov_b32 %1, 0x100000\n"
                 "0:\n\t"
                 "ds_read_b32 %0, %2\n\t"
                 "s_sub_u32 %1, %1, 1\n\t"
                 "s_waitcnt lgkmcnt(0)\n\t"
                 "s_cbranch_scc1 1f\n\t"
                 "v_cmp_le_u32 vcc, %3, %0\n\t"
                 "s_cmp_eq_u64 vcc, -1\n\t"
                 "s_cbranch_scc0 0b\n"
                 "1:" : "+v"(f), "=&s"(cnt) : "v"(raddr), "s"(need) : "vcc", "scc", "memory");
}
template <int OFF, int SL = 0> __device__ __forceinline__ void zero_chk(unsigned& f, unsigned raddr) {
    unsigned cnt;
    asm volatile("v_cmp_ne_u32 vcc, 0, %0\n\t"
                 "s_cbranch_vccz 1f\n\t"
                 "s_mov_b32 %1, 0x100000\n"
                 "0:\n\t"
                 ".if %4\n\ts_sleep %4\n\t.endif\n\t"
                 "ds_read_b32 %0, %2 offset:%3\n\t"
                 "s_sub_u32 %1, %1, 1\n\t"
                 "s_waitcnt lgkmcnt(0)\n\t"
                 "s_cbranch_scc1 1f\n\t"
                 "v_cmp_ne_u32 vcc, 0, %0\n\t"
                 "s_cbranch_vccnz 0b\n"
                 "1:" : "+v"(f), "=&s"(cnt) : "v"(raddr), "n"(OFF), "n"(SL) : "vcc", "scc", "memory");
}
template <int IMM, class T> __device__ __forceinline__ void gld(T& d, unsigned voff, const u32x4& rs, unsigned soff) {
    if constexpr (sizeof(T) == 16) asm volatile("buffer_load_dwordx4 %0, %1, %2, %3 offen offset:%4" : "=v"(d) : "v"(voff), "s"(rs), "s"(soff), "n"(IMM) : "memory");
    else if constexpr (sizeof(T) == 8) asm volatile("buffer_load_dwordx2 %0, %1, %2, %3 offen offset:%4" : "=v"(d) : "v"(voff), "s"(rs), "s"(soff), "n"(IMM) : "memory");
    else asm volatile("buffer_load_ushort %0, %1, %2, %3 offen offset:%4" : "=v"(d) : "v"(voff), "s"(rs), "s"(soff), "n"(IMM) : "memory");
}
__device__ __forceinline__ void gld32(unsigned& d, unsigned voff, const u32x4& rs, unsigned soff) { asm volatile("buffer_load_dword %0, %1, %2, %3 offen" : "=v"(d) : "v"(voff), "s"(rs), "s"(soff) : "memory"); }
__device__ __forceinline__ void sld(unsigned& d, const void* p) { asm volatile("s_load_dword %0, %1, 0x0" : "=s"(d) : "s"(p) : "memory"); }
__device__ __forceinline__ void stouch(unsigned long long p) { asm volatile("s_load_dword s101, %0, 0x0\n\ts_load_dword s101, %0, 0x80\n\ts_load_dword s101, %0, 0x100" :: "s"(p) : "s101", "memory"); }
template <class T> __device__ __forceinline__ void ties(T& x) { asm volatile("" : "+s"(x)); }
template <int IMM, class T> __device__ __forceinline__ void gst(const T& d, unsigned voff, const u32x4& rs, unsigned soff) {
    if constexpr (sizeof(T) == 16) asm volatile("buffer_store_dwordx4 %0, %1, %2, %3 offen offset:%4 sc1" :: "v"(d), "v"(voff), "s"(rs), "s"(soff), "n"(IMM) : "memory");
    else asm volatile("buffer_store_dwordx2 %0, %1, %2, %3 offen offset:%4 sc1" :: "v"(d), "v"(voff), "s"(rs), "s"(soff), "n"(IMM) : "memory");
}
template <int SLQ> __device__ __forceinline__ void gate(unsigned raddr, unsigned need) {
    unsigned cnt, f;
    asm volatile("s_mov_b32 %1, 0x40000\n"
                 "0:\n\t"
                 "ds_read_b32 %0, %2\n\t"
                 "s_sub_u32 %1, %1, 1\n\t"
                 "s_waitcnt lgkmcnt(0)\n\t"
                 "s_cbranch_scc1 1f\n\t"
                 "v_cmp_le_u32 vcc, %3, %0\n\t"
                 "s_cmp_eq_u64 vcc, -1\n\t"
                 "s_cbranch_scc1 1f\n\t"
                 "s_sleep %4\n\t"
                 "s_branch 0b\n"
                 "1:" : "=&v"(f), "=&s"(cnt) : "v"(raddr), "s"(need), "n"(SLQ) : "vcc", "scc", "memory");
}
template <int SL = SLP, int AT_ = 0> __device__ __forceinline__ void tag_chk(unsigned& f, unsigned expect, unsigned voff, const u32x4& rs, unsigned soff) {
    unsigned cnt;
    asm volatile("v_cmp_eq_u32 vcc, %2, %0\n\t"
                 "s_cmp_eq_u64 vcc, -1\n\t"
                 "s_cbranch_scc1 1f\n\t"
                 "s_mov_b32 %1, 0x10000\n"
                 "0:\n\t"
                 ".if %7\n\tv_mov_b32 %0, 0\n\t.endif\n\t"
                 "s_sleep %6\n\t"
                 ".if %7\n\tbuffer_atomic_or %0, %3, %4, %5 offen sc0 sc1\n\t.else\n\tbuffer_load_dword %0, %3, %4, %5 offen sc1\n\t.endif\n\t"
                 "s_sub_u32 %1, %1, 1\n\t"
                 "s_waitcnt vmcnt(0)\n\t"
                 "s_cbranch_scc1 1f\n\t"
                 "v_cmp_eq_u32 vcc, %2, %0\n\t"
                 "s_cmp_eq_u64 vcc, -1\n\t"
                 "s_cbranch_scc0 0b\n"
                 "1:" : "+v"(f), "=&s"(cnt) : "v"(expect), "v"(voff), "s"(rs), "s"(soff), "n"(SL), "n"(AT_) : "vcc", "scc", "memory");
}

__device__ __forceinline__ unsigned race(unsigned w, const void* ws, unsigned long long rnd) {
    unsigned v;
    asm volatile("s_cmp_lt_u32 %1, 32\n\t"
                 "s_cbranch_scc0 1f\n\t"
                 "s_mov_b64 s[98:99], %3\n\t"
                 "s_lshl_b32 s96, s98, 7\n\t"
                 "s_lshl_b32 s97, %1, 13\n\t"
                 "s_andn2_b32 s96, s96, 0x2000\n\t"
                 "s_xor_b32 s96, s96, s97\n\t"
                 "s_and_b32 s96, s96, 0x7ff80\n\t"
                 "s_add_u32 s96, s96, %4\n\t"
                 "s_xor_b32 s97, s96, 0x2000\n\t"
                 "s_mov_b32 %0, s98\n\t"
                 "s_load_dword s99, %2, s96\n\t"
                 "s_load_dword s98, %2, s97\n\t"
                 "s_lshr_b32 s96, %1, 4\n\t"
                 "s_xor_b32 s96, s96, %1\n\t"
                 "s_waitcnt lgkmcnt(1)\n\t"
                 "s_sleep %5\n\t"
                 "s_cmp_lg_u32 %0, s98\n\t"
                 "s_addc_u32 %0, s96, 0\n\t"
                 "s_and_b32 %0, %0, 1\n"
                 "1:" : "=&s"(v) : "s"(w), "s"(ws), "s"(rnd), "n"(WS_P), "n"(RSL) : "s96", "s97", "s98", "s99", "scc", "memory");
    return v;
}
__device__ __forceinline__ void vote_chk(unsigned& f, unsigned expect, unsigned odd, unsigned raddr, unsigned lds, unsigned voff, const u32x4& rs, unsigned soff, unsigned& cnt) {
    unsigned t;
    asm volatile("s_mov_b32 %1, 0x10000\n\t"
                 "v_xor_b32 %2, %0, %3\n\t"
                 "v_cmp_gt_u32 vcc, %2, %4\n\t"
                 "s_cbranch_vccz 1f\n\t"
                 "s_mov_b32 m0, %6\n"
                 "0:\n\t"
                 "s_sleep 4\n\t"
                 "buffer_load_dword %7, %8, %9 offen sc1 lds\n\t"
                 "s_sub_u32 %1, %1, 1\n\t"
                 "s_waitcnt vmcnt(0)\n\t"
                 "ds_read_b32 %0, %5\n\t"
                 "s_waitcnt lgkmcnt(0)\n\t"
                 "s_cbranch_scc1 1f\n\t"
                 "v_xor_b32 %2, %0, %3\n\t"
                 "v_cmp_gt_u32 vcc, %2, %4\n\t"
                 "s_cbranch_vccnz 0b\n"
                 "1:" : "+v"(f), "=&s"(cnt), "=&v"(t) : "v"(expect), "v"(odd), "v"(raddr), "s"(lds), "v"(voff), "s"(rs), "s"(soff) : "vcc", "scc", "memory");
}
__device__ __forceinline__ unsigned vote_cnt(unsigned f, unsigned tag, unsigned up, unsigned& n) {
    unsigned r;
    asm volatile("s_xnor_b32 %0, %3, 1\n\tv_cmp_eq_u32 vcc, %0, %2\n\ts_bcnt1_i32_b64 %1, vcc\n\ts_cmp_gt_u32 %1, 16\n\ts_cselect_b32 %0, 0x2000, 0\n\ts_xor_b32 %0, %0, %4"
                 : "=&s"(r), "=&s"(n) : "v"(f), "s"(tag), "s"(up) : "vcc", "scc");
    return r;
}

template <int H> __device__ __forceinline__ void pp_ld(u32x4& d, unsigned voff, const u32x4& rs, unsigned soff) {
    if constexpr (H) asm volatile("buffer_load_dwordx4 v[244:247], %1, %2, %3 offen offset:1024 sc1" : "={v[244:247]}"(d) : "v"(voff), "s"(rs), "s"(soff) : "memory");
    else asm volatile("buffer_load_dwordx4 v[240:243], %1, %2, %3 offen sc1" : "={v[240:243]}"(d) : "v"(voff), "s"(rs), "s"(soff) : "memory");
}
__device__ __forceinline__ void pf_ld(unsigned& d, unsigned voff, const u32x4& rs, unsigned soff) { asm volatile("buffer_load_dword v248, %1, %2, %3 offen sc1" : "={v248}"(d) : "v"(voff), "s"(rs), "s"(soff) : "memory"); }
__device__ __forceinline__ void pf_keep(unsigned d) { asm volatile("" :: "{v248}"(d)); }
__device__ __forceinline__ void pf_zero(unsigned& d) { asm volatile("v_mov_b32 v248, 0" : "={v248}"(d)); }
template <int OFF> __device__ __forceinline__ void pf_at(unsigned& d, unsigned voff, const u32x4& rs, unsigned soff) {
    asm volatile("buffer_atomic_or v248, %1, %2, %3 offen offset:%4 sc0 sc1" : "+{v248}"(d) : "v"(voff), "s"(rs), "s"(soff), "n"(OFF) : "memory");
}
__device__ __forceinline__ void pp_sel(unsigned& so, unsigned& fail, unsigned d, unsigned tn, unsigned yes, unsigned no) {
    asm volatile("v_readlane_b32 %1, v248, 63\n\ts_and_b32 %1, %1, 0xf000f\n\ts_cmp_eq_u32 %1, %3\n\ts_cselect_b32 %0, %4, %5\n\ts_cselect_b32 %1, 0, -1"
                 : "=&s"(so), "=&s"(fail) : "{v248}"(d), "s"(tn), "s"(yes), "s"(no) : "scc");
}
#define PP_CHK(R, R1, R2, R3, OFF) \
    asm volatile("v_cmp_ne_u32 vcc, 0, %2\n\t" \
                 "s_cbranch_vccz 1f\n\t" \
                 "s_mov_b32 %1, 0x10000\n" \
                 "0:\n\t" \
                 ".if %9\n\t" \
                 "v_mov_b32 %2, 0\n\t" \
                 "s_sub_u32 %1, %1, 1\n\t" \
                 "s_cbranch_scc1 1f\n\t" \
                 "buffer_atomic_or %2, %8, %6, %7 offen offset:" OFF " sc0 sc1\n\t" \
                 "s_lshr_b32 vcc_lo, %4, 4\n\t" \
                 "s_waitcnt vmcnt(0)\n\t" \
                 "v_readlane_b32 %3, %2, 63\n\t" \
                 "s_xor_b32 %3, %3, vcc_lo\n\t" \
                 "s_and_b32 %3, %3, 0xf000f\n\t" \
                 "s_cbranch_scc1 0b\n\t" \
                 ".endif\n\t" \
                 "buffer_load_dwordx4 " R ", %5, %6, %7 offen offset:" OFF " sc1\n\t" \
                 "s_waitcnt vmcnt(0)\n\t" \
                 "s_and_b32 %3, %4, 0xf000f\n\t" \
                 "v_xor_b32 " R1 ", %3, " R1 "\n\t" \
                 "s_lshr_b32 %3, %4, 8\n\t" \
                 "s_and_b32 %3, %3, 0xf000f\n\t" \
                 "v_xor_b32 " R2 ", %3, " R2 "\n\t" \
                 "s_lshr_b32 %3, %4, 4\n\t" \
                 "s_and_b32 %3, %3, 0xf000f\n\t" \
                 "v_xor_b32 " R3 ", %3, " R3 "\n\t" \
                 "v_or3_b32 %2, " R1 ", " R2 ", " R3 "\n\t" \
                 "v_and_b32 %2, 0xf000f, %2\n\t" \
                 "s_sub_u32 %1, %1, 1\n\t" \
                 "s_cbranch_scc1 1f\n\t" \
                 "v_cmp_ne_u32 vcc, 0, %2\n\t" \
                 "s_cbranch_vccnz 0b\n" \
                 "1:" : "+{" R "}"(p), "=&s"(cnt), "+v"(bad), "=&s"(t) : "s"(tag), "v"(voff), "s"(rs), "s"(soff), "v"(vp), "n"(PB) : "vcc", "scc", "memory")
template <int H> __device__ __forceinline__ void pp_chk(u32x4& p, unsigned bad, unsigned tag, unsigned voff, const u32x4& rs, unsigned soff, unsigned vp) {
    unsigned cnt, t;
    if constexpr (H) PP_CHK("v[244:247]", "v245", "v246", "v247", "1024"); else PP_CHK("v[240:243]", "v241", "v242", "v243", "0");
}
template <int H> __device__ __forceinline__ void pp_tie(u32x4& p) { if constexpr (H) asm volatile("" : "+{v[244:247]}"(p)); else asm volatile("" : "+{v[240:243]}"(p)); }
__device__ __forceinline__ float amax3(float a, float b, float c) { float r; asm volatile("v_max3_f32 %0, |%1|, |%2|, |%3|" : "=v"(r) : "v"(a), "v"(b), "v"(c)); return r; }
__device__ __forceinline__ unsigned bfi_v(unsigned m, unsigned a, unsigned b) { unsigned r; asm volatile("v_bfi_b32 %0, %1, %2, %3" : "=v"(r) : "v"(m), "v"(a), "v"(b)); return r; }
__device__ __forceinline__ unsigned bfi_s(unsigned m, unsigned a, unsigned b) { unsigned r; asm volatile("v_bfi_b32 %0, %1, %2, %3" : "=v"(r) : "v"(m), "v"(a), "s"(b)); return r; }
__device__ __forceinline__ unsigned bfi_f(unsigned m, unsigned a, unsigned b) { unsigned r; asm volatile("v_bfi_b32 %0, %1, %2, %3" : "=v"(r) : "v"(m), "s"(a), "v"(b)); return r; }
template <int H> __device__ __forceinline__ float cvt16(unsigned w) {
    float r;
    if constexpr (H) asm volatile("v_cvt_f32_i32_sdwa %0, sext(%1) dst_sel:DWORD dst_unused:UNUSED_PAD src0_sel:WORD_1" : "=v"(r) : "v"(w));
    else asm volatile("v_cvt_f32_i32_sdwa %0, sext(%1) dst_sel:DWORD dst_unused:UNUSED_PAD src0_sel:WORD_0" : "=v"(r) : "v"(w));
    return r;
}
__device__ __forceinline__ float sel8(float a, float b, unsigned long long mask) {
    float r;
    asm volatile("s_mov_b64 vcc, %3\n\tv_cndmask_b32_dpp %0, %1, %2, vcc row_ror:8 row_mask:0xf bank_mask:0xf" : "=v"(r) : "v"(a), "v"(b), "s"(mask) : "vcc");
    return r;
}
__device__ __forceinline__ unsigned sel0(unsigned a, unsigned long long mask) { unsigned r; asm volatile("v_cndmask_b32_e64 %0, 0, %1, %2" : "=v"(r) : "v"(a), "s"(mask)); return r; }
constexpr unsigned long long diag_mask(int R) {
    unsigned long long m = 0;
    for (int l = 0; l < 64; ++l) if ((l >> 4) == ((l & 7) >> 1) && 2 * (l & 1) + ((l >> 3) & 1) == R) m |= 1ull << l;
    return m;
}
template <int IMM, int X, bool SC1> __device__ __forceinline__ void dma_x(unsigned& so, unsigned lds, unsigned voff, const u32x4& rs, unsigned base) {
    if constexpr (SC1) asm volatile("s_add_u32 m0, %1, %2\n\ts_xor_b32 %0, %5, %6\n\tbuffer_load_dwordx4 %3, %4, %0 offen sc1 lds" : "=&s"(so) : "s"(lds), "n"(IMM), "v"(voff), "s"(rs), "s"(base), "n"(X) : "memory", "scc");
    else asm volatile("s_add_u32 m0, %1, %2\n\ts_xor_b32 %0, %5, %6\n\tbuffer_load_dwordx4 %3, %4, %0 offen lds" : "=&s"(so) : "s"(lds), "n"(IMM), "v"(voff), "s"(rs), "s"(base), "n"(X) : "memory", "scc");
}
template <int OFF> __device__ __forceinline__ void dma_n(unsigned so, unsigned voff, const u32x4& rs) {
    asm volatile("buffer_load_dwordx4 %0, %1, %2 offen offset:%3 sc1 lds" :: "v"(voff), "s"(rs), "s"(so), "n"(OFF) : "memory");
}
template <int IMM, int OFF> __device__ __forceinline__ void dma_m(unsigned so, unsigned lds, unsigned voff, const u32x4& rs) {
    asm volatile("s_add_u32 m0, %4, %5\n\ts_nop 0\n\tbuffer_load_dwordx4 %0, %1, %2 offen offset:%3 sc1 lds" :: "v"(voff), "s"(rs), "s"(so), "n"(OFF), "s"(lds), "n"(IMM) : "memory", "scc");
}
__device__ __forceinline__ unsigned and_or(unsigned a, unsigned m, unsigned o) { unsigned r; asm("v_and_or_b32 %0, %1, %2, %3" : "=v"(r) : "v"(a), "v"(m), "s"(o)); return r; }

#define HOPS 1
#define HM 0x1ff221125B082ull
#define PG1 95
#define PG2 288
#define PG3 8
#define PG4 24
#define PGF 5
#define HW 4
constexpr int hop_nth(int n) { for (int k = 0; k < 62; ++k) if (((HM >> k) & 1) && n-- == 0) return k; return 63; }
constexpr int hop_next(int K) { int n = 0; for (int k = 0; k < K; ++k) n += (HM >> k) & 1; return hop_nth(n + HW); }
template <int K> __device__ __forceinline__ void hop() {
    if constexpr (HOPS && ((HM >> K) & 1)) asm volatile(".Lfkh%0:\n\ts_cbranch_execz .Lfkh%1" :: "n"(K), "n"(hop_next(K)));
}
template <int K> __device__ __forceinline__ void page() {
    if constexpr (HOPS && K >= 0 && ((HM >> (K & 63)) & 1)) asm volatile("s_branch 1f\n\t.p2align 12\n.Lfkh%0:\n\ts_cbranch_execz .Lfkh%1\n1:" :: "n"(K), "n"(hop_next(K)));
    else if constexpr (HOPS) asm volatile("s_branch 1f\n\t.p2align 12\n1:");
}
__device__ __forceinline__ void hop_all(int w) {
    if constexpr (HOPS) asm volatile("s_mov_b64 exec, 0\n\ts_cmp_eq_u32 %0, 0\n\ts_cbranch_scc1 .Lfkh%1\n\ts_cmp_eq_u32 %0, 1\n\ts_cbranch_scc1 .Lfkh%2\n\t"
                                     "s_cmp_eq_u32 %0, 2\n\ts_cbranch_scc1 .Lfkh%3\n\ts_branch .Lfkh%4\n.Lfkh63:\n\ts_mov_b64 exec, -1"
                                     :: "s"(w), "n"(hop_nth(0)), "n"(hop_nth(1)), "n"(hop_nth(2)), "n"(hop_nth(3)) : "scc");
}
#define NB 1
#define PFQ 0
#define CVR 3
#define SPR 3
#define ELP 6
#define PW 1
#define CPG 95
#define GQ -1
#define GE 3
#define KDL 0
#define GP 1
#define SG 507
#define KO 0
#define DP 0
#define B13 460
#define PRE 0
enum { OP_NONE, OP_A, OP_B, OP_FLAG, OP_SIG, OP_VOTE };
struct Op { int kind, a, b; };
constexpr Op g1_op(int G) {
    const int st = (G + 32) / 32 - 1, q = G - 32 * st, i = q >> 2, j = q & 3;
    if (i < 5) {
        if (st < 0) return {OP_NONE, 0, 0};
        if ((j & 1) == 0) return {OP_A, st * 8 + i + 3, j >> 1};
        if (q == FLQ && st < 15) return {OP_FLAG, 0, 0};
        if (NB && G == SG) return {OP_SIG, 2, 0};
        if (G == GCR) return {OP_VOTE, 0, 0};
        if (NB == 2 && G == 435) return {OP_SIG, 3, 0};
        return {OP_NONE, 0, 0};
    }
    if (st == 15) return NB && G == SG ? Op{OP_SIG, 2, 0} : Op{OP_NONE, 0, 0};
    const int n = (st + 1) * 8;
    switch (q) {
        case 20: return {OP_B, st + 1, 0}; case 21: return {OP_B, st + 1, 1}; case 22: return {OP_A, n, 0}; case 23: return {OP_B, st + 1, 2};
        case 24: return {OP_B, st + 1, 3}; case 25: return {OP_A, n, 1}; case 26: return {OP_A, n + 1, 0}; case 27: return {OP_A, n + 1, 1};
        case 28: return {OP_A, n + 2, 0}; case 30: return {OP_A, n + 2, 1};
    }
    return {OP_NONE, 0, 0};
}
constexpr int g1_need(int G) {
    const int st = G >> 5, j = G & 3;
    for (int h = G - 1; h >= -12; --h) {
        const Op o = g1_op(h);
        if (o.kind == OP_A && o.a == (G >> 2) && o.b == (j >> 1)) return h;
        if (o.kind == OP_B && o.a == st && o.b == j) return h;
        if (o.kind == OP_FLAG && (G & 31) == 20 && (h >> 5) == st) return h;
    }
    return -100;
}
constexpr int g1_wait(int G) {
    int n = -100, c = 0;
    for (int h = G; h < (G ? G + 4 : 2) && h < 512; ++h) if (g1_need(h) > n) n = g1_need(h);
    for (int h = n + 1; h < G; ++h) c += g1_op(h).kind != OP_NONE;
    return n < G ? c : -1;
}
#define KV0 3
#define KVN 0x2222
constexpr int kv_first(int k) { int n = 0; for (int i = KV0; i < k && i < KV0 + 8; ++i) n += (KVN >> (4 * (i - KV0))) & 15; return n > 8 ? 8 : n; }
constexpr int kv_last() { int k = KV0; while (kv_first(k + 1) < 8) ++k; return k; }
#define CQ -1
#define CTM 0
#define EC 2
#define ED 2
#define EB 0
#define KST 0
#define KFD 3
#define KPL 11
#define KPI 0
#define KSL SLP
#define CBQ 20
constexpr int KVL = kv_last(), KFL = KVL + KFD, KSD = KVS ? KCC : -1;
static_assert(KFL <= KPL && KPL <= 12 && KPL > KVL && (KPI & 3) == 0 && KTI != KCC && KTI != KFL && KTI < KPL && KVP >= 0 && KVP < KCQ && KCQ < KCC && KSD <= KVL && KCC <= KPL && GCR > 32 * KCC + 62, "");
#define ROT 0x824
#define KLP 13
#define KLS 4
constexpr int ld_pos(int j) { return j < KLP ? -100 : KL0 + (j - KLP) * KLS / 4; }
constexpr int ld_cnt(int k) { int c = 0; for (int j = KLP; j < 21; ++j) c += (ld_pos(j) + 8) / 8 - 1 == k; return c; }
constexpr int KLL = (ld_pos(20) + 8) / 8 - 1, KLI = ld_pos(20) - 8 * KLL;
static_assert(KLP == 21 || KLL < KV0, "");
constexpr int hv_cnt(int k0, int k1) {
    int c = 0;
    for (int k = k0; k < k1; ++k) c += 9 + (KST ? 4 * (k == KVL) : k < KSD ? 0 : kv_first(k + 1) / 2 - (k == KSD ? 0 : kv_first(k) / 2)) + (k == KFL) + (k == KPL) + (k == KCQ) + (k == KVP) + (TM && TM < 16 && k == KTI) + ld_cnt(k);
    return c;
}
#define ZM 1
#define CL 1
#define EP 0
constexpr unsigned long long lag0_mask(int w) {
    unsigned long long m = 0;
    for (int l = 0; l < 64; ++l) if ((l >> 4) == 2 + ((l >> 3) & 1) && 3 - ((l & 7) >> 1) == w) m |= 1ull << l;
    return m;
}
constexpr int cv_start(int n) { return n * 8 - n * (n - 1) / 2; }
constexpr int cv_n(int q) { int n = 0; while (cv_start(n + 1) <= q) ++n; return n; }
constexpr int cv_m(int q) { return 7 - cv_n(q) - (q - cv_start(cv_n(q))); }
enum { CO_NONE, CO_A, CO_B, CO_C };
constexpr Op cv_op(int G) {
    const int s = (G + 36) / 36 - 1, q = G - 36 * s;
    if (q >= 1 && q <= 6) return s >= 0 ? Op{CO_A, s * 8 + 5 + (q - 1) / 2, (q - 1) & 1} : Op{CO_NONE, 0, 0};
    if (q >= 11 && q <= 14) return s >= 0 && s >= CL && s - CL < 8 ? Op{CO_C, s - CL, q - 11} : Op{CO_NONE, 0, 0};
    if (s == 7) return {CO_NONE, 0, 0};
    for (int m = 0; m < 8; ++m) if (q == cv_start(7 - m)) return {CO_B, (s + 1) * 8 + m, 0};
    for (int n = 0; n < 5; ++n) if (q == cv_start(n + 1) + 1 || q == cv_start(n + 1) + 2) return {CO_A, (s + 1) * 8 + n, q - cv_start(n + 1) - 1};
    return {CO_NONE, 0, 0};
}
constexpr int cv_need(int G) {
    const int s = G / 36, q = G % 36;
    for (int h = G - 1; h >= -36; --h) {
        const Op o = cv_op(h);
        if (o.kind == CO_A && o.a == s * 8 + cv_n(q)) return h;
        if (o.kind == CO_B && o.a == s * 8 + cv_m(q)) return h;
        if (o.kind == CO_C && q == 24 && o.a == s - CL) return h;
    }
    return -100;
}
constexpr int cv_wait(int G) {
    const int n = cv_need(G);
    for (int h = G - 1; h >= 0 && h >= G - 40; --h) if (cv_need(h) >= n) return -1;
    int c = 0;
    for (int h = n + 1; h < G; ++h) c += cv_op(h).kind != CO_NONE;
    return c;
}
#define AT(n, m) ((n) * 8 + (m))
#define FM 0x326260
constexpr int fill_pos(int s, int i) {
    const int f = (FM >> (4 * (s - 2))) & 15, n = s == 2 ? 0 : 4 * s - 9;
    switch (f) {
        case 1: return AT(n + i, 5) - (s == 2 && i == 3) * 6;
        case 2: return AT(n + 1 + i / 2, 5 + 2 * (i & 1));
        case 3: return i < 2 ? AT(n, 5 + 2 * i) : AT(n + i - 1, 5);
        case 4: return AT(n + 2 + i / 2, 5 + 2 * (i & 1));
        case 5: return AT(n, 1 + 2 * i) + (s == 2 ? 4 : 0);
        case 6: return AT(n + 3 + i / 2, 5 + 2 * (i & 1));
        case 7: return AT(n + 3, 5 + 2 * i);
        case 8: return i < 2 ? AT(n + 1, 5 + 2 * i) : AT(n + i, 5);
        case 9: return AT(n + 1 + i, 1);
        case 10: return i < 2 ? AT(n, 1 + 2 * i) : AT(n + 3, 1 + 2 * i);
    }
    return AT(n + i / 2, 5 + 2 * (i & 1));
}
constexpr int g2_fill(int p) {
    for (int k = 8; k < 32; ++k) if (fill_pos(k >> 2, k & 3) == p) return k;
    return -1;
}
#define QX AT(2, 5)
#define QQ AT(3, 2)
#define QZ AT(11, 2)
#define QT AT(3, 4)
constexpr int P_X = QX, P_Q = QQ, P_Z = QZ, P_T = QT;
constexpr bool XL = false;
constexpr bool fill_m0(int k) {
    const int a = fill_pos(k >> 2, (k & 3) - 1), b = fill_pos(k >> 2, k & 3);
    return (P_Q + 2 >= a && P_Q < b) || (P_Z + 2 >= a && P_Z < b) || (P_X + 2 >= a && P_X < b);
}
#define DBG 0
#define XW 4
#define XW2 4
#define QPP0 AT(9, 4)
#define QL0 AT(21, 1)
#define QR0 AT(24, 6)
#define QF2 AT(28, 3)
#define QL1 AT(26, 1)
#define QR1 AT(29, 3)
#define QA AT(16, 4)
#define AD 0
#define D1 2
#define D0 1
constexpr int NS = 33, NR = 29;
constexpr int P_PP0 = QPP0, P_PP1 = AT(16, 4), P_L0 = QL0, P_R0 = QR0, P_F2 = QF2, P_L1 = QL1, P_R1 = QR1, P_ZC = AT(10, 6), P_A = QA;
#define QB 24
#define QW -2
constexpr int P_W = QW;
static_assert(P_W < AT(ZP, 4), "");
constexpr int P_B0 = P_L0 - QB, P_B1 = P_L1 - QB;
static_assert(P_B0 > P_ZC + 1 && P_B1 > P_L0 + 1, "");
constexpr int E_PP0 = P_PP0 + NS - 1, E_PP1 = P_PP1 + (NS - 1) / D1, E_R0 = P_R0 + (NR - 1) / D0;
constexpr int ZW = P_A >= AT(20, 4) ? 19 : 15;
static_assert(P_R0 + 14 / D0 >= AT(24, 3) && E_R0 < P_F2 && P_F2 >= AT(24, 4) && P_F2 + 8 <= AT(31, 0) && E_R0 < P_R1, "");
static_assert(P_PP0 >= AT(9, 2) && P_PP0 + 8 <= AT(15, 4) && E_PP0 < P_PP1 && P_A >= AT(16, 4), "");
constexpr bool a_free(int p) {
    return p >= P_A && (AD || !((p >= P_PP1 && p <= E_PP1) || (p >= P_R0 && p <= E_R0) || (p >= P_F2 && p < P_F2 + 16)));
}
constexpr int a_step(int p) {
    if (NOA || !a_free(p)) return -1;
    int c = 0;
    for (int h = P_A; h < p; ++h) c += a_free(h);
    return c <= 48 ? c : -1;
}
constexpr int a_pos(int s) { int p = P_A; while (a_step(p) != s) ++p; return p; }
constexpr int q_rd(int r) { int p = r < 2 ? AT(ZW, 6) + r : a_pos(12 * r - 16) + 1; while ((p & 7) < 4) ++p; return p; }
constexpr int q_tie(int r) { const int n = (q_rd(r) >> 3) + 1; return AT(n, (n & 3) == 3 ? 0 : 4); }
static_assert(NOA || (a_pos(48) < 256 && q_tie(2) <= a_pos(23) && q_tie(3) <= a_pos(35)), "");
constexpr int g2_vm(int p) {
    int c = g2_fill(p) >= 0;
    c += a_step(p) > 0 && a_step(p) % 12 == 0 && !(NOST & 1);
    if (!NOA) c += (p == P_T) + (p == P_Q || p == P_Q + 2) + (p == AT(ZP, 4)) + (p == P_Z || p == P_Z + 2);
    c += (p == P_X || p == P_X + 2) + (p == P_W);
    c += !(NOST & 2) && (p == E_PP0 || p == E_PP1);
    c += (p == P_L0 || p == P_L1) + (PB && (p == P_B0 || p == P_B1));
    c += !(NOST & 4) && (p == P_F2 + 11 || p == P_F2 + 15);
    return c;
}
constexpr int g2_lx(int n) {
    int c = 2 * (n == 7);
    if (n >= 0 && (n & 7) >= 6) c += 2;
    if (!NOA) { c += 4 * (n == ZW); for (int r = 0; r < 4; ++r) c += (q_rd(r) >> 3) == n; }
    return c;
}
constexpr int g2_tile(int n, int j) { return (n & 4) ? (n & 2) == 0 : j; }
constexpr int g2_unit(int n, int j) { return (n & 4) ? 4 + 2 * (n & 1) + j : n & 7; }
constexpr int imax(int a, int b) { return a > b ? a : b; }
constexpr int g2_need(int n) {
    int r = n == 3 ? -1 : fill_pos((n + 1) / 4, 3);
    if (n == 7 && !XL) r = imax(r, P_X + 2);
    if (n == ZW && !NOA) r = imax(r, imax(P_Q, P_Z) + 2);
    return r;
}
static_assert(P_Z > AT(11, 0) && P_T > AT(3, 0) && P_T < AT(ZP, 4) && (!XL || (P_X + 2 < P_R0 && P_R0 + 2 >= AT(24, 2) && P_R0 < AT(27, 0) && D0 == 1)), "");
constexpr bool rd1(int n) { return RD == 1 || (RD == 3 && (n & 3) == 2); }
constexpr int g2_allow(int e, int p) { int c = 0; for (int h = p + 1; h < e; ++h) c += g2_vm(h); return c; }

__global__ __launch_bounds__(512, 2) __attribute__((aligned(4096))) void k1(const uint16_t* __restrict__ x, const uint16_t* __restrict__ bre, const uint16_t* __restrict__ bim,
                                             const uint16_t* __restrict__ lre, const uint16_t* __restrict__ lim, const uint16_t* __restrict__ logdt,
                                             const uint16_t* __restrict__ cre, const uint16_t* __restrict__ cim, const uint16_t* __restrict__ dskip,
                                             const uint16_t* __restrict__ ssmk, const uint16_t* __restrict__ ssmD, const uint16_t* __restrict__ qin,
                                             const uint16_t* __restrict__ kin, const uint16_t* __restrict__ vin, const uint16_t* __restrict__ sk,
                                             char* __restrict__ ws, float* __restrict__ out TS_ARG) {
    unsigned long long pc0, rnd;
    asm volatile("s_getpc_b64 %0\n\ts_memtime %1" : "=s"(pc0), "=s"(rnd));
    TS_DECL TS(0);
    TL_DECL
    __shared__ __attribute__((aligned(1024))) char shm[LDS_TOTAL];
    const int tid = threadIdx.x, lane = tid & 63, wid = __builtin_amdgcn_readfirstlane(tid >> 6);
    const int b = blockIdx.x, xcd = b & 7, idx = b >> 3, hb = xcd & 1, tq = idx & 7;
    const int tm = (xcd >> 1) * 4 + (idx >> 3), tn = hb * 8 + tq;
    unsigned nr;
    const int tm2 = (xcd & 4) * 2 + tq, dt = (xcd & 2) * 2 + (idx >> 3), rot2 = tq + JR(idx >> 3);
    const unsigned lbase = (unsigned)(uintptr_t)shm;
    const int l15 = lane & 15, g4 = lane >> 4, lw = wid & 3, kg = wid >> 2;
    dma<U0 + 4096, false>(lbase + wid * 1024, lane * 16, mk_rsrc((const void*)(pc0 & ~255ull), CODE_KB * 1024), ((idx * 8 + wid) & 63) * 1024);
    const unsigned mk0 = lbase + POLL + lw * 256 + kg * (FULL - POLL + lw * (FST - 256));
    flag_set(mk0 + lane * 4, 1u);
    flag_set(lbase + HGO + lane * 4, 0u);
    if constexpr (NB) flag_set(lbase + DONE + lw * FST + lane * 4, 1u);
    unsigned vo[4];
    const bool xw = lw < 2;
#pragma unroll
    for (int i = 0; i < 4; ++i) {
        const int v = l15 ^ (i * 4 + g4);
        vo[i] = ((xw ? 64 * (i & 1) + 4 * (i >> 1) + (lane & 32) + 2 * (g4 & 1) : 8 * (i & 1) + 2 * g4) + (v >> 3)) * 2048 + (v & 7) * 16 - (i & 1) * 1024;
    }
    const int bw = lw >> 1;
    unsigned r0 = (tm * 128 + lw * 16 + bw * (tn * 64 + lw * 16 - 64 - tm * 128)) * 2048, rsz = 229376 - bw * 163840;
    unsigned rot = (ROT >> 8) * ((tq + (idx >> 3)) & 1) + ((ROT >> 4) & 15) * (xcd >> 1) + (ROT & 15) * hb, rst = 16384 << bw, lb = lbase + lw * 8192, oob = OOB;
    unsigned lb0 = lb + g1_buf(0) + kg * (g1_buf(1) - g1_buf(0)), so0 = ((kg + rot) & 15) * 128;
    unsigned co = l15 * 2 + (tn * 128 + lw * 32 + kg * 2048), vw = b * 4 + lw + 32 - 32 * kg;
    tie(vo[0]); tie(vo[1]); tie(vo[2]); tie(vo[3]); tie(oob); tie(co);
    ties(r0); ties(rsz); ties(rot); ties(rst); ties(lb); ties(lb0); ties(so0); ties(vw);
    sched();
    asm volatile("" :: "s"(x), "s"(bre), "s"(bim), "s"(lre), "s"(lim), "s"(logdt), "s"(cre), "s"(cim), "s"(dskip), "s"(ssmk), "s"(ssmD), "s"(qin),
                       "s"(kin), "s"(vin), "s"(sk), "s"(ws), "s"(out));
    const bool xv = fresh(bw) == 0;
    const u32x4 rs0 = mk_rsrc((const char*)(xv ? x : bre) + r0, rsz), rs1 = mk_rsrc((const char*)(xv ? x : bim) + r0 - 2048, rsz + 2048);
    unsigned c_dt, c_re, c_im;
    gld<0>(c_dt, co, mk_rsrc(logdt, 2048), 0);
    gld<0>(c_re, co, mk_rsrc(lre, 2048), 0);
    gld<0>(c_im, co, mk_rsrc(lim, 2048), 0);
    const unsigned vb = race(vw, ws, rnd);
    static_for<0, 8>([&](auto ic) { constexpr int i = CV(ic); dma4<(i & 4) * 1024, i & 3>(lb0, vo[i & 3], (i & 2) ? rs1 : rs0, so0 + (i >> 2) * rst); });
    wait_l<0>();
    barrier();
    dma1<0>(mk0 + kg * 120, oob, rs0, 0);
    sched();
    const unsigned a00 = (l15 >> 1) * 256 + ((((l15 & 1) * 8 + g4) ^ (l15 >> 1)) << 4);
    auto frag = [&](int par, int ks) { return (a00 ^ (ks * 64) ^ (par * 128)) + par * 2048; };
    bf16x8 cf[32];
    unsigned vc[4], cad[4];
    auto c_setup = [&]() {
#pragma unroll
        for (int i = 0; i < 4; ++i) { const int v = l15 ^ (i * 4 + g4); vc[i] = (4 * (2 * g4 + (v >> 3)) + 2 * (i & 1) + (i >> 1)) * 2048 + (v & 7) * 16 - i * 1024; }
    };
    const u32x4 rsc = mk_rsrc((kg ? cim : cre) + (size_t)dt * 131072, 262144);
    auto c_off = [&](int dg, int j) { return (unsigned)(dg * 65536 + hb * 1024 + ((rot2 + j) & 7) * 128); };
    const unsigned cst = lbase + (kg ? BUF : 2 * BUF) + lw * 8192;
    unsigned tag;
    const u32x4 rsf = mk_rsrc(ws + WS_F, 65536);
    const __amdgpu_buffer_rsrc_t rf = mk_brs(ws + WS_F, 65536);
    const int qx = (hb ? 0 : 2) ^ JQ(idx >> 3), wid_ = wid, b_ = b;

    if (wid >= 4) {
        page<-1>();
        const int ch = xcd * 128 + (idx >> 2) * 16 + lw * 4 + (idx & 3);
        unsigned dw, nx; u32x4 ad;
        auto c_unit = [&](auto uc) { static_for<0, 4>([&](auto ic) { constexpr int u = CV(uc), i = CV(ic); dma4<(u & 1) * 4096, i>(cst, vc[i], rsc, c_off(lw, u)); }); };
        constexpr int XV = 4 * (EC == 1) + 8 * (PFQ == 2);
        auto ctouch = [&](int r) { const unsigned j = (fresh(idx * 4 + lw) + r * 37) & 127; stouch((pc0 & ~255ull) + (j < CODE_KB * 8 / 3 ? j : j - 64) * 384); };
        {
            const unsigned fb = lbase + FULL + lw * FST;
            auto piece = [&](auto sc, auto ic) {
                constexpr int s = CV(sc), i = CV(ic);
                dma4<g1_buf(s) + (i & 4) * 1024, i & 3>(lb, vo[i & 3], (i & 2) ? rs1 : rs0, ((s + rot) & 15) * 128 + (i >> 2) * rst);
            };
            auto mark = [&](auto sc) { dma1<(31 - CV(sc)) * 4>(fb, oob, rs0, 0); };
            auto feed = [&](auto kc, auto ic) {
                constexpr int k = CV(kc), i = CV(ic);
                if constexpr (i < 4) piece(IC(k + 3), IC(i + 4));
                if constexpr (i == 3) mark(IC(k + 3));
                if constexpr (i >= 4 && k < 12) piece(IC(k + 4), IC(i - 4));
            };
            __builtin_amdgcn_s_setprio(HP);
            TS(7);
            sld(tag, ws + WS_F + F_EP);
            sld(dw, ssmD + (ch & ~1));
            const int sb = idx * 2 + (lw >> 1), kh = lw & 1;
            u32x2 kk[11], vv[8]; u32x4 w4[2], kd[4], ke[4], kvo[4];
            const unsigned ko = g4 * 16384 + l15 * 8, so = sb * 65536 + xcd * 256 + kh * 128;
            auto kv_ld = [&](auto jc) {
                constexpr int j = CV(jc), i = j < 13 ? j - 5 : j - 13;
                if constexpr (j < 2) gld<16 * j>(w4[j], l15 * 32, mk_rsrc(sk, 8192), xcd * 1024 + kh * 512);
                else if constexpr (j < 5) gld<0>(kk[j - 2], ko + so - 6144 + (j - 2) * 2048, mk_rsrc(kin, 4 << 20), 0);
                else if constexpr (j < 13) gld<(i & 1) * 2048>(kk[3 + i], ko, mk_rsrc(kin, 4 << 20), so + (i >> 1) * 4096);
                else gld<(i & 1) * 2048>(vv[i], ko, mk_rsrc(vin, 4 << 20), so + (i >> 1) * 4096);
            };
            {
                static_for<0, KLP>(kv_ld);
                dma1<DSK>(lbase, lane * 4, mk_rsrc(dskip, 2048), dt * 256);
                dma<U0, false>(lbase + kh * 1024, oob, rs0, 0);
            }
            static_for<4, 8>([&](auto ic) { feed(IC(-2), ic); });
            const __amdgpu_buffer_rsrc_t rk = mk_brs(ws + WS_KVT + (size_t)((xcd & 3) * 32 + kh * 16) * 32768, 524288);
            const unsigned K = lbase + KC + lw * KCH;
            unsigned kfo;
            const unsigned n0 = (xcd & 4) << 11;
            auto st = [&](int c) { st16(kvo[c], rk, (l15 >> 2) * 131072 + g4 * 64 + (l15 & 3) * 16, c * 32768 + (sb >> 5) * 16384 + (KVS ? nx : n0 ^ ((sb & 32) << 8)) + (sb & 31) * 256); };
            float kf[7], pa[4], pb[4];
            auto kv_step = [&](auto uc, auto jc) {
                constexpr int u = CV(uc), j = CV(jc), c = u >> 1, h = u & 1;
                auto el = [&](const u32x2& q) -> float { return (c & 1) ? bfhi(q[c >> 1]) : bflo(q[c >> 1]); };
                auto tap = [&](int t) { const unsigned w = w4[c >> 1][(c & 1) * 2 + (t >> 1)]; return (t & 1) ? bfhi(w) : bflo(w); };
                auto all = [&](auto f) {
#pragma unroll
                    for (int i = 0; i < 4; ++i) f(i);
                };
                if constexpr (j == 0) {
#pragma unroll
                    for (int i = 0; i < 7; ++i) { kf[i] = (h && i < 3) ? kf[i + 4] : el(kk[4 * h + i]); tie(kf[i]); }
                }
                if constexpr (EXACT) {
                    if constexpr (j == 1) { const float t = tap(3); all([&](int i) { pa[i] = kf[i] * t; }); all([&](int i) { pa[i] = rbf(pa[i]); tie(pa[i]); }); }
                    if constexpr (j == 2 || j == 4 || j == 6) { const float t = tap(3 - j / 2); all([&](int i) { pb[i] = kf[i + j / 2] * t; }); all([&](int i) { pb[i] = rbf(pb[i]); tie(pb[i]); }); }
                    if constexpr (j == 3 || j == 5 || j == 7) { all([&](int i) { pa[i] += pb[i]; }); all([&](int i) { pa[i] = rbf(pa[i]); tie(pa[i]); }); }
                } else {
                    if constexpr (j == 1) { const float t = tap(3); all([&](int i) { pa[i] = kf[i] * t; tie(pa[i]); }); }
                    if constexpr (j >= 2 && j <= 4) { const float t = tap(4 - j); all([&](int i) { pa[i] = fma_(kf[i + j - 1], t, pa[i]); tie(pa[i]); }); }
                    if constexpr (j == 5) all([&](int i) { pa[i] = rbf(pa[i]); tie(pa[i]); });
                }
                if constexpr (j == (EXACT ? 7 : 6)) all([&](int i) { pb[i] = el(vv[4 * h + i]); tie(pb[i]); });
                if constexpr (j == 7) {
                    all([&](int i) { pa[i] *= pb[i]; });
                    kvo[c][3 - 2 * h] = pack2(pa[1], pa[0]); kvo[c][2 - 2 * h] = pack2(pa[3], pa[2]);
                    if constexpr (h && !KST && u >= kv_first(KSD)) st(c);
                    else if constexpr (u == 7) all(st);
                    else tie(kvo[c]);
                }
            };
            static_for<-1, 13>([&](auto kc) {
                constexpr int k = CV(kc);
                if constexpr (k == PGF) page<1>();
                if constexpr (k == 2) hop<0>();
                if constexpr (k == 6) hop<6>();
                if constexpr (k == 8) hop<9>();
                if constexpr (k >= 0) barrier();
                if constexpr (k >= 0 && ((CTM >> k) & 1)) ctouch(k);
                if constexpr (k == KV0) {
                    wait_v<KLP == 21 ? 4 + hv_cnt(-1, k) : 9 - KLI - (KLI > 3) + hv_cnt(KLL + 1, k)>();
                    static_for<0, 11>([&](auto ic) { tie(kk[CV(ic)]); });
                    static_for<0, 8>([&](auto ic) { tie(vv[CV(ic)]); });
                    tie(w4[0]); tie(w4[1]); keep(c_dt); keep(c_re); keep(c_im);
                    TS(8);
                }
                if constexpr (k == KVP) {
                    wait_l<0>();
                    ties(tag); ties(dw);
                    tag += 0x01010101u;
                    __builtin_amdgcn_raw_buffer_store_b64(u32x2{tag, ~tag ^ vb}, rf, lane || vw >= 32 ? OOB : 0u, F_C + vw * 8, 16);
                }
                if constexpr (k == KCQ) dma1<POLL>(lbase + lw * 256, lane * 4, rsf, F_C);
                if constexpr (TM == 16 && k == KTI) stouch((unsigned long long)(ws + WS_E) + (TX ? (xcd * 128 + idx * 4 + lw) : b * 4 + lw) * 384);
                if constexpr (TM && TM < 16 && k == KTI) {
                    const unsigned wv = b * 4 + lw, a = lane < 32 ? ((TM & 1) ? wv * 4096 + lane * 128 : (TM & 4) && lane < 4 ? wv * 4096 + lane * 1024 + 896 : OOB) : lane < 35 && (TM & 2) ? (unsigned)(WS_E - WS_PP) + (wv * 3 + lane - 32) * 128 : OOB;
                    if constexpr (TM & 8) dma1<DUMP>(lbase + lw * 256, a, mk_rsrc(ws + WS_PP, WS_F + F_EP - WS_PP), 0);
                    else asm volatile("buffer_atomic_or %0, %1, %2, 0 offen sc1" :: "v"(0u), "v"(a), "s"(mk_rsrc(ws + WS_PP, WS_F + F_EP - WS_PP)) : "memory");
                }
                if constexpr (k == KCC) {
                    unsigned f, n, sp;
                    wait_v<hv_cnt(KCQ, k) - 1>();
                    dsr32<POLL - 131072>(f, lbase + 131072 + lw * 256 + lane * 4);
                    wait_l<0>();
                    vote_chk(f, (lane & 1) ? ~tag : tag, lane & 1, lbase + POLL + lw * 256 + lane * 4, lbase + POLL + lw * 256, lane * 4, rsf, F_C, sp);
                    nr = vote_cnt(f, tag, (xcd & 4) << 11, n);
                    nx = nr ^ ((xcd & 2) << 12);
                    kfo = F_K + (KVS ? nr : n0) + (((xcd & 3) * 2 + (idx >> 4)) * 64 + lw * 16) * 8;
                    TSV(11, n + 100 * (0x10000 - sp));
                }
                if constexpr (k == KFL) {
                    wait_v<hv_cnt(KVL + 1, k)>();
                    __builtin_amdgcn_raw_buffer_store_b64(u32x2{tag, ~tag}, rf, lane ? OOB : 0u, F_K + (KVS ? nr : n0) + (((xcd & 3) * 2 + kh) * 64 + sb) * 8, 16);
                    TS(10);
                }
                if constexpr (k == 12) {
                    dma1<0>(K, oob, rs0, 0);
                    dma1<KCP>(K, oob, rs0, 0);
                }
                static_for<0, 8>([&](auto ic) {
                    constexpr int i = CV(ic), u0 = kv_first(k), nu = kv_first(k + 1) - u0;
                    static_for<KLP, 21>([&](auto jc) { if constexpr (ld_pos(CV(jc)) == 8 * k + i) kv_ld(jc); });
                    if constexpr (k == KPL && i == KPI) dma1<POLL>(lbase + wid * 256, (lane & 31) * 4, rsf, kfo);
                    feed(kc, ic);
                    if constexpr (k == 4 && i == 4) hop<3>();
                    if constexpr (!KST && k == KSD && (i & 1) && i / 2 < kv_first(KSD) / 2) st(i / 2);
                    if constexpr (nu == 1) kv_step(IC(u0), ic);
                    if constexpr (nu == 2) { kv_step(IC(u0 + i / 4), IC(2 * (i & 3))); kv_step(IC(u0 + i / 4), IC(2 * (i & 3) + 1)); }
                    if constexpr (!KDL && k == 12 && i >= 4) gld<(i - 4) * 1024>(kd[i - 4], lane * 16, mk_rsrc(ssmk, 4 << 20), ch * 4096);
                    if constexpr (k == 11 && i == 4) c_setup();
                    if constexpr (k == 10 && i >= 4) { cad[i - 4] = cst + frag(i & 1, (i >> 1) & 1); tie(cad[i - 4]); }
                    sched();
                });
            });
            page<12>();
            TS(1);
            if constexpr (EB == 2) barrier();
            unsigned pf;
            const unsigned fr = lbase + FULL + (lane & 3) * FST;
            wait_v<hv_cnt(KPL, 12) - (KPL < 12 ? 1 : 2) - (KPL == KFL) - KPI - (KPI > 3) + (KDL ? 7 : 11)>();
            dsr32<POLL - 131072>(pf, lbase + 131072 + wid * 256 + lane * 4);
            wait_l<0>();
            tag_chk<KSL>(pf, (lane & 1) ? ~tag : tag, (lane & 31) * 4, rsf, kfo);
            if constexpr (KDL) static_for<0, 4>([&](auto ic) { gld<CV(ic) * 1024>(kd[CV(ic)], lane * 16, mk_rsrc(ssmk, 4 << 20), ch * 4096); });
            auto b13 = [&]() { if constexpr (NB == 2) { unsigned f = 1; zero_chk<12, 1>(f, lbase + DONE + (lane & 3) * FST); } else if constexpr (NB < 2) barrier(); };
            if constexpr (EB == 1) b13();
            {
                const u32x4 rs = mk_rsrc(ws + WS_KVT + (size_t)((xcd & 3) * 32 + idx) * 32768, 32768);
                static_for<0, 4>([&](auto ic) { constexpr int i = CV(ic); dma<U0 + 2048 + i * 1024, true>(lbase + lw * 4096, (lane >> 2) * 64 + (((lane & 3) ^ g4) << 4), rs, (lw >> 1) * 16384 + (KVS ? nx : n0 ^ ((lw & 2) << 12)) + (lw & 1) * 4096 + i * 1024); });
            }
            mark(IC(16));
            auto warm = [&]() {
                const unsigned wo = g4 * 2048 + l15 * 16, dl = lbase + lw * 1024;
                const unsigned qo = (512 * (idx >> 3) + 128 * (idx & 1) + 32 * lw + 4 * ((idx >> 1) & 3)) * 2048 + xcd * 256, xo = (tm2 * 128 + 32 * lw + 8 * (qx & 2)) * 2048 + dt * 256;
                static_for<0, 4>([&](auto ic) { constexpr int i = CV(ic); dma<DUMP, false>(dl, wo, mk_rsrc(qin, 4 << 20), qo + ((i & 1) * 16 + (i >> 1) * 256) * 2048); });
                static_for<0, 4>([&](auto ic) { constexpr int i = CV(ic); dma<DUMP, false>(dl, wo, mk_rsrc(x, 4 << 20), xo + i * 8192); });
            };
            if constexpr (PFQ == 1) warm();
            if constexpr (EB == 0) b13();
            auto c_first = [&]() {
                if constexpr (CL <= 1) c_unit(IC(0));
                if constexpr (CL <= 0) c_unit(IC(1));
                if constexpr (EC == 1) static_for<0, 4>([&](auto ic) { constexpr int i = CV(ic); dma4<DUMP, i>(lbase, vc[i], mk_rsrc(cre + (size_t)dt * 131072, 262144), c_off(lw, 0)); });
                if constexpr (PFQ == 2) warm();
            };
            static_assert(NB != 3 || CL == 2, "");
            if constexpr (!KO) c_first();
            wait_v<5 + 8 * (PFQ == 1) + (KO ? 0 : 4 * (CL <= 1) + 4 * (CL <= 0) + XV)>();
            static_for<0, 4>([&](auto ic) { tie(kd[CV(ic)]); });
#pragma unroll
            for (int n = 0; n < 4; ++n) {
                const unsigned first = n ? __builtin_amdgcn_readlane(kd[n ? n - 1 : 0][3], 63) : 0u;
                const unsigned p = __builtin_amdgcn_update_dpp(first, kd[n][3], 0x138, 0xf, 0xf, false);
                ke[n] = u32x4{__builtin_amdgcn_alignbyte(kd[n][0], p, 2), __builtin_amdgcn_alignbyte(kd[n][1], kd[n][0], 2),
                              __builtin_amdgcn_alignbyte(kd[n][2], kd[n][1], 2), __builtin_amdgcn_alignbyte(kd[n][3], kd[n][2], 2)};
                tie(ke[n]);
            }
            if constexpr (ZM) {
                const unsigned dl = (ch & 1) ? dw >> 16 : dw & 0xffffu, dv = dl << ((~lane & 1) * 16);
                static_for<0, 4>([&](auto ic) { constexpr unsigned long long k = lag0_mask(CV(ic)); ad[CV(ic)] = sel0(dv, k); });
                tie(ad);
            }
            hop<14>();
            static_for<0, 4>([&](auto ic) { constexpr int i = CV(ic); dsw128<64 + i * 1024>(K + lane * 16, kd[i]); dsw128<KCP + 64 + i * 1024>(K + lane * 16, ke[i]); });
            if constexpr (KO) c_first();
            TS(9);
            static_for<0, -CL>([&](auto uc) {
                constexpr int u = CV(uc);
                wait_v<4 + (u < 2 ? XV : 0)>();
                static_for<0, 4>([&](auto ic) { constexpr int i = CV(ic); dsr128<(u & 1) * 4096>(cf[u * 4 + i], cad[i]); });
                wait_l<0>();
                static_for<0, 4>([&](auto ic) { tie(cf[u * 4 + CV(ic)]); });
                c_unit(IC(u + 2));
            });
            pf = 1;
            zero_chk<15 * 4>(pf, fr);
            if constexpr (PRE) { pf = 1; zero_chk<PRE == 1 ? 8 : 4>(pf, lbase + DONE + (lane & 3) * FST); }
            __builtin_amdgcn_s_setprio(0);
        }
        TS(2);
        TL(1);
        unsigned ka[4], ub[4];
#pragma unroll
        for (int i = 0; i < 4; ++i) {
            ka[i] = lbase + KC + lw * KCH + ((l15 & 1) ? 0 : KCP) + 84 + 2 * (l15 & 14) - 16 * g4 + i * 1024;
            const int e = 2 * l15 + g4 + 2;
            ub[i] = lbase + U0 + 64 * e + ((lw ^ (((e >> 2) + 3 - i) & 3)) << 4);
        }
        unsigned gad = lbase + DONE + lw * FST;
        if constexpr (GQ >= 0) tie(gad);
        f32x4 acc[8];
        bf16x4 FA[8][2]; bf16x8 FB[8];
        auto op = [&](auto gc) {
            constexpr Op o = cv_op(CV(gc));
            if constexpr (o.kind == CO_A) { constexpr int s = o.a >> 3, n = o.a & 7, d = (n & 1) * 128 + s * 16 + o.b * 2; dsr2<d, d + 1>(FA[n][o.b], ka[n >> 1]); }
            if constexpr (o.kind == CO_B) { constexpr int s = o.a >> 3, m = o.a & 7; dsr128<2048 * m + 1792 - 256 * s>(FB[m], ub[s & 3]); }
            if constexpr (o.kind == CO_C) dsr128<(o.a & 1) * 4096>(cf[o.a * 4 + o.b], cad[o.b]);
        };
        static_for<-36, 0>(op);
        static_for<0, 288>([&](auto gc) {
            constexpr int G = CV(gc), s = G / 36, q = G % 36, n = cv_n(q), m = cv_m(q);
            if constexpr (cv_wait(G) >= 0) wait_lr<cv_wait(G)>();
            if constexpr (q == 24 && s >= CL && s - CL < 8) static_for<0, 4>([&](auto ic) { tie(cf[(s - CL) * 4 + CV(ic)]); });
            if constexpr (ZM && G < 8) mfma<true>(acc[m], __builtin_bit_cast(bf16x8, ad), FB[m]);
            mfma<s == 0 && n == 0 && !ZM>(acc[n + m], cat(FA[n][0], FA[n][1]), FB[m]);
            sched();
            if constexpr (q == 11) TL(2 + 2 * s);
            if constexpr (q == 11 && s >= CL && s - CL < 8) wait_v<(s - CL < 7 ? 4 : 0) + (s - CL < (CL <= 1) + (CL <= 0) ? XV : 0) + 8 * (PFQ >= 3 && (s == PFQ - 2 || s == PFQ - 1))>();
            if constexpr (q == 11) TL(3 + 2 * s);
            op(gc);
            if constexpr (q == 30 && ((CTM >> (16 + s)) & 1)) ctouch(16 + s);
            if constexpr (G == 16) hop<17>();
            if constexpr (G == 66) hop<19>();
            if constexpr (G == PG1) page<21>();
            if constexpr (G == PG2) page<28>();
            if constexpr (G == 158) hop<23>();
            if constexpr (G == 209) hop<25>();
            if constexpr (G == 254) hop<27>();
            if constexpr (G == GQ) { unsigned f = 1; zero_chk<4, 1>(f, gad); }
            if constexpr (!NB && G == CBQ) barrier();
            if constexpr (NB == 3 && G == 23) { unsigned f = 1; zero_chk<8, 1>(f, lbase + DONE + (lane & 3) * FST); }
            if constexpr ((q == 24 || q == 25 || q == 29 || q == 34) && s + 2 - CL < 8) {
                constexpr int pc = q == 24 ? 0 : q == 25 ? 1 : q == 29 ? 2 : 3, u = s + 2 - CL;
                dma4<(u & 1) * 4096, pc>(cst, vc[pc], rsc, c_off(lw, u));
            }
            sched();
        });
        if constexpr (PG2 >= 288) page<28>();
        TL(18);
        TS(3);
        {
            const __amdgpu_buffer_rsrc_t rz = mk_brs(ws + WS_ZT, 4 << 20);
            if constexpr (ZM) {
                asm volatile("s_nop 7");
#pragma unroll
                for (int jj = 0; jj < 4; ++jj) {
                    tie(acc[2 * jj]); tie(acc[2 * jj + 1]);
                    const f32x4 a = acc[2 * jj], c = acc[2 * jj + 1];
                    st16(u32x4{pack2(a[0], a[1]), pack2(a[2], a[3]), pack2(c[0], c[1]), pack2(c[2], c[3])}, rz, lane * 16 + jj * 1024, (ch & 510) * 8192 + nx + (ch & 1) * 4096);
                }
            } else {
            u32x2 kvr[8];
            const unsigned kva = lbase + U0 + 2048 + 128 * l15 + 64 * (g4 >> 1) + ((lw ^ ((l15 >> 1) & 3)) << 4) + 8 * (1 - (g4 & 1));
            static_for<0, 8>([&](auto ic) { dsr64<CV(ic) * 2048>(kvr[CV(ic)], kva); });
            wait_l<0>();
            const float Dd = (ch & 1) ? bfhi(dw) : bflo(dw);
#pragma unroll
            for (int jj = 0; jj < 4; ++jj) {
                u32x4 o;
#pragma unroll
                for (int u = 0; u < 2; ++u) {
                    const int j = 2 * jj + u;
                    tie(kvr[j]); tie(acc[j]);
                    const float kv3 = bflo(kvr[j][0]), kv2 = bfhi(kvr[j][0]), kv1 = bflo(kvr[j][1]), kv0 = bfhi(kvr[j][1]);
                    o[2 * u] = pack2(rbf(acc[j][0]) + rbf(kv0 * Dd), rbf(acc[j][1]) + rbf(kv1 * Dd));
                    o[2 * u + 1] = pack2(rbf(acc[j][2]) + rbf(kv2 * Dd), rbf(acc[j][3]) + rbf(kv3 * Dd));
                }
                st16(o, rz, lane * 16 + jj * 1024, (ch & 510) * 8192 + nx + (ch & 1) * 4096);
                if (jj == 1) { sched(); hop<29>(); sched(); }
            }
            }
            TS(8);
            hop<30>();
            if constexpr (CL > 0) {
                wait_v<4>();
                static_for<32 - 4 * CL, 32>([&](auto ic) { constexpr int i = CV(ic); dsr128<((i >> 2) & 1) * 4096>(cf[i], cad[i & 3]); });
                wait_l<0>();
                static_for<32 - 4 * CL, 32>([&](auto ic) { tie(cf[CV(ic)]); });
            }
        }
        TL(19);
        TS(4);
    } else {
        f32x4 acc[8][2];
        cf32 L, cc, M, M2, MG, NG, N4, N8, ag;
        {
            unsigned aad[4], bad[4];
#pragma unroll
            for (int i = 0; i < 4; ++i) { aad[i] = lbase + frag(i & 1, i >> 1); bad[i] = aad[i] + 16384 + wid * 4096; }
            const unsigned fr = lbase + FULL + (lane & 3) * FST;
            sld(tag, ws + WS_F + F_EP);
            bf16x8 fa[4][2], fb[2][4];
            unsigned fl = 1, dz = 0, dw_ = lbase + DONE + wid * FST + lane * 4, vt = lbase + POLL + wid * 256 + lane * 4;
            tie(dz); tie(dw_); tie(vt);
            auto op = [&](auto gc) {
                constexpr int G = CV(gc);
                constexpr Op o = g1_op(G);
                if constexpr (o.kind == OP_A) {
                    constexpr int bo = g1_buf(o.a >> 3), hi = bo >= 65536 ? 65536 : 0, i = o.a & 7;
                    dsr128<bo - hi + (i >> 1) * 4096>(fa[i & 3][o.b], aad[(i & 1) + 2 * o.b] + hi);
                }
                if constexpr (o.kind == OP_B) { constexpr int bo = g1_buf(o.a), hi = bo >= 65536 ? 65536 : 0; dsr128<bo - hi>(fb[o.a & 1][o.b], bad[o.b] + hi); }
                if constexpr (o.kind == OP_FLAG) dsr32<(30 - (G >> 5)) * 4>(fl, fr);
                if constexpr (o.kind == OP_SIG) dsw32<o.a * 4>(dw_, dz);
                if constexpr (o.kind == OP_VOTE) dsr32<0>(vt, vt);
            };
            auto consts = [&]() {
                float ld, ar, ai;
                asm volatile("s_waitcnt vmcnt(9)\n\tv_lshlrev_b32 %0, 16, %3\n\tv_lshlrev_b32 %1, 16, %4\n\tv_lshlrev_b32 %2, 16, %5"
                             : "=&v"(ld), "=&v"(ar), "=&v"(ai) : "v"(c_dt), "v"(c_re), "v"(c_im) : "memory");
                const float dtv = rbf(expf(ld)), ea = expf(dtv * ar);
                float sn, cs;
                sincosf(dtv * ai, &sn, &cs);
                L = cf32{ea * cs, ea * sn};
                M = L;
#pragma unroll
                for (int e = 0; e < 5; ++e) M = cmul(M, M);
                M2 = cmul(M, M);
                const cf32 N = cmul(M2, M2), N2 = cmul(N, N), one = {1.f, 0.f};
                N4 = cmul(N2, N2); N8 = cmul(N4, N4);
                auto sel = [](bool c, cf32 a, cf32 b) { return cf32{c ? a.r : b.r, c ? a.i : b.i}; };
                MG = cmul(sel(g4 & 1, M, one), sel(g4 & 2, M2, one));
                NG = cmul(sel(g4 & 1, N, one), sel(g4 & 2, N2, one));
                const float nm = L.r - 1.f, den = ar * ar + ai * ai;
                cc = cf32{2.f * ((nm * ar + L.i * ai) / den), 2.f * ((L.i * ar - nm * ai) / den)};
                auto tc = [&](cf32& v) { tie(v.r); tie(v.i); };
                tc(L); tc(cc); tc(M); tc(M2); tc(MG); tc(NG); tc(N4); tc(N8);
                sched();
            };
            if constexpr (CQ < 0) consts();
            __builtin_amdgcn_s_setprio(CP);
            zero_chk<0>(fl, lbase + POLL + (lane & 3) * 256);
            ties(tag);
            tag += 0x01010101u;
            TS(1);
            static_for<-12, 0>(op);
            static_for<0, 512>([&](auto gc) {
                constexpr int G = CV(gc), st = G >> 5, i = (G >> 2) & 7, j = G & 3;
                if constexpr ((G == 0 || j == 2) && G <= 506) { static_assert(g1_wait(G) >= 0, ""); wait_lr<G == 506 ? 0 : g1_wait(G)>(); }
                if constexpr (G == CQ) { asm volatile("s_waitcnt lgkmcnt(0)\n\ts_nop 7" ::: "memory"); consts(); }
                if constexpr (G == HQ) hop_all(wid);
                if constexpr ((G & 31) == 20 && st < 15) zero_chk<(30 - st) * 4>(fl, fr);
                if constexpr (((G & 31) == 30 && st < 13) || (NB < 2 && G == B13)) barrier();
                if constexpr ((G & 31) == 20) TL(st);
                mfma<st == 0 && j < 2>(acc[i][j & 1], fa[i & 3][j >> 1], fb[st & 1][j]);
                sched();
                op(gc);
                if constexpr (G == GCR + 16) { unsigned n; nr = vote_cnt(vt, tag, (xcd & 4) << 11, n); }
                if constexpr (G == 31) page<-1>();
                if constexpr (G == 95) hop<2>();
                if constexpr (G == 159) hop<4>();
                if constexpr (G == 207) hop<5>();
                if constexpr (G == 255) page<7>();
                if constexpr (G == 319) hop<8>();
                if constexpr (G == 383) hop<10>();
                if constexpr (G == 431) hop<11>();
                if constexpr (G == 479) page<13>();
                if constexpr (G == 509) hop<15>();
                if constexpr (G == 489) hop<40>();
                if constexpr (G == 494) hop<41>();
                if constexpr (G == 499) hop<42>();
                if constexpr (G == 504) hop<43>();
                if constexpr (G >= 487) {
                    constexpr int k = G - 486;
                    if constexpr (k == 1) { asm volatile("s_nop 3"); ag = cf32{acc[0][0][0], acc[0][1][0]}; }
                    cf32 t;
                    asm volatile("v_fma_f32 %0, -%3, %5, %6\n\tv_fma_f32 %1, %3, %4, %7\n\tv_fmac_f32 %0, %2, %4\n\tv_fmac_f32 %1, %2, %5"
                                 : "=&v"(t.r), "=&v"(t.i) : "v"(L.r), "v"(L.i), "v"(ag.r), "v"(ag.i), "v"(acc[k >> 2][0][k & 3]), "v"(acc[k >> 2][1][k & 3]));
                    ag = t;
                }
                sched();
            });
        }
        __builtin_amdgcn_s_setprio(3);
        TL(16);
        TS(2);
#pragma unroll
        for (int k = 26; k < 32; ++k) ag = cfma(L, ag, cf32{acc[k >> 2][0][k & 3], acc[k >> 2][1][k & 3]});
        tie(ag.r); tie(ag.i); sched(); hop<44>(); sched();
        cf32 ev_, od, p01, p23;
        sw16(ag.r, ev_.r, od.r); sw16(ag.i, ev_.i, od.i);
        const cf32 pp = cfma(M, ev_, od);
        sw32(pp.r, p01.r, p23.r); sw32(pp.i, p01.i, p23.i);
        cf32 E = cfma(M2, p01, p23);

        const __amdgpu_buffer_rsrc_t rse = mk_brs(ws + WS_E + tn * 16384 + wid * 2048, 16384);
        st16(u32x4{f2u(E.r), f2u(E.i), tag, ~tag}, rse, g4 ? OOB : l15 * 16, nr + (tm & 7) * 256);
        sched();
        TL(17);
        TS(3);
        hop<45>();
        sched();
        c_setup();
#pragma unroll
        for (int i = 0; i < 4; ++i) cad[i] = lb + frag(i & 1, i >> 1);
        auto c_read = [&](auto rc) { static_for<0, 8>([&](auto ic) { constexpr int r = CV(rc), i = CV(ic); dsr128<(i >> 2) * 4096>(cf[r * 8 + i], cad[i & 3] + ((r & 1) ? 0 : 2 * BUF)); }); };
        auto c_tie = [&](auto rc) { static_for<0, 8>([&](auto ic) { tie(cf[CV(rc) * 8 + CV(ic)]); }); };
        auto c_unit = [&](auto uc) { static_for<0, 4>([&](auto ic) { constexpr int u = CV(uc), i = CV(ic); dma4<((u & 2) ? 0 : 2 * BUF) + (u & 1) * 4096, i>(lb, vc[i], rsc, c_off(wid, u)); }); };
        auto c_req = [&](auto rc) { c_unit(IC(CV(rc) * 2)); c_unit(IC(CV(rc) * 2 + 1)); };
        const float c2r = cc.r, c2i = cc.i;
        auto scale = [&](int i) {
#pragma unroll
            for (int j = 0; j < 4; ++j) {
                const float br = acc[i][0][j], bi = acc[i][1][j];
                acc[i][0][j] = fma_(c2r, br, -(c2i * bi)); acc[i][1][j] = fma_(-c2r, bi, -(c2i * br));
            }
            tie(acc[i][0]); tie(acc[i][1]);
        };
        auto scales = [&](auto ac, auto bc) {
            static_for<CV(ac), CV(bc)>([&](auto ic) {
                constexpr int i = CV(ic);
                scale(i);
                if constexpr (i & 1) { sched(); hop<i == 1 ? 46 : i == 3 ? 16 : i == 5 ? 47 : 48>(); sched(); }
            });
        };
        constexpr int CA = CVR == 0 ? 0 : CVR == 4 ? 1 : 2;
        if constexpr (NB) {
            unsigned dn = 1;
            scales(IC(0), IC(DP));
            if constexpr (CA) { zero_chk<8>(dn, lbase + DONE + (lane & 3) * FST); static_for<0, CA>(c_unit); }
            if constexpr (SPR != 3) __builtin_amdgcn_s_setprio(SPR);
            sched();
            scales(IC(DP), IC(ELP));
            if constexpr (!CA) zero_chk<8>(dn, lbase + DONE + (lane & 3) * FST);
        } else {
            scales(IC(0), IC(ELP));
            barrier();
            static_for<0, CA>(c_unit);
        }
        TL(18);
        u32x4 ev[4]; unsigned eo[4]; unsigned long long none[4];
#pragma unroll
        for (int i = 0; i < 4; ++i) {
            const int t = tm - 1 - 4 * i - g4;
            eo[i] = t < 0 ? OOB : ((((t ^ tm) & 8) << 10) ^ nr) + (t & 7) * 256 + l15 * 16; none[i] = __builtin_amdgcn_ballot_w64(t < 0);
            asm volatile("buffer_load_dwordx4 %0, %1, %2, 0 offen sc1" : "=v"(ev[i]) : "v"(eo[i]), "s"(mk_rsrc(ws + WS_E + tn * 16384 + wid * 2048, 16384)) : "memory");
        }
        asm volatile("" ::: "memory");
        sched();
        constexpr int CB = CVR == 0 ? 1 : CVR == 4 ? 2 : 4;
        static_for<CA, CB>(c_unit);
        scales(IC(ELP), IC(8));
        cf32 X = cfma(cf32{g4 == 3 ? M.r : g4 == 2 ? 1.f : 0.f, g4 == 3 ? M.i : 0.f}, p01, cf32{(g4 & 1) ? ev_.r : 0.f, (g4 & 1) ? ev_.i : 0.f});
        tie(X.r); tie(X.i);
        sched();
        hop<24>();
        if constexpr (SPR != 3) __builtin_amdgcn_s_setprio(3);
        if constexpr (CVR == 2) { wait_v<12>(); c_read(IC(0)); wait_l<0>(); c_tie(IC(0)); c_req(IC(2)); }
        wait_v<4 * (CB - CA) + 8 * (CVR == 2)>();
        TL(19);
        tie(ev[0]); tie(ev[1]); tie(ev[2]); tie(ev[3]);
        {
            const unsigned long long tag64 = (unsigned long long)tag | ((unsigned long long)~tag << 32);
#pragma unroll 1
            for (unsigned spins = 0; spins < SPIN_CAP; ++spins) {
                unsigned long long ok = ~0ull;
#pragma unroll
                for (int i = 0; i < 4; ++i) ok &= __builtin_amdgcn_ballot_w64(((unsigned long long)ev[i][2] | ((unsigned long long)ev[i][3] << 32)) == tag64) | none[i];
                TSV(11, spins);
                if (ok == ~0ull) break;
                asm volatile("buffer_load_dwordx4 %0, %4, %8, 0 offen sc1\n\tbuffer_load_dwordx4 %1, %5, %8, 0 offen sc1\n\tbuffer_load_dwordx4 %2, %6, %8, 0 offen sc1\n\t"
                             "buffer_load_dwordx4 %3, %7, %8, 0 offen sc1\n\ts_waitcnt vmcnt(0)" : "=&v"(ev[0]), "=&v"(ev[1]), "=&v"(ev[2]), "=&v"(ev[3])
                             : "v"(eo[0]), "v"(eo[1]), "v"(eo[2]), "v"(eo[3]), "s"(mk_rsrc(ws + WS_E + tn * 16384 + wid * 2048, 16384)) : "memory");
            }
        }
        page<18>();
        cf32 h;
        {
            auto e = [&](int i) { return cf32{u2f(ev[i][0]), u2f(ev[i][1])}; };
            cf32 c = cmul(NG, cfma(N8, cfma(N4, e(3), e(2)), cfma(N4, e(1), e(0)))), u, v;
            sw16(c.r, u.r, v.r); sw16(c.i, u.i, v.i);
            sw32(u.r + v.r, u.r, v.r); sw32(u.i + v.i, u.i, v.i);
            c = cfma(MG, cf32{u.r + v.r, u.i + v.i}, X);
            h = cf32{fma_(c2r, c.r, -(c2i * c.i)), fma_(-c2r, c.i, -(c2i * c.r))};
        }
        TS(4);
        {
            const __amdgpu_buffer_rsrc_t rh = mk_brs(ws + WS_H + (size_t)((tm & 7) * 16 + tn) * 65536 + (nr ^ ((tm & 2) << 12)) + wid * 2048, 65536);
            const unsigned ho = g4 * 256 + ((l15 ^ (4 * (g4 & 1))) << 4);
            const cf32 Lc = {L.r, -L.i};
            static_for<0, 4>([&](auto qc) {
                constexpr int q = CV(qc);
                if constexpr (q >= CB) c_unit(qc);
                if constexpr (q == 2) hop<20>();
                u32x4 wr, wi;
#pragma unroll
                for (int u = 0; u < 4; ++u) {
                    const int i = 2 * q + (u >> 1), j = 2 * (u & 1);
                    const cf32 h0 = cfma(Lc, h, cf32{acc[i][0][j], acc[i][1][j]});
                    h = cfma(Lc, h0, cf32{acc[i][0][j + 1], acc[i][1][j + 1]});
                    wr[u] = pack2(h0.r, h.r); wi[u] = pack2(h0.i, h.i);
                }
                if constexpr ((PRE == 2 || GQ >= 0) && q == GE) { tie(wr); tie(wi); dsw32<4>(lbase + DONE + wid * FST + lane * 4, 0u); }
                __builtin_amdgcn_raw_buffer_store_b128(wr, rh, ho, q * 16384, 16);
                st16(wi, rh, ho + 1024, q * 16384); keep(wr);
                sched();
            });
        }
        TS(10);
        auto flag = [&]() { __builtin_amdgcn_raw_buffer_store_b64(u32x2{tag, ~tag}, rf, lane ? OOB : 0u, F_H + nr + (((tm & 7) * 16 + tn) * 4 + wid) * 8, 16); TS(5); };
        hop<22>();
        if constexpr (CVR == 2) {
            wait_v<16>();
            c_read(IC(1));
            wait_l<0>();
            c_req(IC(3));
            wait_v<8>();
            flag();
        } else {
            if constexpr (CVR == 1) wait_v<8>(); else { wait_v<0>(); flag(); }
            c_read(IC(0)); c_read(IC(1));
            wait_l<8>();
            c_req(IC(2));
            wait_l<0>();
            c_req(IC(3));
            if constexpr (CVR == 1) { wait_v<16>(); flag(); }
        }
        c_tie(IC(0)); c_tie(IC(1));
        static_for<16, 32>([&](auto ic) { undef(cf[CV(ic)]); });
    }
    {
        unsigned ln = lane, dsw_, pf;
        tie(ln);
        const int wid = fresh(wid_), lw = wid & 3, kg = wid >> 2, b = fresh(b_), xcd = b & 7, idx = b >> 3, tq = (idx & 7) + JR(idx >> 3);
        nr = fresh(__builtin_amdgcn_readfirstlane(nr));
        const unsigned nx = nr ^ ((xcd & 2) << 12), nh = nr ^ ((idx & 2) << 12), hp = (idx & 7) * 16 + (b & 1) * 8;
        const int tm2 = (xcd & 4) * 2 + (idx & 7), dt = (xcd & 2) * 2 + (idx >> 3), qx = ((~b & 1) * 2) ^ JQ(idx >> 3);
        const int ta = 512 * (idx >> 3) + 128 * (idx & 1) + 16 * wid + 4 * ((idx >> 1) & 3);
        const int tb = tm2 * 128 + 64 * kg;
        dsr32<DSK - 131072>(dsw_, lbase + 131072 + lw * 64 + (ln & 7) * 8 + ((ln >> 1) & 4));
        unsigned wl = lbase + wid * 4096, w2k = lbase + wid * 2048, hs0 = (hp + ((tq + (wid >> 1)) & 7)) * 65536 + nh + (wid & 1) * 4096 + qx * 16384;
        unsigned s_x = (tb + qx * 8) * 2048 + dt * 256 + lw * 64, s_q = ta * 2048 + xcd * 256, s_z = WS_ZT + (xcd & 3) * 1048576 + nx + idx * 128 + wid * 131072, zfo = WS_F + F_Z + (xcd & 2) * 8192 + nr + (xcd & 1) * 4096;
        unsigned ppo = PK ? WS_KVT + (xcd & 3) * 1048576 + idx * 32768 + kg * 16384 + (KVS ? nx : ((xcd ^ wid) & 4) << 11) + lw * 2048
                          : WS_PP + (xcd & 2) * 1048576 + idx * 65536 + (xcd & 1) * 32768 + kg * 16384 + nx + lw * 2048, s_a = ta * 8192 + xcd * 512, s_b = (tb + (2 ^ qx) * 8) * 8192 + 4096 + dt * 512 + lw * 128;
        const unsigned tagg = fresh(__builtin_amdgcn_readfirstlane(tag));
        u32x4 rsx = mk_rsrc(x, (NOX & 1) ? 0 : 4 << 20), rsq = mk_rsrc(qin, (NOX & 2) ? 0 : 4 << 20), rso = mk_rsrc(out, 16 << 20), rws = mk_rsrc(ws, 32 << 20);
        ties(wl); ties(w2k); ties(hs0); ties(s_x); ties(s_q); ties(s_z); ties(zfo); ties(ppo); ties(s_a); ties(s_b);
        ties(rsx); ties(rsq); ties(rso); ties(rws);
        const unsigned l31 = ln & 31, l5 = ln >> 5;
        unsigned l16 = ln * 16, ha[8];
        ha[0] = (lbase + kg * 1536 + l5 * 2048 + (ln & 2) * 128 + (ln & 1) * 8) | ((((ln >> 1) & 8) | ((ln >> 2) & 3) | ((ln & 2) << 1)) << 4);
        ha[1] = ha[0] ^ 64; ha[2] = ha[0] ^ 512; ha[3] = ha[0] ^ 576;
        static_for<0, 4>([&](auto ic) { ha[4 + CV(ic)] = ha[CV(ic)] + 65536; tie(ha[CV(ic)]); tie(ha[4 + CV(ic)]); });
        unsigned a_x = ((((ln >> 2) & 7) | (ln & 32)) << 11) | (l16 & 48), a_xr = (((l16 & 240) << 2) | (ln & 48)) + (w2k + ZST);
        unsigned a_q = ((ln & 48) << 7) | (l16 & 240), a_qr = (l5 << 10) + l31 * 8 + (w2k + QST), a_iw = w2k + IMG + l16, a_ir = a_iw ^ 8192;
        unsigned a_t = wid == 0 && ln == 0 ? idx * 128 : OOB, msk = 0xfff0fff0u;
        unsigned a_f = wid == 1 ? (ln >> 1) * 128 + (ln & 1) * 4 : OOB, expect = wid == 1 ? ((ln & 1) ? ~tagg : tagg) : 0u;
        unsigned a_z = (((ln ^ l5 ^ (wid * 4)) & 7) << 4) | ((ln & 48) << 10) | ((ln & 8) << 9), a_z2 = a_z ^ 32, a_zr = (l31 << 9) + (((((wid ^ ln) & 7) << 1) | l5) << 3) + (lbase + ZST);
        unsigned a_oa = (l5 << 21) | (l31 << 4), a_ob = ((((ln >> 2) & 4) | (ln & 32) | ((ln >> 3) & 1)) << 13) | (l16 & 112);
        unsigned a_p = PB ? (ln == 63 ? 1020u : OOB) : l16, a_w = ln < 16 ? ln * 128 : OOB;
        if constexpr (PB) tie(a_p);
        if constexpr (P_W >= -1) tie(a_w);
        if constexpr (P_W == -1) pf_ld(pf, a_w, rws, ppo);
        u32x2 tg2 = {tagg, ~tagg};
        tie(l16); tie(a_x); tie(a_xr); tie(a_q); tie(a_qr); tie(a_iw); tie(a_ir); tie(a_t); tie(msk); tie(a_f); tie(expect); tie(a_z); tie(a_z2); tie(a_zr);
        tie(a_oa); tie(a_ob); tie(tg2);
        wait_l<0>();
        tie(dsw_);
        u32x4 dg[2];
        static_for<0, 4>([&](auto ic) { constexpr unsigned long long k = diag_mask(CV(ic)); dg[0][CV(ic)] = sel0(dsw_ & 0xffffu, k); dg[1][CV(ic)] = sel0(dsw_ & 0xffff0000u, k); });
        tie(dg[0]); tie(dg[1]);
        unsigned fso, xso, ppi = ppo ^ (PK ? 1048576 : 32768), tn_[3] = {tagg & 0xf000fu, (tagg >> 8) & 0xf000fu, (tagg >> 4) & 0xf000fu}, psel = 0x05040100u;
        unsigned long long lo8 = 0x00ff00ff00ff00ffull, hi8 = ~lo8;
        ties(ppi); ties(tn_[0]); ties(tn_[1]); ties(tn_[2]); ties(psel); ties(lo8); ties(hi8);
        auto fill = [&](auto sc, auto pc) {
            constexpr int s = CV(sc), p = CV(pc);
            if constexpr (p == 0) dma_x<(2 * s % 3) * BUF, (s >> 1) * 16384 + (s & 1) * 262144, true>(fso, wl, l16, rws, hs0);
            else if constexpr (fill_m0(4 * s + p)) dma_m<(2 * s % 3) * BUF, p * 1024>(fso, wl, l16, rws);
            else dma_n<p * 1024>(fso, l16, rws);
        };
        bf16x4 hf[2][8];
        auto ldh = [&](auto nc, auto ic) {
            constexpr int n = CV(nc), i = CV(ic), j = (i >> 1) & 1, bo = (2 * (n >> 2) % 3) * BUF + (g2_unit(n, j) & 3) * 8192 + (i >> 2) * 4096, hi = bo >= 65536 ? 65536 : 0;
            dsr64tr<bo - hi>(hf[n & 1][i], ha[2 * g2_tile(n, j) + (i & 1) + (hi >> 14)]);
        };
        f32x4 yc[2][2][2];
        f32x4 im[2], ao, ob[2]; u32x4 pl[2], po;
        bf16x8 xf[2];
        u32x2 zv[4], qv[2];
        unsigned ck, ex, fail[2];
        float tq_[2], tz, sm[8], mx[2], cv[8];
        auto finB = [&](auto pc, auto jc) {
            constexpr int p = CV(pc), j = CV(jc);
            if constexpr (j < 8) { yc[p & 1][0][j & 1][j >> 1] += im[j & 1][j >> 1]; tie(yc[p & 1][0][j & 1]); }
            else {
                constexpr int i = (j - 8) >> 2, k = j & 3, dd = k & 1, h = k >> 1;
                ob[i][k] = sel8(yc[p & 1][0][dd][2 * i + 1 - h], yc[p & 1][0][dd][2 * i + h], h ? hi8 : lo8);
                if constexpr (k == 3 && (p == 3 || !(NOST & 4))) gst<0>(ob[i], a_ob, rso, s_b ^ ((p & 1) * 65536 + i * 16384));
                if constexpr (j == 12) keep(ob[0]);
            }
        };
        auto snd = [&](auto pc, auto jc) {
            constexpr int p = CV(pc), j = CV(jc);
            if constexpr (j < 8) { sm[j] = yc[p][0][j >> 2][j & 3] + im[j >> 2][j & 3]; tie(sm[j]); }
            if constexpr (j == 8) mx[0] = amax3(sm[0], sm[1], sm[2]);
            if constexpr (j == 9) mx[1] = amax3(sm[3], sm[4], sm[5]);
            if constexpr (j == 10) mx[0] = amax3(mx[0], sm[6], sm[7]);
            if constexpr (j == 11) mx[0] = amax3(mx[0], mx[1], mx[1]);
            if constexpr (j == 12) { ex = f2u(mx[0]) + 0x4000u; tie(ex); }
            if constexpr (j == 13) { ex &= 0x7f800000u; tie(ex); }
            if constexpr (j == 14) { mx[0] = u2f(ex + 0x04c00008u); tie(mx[0]); }
            if constexpr (j == 15) { ex >>= 23; tie(ex); }
            if constexpr (j >= 16 && j < 24) { sm[j - 16] += mx[0]; tie(sm[j - 16]); }
            if constexpr (j == 24) { ex |= ex << 12; tie(ex); }
            if constexpr (j >= 25 && j < 29) { po[j - 25] = __builtin_amdgcn_perm(f2u(sm[2 * (j - 25) + 1]), f2u(sm[2 * (j - 25)]), psel); tie(po); }
            if constexpr (j == 29) { po[0] = bfi_v(msk, po[0], ex); tie(po); }
            if constexpr (j >= 30 && j < NS) { po[j - 29] = bfi_s(msk, po[j - 29], tn_[j - 30]); if constexpr (j < NS - 1) tie(po); }
            if constexpr (j == NS - 1 && !(NOST & 2)) gst<p * 1024>(po, l16, rws, ppo);
        };
        auto rcv = [&](auto hc, auto jc) {
            constexpr int h = CV(hc), j = CV(jc);
            if constexpr (j < 3) { pl[h][j + 1] ^= tn_[j]; pp_tie<h>(pl[h]); }
            if constexpr (j == 3) { ck = pl[h][1] | pl[h][2] | pl[h][3]; tie(ck); }
            if constexpr (j == 4) { if constexpr (PB) ck = bfi_f(msk, fail[h], ck); else ck &= 0xf000fu; tie(ck); }
            if constexpr (j == 6 && !(NOST & 2)) pp_chk<h>(pl[h], ck, tagg, l16, rws, ppi, a_p);
            if constexpr (j == 7) { ex = pl[h][0] >> 12; tie(ex); }
            if constexpr (j == 8) ex = bfi_v(msk, ex, pl[h][0]);
            if constexpr (j == 9) { pl[h][0] &= msk; pp_tie<h>(pl[h]); }
            if constexpr (j == 10) { ex <<= 23; tie(ex); }
            if constexpr (j == 11) { ex &= 0x7f800000u; tie(ex); }
            if constexpr (j == 12) { mx[1] = u2f(ex) * 0x1p-14f; tie(mx[1]); }
            if constexpr (h == 0 && j >= 13) {
                constexpr int k = j - 13, a = (k + 1) / 2, e = k == 15 ? 7 : k / 2 - 1;
                if constexpr (k == 0 || ((k & 1) && k < 15)) cv[a] = cvt16<a & 1>(pl[h][a >> 1]);
                else { yc[0][0][e >> 2][e & 3] = fma_(cv[e], mx[1], yc[0][0][e >> 2][e & 3]); tie(yc[0][0][e >> 2]); }
            }
            if constexpr (h == 1 && j >= 13 && j < 21) cv[j - 13] = cvt16<(j - 13) & 1>(pl[h][(j - 13) >> 1]);
            if constexpr (h == 1 && j >= 21) { constexpr int e = j - 21; im[e >> 2][e & 3] = fma_(cv[e], mx[1], im[e >> 2][e & 3]); tie(im[e >> 2]); }
        };
        auto side = [&](auto pc) {
            constexpr int P = CV(pc), n = P >> 3, m = P & 7, p = n >> 3;
            if constexpr (P == P_X || P == P_X + 2) dma_x<ZST + (P != P_X) * 1024, (P != P_X) * 16384, false>(xso, w2k, a_x, rsx, s_x);
            if constexpr (P == P_W) pf_ld(pf, a_w, rws, ppo);
            if constexpr (!NOA) {
            if constexpr (P == P_Q || P == P_Q + 2) dma_x<QST + (P != P_Q) * 1024, (P != P_Q) * 524288, false>(xso, w2k, a_q, rsq, s_q);
            if constexpr (P == P_T) gst<0>(tg2, a_t, rws, zfo);
            if constexpr (P == AT(ZP, 4)) { if constexpr (P_W >= 0) pf_keep(pf); pf_ld(pf, a_f, rws, zfo); }
            if constexpr (P == P_ZC) { wait_v<g2_allow(P, AT(ZP, 4))>(); tag_chk(pf, expect, a_f, rws, zfo); }
            if constexpr (P == P_Z || P == P_Z + 2) dma_x<ZST + (P != P_Z) * 1024, (P != P_Z) * 65536, true>(xso, w2k, P == P_Z ? a_z : a_z2, rws, s_z);
            }
            if constexpr (P == AT(7, 5) || P == AT(7, 7)) dsr128<(m - 5) * 512>(xf[(m - 5) / 2], a_xr);
            if constexpr (P == AT(8, 4)) { tie(xf[0]); tie(xf[1]); }
            if constexpr (P == AT(8, 5) || P == AT(9, 3)) static_for<0, 2>([&](auto dc) { mfma<false>(yc[n & 1][0][CV(dc)], xf[n & 1], __builtin_bit_cast(bf16x8, dg[CV(dc)])); });
            if constexpr ((n & 7) == 6 && m == XW && !(DBG & 2)) dsw128<0>(a_iw, yc[p & 1][1][0]);
            if constexpr ((n & 7) == 6 && m == XW2 && !(DBG & 2)) dsw128<1024>(a_iw, yc[p & 1][1][1]);
            if constexpr ((n & 7) == 7 && (m & 5) == (n < 31 ? 4 : 0)) dsr128<(m & 2) * 512>(im[(m >> 1) & 1], a_ir);
            if constexpr (n >= 8 && (n & 7) == 0 && m == 4) { tie(im[0]); tie(im[1]); }
            if constexpr (P >= P_PP0 && P <= E_PP0) snd(IC(0), IC(P - P_PP0));
            if constexpr (P >= P_PP1 && P <= E_PP1) static_for<0, D1>([&](auto ic) { snd(IC(1), IC((P - P_PP1) * D1 + CV(ic))); });
            if constexpr (PB && (P == P_B0 - 1 || P == P_B1 - 1)) pf_zero(pf);
            if constexpr (PB && (P == P_B0 || P == P_B1)) pf_at<(P == P_B1) * 1024>(pf, a_p, rws, ppi);
            if constexpr (P == P_L0 || P == P_L1) {
                unsigned so = ppi;
                if constexpr (PB) { wait_v<g2_allow(P, P - QB) + (g2_fill(P) >= 0)>(); pp_sel(so, fail[P == P_L1], pf, tn_[2], ppi, ppo); }
                pp_ld<P == P_L1>(pl[P == P_L1], l16, rws, so);
            }
            if constexpr (P == P_R0 || P == P_R1) { wait_v<g2_allow(P, P == P_R0 ? imax(P_L0, XL ? P_X + 2 : 0) : P_L1)>(); pp_tie<P == P_R1>(pl[P == P_R1]); }
            if constexpr (P >= P_R0 && P <= E_R0) static_for<0, D0>([&](auto ic) { if constexpr ((P - P_R0) * D0 + CV(ic) < NR) rcv(IC(0), IC((P - P_R0) * D0 + CV(ic))); });
            if constexpr (P >= P_R1 && P < P_R1 + 21) rcv(IC(1), IC(P - P_R1));
            if constexpr (!NOA) {
            if constexpr (n == ZW && m >= 4) dsr64<(m - 4) * 128>(zv[m - 4], a_zr);
            static_for<0, 4>([&](auto rc) {
                constexpr int r = CV(rc);
                if constexpr (P == q_tie(r)) tie(qv[r & 1]);
                if constexpr (P == q_rd(r)) dsr64<r * 256>(qv[r & 1], a_qr);
            });
            if constexpr (P == AT(ZW + 1, 4)) { tie(zv[0]); tie(zv[1]); tie(zv[2]); tie(zv[3]); }
            }
            if constexpr (a_step(P) >= 0) {
                constexpr int s = a_step(P), e = s == 0 ? 0 : s % 3 == 2 ? (s + 1) / 3 : s % 3 == 1 ? s / 3 : s / 3 - 1, r = e >> 2, c = e & 3;
                if constexpr (s == 0 || (s % 3 == 2 && e < 16)) { tq_[e & 1] = (c & 1) ? bfhi(qv[r & 1][c >> 1]) : bflo(qv[r & 1][c >> 1]); tie(tq_[e & 1]); }
                else if constexpr (s % 3 == 1) { tz = (r & 1) ? bfhi(zv[c][r >> 1]) : bflo(zv[c][r >> 1]); tie(tz); }
                else if constexpr (s % 3 == 0) {
                    ao[c] = tq_[e & 1] * tz;
                    if constexpr (c == 3 && !(NOST & 1)) gst<0>(ao, a_oa, rso, s_a + r * 8192);
                    else tie(ao);
                }
            }
            if constexpr (P >= P_F2 && P < P_F2 + 16) finB(IC(2), IC(P - P_F2));
            if constexpr (a_step(P - 1) > 0 && a_step(P - 1) % 12 == 0) keep(ao);
            if constexpr (P == E_PP0 + 1 || P == E_PP1 + 1) keep(po);
            if constexpr (P == P_F2 + 16) keep(ob[1]);
        };
        {
            unsigned e_h = wid == 3 ? ((ln & 1) ? ~tagg : tagg) : 0u, hsl = lbase + lw * 8192, hso = (hp + ((tq + wid) & 7)) * 65536 + nh + qx * 16384;
            unsigned a_h = wid == 3 ? ln * 4 : OOB, s_h = WS_F + F_H + nr + hp * 32, ca2[4], a_g = lbase + HGO + ln * 4;
            static_for<0, 4>([&](auto ic) { ca2[CV(ic)] = cad[CV(ic)] + 2 * BUF; tie(ca2[CV(ic)]); });
            tie(e_h); tie(a_h); tie(a_g); ties(hsl); ties(hso); ties(s_h);
            sched();
            if (wid < 4) {
                auto look = [&]() { if constexpr (HA) pf_at<0>(pf, a_h, rws, s_h); else pf_ld(pf, a_h, rws, s_h); };
                if constexpr (HA) pf_zero(pf);
                if constexpr (PW == 0) { look(); wait_v<1>(); }
                if constexpr (PW == 1) { wait_v<0>(); look(); }
                if constexpr (PW == 2) { wait_v<8>(); look(); wait_v<1>(); }
                static_for<16, 32>([&](auto ic) { constexpr int i = CV(ic); dsr128u<((i >> 2) & 1) * 4096>(cf[i], (i & 8) ? cad[i & 3] : ca2[i & 3]); });
                wait_l<0>();
                static_for<16, 32>([&](auto ic) { tie(cf[CV(ic)]); });
                wait_v<0>();
                tag_chk<1, HA>(pf, e_h, a_h, rws, s_h);
                if (wid == 3) flag_set(a_g, 1u); else gate<1>(a_g, 1);
                TS(6);
                hop<26>();
                static_for<0, 4>([&](auto ic) {
                    constexpr int i = CV(ic);
                    dma_x<(i >> 1) * 2 * BUF + (i & 1) * 4096, (i >> 1) * 262144 + (i & 1) * 4096, true>(fso, hsl, l16, rws, hso);
                    dma_n<1024>(fso, l16, rws); dma_n<2048>(fso, l16, rws); dma_n<3072>(fso, l16, rws);
                });
            }
        }
        wait_v<8>();
        if (wid < 4) TS(7);
        TG_DECL
        barrier();
        static_for<0, 8>([&](auto ic) { ldh(IC(0), ic); });
        static_for<0, 32>([&](auto nc) {
            constexpr int n = CV(nc), p = n >> 3;
            if constexpr (n == PG3) page<33>();
            if constexpr (n == PG4) page<37>();
            wait_lr<(n & 3) == 3 ? 0 : 4 + g2_lx(n - 1)>();
            if constexpr (n == 31) barrier();
            if constexpr ((n & 3) == 3 && n < 31) {
                constexpr int need = g2_need(n);
                TL(21 + n / 4 * 3);
                wait_v<g2_allow(8 * n, need)>();
                TL(22 + n / 4 * 3);
                barrier();
                TL(23 + n / 4 * 3);
            }
            if constexpr (n == 1) hop<31>();
            if constexpr (n == 4) hop<32>();
            if constexpr (n == 11) hop<34>();
            if constexpr (n == 15) hop<35>();
            if constexpr (n == 19) hop<36>();
            if constexpr (n == 27) hop<38>();
            if constexpr (n == 31) hop<39>();
            if constexpr (n == 0) TL(20);
            TG(20 + n);
            if constexpr (n == 31) TL(42);
            if constexpr (PRG) __builtin_amdgcn_s_setprio(n == 31 ? 0 : 3 - ((n + 1) & 3));
            sched();
            static_for<0, 8>([&](auto mc) {
                constexpr int m = CV(mc), ks = m >> 2, tt = (m >> 1) & 1, dd = m & 1, P = n * 8 + m;
                if constexpr (m == 4 && (n & 3) != 3) wait_lr<(rd1(n) ? 8 : 4) + ((n & 7) == 6 ? (XW < 4) + (XW2 < 4) : 0)>();
                mfma<(n & 7) == 0 && ks == 0>(yc[p & 1][(DBG & 1) ? tt : g2_tile(n, tt)][dd], cat(hf[n & 1][4 * ks + 2 * tt], hf[n & 1][4 * ks + 2 * tt + 1]), cf[g2_unit(n, tt) * 4 + 2 * ks + dd]);
                sched();
                if constexpr (n < 31 && rd1(n) && m < 4) { ldh(IC(n + 1), IC(2 * m)); ldh(IC(n + 1), IC(2 * m + 1)); }
                if constexpr (n < 31 && !rd1(n)) ldh(IC(n + 1), mc);
                if constexpr (g2_fill(P) >= 0) fill(IC(g2_fill(P) >> 2), IC(g2_fill(P) & 3));
                side(IC(P));
                sched();
            });
        });
        TG(52);
        if (wid >= 4) TS(5); else TS(8);
        TG(53);
        static_for<256 - P_R1, 21>([&](auto jc) { rcv(IC(1), jc); });
        wait_l<0>();
        tie(im[0]); tie(im[1]);
        static_for<21, NR>([&](auto jc) { rcv(IC(1), jc); });
        asm volatile("s_nop 3");
        sched();
        static_for<0, 16>([&](auto jc) { finB(IC(3), jc); });
        if (b == 255 && wid == 0) gst<0>(u32x2{tagg, tagg}, l16 ? OOB : 0u, rws, WS_F + F_EP);
    }
}

}

#if defined(__HIP_DEVICE_COMPILE__)
#pragma clang attribute pop
#endif

extern "C" void kernel_launch(void* const* d_in, const int* in_sizes, int n_in, void* d_out, int out_size, void* d_ws, size_t ws_size,
                              hipStream_t stream) {
    (void)in_sizes; (void)n_in; (void)out_size; (void)ws_size;
    using namespace fk;
    const uint16_t* q = (const uint16_t*)d_in[0];
    const uint16_t* kin = (const uint16_t*)d_in[1];
    const uint16_t* vin = (const uint16_t*)d_in[2];
    const uint16_t* sk = (const uint16_t*)d_in[3];
    const uint16_t* ssmk = (const uint16_t*)d_in[4];
    const uint16_t* ssmD = (const uint16_t*)d_in[5];
    const uint16_t* x = (const uint16_t*)d_in[6];
    const uint16_t* lre = (const uint16_t*)d_in[7];
    const uint16_t* lim = (const uint16_t*)d_in[8];
    const uint16_t* bre = (const uint16_t*)d_in[9];
    const uint16_t* bim = (const uint16_t*)d_in[10];
    const uint16_t* cre = (const uint16_t*)d_in[11];
    const uint16_t* cim = (const uint16_t*)d_in[12];
    const uint16_t* logdt = (const uint16_t*)d_in[13];
    const uint16_t* dskip = (const uint16_t*)d_in[14];
#define WOFF 0
    char* ws = (char*)(((uintptr_t)d_ws + ((size_t)WOFF << 20) + 0x1fffff) & ~(uintptr_t)0x1fffff);
    hipLaunchKernelGGL(k1, dim3(256), dim3(512), 0, stream, x, bre, bim, lre, lim, logdt, cre, cim, dskip, ssmk, ssmD, q, kin, vin, sk, ws, (float*)d_out TS_PASS(0));
}
```

```cpp
#include <hip/hip_runtime.h>
#include <stdint.h>
#include <type_traits>

#if defined(__HIP_DEVICE_COMPILE__)
#pragma clang attribute push(__attribute__((target("no-packed-fp32-ops"))), apply_to = function)
#endif

namespace fk {

typedef __attribute__((ext_vector_type(8))) short bf16x8;
typedef __attribute__((ext_vector_type(4))) short bf16x4;
typedef __attribute__((ext_vector_type(4))) float f32x4;
typedef __attribute__((ext_vector_type(4))) unsigned u32x4;
typedef __attribute__((ext_vector_type(2))) unsigned u32x2;
typedef __attribute__((ext_vector_type(2))) __bf16 bf16x2_t;
typedef __attribute__((address_space(1))) unsigned gu32;
typedef __attribute__((address_space(1))) unsigned long long gu64;
#define RLX_AGENT __ATOMIC_RELAXED, __HIP_MEMORY_SCOPE_AGENT

__device__ __forceinline__ float u2f(unsigned v) { return __builtin_bit_cast(float, v); }
__device__ __forceinline__ unsigned f2u(float v) { return __builtin_bit_cast(unsigned, v); }
__device__ __forceinline__ bool wall(bool p) { return __builtin_amdgcn_ballot_w64(p) == ~0ull; }
__device__ __forceinline__ float bflo(unsigned w) { return u2f(w << 16); }
__device__ __forceinline__ float bfhi(unsigned w) { return u2f(w & 0xffff0000u); }
__device__ __forceinline__ unsigned pack2(float lo, float hi) {
    bf16x2_t v; v[0] = (__bf16)lo; v[1] = (__bf16)hi; return __builtin_bit_cast(unsigned, v);
}
__device__ __forceinline__ float rbf(float f) { float r; asm("v_cvt_pk_bf16_f32 %0, 0, %1" : "=v"(r) : "v"(f)); return r; }
__device__ __forceinline__ float fma_(float a, float b, float c) { return __builtin_fmaf(a, b, c); }

template <int I, int N, class F>
__device__ __forceinline__ void static_for(F&& f) {
    if constexpr (I + 1 == N) f(std::integral_constant<int, I>{});
    else if constexpr (I < N) { static_for<I, I + (N - I) / 2>(f); static_for<I + (N - I) / 2, N>(f); }
}
#define IC(v) std::integral_constant<int, (v)>{}
#define CV(c) decltype(c)::value

#define TS_ARG
#define TS_PASS(off)
#define TS_DECL
#define TS(i)
#define TSV(i, v)
#define TL_DECL
#define TL(i)
#define TS_FLUSH(slot)

#define TG_DECL
#define TG(i)
template <int N> __device__ __forceinline__ void wait_v() { asm volatile("s_waitcnt vmcnt(%0)" ::"n"(N > 63 ? 63 : N) : "memory"); }
template <int N> __device__ __forceinline__ void wait_l() { asm volatile("s_waitcnt lgkmcnt(%0)" ::"n"(N > 14 ? 14 : N) : "memory"); }
template <int N> __device__ __forceinline__ void wait_lr() { __builtin_amdgcn_s_waitcnt(0xc07f | ((N > 14 ? 14 : N) << 8)); }
template <class T> __device__ __forceinline__ void tie(T& x) { asm volatile("" : "+v"(x)); }
template <class T> __device__ __forceinline__ void undef(T& x) { asm volatile("" : "=v"(x)); }
template <int OFF, class T> __device__ __forceinline__ void dsr128u(T& d, unsigned a) { asm volatile("ds_read_b128 %0, %1 offset:%2" : "+v"(d) : "v"(a), "n"(OFF)); }
template <class T> __device__ __forceinline__ void keep(const T& x) { asm volatile("" :: "v"(x)); }
template <int OFF, class T> __device__ __forceinline__ void dsr128(T& d, unsigned a) { asm volatile("ds_read_b128 %0, %1 offset:%2" : "=v"(d) : "v"(a), "n"(OFF)); }
template <int OFF, class T> __device__ __forceinline__ void dsr64(T& d, unsigned a) { asm volatile("ds_read_b64 %0, %1 offset:%2" : "=v"(d) : "v"(a), "n"(OFF)); }
template <int OFF> __device__ __forceinline__ void dsr32(unsigned& d, unsigned a) { asm volatile("ds_read_b32 %0, %1 offset:%2" : "=v"(d) : "v"(a), "n"(OFF)); }
template <int O0, int O1, class T> __device__ __forceinline__ void dsr2(T& d, unsigned a) { asm volatile("ds_read2_b32 %0, %1 offset0:%2 offset1:%3" : "=v"(d) : "v"(a), "n"(O0), "n"(O1)); }
template <int OFF> __device__ __forceinline__ void dsr64tr(bf16x4& d, unsigned a) { asm volatile("ds_read_b64_tr_b16 %0, %1 offset:%2" : "=v"(d) : "v"(a), "n"(OFF)); }
template <int OFF, class T> __device__ __forceinline__ void dsw128(unsigned a, const T& v) { asm volatile("ds_write_b128 %0, %1 offset:%2" :: "v"(a), "v"(v), "n"(OFF) : "memory"); }
template <int OFF> __device__ __forceinline__ void dsw32(unsigned a, unsigned v) { asm volatile("ds_write_b32 %0, %1 offset:%2" :: "v"(a), "v"(v), "n"(OFF) : "memory"); }
__device__ __forceinline__ bf16x8 cat(const bf16x4& lo, const bf16x4& hi) { return __builtin_shufflevector(lo, hi, 0, 1, 2, 3, 4, 5, 6, 7); }
__device__ __forceinline__ void barrier() { asm volatile("" ::: "memory"); __builtin_amdgcn_s_barrier(); asm volatile("" ::: "memory"); }
__device__ __forceinline__ int fresh(int v) { asm volatile("" : "+s"(v)); return v; }
__device__ __forceinline__ void sched() { __builtin_amdgcn_sched_barrier(0); }
template <bool FIRST> __device__ __forceinline__ void mfma(f32x4& c, const bf16x8& a, const bf16x8& b) {
    if constexpr (FIRST) asm volatile("v_mfma_f32_16x16x32_bf16 %0, %1, %2, 0" : "=v"(c) : "v"(a), "v"(b));
    else asm volatile("v_mfma_f32_16x16x32_bf16 %0, %1, %2, %0" : "+v"(c) : "v"(a), "v"(b));
}
struct cf32 { float r, i; };
__device__ __forceinline__ cf32 cmul(cf32 a, cf32 b) { return cf32{fma_(a.r, b.r, -(a.i * b.i)), fma_(a.r, b.i, a.i * b.r)}; }
__device__ __forceinline__ cf32 cfma(cf32 a, cf32 b, cf32 c) { return cf32{fma_(a.r, b.r, fma_(-a.i, b.i, c.r)), fma_(a.r, b.i, fma_(a.i, b.r, c.i))}; }
__device__ __forceinline__ void sw16(float v, float& e, float& o) { const auto r = __builtin_amdgcn_permlane16_swap(f2u(v), f2u(v), false, false); e = u2f(r[0]); o = u2f(r[1]); }
__device__ __forceinline__ void sw32(float v, float& e, float& o) { const auto r = __builtin_amdgcn_permlane32_swap(f2u(v), f2u(v), false, false); e = u2f(r[0]); o = u2f(r[1]); }
constexpr unsigned SPIN_CAP = 1u << 16;
constexpr unsigned OOB = 0x40000000u;

constexpr size_t WS_H = 0;
constexpr size_t WS_KVT = (size_t)8 << 20;
constexpr size_t WS_ZT = (size_t)12 << 20;
constexpr size_t WS_PP = (size_t)16 << 20;
constexpr size_t WS_E = (size_t)24 << 20;
constexpr size_t WS_F = WS_E + (256 << 10);
constexpr int F_H = 0;
constexpr int F_K = 4096;
constexpr int F_Z = 16384;
constexpr int F_EP = F_Z + 32768;
constexpr int F_C = F_EP + 256;
constexpr unsigned WS_P = 25 << 20;
#define KCQ 2
#define KCC 4
#define KVP 0
#define RSL 1
#define PB 0
#define KTI 1
#define TM 0
#define TX 0
#define PK 1
#define HA 0
#define KVS 1
#define GCR 273
constexpr int LDS_TOTAL = 163840, BUF = 32768;
constexpr int KC = 98304, KCH = 8320, KCP = 4160;
constexpr int U0 = KC + 4 * KCH;
constexpr int FULL = U0 + 18432 + 8192, FST = 388;
constexpr int DONE = U0 + 18432, DUMP = DONE + 4096;
constexpr int HGO = FULL + 1792, DSK = HGO + 256, POLL = DSK + 256;
static_assert(POLL + 8 * 256 <= LDS_TOTAL && 4 * FST <= 1792, "lds");
constexpr int g1_buf(int s) { return ((0x0213 >> ((s & 3) * 4)) & 3) * BUF; }
constexpr int IMG = KC, QST = KC + 16384, ZST = U0, PLD = 2 * BUF;
#define CODE_KB 47
#define EXACT 0
#define HP 1
#define CP 2
#define FLQ 11
#define R01 3
#define ZP 7
#define SLP 8
#define STR_(x) #x
#define STR(x) STR_(x)
#define NOST 0
#define G2M 0
#define RD 2
#define PRG 1
#define OSC 16
#define NOA 0
#define PPH 0
#define HS 0
#define JQ(j) (HS == 0 ? 0 : HS == 1 ? (j) & 1 : HS == 2 ? (j) : (j) & 2)
#define JR(j) (HS == 0 ? 2 * (j) : HS == 1 ? 2 * ((j) & 2) : HS == 2 ? 0 : 4 * ((j) & 1))
#define NOX 0

__device__ __forceinline__ u32x4 mk_rsrc(const void* p, unsigned bytes) {
    const unsigned long long a = (unsigned long long)p;
    return u32x4{(unsigned)a, (unsigned)(a >> 32) & 0xffffu, bytes, 0x00020000u};
}
__device__ __forceinline__ __amdgpu_buffer_rsrc_t mk_brs(const void* p, unsigned bytes) { return __builtin_amdgcn_make_buffer_rsrc((void*)p, (short)0, bytes, 0x00020000); }
__device__ __forceinline__ void st16(const u32x4& d, __amdgpu_buffer_rsrc_t r, unsigned voff, unsigned soff) {
    __builtin_amdgcn_raw_buffer_store_b128(d, r, voff, soff, 16);
    asm volatile("s_nop 1" :: "v"(d) : "memory");
}
template <int IMM, bool SC1> __device__ __forceinline__ void dma(unsigned lds, unsigned voff, const u32x4& rs, unsigned soff) {
    if constexpr (SC1) asm volatile("s_add_u32 m0, %0, %1\n\ts_nop 0\n\tbuffer_load_dwordx4 %2, %3, %4 offen sc1 lds" :: "s"(lds), "n"(IMM), "v"(voff), "s"(rs), "s"(soff) : "memory", "scc");
    else asm volatile("s_add_u32 m0, %0, %1\n\ts_nop 0\n\tbuffer_load_dwordx4 %2, %3, %4 offen lds" :: "s"(lds), "n"(IMM), "v"(voff), "s"(rs), "s"(soff) : "memory", "scc");
}
template <int IMM, int I> __device__ __forceinline__ void dma4(unsigned lds, unsigned voff, const u32x4& rs, unsigned soff) {
    if constexpr (I == 0) dma<IMM, false>(lds, voff, rs, soff);
    else asm volatile("buffer_load_dwordx4 %0, %1, %2 offen offset:%3 lds" :: "v"(voff), "s"(rs), "s"(soff), "n"(I * 1024) : "memory");
}
template <int IMM> __device__ __forceinline__ void dma1(unsigned lds, unsigned voff, const u32x4& rs, unsigned soff) {
    asm volatile("s_add_u32 m0, %0, %1\n\ts_nop 0\n\tbuffer_load_dword %2, %3, %4 offen sc1 lds" :: "s"(lds), "n"(IMM), "v"(voff), "s"(rs), "s"(soff) : "memory", "scc");
}
__device__ __forceinline__ void flag_set(unsigned waddr, unsigned v) { asm volatile("ds_write_b32 %0, %1" :: "v"(waddr), "v"(v) : "memory"); }
__device__ __forceinline__ void flag_chk(unsigned& f, unsigned raddr, unsigned need) {
    unsigned cnt;
    asm volatile("v_cmp_le_u32 vcc, %3, %0\n\t"
                 "s_cmp_eq_u64 vcc, -1\n\t"
                 "s_cbranch_scc1 1f\n\t"
                 "s_mov_b32 %1, 0x100000\n"
                 "0:\n\t"
                 "ds_read_b32 %0, %2\n\t"
                 "s_sub_u32 %1, %1, 1\n\t"
                 "s_waitcnt lgkmcnt(0)\n\t"
                 "s_cbranch_scc1 1f\n\t"
                 "v_cmp_le_u32 vcc, %3, %0\n\t"
                 "s_cmp_eq_u64 vcc, -1\n\t"
                 "s_cbranch_scc0 0b\n"
                 "1:" : "+v"(f), "=&s"(cnt) : "v"(raddr), "s"(need) : "vcc", "scc", "memory");
}
template <int OFF, int SL = 0> __device__ __forceinline__ void zero_chk(unsigned& f, unsigned raddr) {
    unsigned cnt;
    asm volatile("v_cmp_ne_u32 vcc, 0, %0\n\t"
                 "s_cbranch_vccz 1f\n\t"
                 "s_mov_b32 %1, 0x100000\n"
                 "0:\n\t"
                 ".if %4\n\ts_sleep %4\n\t.endif\n\t"
                 "ds_read_b32 %0, %2 offset:%3\n\t"
                 "s_sub_u32 %1, %1, 1\n\t"
                 "s_waitcnt lgkmcnt(0)\n\t"
                 "s_cbranch_scc1 1f\n\t"
                 "v_cmp_ne_u32 vcc, 0, %0\n\t"
                 "s_cbranch_vccnz 0b\n"
                 "1:" : "+v"(f), "=&s"(cnt) : "v"(raddr), "n"(OFF), "n"(SL) : "vcc", "scc", "memory");
}
template <int IMM, class T> __device__ __forceinline__ void gld(T& d, unsigned voff, const u32x4& rs, unsigned soff) {
    if constexpr (sizeof(T) == 16) asm volatile("buffer_load_dwordx4 %0, %1, %2, %3 offen offset:%4" : "=v"(d) : "v"(voff), "s"(rs), "s"(soff), "n"(IMM) : "memory");
    else if constexpr (sizeof(T) == 8) asm volatile("buffer_load_dwordx2 %0, %1, %2, %3 offen offset:%4" : "=v"(d) : "v"(voff), "s"(rs), "s"(soff), "n"(IMM) : "memory");
    else asm volatile("buffer_load_ushort %0, %1, %2, %3 offen offset:%4" : "=v"(d) : "v"(voff), "s"(rs), "s"(soff), "n"(IMM) : "memory");
}
__device__ __forceinline__ void gld32(unsigned& d, unsigned voff, const u32x4& rs, unsigned soff) { asm volatile("buffer_load_dword %0, %1, %2, %3 offen" : "=v"(d) : "v"(voff), "s"(rs), "s"(soff) : "memory"); }
__device__ __forceinline__ void sld(unsigned& d, const void* p) { asm volatile("s_load_dword %0, %1, 0x0" : "=s"(d) : "s"(p) : "memory"); }
__device__ __forceinline__ void stouch(unsigned long long p) { asm volatile("s_load_dword s101, %0, 0x0\n\ts_load_dword s101, %0, 0x80\n\ts_load_dword s101, %0, 0x100" :: "s"(p) : "s101", "memory"); }
template <class T> __device__ __forceinline__ void ties(T& x) { asm volatile("" : "+s"(x)); }
template <int IMM, class T> __device__ __forceinline__ void gst(const T& d, unsigned voff, const u32x4& rs, unsigned soff) {
    if constexpr (sizeof(T) == 16) asm volatile("buffer_store_dwordx4 %0, %1, %2, %3 offen offset:%4 sc1" :: "v"(d), "v"(voff), "s"(rs), "s"(soff), "n"(IMM) : "memory");
    else asm volatile("buffer_store_dwordx2 %0, %1, %2, %3 offen offset:%4 sc1" :: "v"(d), "v"(voff), "s"(rs), "s"(soff), "n"(IMM) : "memory");
}
template <int SLQ> __device__ __forceinline__ void gate(unsigned raddr, unsigned need) {
    unsigned cnt, f;
    asm volatile("s_mov_b32 %1, 0x40000\n"
                 "0:\n\t"
                 "ds_read_b32 %0, %2\n\t"
                 "s_sub_u32 %1, %1, 1\n\t"
                 "s_waitcnt lgkmcnt(0)\n\t"
                 "s_cbranch_scc1 1f\n\t"
                 "v_cmp_le_u32 vcc, %3, %0\n\t"
                 "s_cmp_eq_u64 vcc, -1\n\t"
                 "s_cbranch_scc1 1f\n\t"
                 "s_sleep %4\n\t"
                 "s_branch 0b\n"
                 "1:" : "=&v"(f), "=&s"(cnt) : "v"(raddr), "s"(need), "n"(SLQ) : "vcc", "scc", "memory");
}
template <int SL = SLP, int AT_ = 0> __device__ __forceinline__ void tag_chk(unsigned& f, unsigned expect, unsigned voff, const u32x4& rs, unsigned soff) {
    unsigned cnt;
    asm volatile("v_cmp_eq_u32 vcc, %2, %0\n\t"
                 "s_cmp_eq_u64 vcc, -1\n\t"
                 "s_cbranch_scc1 1f\n\t"
                 "s_mov_b32 %1, 0x10000\n"
                 "0:\n\t"
                 ".if %7\n\tv_mov_b32 %0, 0\n\t.endif\n\t"
                 "s_sleep %6\n\t"
                 ".if %7\n\tbuffer_atomic_or %0, %3, %4, %5 offen sc0 sc1\n\t.else\n\tbuffer_load_dword %0, %3, %4, %5 offen sc1\n\t.endif\n\t"
                 "s_sub_u32 %1, %1, 1\n\t"
                 "s_waitcnt vmcnt(0)\n\t"
                 "s_cbranch_scc1 1f\n\t"
                 "v_cmp_eq_u32 vcc, %2, %0\n\t"
                 "s_cmp_eq_u64 vcc, -1\n\t"
                 "s_cbranch_scc0 0b\n"
                 "1:" : "+v"(f), "=&s"(cnt) : "v"(expect), "v"(voff), "s"(rs), "s"(soff), "n"(SL), "n"(AT_) : "vcc", "scc", "memory");
}

__device__ __forceinline__ unsigned race(unsigned w, const void* ws, unsigned long long rnd) {
    unsigned v;
    asm volatile("s_cmp_lt_u32 %1, 32\n\t"
                 "s_cbranch_scc0 1f\n\t"
                 "s_mov_b64 s[98:99], %3\n\t"
                 "s_lshl_b32 s96, s98, 7\n\t"
                 "s_lshl_b32 s97, %1, 13\n\t"
                 "s_andn2_b32 s96, s96, 0x2000\n\t"
                 "s_xor_b32 s96, s96, s97\n\t"
                 "s_and_b32 s96, s96, 0x7ff80\n\t"
                 "s_add_u32 s96, s96, %4\n\t"
                 "s_xor_b32 s97, s96, 0x2000\n\t"
                 "s_mov_b32 %0, s98\n\t"
                 "s_load_dword s99, %2, s96\n\t"
                 "s_load_dword s98, %2, s97\n\t"
                 "s_lshr_b32 s96, %1, 4\n\t"
                 "s_xor_b32 s96, s96, %1\n\t"
                 "s_waitcnt lgkmcnt(1)\n\t"
                 "s_sleep %5\n\t"
                 "s_cmp_lg_u32 %0, s98\n\t"
                 "s_addc_u32 %0, s96, 0\n\t"
                 "s_and_b32 %0, %0, 1\n"
                 "1:" : "=&s"(v) : "s"(w), "s"(ws), "s"(rnd), "n"(WS_P), "n"(RSL) : "s96", "s97", "s98", "s99", "scc", "memory");
    return v;
}
__device__ __forceinline__ void vote_chk(unsigned& f, unsigned expect, unsigned odd, unsigned raddr, unsigned lds, unsigned voff, const u32x4& rs, unsigned soff, unsigned& cnt) {
    unsigned t;
    asm volatile("s_mov_b32 %1, 0x10000\n\t"
                 "v_xor_b32 %2, %0, %3\n\t"
                 "v_cmp_gt_u32 vcc, %2, %4\n\t"
                 "s_cbranch_vccz 1f\n\t"
                 "s_mov_b32 m0, %6\n"
                 "0:\n\t"
                 "s_sleep 4\n\t"
                 "buffer_load_dword %7, %8, %9 offen sc1 lds\n\t"
                 "s_sub_u32 %1, %1, 1\n\t"
                 "s_waitcnt vmcnt(0)\n\t"
                 "ds_read_b32 %0, %5\n\t"
                 "s_waitcnt lgkmcnt(0)\n\t"
                 "s_cbranch_scc1 1f\n\t"
                 "v_xor_b32 %2, %0, %3\n\t"
                 "v_cmp_gt_u32 vcc, %2, %4\n\t"
                 "s_cbranch_vccnz 0b\n"
                 "1:" : "+v"(f), "=&s"(cnt), "=&v"(t) : "v"(expect), "v"(odd), "v"(raddr), "s"(lds), "v"(voff), "s"(rs), "s"(soff) : "vcc", "scc", "memory");
}
__device__ __forceinline__ unsigned vote_cnt(unsigned f, unsigned tag, unsigned up, unsigned& n) {
    unsigned r;
    asm volatile("s_xnor_b32 %0, %3, 1\n\tv_cmp_eq_u32 vcc, %0, %2\n\ts_bcnt1_i32_b64 %1, vcc\n\ts_cmp_gt_u32 %1, 16\n\ts_cselect_b32 %0, 0x2000, 0\n\ts_xor_b32 %0, %0, %4"
                 : "=&s"(r), "=&s"(n) : "v"(f), "s"(tag), "s"(up) : "vcc", "scc");
    return r;
}

template <int H> __device__ __forceinline__ void pp_ld(u32x4& d, unsigned voff, const u32x4& rs, unsigned soff) {
    if constexpr (H) asm volatile("buffer_load_dwordx4 v[244:247], %1, %2, %3 offen offset:1024 sc1" : "={v[244:247]}"(d) : "v"(voff), "s"(rs), "s"(soff) : "memory");
    else asm volatile("buffer_load_dwordx4 v[240:243], %1, %2, %3 offen sc1" : "={v[240:243]}"(d) : "v"(voff), "s"(rs), "s"(soff) : "memory");
}
__device__ __forceinline__ void pf_ld(unsigned& d, unsigned voff, const u32x4& rs, unsigned soff) { asm volatile("buffer_load_dword v248, %1, %2, %3 offen sc1" : "={v248}"(d) : "v"(voff), "s"(rs), "s"(soff) : "memory"); }
__device__ __forceinline__ void pf_keep(unsigned d) { asm volatile("" :: "{v248}"(d)); }
__device__ __forceinline__ void pf_zero(unsigned& d) { asm volatile("v_mov_b32 v248, 0" : "={v248}"(d)); }
template <int OFF> __device__ __forceinline__ void pf_at(unsigned& d, unsigned voff, const u32x4& rs, unsigned soff) {
    asm volatile("buffer_atomic_or v248, %1, %2, %3 offen offset:%4 sc0 sc1" : "+{v248}"(d) : "v"(voff), "s"(rs), "s"(soff), "n"(OFF) : "memory");
}
__device__ __forceinline__ void pp_sel(unsigned& so, unsigned& fail, unsigned d, unsigned tn, unsigned yes, unsigned no) {
    asm volatile("v_readlane_b32 %1, v248, 63\n\ts_and_b32 %1, %1, 0xf000f\n\ts_cmp_eq_u32 %1, %3\n\ts_cselect_b32 %0, %4, %5\n\ts_cselect_b32 %1, 0, -1"
                 : "=&s"(so), "=&s"(fail) : "{v248}"(d), "s"(tn), "s"(yes), "s"(no) : "scc");
}
#define PP_CHK(R, R1, R2, R3, OFF) \
    asm volatile("v_cmp_ne_u32 vcc, 0, %2\n\t" \
                 "s_cbranch_vccz 1f\n\t" \
                 "s_mov_b32 %1, 0x10000\n" \
                 "0:\n\t" \
                 ".if %9\n\t" \
                 "v_mov_b32 %2, 0\n\t" \
                 "s_sub_u32 %1, %1, 1\n\t" \
                 "s_cbranch_scc1 1f\n\t" \
                 "buffer_atomic_or %2, %8, %6, %7 offen offset:" OFF " sc0 sc1\n\t" \
                 "s_lshr_b32 vcc_lo, %4, 4\n\t" \
                 "s_waitcnt vmcnt(0)\n\t" \
                 "v_readlane_b32 %3, %2, 63\n\t" \
                 "s_xor_b32 %3, %3, vcc_lo\n\t" \
                 "s_and_b32 %3, %3, 0xf000f\n\t" \
                 "s_cbranch_scc1 0b\n\t" \
                 ".endif\n\t" \
                 "buffer_load_dwordx4 " R ", %5, %6, %7 offen offset:" OFF " sc1\n\t" \
                 "s_waitcnt vmcnt(0)\n\t" \
                 "s_and_b32 %3, %4, 0xf000f\n\t" \
                 "v_xor_b32 " R1 ", %3, " R1 "\n\t" \
                 "s_lshr_b32 %3, %4, 8\n\t" \
                 "s_and_b32 %3, %3, 0xf000f\n\t" \
                 "v_xor_b32 " R2 ", %3, " R2 "\n\t" \
                 "s_lshr_b32 %3, %4, 4\n\t" \
                 "s_and_b32 %3, %3, 0xf000f\n\t" \
                 "v_xor_b32 " R3 ", %3, " R3 "\n\t" \
                 "v_or3_b32 %2, " R1 ", " R2 ", " R3 "\n\t" \
                 "v_and_b32 %2, 0xf000f, %2\n\t" \
                 "s_sub_u32 %1, %1, 1\n\t" \
                 "s_cbranch_scc1 1f\n\t" \
                 "v_cmp_ne_u32 vcc, 0, %2\n\t" \
                 "s_cbranch_vccnz 0b\n" \
                 "1:" : "+{" R "}"(p), "=&s"(cnt), "+v"(bad), "=&s"(t) : "s"(tag), "v"(voff), "s"(rs), "s"(soff), "v"(vp), "n"(PB) : "vcc", "scc", "memory")
template <int H> __device__ __forceinline__ void pp_chk(u32x4& p, unsigned bad, unsigned tag, unsigned voff, const u32x4& rs, unsigned soff, unsigned vp) {
    unsigned cnt, t;
    if constexpr (H) PP_CHK("v[244:247]", "v245", "v246", "v247", "1024"); else PP_CHK("v[240:243]", "v241", "v242", "v243", "0");
}
template <int H> __device__ __forceinline__ void pp_tie(u32x4& p) { if constexpr (H) asm volatile("" : "+{v[244:247]}"(p)); else asm volatile("" : "+{v[240:243]}"(p)); }
__device__ __forceinline__ float amax3(float a, float b, float c) { float r; asm volatile("v_max3_f32 %0, |%1|, |%2|, |%3|" : "=v"(r) : "v"(a), "v"(b), "v"(c)); return r; }
__device__ __forceinline__ unsigned bfi_v(unsigned m, unsigned a, unsigned b) { unsigned r; asm volatile("v_bfi_b32 %0, %1, %2, %3" : "=v"(r) : "v"(m), "v"(a), "v"(b)); return r; }
__device__ __forceinline__ unsigned bfi_s(unsigned m, unsigned a, unsigned b) { unsigned r; asm volatile("v_bfi_b32 %0, %1, %2, %3" : "=v"(r) : "v"(m), "v"(a), "s"(b)); return r; }
__device__ __forceinline__ unsigned bfi_f(unsigned m, unsigned a, unsigned b) { unsigned r; asm volatile("v_bfi_b32 %0, %1, %2, %3" : "=v"(r) : "v"(m), "s"(a), "v"(b)); return r; }
template <int H> __device__ __forceinline__ float cvt16(unsigned w) {
    float r;
    if constexpr (H) asm volatile("v_cvt_f32_i32_sdwa %0, sext(%1) dst_sel:DWORD dst_unused:UNUSED_PAD src0_sel:WORD_1" : "=v"(r) : "v"(w));
    else asm volatile("v_cvt_f32_i32_sdwa %0, sext(%1) dst_sel:DWORD dst_unused:UNUSED_PAD src0_sel:WORD_0" : "=v"(r) : "v"(w));
    return r;
}
__device__ __forceinline__ float sel8(float a, float b, unsigned long long mask) {
    float r;
    asm volatile("s_mov_b64 vcc, %3\n\tv_cndmask_b32_dpp %0, %1, %2, vcc row_ror:8 row_mask:0xf bank_mask:0xf" : "=v"(r) : "v"(a), "v"(b), "s"(mask) : "vcc");
    return r;
}
__device__ __forceinline__ unsigned sel0(unsigned a, unsigned long long mask) { unsigned r; asm volatile("v_cndmask_b32_e64 %0, 0, %1, %2" : "=v"(r) : "v"(a), "s"(mask)); return r; }
constexpr unsigned long long diag_mask(int R) {
    unsigned long long m = 0;
    for (int l = 0; l < 64; ++l) if ((l >> 4) == ((l & 7) >> 1) && 2 * (l & 1) + ((l >> 3) & 1) == R) m |= 1ull << l;
    return m;
}
template <int IMM, int X, bool SC1> __device__ __forceinline__ void dma_x(unsigned& so, unsigned lds, unsigned voff, const u32x4& rs, unsigned base) {
    if constexpr (SC1) asm volatile("s_add_u32 m0, %1, %2\n\ts_xor_b32 %0, %5, %6\n\tbuffer_load_dwordx4 %3, %4, %0 offen sc1 lds" : "=&s"(so) : "s"(lds), "n"(IMM), "v"(voff), "s"(rs), "s"(base), "n"(X) : "memory", "scc");
    else asm volatile("s_add_u32 m0, %1, %2\n\ts_xor_b32 %0, %5, %6\n\tbuffer_load_dwordx4 %3, %4, %0 offen lds" : "=&s"(so) : "s"(lds), "n"(IMM), "v"(voff), "s"(rs), "s"(base), "n"(X) : "memory", "scc");
}
template <int OFF> __device__ __forceinline__ void dma_n(unsigned so, unsigned voff, const u32x4& rs) {
    asm volatile("buffer_load_dwordx4 %0, %1, %2 offen offset:%3 sc1 lds" :: "v"(voff), "s"(rs), "s"(so), "n"(OFF) : "memory");
}
template <int IMM, int OFF> __device__ __forceinline__ void dma_m(unsigned so, unsigned lds, unsigned voff, const u32x4& rs) {
    asm volatile("s_add_u32 m0, %4, %5\n\ts_nop 0\n\tbuffer_load_dwordx4 %0, %1, %2 offen offset:%3 sc1 lds" :: "v"(voff), "s"(rs), "s"(so), "n"(OFF), "s"(lds), "n"(IMM) : "memory", "scc");
}
__device__ __forceinline__ unsigned and_or(unsigned a, unsigned m, unsigned o) { unsigned r; asm("v_and_or_b32 %0, %1, %2, %3" : "=v"(r) : "v"(a), "v"(m), "s"(o)); return r; }

#define HOPS 1
#define HM 0x1ff221125B082ull
#define PG1 95
#define PG2 288
#define PG3 8
#define PG4 24
#define SQ 52
#define P18 -1
#define WS0 0
#define SLM 0x12234566ull
#define PGF 5
#define HQ 84
#define HW 4
constexpr int hop_nth(int n) { for (int k = 0; k < 62; ++k) if (((HM >> k) & 1) && n-- == 0) return k; return 63; }
constexpr int hop_next(int K) { int n = 0; for (int k = 0; k < K; ++k) n += (HM >> k) & 1; return hop_nth(n + HW); }
template <int K> __device__ __forceinline__ void hop() {
    if constexpr (HOPS && ((HM >> K) & 1)) asm volatile(".Lfkh%0:\n\ts_cbranch_execz .Lfkh%1" :: "n"(K), "n"(hop_next(K)));
}
template <int K> __device__ __forceinline__ void page() {
    if constexpr (HOPS && K >= 0 && ((HM >> (K & 63)) & 1)) asm volatile("s_branch 1f\n\t.p2align 12\n.Lfkh%0:\n\ts_cbranch_execz .Lfkh%1\n1:" :: "n"(K), "n"(hop_next(K)));
    else if constexpr (HOPS) asm volatile("s_branch 1f\n\t.p2align 12\n1:");
}
__device__ __forceinline__ void hop_all(int w) {
    if constexpr (HOPS) asm volatile("s_mov_b64 exec, 0\n\ts_cmp_eq_u32 %0, 0\n\ts_cbranch_scc1 .Lfkh%1\n\ts_cmp_eq_u32 %0, 1\n\ts_cbranch_scc1 .Lfkh%2\n\t"
                                     "s_cmp_eq_u32 %0, 2\n\ts_cbranch_scc1 .Lfkh%3\n\ts_branch .Lfkh%4\n.Lfkh63:\n\ts_mov_b64 exec, -1"
                                     :: "s"(w), "n"(hop_nth(0)), "n"(hop_nth(1)), "n"(hop_nth(2)), "n"(hop_nth(3)) : "scc");
}
#define NB 1
#define PFQ 0
#define CVR 3
#define SPR 3
#define ELP 8
#define PW 1
#define CPG 95
#define GQ -1
#define GE 3
#define KDL 0
#define GP 1
#define SG 507
#define KO 0
#define DP 0
#define B13 460
#define PRE 0
enum { OP_NONE, OP_A, OP_B, OP_FLAG, OP_SIG, OP_VOTE };
struct Op { int kind, a, b; };
constexpr Op g1_op(int G) {
    const int st = (G + 32) / 32 - 1, q = G - 32 * st, i = q >> 2, j = q & 3;
    if (i < 5) {
        if (st < 0) return {OP_NONE, 0, 0};
        if ((j & 1) == 0) return {OP_A, st * 8 + i + 3, j >> 1};
        if (q == FLQ && st < 15) return {OP_FLAG, 0, 0};
        if (NB && G == SG) return {OP_SIG, 2, 0};
        if (G == GCR) return {OP_VOTE, 0, 0};
        if (NB == 2 && G == 435) return {OP_SIG, 3, 0};
        return {OP_NONE, 0, 0};
    }
    if (st == 15) return NB && G == SG ? Op{OP_SIG, 2, 0} : Op{OP_NONE, 0, 0};
    const int n = (st + 1) * 8;
    switch (q) {
        case 20: return {OP_B, st + 1, 0}; case 21: return {OP_B, st + 1, 1}; case 22: return {OP_A, n, 0}; case 23: return {OP_B, st + 1, 2};
        case 24: return {OP_B, st + 1, 3}; case 25: return {OP_A, n, 1}; case 26: return {OP_A, n + 1, 0}; case 27: return {OP_A, n + 1, 1};
        case 28: return {OP_A, n + 2, 0}; case 30: return {OP_A, n + 2, 1};
    }
    return {OP_NONE, 0, 0};
}
constexpr int g1_need(int G) {
    const int st = G >> 5, j = G & 3;
    for (int h = G - 1; h >= -12; --h) {
        const Op o = g1_op(h);
        if (o.kind == OP_A && o.a == (G >> 2) && o.b == (j >> 1)) return h;
        if (o.kind == OP_B && o.a == st && o.b == j) return h;
        if (o.kind == OP_FLAG && (G & 31) == 20 && (h >> 5) == st) return h;
    }
    return -100;
}
constexpr int g1_wait(int G) {
    int n = -100, c = 0;
    for (int h = G; h < (G ? G + 4 : 2) && h < 512; ++h) if (g1_need(h) > n) n = g1_need(h);
    for (int h = n + 1; h < G; ++h) c += g1_op(h).kind != OP_NONE;
    return n < G ? c : -1;
}
#define KV0 3
#define KVN 0x2222
constexpr int kv_first(int k) { int n = 0; for (int i = KV0; i < k && i < KV0 + 8; ++i) n += (KVN >> (4 * (i - KV0))) & 15; return n > 8 ? 8 : n; }
constexpr int kv_last() { int k = KV0; while (kv_first(k + 1) < 8) ++k; return k; }
#define CQ -1
#define CTM 0
#define EC 2
#define ED 2
#define EB 0
#define KST 0
#define KFD 3
#define KPL 11
#define KPI 0
#define KSL SLP
#define CBQ 20
constexpr int KVL = kv_last(), KFL = KVL + KFD, KSD = KVS ? KCC : -1;
static_assert(KFL <= KPL && KPL <= 12 && KPL > KVL && (KPI & 3) == 0 && KTI != KCC && KTI != KFL && KTI < KPL && KVP >= 0 && KVP < KCQ && KCQ < KCC && KSD <= KVL && KCC <= KPL && GCR > 32 * KCC + 62, "");
#define ROT 0x814
#define KLP 13
#define KL0 8
#define KLS 4
constexpr int ld_pos(int j) { return j < KLP ? -100 : KL0 + (j - KLP) * KLS / 4; }
constexpr int ld_cnt(int k) { int c = 0; for (int j = KLP; j < 21; ++j) c += (ld_pos(j) + 8) / 8 - 1 == k; return c; }
constexpr int KLL = (ld_pos(20) + 8) / 8 - 1, KLI = ld_pos(20) - 8 * KLL;
static_assert(KLP == 21 || KLL < KV0, "");
constexpr int hv_cnt(int k0, int k1) {
    int c = 0;
    for (int k = k0; k < k1; ++k) c += 9 + (KST ? 4 * (k == KVL) : k < KSD ? 0 : kv_first(k + 1) / 2 - (k == KSD ? 0 : kv_first(k) / 2)) + (k == KFL) + (k == KPL) + (k == KCQ) + (k == KVP) + (TM && TM < 16 && k == KTI) + ld_cnt(k);
    return c;
}
#define ZM 1
#define CL 1
#define EP 0
constexpr unsigned long long lag0_mask(int w) {
    unsigned long long m = 0;
    for (int l = 0; l < 64; ++l) if ((l >> 4) == 2 + ((l >> 3) & 1) && 3 - ((l & 7) >> 1) == w) m |= 1ull << l;
    return m;
}
constexpr int cv_start(int n) { return n * 8 - n * (n - 1) / 2; }
constexpr int cv_n(int q) { int n = 0; while (cv_start(n + 1) <= q) ++n; return n; }
constexpr int cv_m(int q) { return 7 - cv_n(q) - (q - cv_start(cv_n(q))); }
enum { CO_NONE, CO_A, CO_B, CO_C };
constexpr Op cv_op(int G) {
    const int s = (G + 36) / 36 - 1, q = G - 36 * s;
    if (q >= 1 && q <= 6) return s >= 0 ? Op{CO_A, s * 8 + 5 + (q - 1) / 2, (q - 1) & 1} : Op{CO_NONE, 0, 0};
    if (q >= 11 && q <= 14) return s >= 0 && s >= CL && s - CL < 8 ? Op{CO_C, s - CL, q - 11} : Op{CO_NONE, 0, 0};
    if (s == 7) return {CO_NONE, 0, 0};
    for (int m = 0; m < 8; ++m) if (q == cv_start(7 - m)) return {CO_B, (s + 1) * 8 + m, 0};
    for (int n = 0; n < 5; ++n) if (q == cv_start(n + 1) + 1 || q == cv_start(n + 1) + 2) return {CO_A, (s + 1) * 8 + n, q - cv_start(n + 1) - 1};
    return {CO_NONE, 0, 0};
}
constexpr int cv_need(int G) {
    const int s = G / 36, q = G % 36;
    for (int h = G - 1; h >= -36; --h) {
        const Op o = cv_op(h);
        if (o.kind == CO_A && o.a == s * 8 + cv_n(q)) return h;
        if (o.kind == CO_B && o.a == s * 8 + cv_m(q)) return h;
        if (o.kind == CO_C && q == 24 && o.a == s - CL) return h;
    }
    return -100;
}
constexpr int cv_wait(int G) {
    const int n = cv_need(G);
    for (int h = G - 1; h >= 0 && h >= G - 40; --h) if (cv_need(h) >= n) return -1;
    int c = 0;
    for (int h = n + 1; h < G; ++h) c += cv_op(h).kind != CO_NONE;
    return c;
}
#define AT(n, m) ((n) * 8 + (m))
#define FM 0x326260
constexpr int fill_pos(int s, int i) {
    const int f = (FM >> (4 * (s - 2))) & 15, n = s == 2 ? 0 : 4 * s - 9;
    switch (f) {
        case 1: return AT(n + i, 5) - (s == 2 && i == 3) * 6;
        case 2: return AT(n + 1 + i / 2, 5 + 2 * (i & 1));
        case 3: return i < 2 ? AT(n, 5 + 2 * i) : AT(n + i - 1, 5);
        case 4: return AT(n + 2 + i / 2, 5 + 2 * (i & 1));
        case 5: return AT(n, 1 + 2 * i) + (s == 2 ? 4 : 0);
        case 6: return AT(n + 3 + i / 2, 5 + 2 * (i & 1));
        case 7: return AT(n + 3, 5 + 2 * i);
        case 8: return i < 2 ? AT(n + 1, 5 + 2 * i) : AT(n + i, 5);
        case 9: return AT(n + 1 + i, 1);
        case 10: return i < 2 ? AT(n, 1 + 2 * i) : AT(n + 3, 1 + 2 * i);
    }
    return AT(n + i / 2, 5 + 2 * (i & 1));
}
constexpr int g2_fill(int p) {
    for (int k = 8; k < 32; ++k) if (fill_pos(k >> 2, k & 3) == p) return k;
    return -1;
}
#define QX AT(2, 5)
#define QQ AT(3, 2)
#define QZ AT(11, 2)
#define QT AT(3, 4)
constexpr int P_X = QX, P_Q = QQ, P_Z = QZ, P_T = QT;
constexpr bool XL = false;
constexpr bool fill_m0(int k) {
    const int a = fill_pos(k >> 2, (k & 3) - 1), b = fill_pos(k >> 2, k & 3);
    return (P_Q + 2 >= a && P_Q < b) || (P_Z + 2 >= a && P_Z < b) || (P_X + 2 >= a && P_X < b);
}
#define DBG 0
#define XW 4
#define XW2 4
#define QPP0 AT(9, 4)
#define QL0 AT(21, 1)
#define QR0 AT(24, 6)
#define QF2 AT(28, 3)
#define QL1 AT(26, 1)
#define QR1 AT(29, 3)
#define QA AT(16, 4)
#define AD 0
#define D1 2
#define D0 1
constexpr int NS = 33, NR = 29;
constexpr int P_PP0 = QPP0, P_PP1 = AT(16, 4), P_L0 = QL0, P_R0 = QR0, P_F2 = QF2, P_L1 = QL1, P_R1 = QR1, P_ZC = AT(10, 6), P_A = QA;
#define QB 24
#define QW -2
constexpr int P_W = QW;
static_assert(P_W < AT(ZP, 4), "");
constexpr int P_B0 = P_L0 - QB, P_B1 = P_L1 - QB;
static_assert(P_B0 > P_ZC + 1 && P_B1 > P_L0 + 1, "");
constexpr int E_PP0 = P_PP0 + NS - 1, E_PP1 = P_PP1 + (NS - 1) / D1, E_R0 = P_R0 + (NR - 1) / D0;
constexpr int ZW = P_A >= AT(20, 4) ? 19 : 15;
static_assert(P_R0 + 14 / D0 >= AT(24, 3) && E_R0 < P_F2 && P_F2 >= AT(24, 4) && P_F2 + 8 <= AT(31, 0) && E_R0 < P_R1, "");
static_assert(P_PP0 >= AT(9, 2) && P_PP0 + 8 <= AT(15, 4) && E_PP0 < P_PP1 && P_A >= AT(16, 4), "");
constexpr bool a_free(int p) {
    return p >= P_A && (AD || !((p >= P_PP1 && p <= E_PP1) || (p >= P_R0 && p <= E_R0) || (p >= P_F2 && p < P_F2 + 16)));
}
constexpr int a_step(int p) {
    if (NOA || !a_free(p)) return -1;
    int c = 0;
    for (int h = P_A; h < p; ++h) c += a_free(h);
    return c <= 48 ? c : -1;
}
constexpr int a_pos(int s) { int p = P_A; while (a_step(p) != s) ++p; return p; }
constexpr int q_rd(int r) { int p = r < 2 ? AT(ZW, 6) + r : a_pos(12 * r - 16) + 1; while ((p & 7) < 4) ++p; return p; }
constexpr int q_tie(int r) { const int n = (q_rd(r) >> 3) + 1; return AT(n, (n & 3) == 3 ? 0 : 4); }
static_assert(NOA || (a_pos(48) < 256 && q_tie(2) <= a_pos(23) && q_tie(3) <= a_pos(35)), "");
constexpr int g2_vm(int p) {
    int c = g2_fill(p) >= 0;
    c += a_step(p) > 0 && a_step(p) % 12 == 0 && !(NOST & 1);
    if (!NOA) c += (p == P_T) + (p == P_Q || p == P_Q + 2) + (p == AT(ZP, 4)) + (p == P_Z || p == P_Z + 2);
    c += (p == P_X || p == P_X + 2) + (p == P_W);
    c += !(NOST & 2) && (p == E_PP0 || p == E_PP1);
    c += (p == P_L0 || p == P_L1) + (PB && (p == P_B0 || p == P_B1));
    c += !(NOST & 4) && (p == P_F2 + 11 || p == P_F2 + 15);
    return c;
}
constexpr int g2_lx(int n) {
    int c = 2 * (n == 7);
    if (n >= 0 && (n & 7) >= 6) c += 2;
    if (!NOA) { c += 4 * (n == ZW); for (int r = 0; r < 4; ++r) c += (q_rd(r) >> 3) == n; }
    return c;
}
constexpr int g2_tile(int n, int j) { return (n & 4) ? (n & 2) == 0 : j; }
constexpr int g2_unit(int n, int j) { return (n & 4) ? 4 + 2 * (n & 1) + j : n & 7; }
constexpr int imax(int a, int b) { return a > b ? a : b; }
constexpr int g2_need(int n) {
    int r = n == 3 ? -1 : fill_pos((n + 1) / 4, 3);
    if (n == 7 && !XL) r = imax(r, P_X + 2);
    if (n == ZW && !NOA) r = imax(r, imax(P_Q, P_Z) + 2);
    return r;
}
static_assert(P_Z > AT(11, 0) && P_T > AT(3, 0) && P_T < AT(ZP, 4) && (!XL || (P_X + 2 < P_R0 && P_R0 + 2 >= AT(24, 2) && P_R0 < AT(27, 0) && D0 == 1)), "");
constexpr bool rd1(int n) { return RD == 1 || (RD == 3 && (n & 3) == 2); }
constexpr int g2_allow(int e, int p) { int c = 0; for (int h = p + 1; h < e; ++h) c += g2_vm(h); return c; }

__global__ __launch_bounds__(512, 2) __attribute__((aligned(4096))) void k1(const uint16_t* __restrict__ x, const uint16_t* __restrict__ bre, const uint16_t* __restrict__ bim,
                                             const uint16_t* __restrict__ lre, const uint16_t* __restrict__ lim, const uint16_t* __restrict__ logdt,
                                             const uint16_t* __restrict__ cre, const uint16_t* __restrict__ cim, const uint16_t* __restrict__ dskip,
                                             const uint16_t* __restrict__ ssmk, const uint16_t* __restrict__ ssmD, const uint16_t* __restrict__ qin,
                                             const uint16_t* __restrict__ kin, const uint16_t* __restrict__ vin, const uint16_t* __restrict__ sk,
                                             char* __restrict__ ws, float* __restrict__ out TS_ARG) {
    unsigned long long pc0, rnd;
    asm volatile("s_getpc_b64 %0\n\ts_memtime %1" : "=s"(pc0), "=s"(rnd));
    TS_DECL TS(0);
    TL_DECL
    __shared__ __attribute__((aligned(1024))) char shm[LDS_TOTAL];
    const int tid = threadIdx.x, lane = tid & 63, wid = __builtin_amdgcn_readfirstlane(tid >> 6);
    const int b = blockIdx.x, xcd = b & 7, idx = b >> 3, hb = xcd & 1, tq = idx & 7;
    const int tm = (xcd >> 1) * 4 + (idx >> 3), tn = hb * 8 + tq;
    unsigned nr;
    const int tm2 = (xcd & 4) * 2 + tq, dt = (xcd & 2) * 2 + (idx >> 3), rot2 = tq + JR(idx >> 3);
    const unsigned lbase = (unsigned)(uintptr_t)shm;
    const int l15 = lane & 15, g4 = lane >> 4, lw = wid & 3, kg = wid >> 2;
    dma<U0 + 4096, false>(lbase + wid * 1024, lane * 16, mk_rsrc((const void*)(pc0 & ~255ull), CODE_KB * 1024), ((idx * 8 + wid) & 63) * 1024);
    const unsigned mk0 = lbase + POLL + lw * 256 + kg * (FULL - POLL + lw * (FST - 256));
    flag_set(mk0 + lane * 4, 1u);
    flag_set(lbase + HGO + lane * 4, 0u);
    if constexpr (NB) flag_set(lbase + DONE + lw * FST + lane * 4, 1u);
    unsigned vo[4];
    const bool xw = lw < 2;
#pragma unroll
    for (int i = 0; i < 4; ++i) {
        const int v = l15 ^ (i * 4 + g4);
        vo[i] = ((xw ? 64 * (i & 1) + 4 * (i >> 1) + (lane & 32) + 2 * (g4 & 1) : 8 * (i & 1) + 2 * g4) + (v >> 3)) * 2048 + (v & 7) * 16 - (i & 1) * 1024;
    }
    const int bw = lw >> 1;
    unsigned r0 = (tm * 128 + lw * 16 + bw * (tn * 64 + lw * 16 - 64 - tm * 128)) * 2048, rsz = 229376 - bw * 163840;
    unsigned rot = (ROT >> 8) * ((tq + (idx >> 3)) & 1) + ((ROT >> 4) & 15) * (xcd >> 1) + (ROT & 15) * hb, rst = 16384 << bw, lb = lbase + lw * 8192, oob = OOB;
    unsigned lb0 = lb + g1_buf(0) + kg * (g1_buf(1) - g1_buf(0)), so0 = ((kg + rot) & 15) * 128;
    unsigned co = l15 * 2 + (tn * 128 + lw * 32 + kg * 2048), vw = b * 4 + lw + 32 - 32 * kg;
    tie(vo[0]); tie(vo[1]); tie(vo[2]); tie(vo[3]); tie(oob); tie(co);
    ties(r0); ties(rsz); ties(rot); ties(rst); ties(lb); ties(lb0); ties(so0); ties(vw);
    sched();
    asm volatile("" :: "s"(x), "s"(bre), "s"(bim), "s"(lre), "s"(lim), "s"(logdt), "s"(cre), "s"(cim), "s"(dskip), "s"(ssmk), "s"(ssmD), "s"(qin),
                       "s"(kin), "s"(vin), "s"(sk), "s"(ws), "s"(out));
    const bool xv = fresh(bw) == 0;
    const u32x4 rs0 = mk_rsrc((const char*)(xv ? x : bre) + r0, rsz), rs1 = mk_rsrc((const char*)(xv ? x : bim) + r0 - 2048, rsz + 2048);
    unsigned c_dt, c_re, c_im;
    gld<0>(c_dt, co, mk_rsrc(logdt, 2048), 0);
    gld<0>(c_re, co, mk_rsrc(lre, 2048), 0);
    gld<0>(c_im, co, mk_rsrc(lim, 2048), 0);
    const unsigned vb = race(vw, ws, rnd);
    static_for<0, 8>([&](auto ic) { constexpr int i = CV(ic); dma4<(i & 4) * 1024, i & 3>(lb0, vo[i & 3], (i & 2) ? rs1 : rs0, so0 + (i >> 2) * rst); });
    wait_l<0>();
    barrier();
    dma1<0>(mk0 + kg * 120, oob, rs0, 0);
    sched();
    const unsigned a00 = (l15 >> 1) * 256 + ((((l15 & 1) * 8 + g4) ^ (l15 >> 1)) << 4);
    auto frag = [&](int par, int ks) { return (a00 ^ (ks * 64) ^ (par * 128)) + par * 2048; };
    bf16x8 cf[32];
    unsigned vc[4], cad[4];
    auto c_setup = [&]() {
#pragma unroll
        for (int i = 0; i < 4; ++i) { const int v = l15 ^ (i * 4 + g4); vc[i] = (4 * (2 * g4 + (v >> 3)) + 2 * (i & 1) + (i >> 1)) * 2048 + (v & 7) * 16 - i * 1024; }
    };
    const u32x4 rsc = mk_rsrc((kg ? cim : cre) + (size_t)dt * 131072, 262144);
    auto c_off = [&](int dg, int j) { return (unsigned)(dg * 65536 + hb * 1024 + ((rot2 + j) & 7) * 128); };
    const unsigned cst = lbase + (kg ? BUF : 2 * BUF) + lw * 8192;
    unsigned tag;
    const u32x4 rsf = mk_rsrc(ws + WS_F, 65536);
    const __amdgpu_buffer_rsrc_t rf = mk_brs(ws + WS_F, 65536);
    const int qx = (hb ? 0 : 2) ^ JQ(idx >> 3), wid_ = wid, b_ = b;

    if (wid >= 4) {
        page<-1>();
        const int ch = xcd * 128 + (idx >> 2) * 16 + lw * 4 + (idx & 3);
        unsigned dw, nx; u32x4 ad;
        auto c_unit = [&](auto uc) { static_for<0, 4>([&](auto ic) { constexpr int u = CV(uc), i = CV(ic); dma4<(u & 1) * 4096, i>(cst, vc[i], rsc, c_off(lw, u)); }); };
        constexpr int XV = 4 * (EC == 1) + 8 * (PFQ == 2);
        auto ctouch = [&](int r) { const unsigned j = (fresh(idx * 4 + lw) + r * 37) & 127; stouch((pc0 & ~255ull) + (j < CODE_KB * 8 / 3 ? j : j - 64) * 384); };
        {
            const unsigned fb = lbase + FULL + lw * FST;
            auto piece = [&](auto sc, auto ic) {
                constexpr int s = CV(sc), i = CV(ic);
                dma4<g1_buf(s) + (i & 4) * 1024, i & 3>(lb, vo[i & 3], (i & 2) ? rs1 : rs0, ((s + rot) & 15) * 128 + (i >> 2) * rst);
            };
            auto mark = [&](auto sc) { dma1<(31 - CV(sc)) * 4>(fb, oob, rs0, 0); };
            auto feed = [&](auto kc, auto ic) {
                constexpr int k = CV(kc), i = CV(ic);
                if constexpr (i < 4) piece(IC(k + 3), IC(i + 4));
                if constexpr (i == 3) mark(IC(k + 3));
                if constexpr (i >= 4 && k < 12) piece(IC(k + 4), IC(i - 4));
            };
            __builtin_amdgcn_s_setprio(HP);
            TS(7);
            sld(tag, ws + WS_F + F_EP);
            sld(dw, ssmD + (ch & ~1));
            const int sb = idx * 2 + (lw >> 1), kh = lw & 1;
            u32x2 kk[11], vv[8]; u32x4 w4[2], kd[4], ke[4], kvo[4];
            const unsigned ko = g4 * 16384 + l15 * 8, so = sb * 65536 + xcd * 256 + kh * 128;
            auto kv_ld = [&](auto jc) {
                constexpr int j = CV(jc), i = j < 13 ? j - 5 : j - 13;
                if constexpr (j < 2) gld<16 * j>(w4[j], l15 * 32, mk_rsrc(sk, 8192), xcd * 1024 + kh * 512);
                else if constexpr (j < 5) gld<0>(kk[j - 2], ko + so - 6144 + (j - 2) * 2048, mk_rsrc(kin, 4 << 20), 0);
                else if constexpr (j < 13) gld<(i & 1) * 2048>(kk[3 + i], ko, mk_rsrc(kin, 4 << 20), so + (i >> 1) * 4096);
                else gld<(i & 1) * 2048>(vv[i], ko, mk_rsrc(vin, 4 << 20), so + (i >> 1) * 4096);
            };
            {
                static_for<0, KLP>(kv_ld);
                dma1<DSK>(lbase, lane * 4, mk_rsrc(dskip, 2048), dt * 256);
                dma<U0, false>(lbase + kh * 1024, oob, rs0, 0);
            }
            static_for<4, 8>([&](auto ic) { feed(IC(-2), ic); });
            const __amdgpu_buffer_rsrc_t rk = mk_brs(ws + WS_KVT + (size_t)((xcd & 3) * 32 + kh * 16) * 32768, 524288);
            const unsigned K = lbase + KC + lw * KCH;
            unsigned kfo;
            const unsigned n0 = (xcd & 4) << 11;
            auto st = [&](int c) { st16(kvo[c], rk, (l15 >> 2) * 131072 + g4 * 64 + (l15 & 3) * 16, c * 32768 + (sb >> 5) * 16384 + (KVS ? nx : n0 ^ ((sb & 32) << 8)) + (sb & 31) * 256); };
            float kf[7], pa[4], pb[4];
            auto kv_step = [&](auto uc, auto jc) {
                constexpr int u = CV(uc), j = CV(jc), c = u >> 1, h = u & 1;
                auto el = [&](const u32x2& q) -> float { return (c & 1) ? bfhi(q[c >> 1]) : bflo(q[c >> 1]); };
                auto tap = [&](int t) { const unsigned w = w4[c >> 1][(c & 1) * 2 + (t >> 1)]; return (t & 1) ? bfhi(w) : bflo(w); };
                auto all = [&](auto f) {
#pragma unroll
                    for (int i = 0; i < 4; ++i) f(i);
                };
                if constexpr (j == 0) {
#pragma unroll
                    for (int i = 0; i < 7; ++i) { kf[i] = (h && i < 3) ? kf[i + 4] : el(kk[4 * h + i]); tie(kf[i]); }
                }
                if constexpr (EXACT) {
                    if constexpr (j == 1) { const float t = tap(3); all([&](int i) { pa[i] = kf[i] * t; }); all([&](int i) { pa[i] = rbf(pa[i]); tie(pa[i]); }); }
                    if constexpr (j == 2 || j == 4 || j == 6) { const float t = tap(3 - j / 2); all([&](int i) { pb[i] = kf[i + j / 2] * t; }); all([&](int i) { pb[i] = rbf(pb[i]); tie(pb[i]); }); }
                    if constexpr (j == 3 || j == 5 || j == 7) { all([&](int i) { pa[i] += pb[i]; }); all([&](int i) { pa[i] = rbf(pa[i]); tie(pa[i]); }); }
                } else {
                    if constexpr (j == 1) { const float t = tap(3); all([&](int i) { pa[i] = kf[i] * t; tie(pa[i]); }); }
                    if constexpr (j >= 2 && j <= 4) { const float t = tap(4 - j); all([&](int i) { pa[i] = fma_(kf[i + j - 1], t, pa[i]); tie(pa[i]); }); }
                    if constexpr (j == 5) all([&](int i) { pa[i] = rbf(pa[i]); tie(pa[i]); });
                }
                if constexpr (j == (EXACT ? 7 : 6)) all([&](int i) { pb[i] = el(vv[4 * h + i]); tie(pb[i]); });
                if constexpr (j == 7) {
                    all([&](int i) { pa[i] *= pb[i]; });
                    kvo[c][3 - 2 * h] = pack2(pa[1], pa[0]); kvo[c][2 - 2 * h] = pack2(pa[3], pa[2]);
                    if constexpr (h && !KST && u >= kv_first(KSD)) st(c);
                    else if constexpr (u == 7) all(st);
                    else tie(kvo[c]);
                }
            };
            static_for<-1, 13>([&](auto kc) {
                constexpr int k = CV(kc);
                if constexpr (k == PGF) page<1>();
                if constexpr (k == 2) hop<0>();
                if constexpr (k == 6) hop<6>();
                if constexpr (k == 8) hop<9>();
                if constexpr (k >= 0) barrier();
                if constexpr (k >= 0 && ((CTM >> k) & 1)) ctouch(k);
                if constexpr (k == KV0) {
                    wait_v<KLP == 21 ? 4 + hv_cnt(-1, k) : 9 - KLI - (KLI > 3) + hv_cnt(KLL + 1, k)>();
                    static_for<0, 11>([&](auto ic) { tie(kk[CV(ic)]); });
                    static_for<0, 8>([&](auto ic) { tie(vv[CV(ic)]); });
                    tie(w4[0]); tie(w4[1]); keep(c_dt); keep(c_re); keep(c_im);
                    TS(8);
                }
                if constexpr (k == KVP) {
                    wait_l<0>();
                    ties(tag); ties(dw);
                    tag += 0x01010101u;
                    __builtin_amdgcn_raw_buffer_store_b64(u32x2{tag, ~tag ^ vb}, rf, lane || vw >= 32 ? OOB : 0u, F_C + vw * 8, 16);
                }
                if constexpr (k == KCQ) dma1<POLL>(lbase + lw * 256, lane * 4, rsf, F_C);
                if constexpr (TM == 16 && k == KTI) stouch((unsigned long long)(ws + WS_E) + (TX ? (xcd * 128 + idx * 4 + lw) : b * 4 + lw) * 384);
                if constexpr (TM && TM < 16 && k == KTI) {
                    const unsigned wv = b * 4 + lw, a = lane < 32 ? ((TM & 1) ? wv * 4096 + lane * 128 : (TM & 4) && lane < 4 ? wv * 4096 + lane * 1024 + 896 : OOB) : lane < 35 && (TM & 2) ? (unsigned)(WS_E - WS_PP) + (wv * 3 + lane - 32) * 128 : OOB;
                    if constexpr (TM & 8) dma1<DUMP>(lbase + lw * 256, a, mk_rsrc(ws + WS_PP, WS_F + F_EP - WS_PP), 0);
                    else asm volatile("buffer_atomic_or %0, %1, %2, 0 offen sc1" :: "v"(0u), "v"(a), "s"(mk_rsrc(ws + WS_PP, WS_F + F_EP - WS_PP)) : "memory");
                }
                if constexpr (k == KCC) {
                    unsigned f, n, sp;
                    wait_v<hv_cnt(KCQ, k) - 1>();
                    dsr32<POLL - 131072>(f, lbase + 131072 + lw * 256 + lane * 4);
                    wait_l<0>();
                    vote_chk(f, (lane & 1) ? ~tag : tag, lane & 1, lbase + POLL + lw * 256 + lane * 4, lbase + POLL + lw * 256, lane * 4, rsf, F_C, sp);
                    nr = vote_cnt(f, tag, (xcd & 4) << 11, n);
                    nx = nr ^ ((xcd & 2) << 12);
                    kfo = F_K + (KVS ? nr : n0) + (((xcd & 3) * 2 + (idx >> 4)) * 64 + lw * 16) * 8;
                    TSV(11, n + 100 * (0x10000 - sp));
                }
                if constexpr (k == KFL) {
                    wait_v<hv_cnt(KVL + 1, k)>();
                    __builtin_amdgcn_raw_buffer_store_b64(u32x2{tag, ~tag}, rf, lane ? OOB : 0u, F_K + (KVS ? nr : n0) + (((xcd & 3) * 2 + kh) * 64 + sb) * 8, 16);
                    TS(10);
                }
                if constexpr (k == 12) {
                    dma1<0>(K, oob, rs0, 0);
                    dma1<KCP>(K, oob, rs0, 0);
                }
                static_for<0, 8>([&](auto ic) {
                    constexpr int i = CV(ic), u0 = kv_first(k), nu = kv_first(k + 1) - u0;
                    static_for<KLP, 21>([&](auto jc) { if constexpr (ld_pos(CV(jc)) == 8 * k + i) kv_ld(jc); });
                    if constexpr (k == KPL && i == KPI) dma1<POLL>(lbase + wid * 256, (lane & 31) * 4, rsf, kfo);
                    feed(kc, ic);
                    if constexpr (k == 4 && i == 4) hop<3>();
                    if constexpr (!KST && k == KSD && (i & 1) && i / 2 < kv_first(KSD) / 2) st(i / 2);
                    if constexpr (nu == 1) kv_step(IC(u0), ic);
                    if constexpr (nu == 2) { kv_step(IC(u0 + i / 4), IC(2 * (i & 3))); kv_step(IC(u0 + i / 4), IC(2 * (i & 3) + 1)); }
                    if constexpr (!KDL && k == 12 && i >= 4) gld<(i - 4) * 1024>(kd[i - 4], lane * 16, mk_rsrc(ssmk, 4 << 20), ch * 4096);
                    if constexpr (k == 11 && i == 4) c_setup();
                    if constexpr (k == 10 && i >= 4) { cad[i - 4] = cst + frag(i & 1, (i >> 1) & 1); tie(cad[i - 4]); }
                    sched();
                });
            });
            page<12>();
            TS(1);
            if constexpr (EB == 2) barrier();
            unsigned pf;
            const unsigned fr = lbase + FULL + (lane & 3) * FST;
            wait_v<hv_cnt(KPL, 12) - (KPL < 12 ? 1 : 2) - (KPL == KFL) - KPI - (KPI > 3) + (KDL ? 7 : 11)>();
            dsr32<POLL - 131072>(pf, lbase + 131072 + wid * 256 + lane * 4);
            wait_l<0>();
            tag_chk<KSL>(pf, (lane & 1) ? ~tag : tag, (lane & 31) * 4, rsf, kfo);
            if constexpr (KDL) static_for<0, 4>([&](auto ic) { gld<CV(ic) * 1024>(kd[CV(ic)], lane * 16, mk_rsrc(ssmk, 4 << 20), ch * 4096); });
            auto b13 = [&]() { if constexpr (NB == 2) { unsigned f = 1; zero_chk<12, 1>(f, lbase + DONE + (lane & 3) * FST); } else if constexpr (NB < 2) barrier(); };
            if constexpr (EB == 1) b13();
            {
                const u32x4 rs = mk_rsrc(ws + WS_KVT + (size_t)((xcd & 3) * 32 + idx) * 32768, 32768);
                static_for<0, 4>([&](auto ic) { constexpr int i = CV(ic); dma<U0 + 2048 + i * 1024, true>(lbase + lw * 4096, (lane >> 2) * 64 + (((lane & 3) ^ g4) << 4), rs, (lw >> 1) * 16384 + (KVS ? nx : n0 ^ ((lw & 2) << 12)) + (lw & 1) * 4096 + i * 1024); });
            }
            mark(IC(16));
            auto warm = [&]() {
                const unsigned wo = g4 * 2048 + l15 * 16, dl = lbase + lw * 1024;
                const unsigned qo = (512 * (idx >> 3) + 128 * (idx & 1) + 32 * lw + 4 * ((idx >> 1) & 3)) * 2048 + xcd * 256, xo = (tm2 * 128 + 32 * lw + 8 * (qx & 2)) * 2048 + dt * 256;
                static_for<0, 4>([&](auto ic) { constexpr int i = CV(ic); dma<DUMP, false>(dl, wo, mk_rsrc(qin, 4 << 20), qo + ((i & 1) * 16 + (i >> 1) * 256) * 2048); });
                static_for<0, 4>([&](auto ic) { constexpr int i = CV(ic); dma<DUMP, false>(dl, wo, mk_rsrc(x, 4 << 20), xo + i * 8192); });
            };
            if constexpr (PFQ == 1) warm();
            if constexpr (EB == 0) b13();
            auto c_first = [&]() {
                if constexpr (CL <= 1) c_unit(IC(0));
                if constexpr (CL <= 0) c_unit(IC(1));
                if constexpr (EC == 1) static_for<0, 4>([&](auto ic) { constexpr int i = CV(ic); dma4<DUMP, i>(lbase, vc[i], mk_rsrc(cre + (size_t)dt * 131072, 262144), c_off(lw, 0)); });
                if constexpr (PFQ == 2) warm();
            };
            static_assert(NB != 3 || CL == 2, "");
            if constexpr (!KO) c_first();
            wait_v<5 + 8 * (PFQ == 1) + (KO ? 0 : 4 * (CL <= 1) + 4 * (CL <= 0) + XV)>();
            static_for<0, 4>([&](auto ic) { tie(kd[CV(ic)]); });
#pragma unroll
            for (int n = 0; n < 4; ++n) {
                const unsigned first = n ? __builtin_amdgcn_readlane(kd[n ? n - 1 : 0][3], 63) : 0u;
                const unsigned p = __builtin_amdgcn_update_dpp(first, kd[n][3], 0x138, 0xf, 0xf, false);
                ke[n] = u32x4{__builtin_amdgcn_alignbyte(kd[n][0], p, 2), __builtin_amdgcn_alignbyte(kd[n][1], kd[n][0], 2),
                              __builtin_amdgcn_alignbyte(kd[n][2], kd[n][1], 2), __builtin_amdgcn_alignbyte(kd[n][3], kd[n][2], 2)};
                tie(ke[n]);
            }
            if constexpr (ZM) {
                const unsigned dl = (ch & 1) ? dw >> 16 : dw & 0xffffu, dv = dl << ((~lane & 1) * 16);
                static_for<0, 4>([&](auto ic) { constexpr unsigned long long k = lag0_mask(CV(ic)); ad[CV(ic)] = sel0(dv, k); });
                tie(ad);
            }
            hop<14>();
            static_for<0, 4>([&](auto ic) { constexpr int i = CV(ic); dsw128<64 + i * 1024>(K + lane * 16, kd[i]); dsw128<KCP + 64 + i * 1024>(K + lane * 16, ke[i]); });
            if constexpr (KO) c_first();
            TS(9);
            static_for<0, -CL>([&](auto uc) {
                constexpr int u = CV(uc);
                wait_v<4 + (u < 2 ? XV : 0)>();
                static_for<0, 4>([&](auto ic) { constexpr int i = CV(ic); dsr128<(u & 1) * 4096>(cf[u * 4 + i], cad[i]); });
                wait_l<0>();
                static_for<0, 4>([&](auto ic) { tie(cf[u * 4 + CV(ic)]); });
                c_unit(IC(u + 2));
            });
            pf = 1;
            zero_chk<15 * 4>(pf, fr);
            if constexpr (PRE) { pf = 1; zero_chk<PRE == 1 ? 8 : 4>(pf, lbase + DONE + (lane & 3) * FST); }
            __builtin_amdgcn_s_setprio(0);
        }
        TS(2);
        TL(1);
        unsigned ka[4], ub[4];
#pragma unroll
        for (int i = 0; i < 4; ++i) {
            ka[i] = lbase + KC + lw * KCH + ((l15 & 1) ? 0 : KCP) + 84 + 2 * (l15 & 14) - 16 * g4 + i * 1024;
            const int e = 2 * l15 + g4 + 2;
            ub[i] = lbase + U0 + 64 * e + ((lw ^ (((e >> 2) + 3 - i) & 3)) << 4);
        }
        unsigned gad = lbase + DONE + lw * FST;
        if constexpr (GQ >= 0) tie(gad);
        f32x4 acc[8];
        bf16x4 FA[8][2]; bf16x8 FB[8];
        auto op = [&](auto gc) {
            constexpr Op o = cv_op(CV(gc));
            if constexpr (o.kind == CO_A) { constexpr int s = o.a >> 3, n = o.a & 7, d = (n & 1) * 128 + s * 16 + o.b * 2; dsr2<d, d + 1>(FA[n][o.b], ka[n >> 1]); }
            if constexpr (o.kind == CO_B) { constexpr int s = o.a >> 3, m = o.a & 7; dsr128<2048 * m + 1792 - 256 * s>(FB[m], ub[s & 3]); }
            if constexpr (o.kind == CO_C) dsr128<(o.a & 1) * 4096>(cf[o.a * 4 + o.b], cad[o.b]);
        };
        static_for<-36, 0>(op);
        static_for<0, 288>([&](auto gc) {
            constexpr int G = CV(gc), s = G / 36, q = G % 36, n = cv_n(q), m = cv_m(q);
            if constexpr (cv_wait(G) >= 0) wait_lr<cv_wait(G)>();
            if constexpr (q == 24 && s >= CL && s - CL < 8) static_for<0, 4>([&](auto ic) { tie(cf[(s - CL) * 4 + CV(ic)]); });
            if constexpr (ZM && G < 8) mfma<true>(acc[m], __builtin_bit_cast(bf16x8, ad), FB[m]);
            mfma<s == 0 && n == 0 && !ZM>(acc[n + m], cat(FA[n][0], FA[n][1]), FB[m]);
            sched();
            if constexpr (q == 11) TL(2 + 2 * s);
            if constexpr (q == 11 && s >= CL && s - CL < 8) wait_v<(s - CL < 7 ? 4 : 0) + (s - CL < (CL <= 1) + (CL <= 0) ? XV : 0) + 8 * (PFQ >= 3 && (s == PFQ - 2 || s == PFQ - 1))>();
            if constexpr (q == 11) TL(3 + 2 * s);
            op(gc);
            if constexpr (q == 30 && ((CTM >> (16 + s)) & 1)) ctouch(16 + s);
            if constexpr (G == 16) hop<17>();
            if constexpr (G == 66) hop<19>();
            if constexpr (G == PG1) page<21>();
            if constexpr (G == PG2) page<28>();
            if constexpr (G == 158) hop<23>();
            if constexpr (G == 209) hop<25>();
            if constexpr (G == 254) hop<27>();
            if constexpr (G == GQ) { unsigned f = 1; zero_chk<4, 1>(f, gad); }
            if constexpr (!NB && G == CBQ) barrier();
            if constexpr (NB == 3 && G == 23) { unsigned f = 1; zero_chk<8, 1>(f, lbase + DONE + (lane & 3) * FST); }
            if constexpr ((q == 24 || q == 25 || q == 29 || q == 34) && s + 2 - CL < 8) {
                constexpr int pc = q == 24 ? 0 : q == 25 ? 1 : q == 29 ? 2 : 3, u = s + 2 - CL;
                dma4<(u & 1) * 4096, pc>(cst, vc[pc], rsc, c_off(lw, u));
            }
            sched();
        });
        if constexpr (PG2 >= 288) page<28>();
        TL(18);
        TS(3);
        {
            const __amdgpu_buffer_rsrc_t rz = mk_brs(ws + WS_ZT, 4 << 20);
            if constexpr (ZM) {
                asm volatile("s_nop 7");
#pragma unroll
                for (int jj = 0; jj < 4; ++jj) {
                    tie(acc[2 * jj]); tie(acc[2 * jj + 1]);
                    const f32x4 a = acc[2 * jj], c = acc[2 * jj + 1];
                    st16(u32x4{pack2(a[0], a[1]), pack2(a[2], a[3]), pack2(c[0], c[1]), pack2(c[2], c[3])}, rz, lane * 16 + jj * 1024, (ch & 510) * 8192 + nx + (ch & 1) * 4096);
                }
            } else {
            u32x2 kvr[8];
            const unsigned kva = lbase + U0 + 2048 + 128 * l15 + 64 * (g4 >> 1) + ((lw ^ ((l15 >> 1) & 3)) << 4) + 8 * (1 - (g4 & 1));
            static_for<0, 8>([&](auto ic) { dsr64<CV(ic) * 2048>(kvr[CV(ic)], kva); });
            wait_l<0>();
            const float Dd = (ch & 1) ? bfhi(dw) : bflo(dw);
#pragma unroll
            for (int jj = 0; jj < 4; ++jj) {
                u32x4 o;
#pragma unroll
                for (int u = 0; u < 2; ++u) {
                    const int j = 2 * jj + u;
                    tie(kvr[j]); tie(acc[j]);
                    const float kv3 = bflo(kvr[j][0]), kv2 = bfhi(kvr[j][0]), kv1 = bflo(kvr[j][1]), kv0 = bfhi(kvr[j][1]);
                    o[2 * u] = pack2(rbf(acc[j][0]) + rbf(kv0 * Dd), rbf(acc[j][1]) + rbf(kv1 * Dd));
                    o[2 * u + 1] = pack2(rbf(acc[j][2]) + rbf(kv2 * Dd), rbf(acc[j][3]) + rbf(kv3 * Dd));
                }
                st16(o, rz, lane * 16 + jj * 1024, (ch & 510) * 8192 + nx + (ch & 1) * 4096);
                if (jj == 1) { sched(); hop<29>(); sched(); }
            }
            }
            TS(8);
            hop<30>();
            if constexpr (CL > 0) {
                wait_v<4>();
                static_for<32 - 4 * CL, 32>([&](auto ic) { constexpr int i = CV(ic); dsr128<((i >> 2) & 1) * 4096>(cf[i], cad[i & 3]); });
                wait_l<0>();
                static_for<32 - 4 * CL, 32>([&](auto ic) { tie(cf[CV(ic)]); });
            }
        }
        TL(19);
        TS(4);
    } else {
        f32x4 acc[8][2];
        cf32 L, cc, M, M2, MG, NG, N4, N8, XC, ag;
        unsigned eo[4], ew, es, dna, ho, hfo, cb, fv; unsigned long long none[4]; u32x4 rse, rh, er;
        {
            unsigned aad[4], bad[4];
#pragma unroll
            for (int i = 0; i < 4; ++i) { aad[i] = lbase + frag(i & 1, i >> 1); bad[i] = aad[i] + 16384 + wid * 4096; }
            const unsigned fr = lbase + FULL + (lane & 3) * FST;
            sld(tag, ws + WS_F + F_EP);
            bf16x8 fa[4][2], fb[2][4];
            unsigned fl = 1, dz = 0, dw_ = lbase + DONE + wid * FST + lane * 4, vt = lbase + POLL + wid * 256 + lane * 4;
            tie(dz); tie(dw_); tie(vt);
            auto op = [&](auto gc) {
                constexpr int G = CV(gc);
                constexpr Op o = g1_op(G);
                if constexpr (o.kind == OP_A) {
                    constexpr int bo = g1_buf(o.a >> 3), hi = bo >= 65536 ? 65536 : 0, i = o.a & 7;
                    dsr128<bo - hi + (i >> 1) * 4096>(fa[i & 3][o.b], aad[(i & 1) + 2 * o.b] + hi);
                }
                if constexpr (o.kind == OP_B) { constexpr int bo = g1_buf(o.a), hi = bo >= 65536 ? 65536 : 0; dsr128<bo - hi>(fb[o.a & 1][o.b], bad[o.b] + hi); }
                if constexpr (o.kind == OP_FLAG) dsr32<(30 - (G >> 5)) * 4>(fl, fr);
                if constexpr (o.kind == OP_SIG) dsw32<o.a * 4>(dw_, dz);
                if constexpr (o.kind == OP_VOTE) dsr32<0>(vt, vt);
            };
            auto consts = [&]() {
                float ld, ar, ai;
                asm volatile("s_waitcnt vmcnt(9)\n\tv_lshlrev_b32 %0, 16, %3\n\tv_lshlrev_b32 %1, 16, %4\n\tv_lshlrev_b32 %2, 16, %5"
                             : "=&v"(ld), "=&v"(ar), "=&v"(ai) : "v"(c_dt), "v"(c_re), "v"(c_im) : "memory");
                const float dtv = rbf(expf(ld)), ea = expf(dtv * ar);
                float sn, cs;
                sincosf(dtv * ai, &sn, &cs);
                L = cf32{ea * cs, ea * sn};
                M = L;
#pragma unroll
                for (int e = 0; e < 5; ++e) M = cmul(M, M);
                M2 = cmul(M, M);
                const cf32 N = cmul(M2, M2), N2 = cmul(N, N), one = {1.f, 0.f};
                N4 = cmul(N2, N2); N8 = cmul(N4, N4);
                auto sel = [](bool c, cf32 a, cf32 b) { return cf32{c ? a.r : b.r, c ? a.i : b.i}; };
                MG = cmul(sel(g4 & 1, M, one), sel(g4 & 2, M2, one));
                NG = cmul(sel(g4 & 1, N, one), sel(g4 & 2, N2, one));
                const float nm = L.r - 1.f, den = ar * ar + ai * ai;
                cc = cf32{2.f * ((nm * ar + L.i * ai) / den), 2.f * ((L.i * ar - nm * ai) / den)};
                auto tc = [&](cf32& v) { tie(v.r); tie(v.i); };
                XC = cf32{g4 == 3 ? M.r : g4 == 2 ? 1.f : 0.f, g4 == 3 ? M.i : 0.f};
                tc(L); tc(cc); tc(M); tc(M2); tc(MG); tc(NG); tc(N4); tc(N8); tc(XC);
                sched();
            };
            if constexpr (CQ < 0) consts();
            __builtin_amdgcn_s_setprio(CP);
            zero_chk<0>(fl, lbase + POLL + (lane & 3) * 256);
            if constexpr (WS0 > 0) { fl = 1; zero_chk<(31 - WS0) * 4, 1>(fl, fr); }
            ties(tag);
            tag += 0x01010101u;
            TS(1);
            static_for<-12, 0>(op);
            static_for<0, 512>([&](auto gc) {
                constexpr int G = CV(gc), st = G >> 5, i = (G >> 2) & 7, j = G & 3;
                if constexpr ((G == 0 || j == 2) && G <= 506) { static_assert(g1_wait(G) >= 0, ""); wait_lr<G == 506 ? 0 : g1_wait(G)>(); }
                if constexpr (G == CQ) { asm volatile("s_waitcnt lgkmcnt(0)\n\ts_nop 7" ::: "memory"); consts(); }
                if constexpr (G == SQ) {
                    c_setup();
                    const int t0 = tm - 1 - g4;
                    static_for<0, 4>([&](auto ic) {
                        constexpr int i = CV(ic);
                        const int t = t0 - 4 * i;
                        cad[i] = lb + frag(i & 1, i >> 1);
                        eo[i] = ((((t ^ tm) & 8) << 10) | ((t & 7) << 8) | (l15 * 16) | (t & OOB)); none[i] = __builtin_amdgcn_ballot_w64(t < 0);
                        tie(vc[i]); tie(cad[i]); tie(eo[i]); ties(none[i]);
                    });
                    ew = (l15 * 16) | (g4 << 20); dna = lbase + DONE + (lane & 3) * FST; ho = g4 * 256 + ((l15 ^ (4 * (g4 & 1))) << 4); fv = lane << 20;
                    tie(ew); tie(dna); tie(ho); tie(fv);
                    sched();
                }
                if constexpr (G == HQ) hop_all(wid);
                {
                    constexpr int n = (SLM >> (4 * st)) & 15;
                    if constexpr (n > 0 && ((G & 31) + 1) * (n & 7) / 32 != (G & 31) * (n & 7) / 32) __builtin_amdgcn_s_sleep(1 + (n >> 3));
                }
                if constexpr ((G & 31) == 20 && st < 15) zero_chk<(30 - st) * 4>(fl, fr);
                if constexpr (((G & 31) == 30 && st < 13) || (NB < 2 && G == B13)) barrier();
                if constexpr ((G & 31) == 20) TL(st);
                mfma<st == 0 && j < 2>(acc[i][j & 1], fa[i & 3][j >> 1], fb[st & 1][j]);
                sched();
                op(gc);
                if constexpr (G == GCR + 16) { unsigned n; nr = vote_cnt(vt, tag, (xcd & 4) << 11, n); }
                if constexpr (G >= GCR + 18 && G < GCR + 22) { eo[G - GCR - 18] ^= nr; tie(eo[G - GCR - 18]); }
                if constexpr (G == GCR + 22) { rse = mk_rsrc(ws + WS_E + tn * 16384 + wid * 2048, 16384); ties(rse); }
                if constexpr (G == GCR + 24) { es = nr + (tm & 7) * 256; cb = wid * 65536 + hb * 1024; er = u32x4{0u, 0u, tag, ~tag}; ties(es); ties(cb); tie(er); }
                if constexpr (G == GCR + 26) { rh = mk_rsrc(ws + WS_H + (size_t)((tm & 7) * 16 + tn) * 65536 + (nr ^ ((tm & 2) << 12)) + wid * 2048, 65536); ties(rh); }
                if constexpr (G == GCR + 28) { hfo = F_H + nr + (((tm & 7) * 16 + tn) * 4 + wid) * 8; ties(hfo); }
                if constexpr (G == 31) page<-1>();
                if constexpr (G == 95) hop<2>();
                if constexpr (G == 159) hop<4>();
                if constexpr (G == 207) hop<5>();
                if constexpr (G == 255) page<7>();
                if constexpr (G == 319) hop<8>();
                if constexpr (G == 383) hop<10>();
                if constexpr (G == 431) hop<11>();
                if constexpr (G == 479) page<13>();
                if constexpr (G == 509) hop<15>();
                if constexpr (G == 489) hop<40>();
                if constexpr (G == 494) hop<41>();
                if constexpr (G == 499) hop<42>();
                if constexpr (G == 504) hop<43>();
                if constexpr (G >= 487) {
                    constexpr int k = G - 486;
                    if constexpr (k == 1) { asm volatile("s_nop 3"); ag = cf32{acc[0][0][0], acc[0][1][0]}; }
                    cf32 t;
                    asm volatile("v_fma_f32 %0, -%3, %5, %6\n\tv_fma_f32 %1, %3, %4, %7\n\tv_fmac_f32 %0, %2, %4\n\tv_fmac_f32 %1, %2, %5"
                                 : "=&v"(t.r), "=&v"(t.i) : "v"(L.r), "v"(L.i), "v"(ag.r), "v"(ag.i), "v"(acc[k >> 2][0][k & 3]), "v"(acc[k >> 2][1][k & 3]));
                    ag = t;
                }
                sched();
            });
        }
        __builtin_amdgcn_s_setprio(3);
        TL(16);
        TS(2);
#pragma unroll
        for (int k = 26; k < 32; ++k) ag = cfma(L, ag, cf32{acc[k >> 2][0][k & 3], acc[k >> 2][1][k & 3]});
        tie(ag.r); tie(ag.i); sched(); hop<44>(); sched();
        cf32 ev_, od, p01, p23;
        sw16(ag.r, ev_.r, od.r); sw16(ag.i, ev_.i, od.i);
        const cf32 pp = cfma(M, ev_, od);
        sw32(pp.r, p01.r, p23.r); sw32(pp.i, p01.i, p23.i);
        cf32 E = cfma(M2, p01, p23);

        er[0] = f2u(E.r); er[1] = f2u(E.i);
        gst<0>(er, ew, rse, es);
        sched();
        TL(17);
        TS(3);
        hop<45>();
        sched();
        auto c_read = [&](auto rc) { static_for<0, 8>([&](auto ic) { constexpr int r = CV(rc), i = CV(ic); dsr128<(i >> 2) * 4096>(cf[r * 8 + i], cad[i & 3] + ((r & 1) ? 0 : 2 * BUF)); }); };
        auto c_tie = [&](auto rc) { static_for<0, 8>([&](auto ic) { tie(cf[CV(rc) * 8 + CV(ic)]); }); };
        auto c_unit = [&](auto uc) { static_for<0, 4>([&](auto ic) { constexpr int u = CV(uc), i = CV(ic); dma4<((u & 2) ? 0 : 2 * BUF) + (u & 1) * 4096, i>(lb, vc[i], rsc, cb + ((rot2 + u) & 7) * 128); }); };
        auto c_req = [&](auto rc) { c_unit(IC(CV(rc) * 2)); c_unit(IC(CV(rc) * 2 + 1)); };
        const float c2r = cc.r, c2i = cc.i;
        auto scale = [&](int i) {
#pragma unroll
            for (int j = 0; j < 4; ++j) {
                const float br = acc[i][0][j], bi = acc[i][1][j];
                acc[i][0][j] = fma_(c2r, br, -(c2i * bi)); acc[i][1][j] = fma_(-c2r, bi, -(c2i * br));
            }
            tie(acc[i][0]); tie(acc[i][1]);
        };
        auto scales = [&](auto ac, auto bc) {
            static_for<CV(ac), CV(bc)>([&](auto ic) {
                constexpr int i = CV(ic);
                scale(i);
                if constexpr (i & 1) { sched(); hop<i == 1 ? 46 : i == 3 ? 16 : i == 5 ? 47 : 48>(); sched(); }
            });
        };
        constexpr int CA = CVR == 0 ? 0 : CVR == 4 ? 1 : 2;
        if constexpr (NB) {
            unsigned dn = 1;
            scales(IC(0), IC(DP));
            if constexpr (CA) { zero_chk<8>(dn, dna); static_for<0, CA>(c_unit); }
            if constexpr (SPR != 3) __builtin_amdgcn_s_setprio(SPR);
            sched();
            scales(IC(DP), IC(ELP));
            if constexpr (!CA) zero_chk<8>(dn, dna);
        } else {
            scales(IC(0), IC(ELP));
            barrier();
            static_for<0, CA>(c_unit);
        }
        TL(18);
        u32x4 ev[4];
#pragma unroll
        for (int i = 0; i < 4; ++i) asm volatile("buffer_load_dwordx4 %0, %1, %2, 0 offen sc1" : "=v"(ev[i]) : "v"(eo[i]), "s"(rse) : "memory");
        asm volatile("" ::: "memory");
        sched();
        constexpr int CB = CVR == 0 ? 1 : CVR == 4 ? 2 : 4;
        static_for<CA, CB>(c_unit);
        scales(IC(ELP), IC(8));
        cf32 X = cfma(XC, p01, cf32{(g4 & 1) ? ev_.r : 0.f, (g4 & 1) ? ev_.i : 0.f});
        tie(X.r); tie(X.i);
        sched();
        hop<24>();
        if constexpr (SPR != 3) __builtin_amdgcn_s_setprio(3);
        if constexpr (CVR == 2) { wait_v<12>(); c_read(IC(0)); wait_l<0>(); c_tie(IC(0)); c_req(IC(2)); }
        wait_v<4 * (CB - CA) + 8 * (CVR == 2)>();
        TL(19);
        tie(ev[0]); tie(ev[1]); tie(ev[2]); tie(ev[3]);
        {
            const unsigned long long tag64 = (unsigned long long)tag | ((unsigned long long)~tag << 32);
#pragma unroll 1
            for (unsigned spins = 0; spins < SPIN_CAP; ++spins) {
                unsigned long long ok = ~0ull;
#pragma unroll
                for (int i = 0; i < 4; ++i) ok &= __builtin_amdgcn_ballot_w64(((unsigned long long)ev[i][2] | ((unsigned long long)ev[i][3] << 32)) == tag64) | none[i];
                TSV(11, spins);
                if (ok == ~0ull) break;
                asm volatile("buffer_load_dwordx4 %0, %4, %8, 0 offen sc1\n\tbuffer_load_dwordx4 %1, %5, %8, 0 offen sc1\n\tbuffer_load_dwordx4 %2, %6, %8, 0 offen sc1\n\t"
                             "buffer_load_dwordx4 %3, %7, %8, 0 offen sc1\n\ts_waitcnt vmcnt(0)" : "=&v"(ev[0]), "=&v"(ev[1]), "=&v"(ev[2]), "=&v"(ev[3])
                             : "v"(eo[0]), "v"(eo[1]), "v"(eo[2]), "v"(eo[3]), "s"(rse) : "memory");
            }
        }
        if constexpr (P18 < 0) page<18>();
        cf32 h;
        {
            auto e = [&](int i) { return cf32{u2f(ev[i][0]), u2f(ev[i][1])}; };
            cf32 c = cmul(NG, cfma(N8, cfma(N4, e(3), e(2)), cfma(N4, e(1), e(0)))), u, v;
            sw16(c.r, u.r, v.r); sw16(c.i, u.i, v.i);
            sw32(u.r + v.r, u.r, v.r); sw32(u.i + v.i, u.i, v.i);
            c = cfma(MG, cf32{u.r + v.r, u.i + v.i}, X);
            h = cf32{fma_(c2r, c.r, -(c2i * c.i)), fma_(-c2r, c.i, -(c2i * c.r))};
        }
        TS(4);
        {
            const cf32 Lc = {L.r, -L.i};
            static_for<0, 4>([&](auto qc) {
                constexpr int q = CV(qc);
                if constexpr (q >= CB) c_unit(qc);
                if constexpr (q == 2) hop<20>();
                u32x4 wr, wi;
#pragma unroll
                for (int u = 0; u < 4; ++u) {
                    const int i = 2 * q + (u >> 1), j = 2 * (u & 1);
                    const cf32 h0 = cfma(Lc, h, cf32{acc[i][0][j], acc[i][1][j]});
                    h = cfma(Lc, h0, cf32{acc[i][0][j + 1], acc[i][1][j + 1]});
                    wr[u] = pack2(h0.r, h.r); wi[u] = pack2(h0.i, h.i);
                }
                if constexpr ((PRE == 2 || GQ >= 0) && q == GE) { tie(wr); tie(wi); dsw32<4>(lbase + DONE + wid * FST + lane * 4, 0u); }
                gst<0>(wr, ho, rh, q * 16384);
                gst<1024>(wi, ho, rh, q * 16384);
                if constexpr (q == P18) page<18>();
                asm volatile("s_nop 1" :: "v"(wr), "v"(wi));
                sched();
            });
        }
        TS(10);
        auto flag = [&]() { gst<0>(u32x2{er[2], er[3]}, fv, rsf, hfo); TS(5); };
        hop<22>();
        if constexpr (CVR == 2) {
            wait_v<16>();
            c_read(IC(1));
            wait_l<0>();
            c_req(IC(3));
            wait_v<8>();
            flag();
        } else {
            if constexpr (CVR == 1) wait_v<8>(); else { wait_v<0>(); flag(); }
            c_read(IC(0)); c_read(IC(1));
            wait_l<8>();
            c_req(IC(2));
            wait_l<0>();
            c_req(IC(3));
            if constexpr (CVR == 1) { wait_v<16>(); flag(); }
        }
        c_tie(IC(0)); c_tie(IC(1));
        static_for<16, 32>([&](auto ic) { undef(cf[CV(ic)]); });
    }
    {
        unsigned ln = lane, dsw_, pf;
        tie(ln);
        const int wid = fresh(wid_), lw = wid & 3, kg = wid >> 2, b = fresh(b_), xcd = b & 7, idx = b >> 3, tq = (idx & 7) + JR(idx >> 3);
        nr = fresh(__builtin_amdgcn_readfirstlane(nr));
        const unsigned nx = nr ^ ((xcd & 2) << 12), nh = nr ^ ((idx & 2) << 12), hp = (idx & 7) * 16 + (b & 1) * 8;
        const int tm2 = (xcd & 4) * 2 + (idx & 7), dt = (xcd & 2) * 2 + (idx >> 3), qx = ((~b & 1) * 2) ^ JQ(idx >> 3);
        const int ta = 512 * (idx >> 3) + 128 * (idx & 1) + 16 * wid + 4 * ((idx >> 1) & 3);
        const int tb = tm2 * 128 + 64 * kg;
        dsr32<DSK - 131072>(dsw_, lbase + 131072 + lw * 64 + (ln & 7) * 8 + ((ln >> 1) & 4));
        unsigned wl = lbase + wid * 4096, w2k = lbase + wid * 2048, hs0 = (hp + ((tq + (wid >> 1)) & 7)) * 65536 + nh + (wid & 1) * 4096 + qx * 16384;
        unsigned s_x = (tb + qx * 8) * 2048 + dt * 256 + lw * 64, s_q = ta * 2048 + xcd * 256, s_z = WS_ZT + (xcd & 3) * 1048576 + nx + idx * 128 + wid * 131072, zfo = WS_F + F_Z + (xcd & 2) * 8192 + nr + (xcd & 1) * 4096;
        unsigned ppo = PK ? WS_KVT + (xcd & 3) * 1048576 + idx * 32768 + kg * 16384 + (KVS ? nx : ((xcd ^ wid) & 4) << 11) + lw * 2048
                          : WS_PP + (xcd & 2) * 1048576 + idx * 65536 + (xcd & 1) * 32768 + kg * 16384 + nx + lw * 2048, s_a = ta * 8192 + xcd * 512, s_b = (tb + (2 ^ qx) * 8) * 8192 + 4096 + dt * 512 + lw * 128;
        const unsigned tagg = fresh(__builtin_amdgcn_readfirstlane(tag));
        u32x4 rsx = mk_rsrc(x, (NOX & 1) ? 0 : 4 << 20), rsq = mk_rsrc(qin, (NOX & 2) ? 0 : 4 << 20), rso = mk_rsrc(out, 16 << 20), rws = mk_rsrc(ws, 32 << 20);
        ties(wl); ties(w2k); ties(hs0); ties(s_x); ties(s_q); ties(s_z); ties(zfo); ties(ppo); ties(s_a); ties(s_b);
        ties(rsx); ties(rsq); ties(rso); ties(rws);
        const unsigned l31 = ln & 31, l5 = ln >> 5;
        unsigned l16 = ln * 16, ha[8];
        ha[0] = (lbase + kg * 1536 + l5 * 2048 + (ln & 2) * 128 + (ln & 1) * 8) | ((((ln >> 1) & 8) | ((ln >> 2) & 3) | ((ln & 2) << 1)) << 4);
        ha[1] = ha[0] ^ 64; ha[2] = ha[0] ^ 512; ha[3] = ha[0] ^ 576;
        static_for<0, 4>([&](auto ic) { ha[4 + CV(ic)] = ha[CV(ic)] + 65536; tie(ha[CV(ic)]); tie(ha[4 + CV(ic)]); });
        unsigned a_x = ((((ln >> 2) & 7) | (ln & 32)) << 11) | (l16 & 48), a_xr = (((l16 & 240) << 2) | (ln & 48)) + (w2k + ZST);
        unsigned a_q = ((ln & 48) << 7) | (l16 & 240), a_qr = (l5 << 10) + l31 * 8 + (w2k + QST), a_iw = w2k + IMG + l16, a_ir = a_iw ^ 8192;
        unsigned a_t = wid == 0 && ln == 0 ? idx * 128 : OOB, msk = 0xfff0fff0u;
        unsigned a_f = wid == 1 ? (ln >> 1) * 128 + (ln & 1) * 4 : OOB, expect = wid == 1 ? ((ln & 1) ? ~tagg : tagg) : 0u;
        unsigned a_z = (((ln ^ l5 ^ (wid * 4)) & 7) << 4) | ((ln & 48) << 10) | ((ln & 8) << 9), a_z2 = a_z ^ 32, a_zr = (l31 << 9) + (((((wid ^ ln) & 7) << 1) | l5) << 3) + (lbase + ZST);
        unsigned a_oa = (l5 << 21) | (l31 << 4), a_ob = ((((ln >> 2) & 4) | (ln & 32) | ((ln >> 3) & 1)) << 13) | (l16 & 112);
        unsigned a_p = PB ? (ln == 63 ? 1020u : OOB) : l16, a_w = ln < 16 ? ln * 128 : OOB;
        if constexpr (PB) tie(a_p);
        if constexpr (P_W >= -1) tie(a_w);
        if constexpr (P_W == -1) pf_ld(pf, a_w, rws, ppo);
        u32x2 tg2 = {tagg, ~tagg};
        tie(l16); tie(a_x); tie(a_xr); tie(a_q); tie(a_qr); tie(a_iw); tie(a_ir); tie(a_t); tie(msk); tie(a_f); tie(expect); tie(a_z); tie(a_z2); tie(a_zr);
        tie(a_oa); tie(a_ob); tie(tg2);
        wait_l<0>();
        tie(dsw_);
        u32x4 dg[2];
        static_for<0, 4>([&](auto ic) { constexpr unsigned long long k = diag_mask(CV(ic)); dg[0][CV(ic)] = sel0(dsw_ & 0xffffu, k); dg[1][CV(ic)] = sel0(dsw_ & 0xffff0000u, k); });
        tie(dg[0]); tie(dg[1]);
        unsigned fso, xso, ppi = ppo ^ (PK ? 1048576 : 32768), tn_[3] = {tagg & 0xf000fu, (tagg >> 8) & 0xf000fu, (tagg >> 4) & 0xf000fu}, psel = 0x05040100u;
        unsigned long long lo8 = 0x00ff00ff00ff00ffull, hi8 = ~lo8;
        ties(ppi); ties(tn_[0]); ties(tn_[1]); ties(tn_[2]); ties(psel); ties(lo8); ties(hi8);
        auto fill = [&](auto sc, auto pc) {
            constexpr int s = CV(sc), p = CV(pc);
            if constexpr (p == 0) dma_x<(2 * s % 3) * BUF, (s >> 1) * 16384 + (s & 1) * 262144, true>(fso, wl, l16, rws, hs0);
            else if constexpr (fill_m0(4 * s + p)) dma_m<(2 * s % 3) * BUF, p * 1024>(fso, wl, l16, rws);
            else dma_n<p * 1024>(fso, l16, rws);
        };
        bf16x4 hf[2][8];
        auto ldh = [&](auto nc, auto ic) {
            constexpr int n = CV(nc), i = CV(ic), j = (i >> 1) & 1, bo = (2 * (n >> 2) % 3) * BUF + (g2_unit(n, j) & 3) * 8192 + (i >> 2) * 4096, hi = bo >= 65536 ? 65536 : 0;
            dsr64tr<bo - hi>(hf[n & 1][i], ha[2 * g2_tile(n, j) + (i & 1) + (hi >> 14)]);
        };
        f32x4 yc[2][2][2];
        f32x4 im[2], ao, ob[2]; u32x4 pl[2], po;
        bf16x8 xf[2];
        u32x2 zv[4], qv[2];
        unsigned ck, ex, fail[2];
        float tq_[2], tz, sm[8], mx[2], cv[8];
        auto finB = [&](auto pc, auto jc) {
            constexpr int p = CV(pc), j = CV(jc);
            if constexpr (j < 8) { yc[p & 1][0][j & 1][j >> 1] += im[j & 1][j >> 1]; tie(yc[p & 1][0][j & 1]); }
            else {
                constexpr int i = (j - 8) >> 2, k = j & 3, dd = k & 1, h = k >> 1;
                ob[i][k] = sel8(yc[p & 1][0][dd][2 * i + 1 - h], yc[p & 1][0][dd][2 * i + h], h ? hi8 : lo8);
                if constexpr (k == 3 && (p == 3 || !(NOST & 4))) gst<0>(ob[i], a_ob, rso, s_b ^ ((p & 1) * 65536 + i * 16384));
                if constexpr (j == 12) keep(ob[0]);
            }
        };
        auto snd = [&](auto pc, auto jc) {
            constexpr int p = CV(pc), j = CV(jc);
            if constexpr (j < 8) { sm[j] = yc[p][0][j >> 2][j & 3] + im[j >> 2][j & 3]; tie(sm[j]); }
            if constexpr (j == 8) mx[0] = amax3(sm[0], sm[1], sm[2]);
            if constexpr (j == 9) mx[1] = amax3(sm[3], sm[4], sm[5]);
            if constexpr (j == 10) mx[0] = amax3(mx[0], sm[6], sm[7]);
            if constexpr (j == 11) mx[0] = amax3(mx[0], mx[1], mx[1]);
            if constexpr (j == 12) { ex = f2u(mx[0]) + 0x4000u; tie(ex); }
            if constexpr (j == 13) { ex &= 0x7f800000u; tie(ex); }
            if constexpr (j == 14) { mx[0] = u2f(ex + 0x04c00008u); tie(mx[0]); }
            if constexpr (j == 15) { ex >>= 23; tie(ex); }
            if constexpr (j >= 16 && j < 24) { sm[j - 16] += mx[0]; tie(sm[j - 16]); }
            if constexpr (j == 24) { ex |= ex << 12; tie(ex); }
            if constexpr (j >= 25 && j < 29) { po[j - 25] = __builtin_amdgcn_perm(f2u(sm[2 * (j - 25) + 1]), f2u(sm[2 * (j - 25)]), psel); tie(po); }
            if constexpr (j == 29) { po[0] = bfi_v(msk, po[0], ex); tie(po); }
            if constexpr (j >= 30 && j < NS) { po[j - 29] = bfi_s(msk, po[j - 29], tn_[j - 30]); if constexpr (j < NS - 1) tie(po); }
            if constexpr (j == NS - 1 && !(NOST & 2)) gst<p * 1024>(po, l16, rws, ppo);
        };
        auto rcv = [&](auto hc, auto jc) {
            constexpr int h = CV(hc), j = CV(jc);
            if constexpr (j < 3) { pl[h][j + 1] ^= tn_[j]; pp_tie<h>(pl[h]); }
            if constexpr (j == 3) { ck = pl[h][1] | pl[h][2] | pl[h][3]; tie(ck); }
            if constexpr (j == 4) { if constexpr (PB) ck = bfi_f(msk, fail[h], ck); else ck &= 0xf000fu; tie(ck); }
            if constexpr (j == 6 && !(NOST & 2)) pp_chk<h>(pl[h], ck, tagg, l16, rws, ppi, a_p);
            if constexpr (j == 7) { ex = pl[h][0] >> 12; tie(ex); }
            if constexpr (j == 8) ex = bfi_v(msk, ex, pl[h][0]);
            if constexpr (j == 9) { pl[h][0] &= msk; pp_tie<h>(pl[h]); }
            if constexpr (j == 10) { ex <<= 23; tie(ex); }
            if constexpr (j == 11) { ex &= 0x7f800000u; tie(ex); }
            if constexpr (j == 12) { mx[1] = u2f(ex) * 0x1p-14f; tie(mx[1]); }
            if constexpr (h == 0 && j >= 13) {
                constexpr int k = j - 13, a = (k + 1) / 2, e = k == 15 ? 7 : k / 2 - 1;
                if constexpr (k == 0 || ((k & 1) && k < 15)) cv[a] = cvt16<a & 1>(pl[h][a >> 1]);
                else { yc[0][0][e >> 2][e & 3] = fma_(cv[e], mx[1], yc[0][0][e >> 2][e & 3]); tie(yc[0][0][e >> 2]); }
            }
            if constexpr (h == 1 && j >= 13 && j < 21) cv[j - 13] = cvt16<(j - 13) & 1>(pl[h][(j - 13) >> 1]);
            if constexpr (h == 1 && j >= 21) { constexpr int e = j - 21; im[e >> 2][e & 3] = fma_(cv[e], mx[1], im[e >> 2][e & 3]); tie(im[e >> 2]); }
        };
        auto side = [&](auto pc) {
            constexpr int P = CV(pc), n = P >> 3, m = P & 7, p = n >> 3;
            if constexpr (P == P_X || P == P_X + 2) dma_x<ZST + (P != P_X) * 1024, (P != P_X) * 16384, false>(xso, w2k, a_x, rsx, s_x);
            if constexpr (P == P_W) pf_ld(pf, a_w, rws, ppo);
            if constexpr (!NOA) {
            if constexpr (P == P_Q || P == P_Q + 2) dma_x<QST + (P != P_Q) * 1024, (P != P_Q) * 524288, false>(xso, w2k, a_q, rsq, s_q);
            if constexpr (P == P_T) gst<0>(tg2, a_t, rws, zfo);
            if constexpr (P == AT(ZP, 4)) { if constexpr (P_W >= 0) pf_keep(pf); pf_ld(pf, a_f, rws, zfo); }
            if constexpr (P == P_ZC) { wait_v<g2_allow(P, AT(ZP, 4))>(); tag_chk(pf, expect, a_f, rws, zfo); }
            if constexpr (P == P_Z || P == P_Z + 2) dma_x<ZST + (P != P_Z) * 1024, (P != P_Z) * 65536, true>(xso, w2k, P == P_Z ? a_z : a_z2, rws, s_z);
            }
            if constexpr (P == AT(7, 5) || P == AT(7, 7)) dsr128<(m - 5) * 512>(xf[(m - 5) / 2], a_xr);
            if constexpr (P == AT(8, 4)) { tie(xf[0]); tie(xf[1]); }
            if constexpr (P == AT(8, 5) || P == AT(9, 3)) static_for<0, 2>([&](auto dc) { mfma<false>(yc[n & 1][0][CV(dc)], xf[n & 1], __builtin_bit_cast(bf16x8, dg[CV(dc)])); });
            if constexpr ((n & 7) == 6 && m == XW && !(DBG & 2)) dsw128<0>(a_iw, yc[p & 1][1][0]);
            if constexpr ((n & 7) == 6 && m == XW2 && !(DBG & 2)) dsw128<1024>(a_iw, yc[p & 1][1][1]);
            if constexpr ((n & 7) == 7 && (m & 5) == (n < 31 ? 4 : 0)) dsr128<(m & 2) * 512>(im[(m >> 1) & 1], a_ir);
            if constexpr (n >= 8 && (n & 7) == 0 && m == 4) { tie(im[0]); tie(im[1]); }
            if constexpr (P >= P_PP0 && P <= E_PP0) snd(IC(0), IC(P - P_PP0));
            if constexpr (P >= P_PP1 && P <= E_PP1) static_for<0, D1>([&](auto ic) { snd(IC(1), IC((P - P_PP1) * D1 + CV(ic))); });
            if constexpr (PB && (P == P_B0 - 1 || P == P_B1 - 1)) pf_zero(pf);
            if constexpr (PB && (P == P_B0 || P == P_B1)) pf_at<(P == P_B1) * 1024>(pf, a_p, rws, ppi);
            if constexpr (P == P_L0 || P == P_L1) {
                unsigned so = ppi;
                if constexpr (PB) { wait_v<g2_allow(P, P - QB) + (g2_fill(P) >= 0)>(); pp_sel(so, fail[P == P_L1], pf, tn_[2], ppi, ppo); }
                pp_ld<P == P_L1>(pl[P == P_L1], l16, rws, so);
            }
            if constexpr (P == P_R0 || P == P_R1) { wait_v<g2_allow(P, P == P_R0 ? imax(P_L0, XL ? P_X + 2 : 0) : P_L1)>(); pp_tie<P == P_R1>(pl[P == P_R1]); }
            if constexpr (P >= P_R0 && P <= E_R0) static_for<0, D0>([&](auto ic) { if constexpr ((P - P_R0) * D0 + CV(ic) < NR) rcv(IC(0), IC((P - P_R0) * D0 + CV(ic))); });
            if constexpr (P >= P_R1 && P < P_R1 + 21) rcv(IC(1), IC(P - P_R1));
            if constexpr (!NOA) {
            if constexpr (n == ZW && m >= 4) dsr64<(m - 4) * 128>(zv[m - 4], a_zr);
            static_for<0, 4>([&](auto rc) {
                constexpr int r = CV(rc);
                if constexpr (P == q_tie(r)) tie(qv[r & 1]);
                if constexpr (P == q_rd(r)) dsr64<r * 256>(qv[r & 1], a_qr);
            });
            if constexpr (P == AT(ZW + 1, 4)) { tie(zv[0]); tie(zv[1]); tie(zv[2]); tie(zv[3]); }
            }
            if constexpr (a_step(P) >= 0) {
                constexpr int s = a_step(P), e = s == 0 ? 0 : s % 3 == 2 ? (s + 1) / 3 : s % 3 == 1 ? s / 3 : s / 3 - 1, r = e >> 2, c = e & 3;
                if constexpr (s == 0 || (s % 3 == 2 && e < 16)) { tq_[e & 1] = (c & 1) ? bfhi(qv[r & 1][c >> 1]) : bflo(qv[r & 1][c >> 1]); tie(tq_[e & 1]); }
                else if constexpr (s % 3 == 1) { tz = (r & 1) ? bfhi(zv[c][r >> 1]) : bflo(zv[c][r >> 1]); tie(tz); }
                else if constexpr (s % 3 == 0) {
                    ao[c] = tq_[e & 1] * tz;
                    if constexpr (c == 3 && !(NOST & 1)) gst<0>(ao, a_oa, rso, s_a + r * 8192);
                    else tie(ao);
                }
            }
            if constexpr (P >= P_F2 && P < P_F2 + 16) finB(IC(2), IC(P - P_F2));
            if constexpr (a_step(P - 1) > 0 && a_step(P - 1) % 12 == 0) keep(ao);
            if constexpr (P == E_PP0 + 1 || P == E_PP1 + 1) keep(po);
            if constexpr (P == P_F2 + 16) keep(ob[1]);
        };
        {
            unsigned e_h = wid == 3 ? ((ln & 1) ? ~tagg : tagg) : 0u, hsl = lbase + lw * 8192, hso = (hp + ((tq + wid) & 7)) * 65536 + nh + qx * 16384;
            unsigned a_h = wid == 3 ? ln * 4 : OOB, s_h = WS_F + F_H + nr + hp * 32, ca2[4], a_g = lbase + HGO + ln * 4;
            static_for<0, 4>([&](auto ic) { ca2[CV(ic)] = cad[CV(ic)] + 2 * BUF; tie(ca2[CV(ic)]); });
            tie(e_h); tie(a_h); tie(a_g); ties(hsl); ties(hso); ties(s_h);
            sched();
            if (wid < 4) {
                auto look = [&]() { if constexpr (HA) pf_at<0>(pf, a_h, rws, s_h); else pf_ld(pf, a_h, rws, s_h); };
                if constexpr (HA) pf_zero(pf);
                if constexpr (PW == 0) { look(); wait_v<1>(); }
                if constexpr (PW == 1) { wait_v<0>(); look(); }
                if constexpr (PW == 2) { wait_v<8>(); look(); wait_v<1>(); }
                static_for<16, 32>([&](auto ic) { constexpr int i = CV(ic); dsr128u<((i >> 2) & 1) * 4096>(cf[i], (i & 8) ? cad[i & 3] : ca2[i & 3]); });
                wait_l<0>();
                static_for<16, 32>([&](auto ic) { tie(cf[CV(ic)]); });
                wait_v<0>();
                tag_chk<1, HA>(pf, e_h, a_h, rws, s_h);
                if (wid == 3) flag_set(a_g, 1u); else gate<1>(a_g, 1);
                TS(6);
                hop<26>();
                static_for<0, 4>([&](auto ic) {
                    constexpr int i = CV(ic);
                    dma_x<(i >> 1) * 2 * BUF + (i & 1) * 4096, (i >> 1) * 262144 + (i & 1) * 4096, true>(fso, hsl, l16, rws, hso);
                    dma_n<1024>(fso, l16, rws); dma_n<2048>(fso, l16, rws); dma_n<3072>(fso, l16, rws);
                });
            }
        }
        wait_v<8>();
        if (wid < 4) TS(7);
        TG_DECL
        barrier();
        static_for<0, 8>([&](auto ic) { ldh(IC(0), ic); });
        static_for<0, 32>([&](auto nc) {
            constexpr int n = CV(nc), p = n >> 3;
            if constexpr (n == PG3) page<33>();
            if constexpr (n == PG4) page<37>();
            wait_lr<(n & 3) == 3 ? 0 : 4 + g2_lx(n - 1)>();
            if constexpr (n == 31) barrier();
            if constexpr ((n & 3) == 3 && n < 31) {
                constexpr int need = g2_need(n);
                TL(21 + n / 4 * 3);
                wait_v<g2_allow(8 * n, need)>();
                TL(22 + n / 4 * 3);
                barrier();
                TL(23 + n / 4 * 3);
            }
            if constexpr (n == 1) hop<31>();
            if constexpr (n == 4) hop<32>();
            if constexpr (n == 11) hop<34>();
            if constexpr (n == 15) hop<35>();
            if constexpr (n == 19) hop<36>();
            if constexpr (n == 27) hop<38>();
            if constexpr (n == 31) hop<39>();
            if constexpr (n == 0) TL(20);
            TG(20 + n);
            if constexpr (n == 31) TL(42);
            if constexpr (PRG) __builtin_amdgcn_s_setprio(n == 31 ? 0 : 3 - ((n + 1) & 3));
            sched();
            static_for<0, 8>([&](auto mc) {
                constexpr int m = CV(mc), ks = m >> 2, tt = (m >> 1) & 1, dd = m & 1, P = n * 8 + m;
                if constexpr (m == 4 && (n & 3) != 3) wait_lr<(rd1(n) ? 8 : 4) + ((n & 7) == 6 ? (XW < 4) + (XW2 < 4) : 0)>();
                mfma<(n & 7) == 0 && ks == 0>(yc[p & 1][(DBG & 1) ? tt : g2_tile(n, tt)][dd], cat(hf[n & 1][4 * ks + 2 * tt], hf[n & 1][4 * ks + 2 * tt + 1]), cf[g2_unit(n, tt) * 4 + 2 * ks + dd]);
                sched();
                if constexpr (n < 31 && rd1(n) && m < 4) { ldh(IC(n + 1), IC(2 * m)); ldh(IC(n + 1), IC(2 * m + 1)); }
                if constexpr (n < 31 && !rd1(n)) ldh(IC(n + 1), mc);
                if constexpr (g2_fill(P) >= 0) fill(IC(g2_fill(P) >> 2), IC(g2_fill(P) & 3));
                side(IC(P));
                sched();
            });
        });
        TG(52);
        if (wid >= 4) TS(5); else TS(8);
        TG(53);
        static_for<256 - P_R1, 21>([&](auto jc) { rcv(IC(1), jc); });
        wait_l<0>();
        tie(im[0]); tie(im[1]);
        static_for<21, NR>([&](auto jc) { rcv(IC(1), jc); });
        asm volatile("s_nop 3");
        sched();
        static_for<0, 16>([&](auto jc) { finB(IC(3), jc); });
        if (b == 255 && wid == 0) gst<0>(u32x2{tagg, tagg}, l16 ? OOB : 0u, rws, WS_F + F_EP);
    }
}

}

#if defined(__HIP_DEVICE_COMPILE__)
#pragma clang attribute pop
#endif

extern "C" void kernel_launch(void* const* d_in, const int* in_sizes, int n_in, void* d_out, int out_size, void* d_ws, size_t ws_size,
                              hipStream_t stream) {
    (void)in_sizes; (void)n_in; (void)out_size; (void)ws_size;
    using namespace fk;
    const uint16_t* q = (const uint16_t*)d_in[0];
    const uint16_t* kin = (const uint16_t*)d_in[1];
    const uint16_t* vin = (const uint16_t*)d_in[2];
    const uint16_t* sk = (const uint16_t*)d_in[3];
    const uint16_t* ssmk = (const uint16_t*)d_in[4];
    const uint16_t* ssmD = (const uint16_t*)d_in[5];
    const uint16_t* x = (const uint16_t*)d_in[6];
    const uint16_t* lre = (const uint16_t*)d_in[7];
    const uint16_t* lim = (const uint16_t*)d_in[8];
    const uint16_t* bre = (const uint16_t*)d_in[9];
    const uint16_t* bim = (const uint16_t*)d_in[10];
    const uint16_t* cre = (const uint16_t*)d_in[11];
    const uint16_t* cim = (const uint16_t*)d_in[12];
    const uint16_t* logdt = (const uint16_t*)d_in[13];
    const uint16_t* dskip = (const uint16_t*)d_in[14];
#define WOFF 0
    char* ws = (char*)(((uintptr_t)d_ws + ((size_t)WOFF << 20) + 0x1fffff) & ~(uintptr_t)0x1fffff);
    hipLaunchKernelGGL(k1, dim3(256), dim3(512), 0, stream, x, bre, bim, lre, lim, logdt, cre, cim, dskip, ssmk, ssmD, q, kin, vin, sk, ws, (float*)d_out TS_PASS(0));
}
```

```cpp
#include <hip/hip_runtime.h>
#include <stdint.h>
#include <type_traits>

#if defined(__HIP_DEVICE_COMPILE__)
#pragma clang attribute push(__attribute__((target("no-packed-fp32-ops"))), apply_to = function)
#endif

namespace fk {

typedef __attribute__((ext_vector_type(8))) short bf16x8;
typedef __attribute__((ext_vector_type(4))) short bf16x4;
typedef __attribute__((ext_vector_type(4))) float f32x4;
typedef __attribute__((ext_vector_type(4))) unsigned u32x4;
typedef __attribute__((ext_vector_type(16))) unsigned u32x16;
typedef __attribute__((ext_vector_type(2))) unsigned u32x2;
typedef __attribute__((ext_vector_type(2))) __bf16 bf16x2_t;
typedef __attribute__((address_space(1))) unsigned gu32;
typedef __attribute__((address_space(1))) unsigned long long gu64;
#define RLX_AGENT __ATOMIC_RELAXED, __HIP_MEMORY_SCOPE_AGENT

__device__ __forceinline__ float u2f(unsigned v) { return __builtin_bit_cast(float, v); }
__device__ __forceinline__ unsigned f2u(float v) { return __builtin_bit_cast(unsigned, v); }
__device__ __forceinline__ bool wall(bool p) { return __builtin_amdgcn_ballot_w64(p) == ~0ull; }
__device__ __forceinline__ float bflo(unsigned w) { return u2f(w << 16); }
__device__ __forceinline__ float bfhi(unsigned w) { return u2f(w & 0xffff0000u); }
__device__ __forceinline__ unsigned pack2(float lo, float hi) {
    bf16x2_t v; v[0] = (__bf16)lo; v[1] = (__bf16)hi; return __builtin_bit_cast(unsigned, v);
}
__device__ __forceinline__ float rbf(float f) { float r; asm("v_cvt_pk_bf16_f32 %0, 0, %1" : "=v"(r) : "v"(f)); return r; }
__device__ __forceinline__ float fma_(float a, float b, float c) { return __builtin_fmaf(a, b, c); }

template <int I, int N, class F>
__device__ __forceinline__ void static_for(F&& f) {
    if constexpr (I + 1 == N) f(std::integral_constant<int, I>{});
    else if constexpr (I < N) { static_for<I, I + (N - I) / 2>(f); static_for<I + (N - I) / 2, N>(f); }
}
#define IC(v) std::integral_constant<int, (v)>{}
#define CV(c) decltype(c)::value

#define TS_ARG
#define TS_ARG0
#define TS_PASS(off)
#define TS_DECL
#define TS(i)
#define TSV(i, v)
#define TL_DECL
#define TL(i)
#define TS_FLUSH(slot)

#define TG_DECL
#define TG(i)
template <int N> __device__ __forceinline__ void wait_v() { asm volatile("s_waitcnt vmcnt(%0)" ::"n"(N > 63 ? 63 : N) : "memory"); }
template <int N> __device__ __forceinline__ void wait_l() { asm volatile("s_waitcnt lgkmcnt(%0)" ::"n"(N > 14 ? 14 : N) : "memory"); }
template <int N> __device__ __forceinline__ void wait_lr() { __builtin_amdgcn_s_waitcnt(0xc07f | ((N > 14 ? 14 : N) << 8)); }
template <class T> __device__ __forceinline__ void tie(T& x) { asm volatile("" : "+v"(x)); }
template <class T> __device__ __forceinline__ void undef(T& x) { asm volatile("" : "=v"(x)); }
template <int OFF, class T> __device__ __forceinline__ void dsr128u(T& d, unsigned a) { asm volatile("ds_read_b128 %0, %1 offset:%2" : "+v"(d) : "v"(a), "n"(OFF)); }
template <class T> __device__ __forceinline__ void keep(const T& x) { asm volatile("" :: "v"(x)); }
template <int OFF, class T> __device__ __forceinline__ void dsr128(T& d, unsigned a) { asm volatile("ds_read_b128 %0, %1 offset:%2" : "=v"(d) : "v"(a), "n"(OFF)); }
template <int OFF, class T> __device__ __forceinline__ void dsr64(T& d, unsigned a) { asm volatile("ds_read_b64 %0, %1 offset:%2" : "=v"(d) : "v"(a), "n"(OFF)); }
template <int OFF> __device__ __forceinline__ void dsr32(unsigned& d, unsigned a) { asm volatile("ds_read_b32 %0, %1 offset:%2" : "=v"(d) : "v"(a), "n"(OFF)); }
template <int O0, int O1, class T> __device__ __forceinline__ void dsr2(T& d, unsigned a) { asm volatile("ds_read2_b32 %0, %1 offset0:%2 offset1:%3" : "=v"(d) : "v"(a), "n"(O0), "n"(O1)); }
template <int OFF> __device__ __forceinline__ void dsr64tr(bf16x4& d, unsigned a) { asm volatile("ds_read_b64_tr_b16 %0, %1 offset:%2" : "=v"(d) : "v"(a), "n"(OFF)); }
template <int OFF, class T> __device__ __forceinline__ void dsw128(unsigned a, const T& v) { asm volatile("ds_write_b128 %0, %1 offset:%2" :: "v"(a), "v"(v), "n"(OFF) : "memory"); }
template <int OFF> __device__ __forceinline__ void dsw32(unsigned a, unsigned v) { asm volatile("ds_write_b32 %0, %1 offset:%2" :: "v"(a), "v"(v), "n"(OFF) : "memory"); }
__device__ __forceinline__ bf16x8 cat(const bf16x4& lo, const bf16x4& hi) { return __builtin_shufflevector(lo, hi, 0, 1, 2, 3, 4, 5, 6, 7); }
__device__ __forceinline__ void barrier() { asm volatile("" ::: "memory"); __builtin_amdgcn_s_barrier(); asm volatile("" ::: "memory"); }
__device__ __forceinline__ int fresh(int v) { asm volatile("" : "+s"(v)); return v; }
__device__ __forceinline__ void sched() { __builtin_amdgcn_sched_barrier(0); }
template <bool FIRST> __device__ __forceinline__ void mfma(f32x4& c, const bf16x8& a, const bf16x8& b) {
    if constexpr (FIRST) asm volatile("v_mfma_f32_16x16x32_bf16 %0, %1, %2, 0" : "=v"(c) : "v"(a), "v"(b));
    else asm volatile("v_mfma_f32_16x16x32_bf16 %0, %1, %2, %0" : "+v"(c) : "v"(a), "v"(b));
}
struct cf32 { float r, i; };
__device__ __forceinline__ cf32 cmul(cf32 a, cf32 b) { return cf32{fma_(a.r, b.r, -(a.i * b.i)), fma_(a.r, b.i, a.i * b.r)}; }
__device__ __forceinline__ cf32 cfma(cf32 a, cf32 b, cf32 c) { return cf32{fma_(a.r, b.r, fma_(-a.i, b.i, c.r)), fma_(a.r, b.i, fma_(a.i, b.r, c.i))}; }
__device__ __forceinline__ void sw16(float v, float& e, float& o) { const auto r = __builtin_amdgcn_permlane16_swap(f2u(v), f2u(v), false, false); e = u2f(r[0]); o = u2f(r[1]); }
__device__ __forceinline__ void sw32(float v, float& e, float& o) { const auto r = __builtin_amdgcn_permlane32_swap(f2u(v), f2u(v), false, false); e = u2f(r[0]); o = u2f(r[1]); }
constexpr unsigned SPIN_CAP = 1u << 16;
constexpr unsigned OOB = 0x40000000u;

constexpr size_t WS_H = 0;
constexpr size_t WS_KVT = (size_t)8 << 20;
constexpr size_t WS_ZT = (size_t)12 << 20;
constexpr size_t WS_PP = (size_t)16 << 20;
constexpr size_t WS_E = (size_t)24 << 20;
constexpr size_t WS_F = WS_E + (256 << 10);
constexpr int F_H = 0;
constexpr int F_K = 4096;
constexpr int F_Z = 16384;
constexpr int F_EP = F_Z + 32768;
constexpr int F_C = F_EP + 256;
constexpr unsigned WS_P = 25 << 20;
#define KP 0
#define SU 0
#define TSX(i) do { if constexpr (!SU) TS(i); } while (0)
#define AB 1
#define SW 1
#define BL 0
#define GS 0
#define KCQ 2
#define KCC 4
#define KVP 0
#define RSL 1
#define PB 0
#define KTI 1
#define TM 0
#define TX 0
#define PK 1
#define HA 0
#define KVS 1
#define GCR 273
constexpr int LDS_TOTAL = 163840, BUF = 32768;
constexpr int KC = 98304, KCH = 8320, KCP = 4160;
constexpr int U0 = KC + 4 * KCH;
constexpr int FULL = U0 + 18432 + 8192, FST = 388;
constexpr int DONE = U0 + 18432, DUMP = DONE + 4096;
constexpr int HGO = FULL + 1792, DSK = HGO + 256, POLL = DSK + 256;
static_assert(POLL + 8 * 256 <= LDS_TOTAL && 4 * FST <= 1792, "lds");
constexpr int g1_buf(int s) { return ((0x0213 >> ((s & 3) * 4)) & 3) * BUF; }
constexpr int IMG = KC, QST = KC + 16384, ZST = U0, PLD = 2 * BUF;
#define CODE_KB 43
#define EXACT 0
#define HP 1
#define CP 2
#define FLQ 11
#define R01 3
#define ZP 7
#define SLP 8
#define STR_(x) #x
#define STR(x) STR_(x)
#define NOST 0
#define G2M 0
#define RD 2
#define PRG 1
#define OSC 16
#define NOA 0
#define PPH 0
#define HS 0
#define JQ(j) (HS == 0 ? 0 : HS == 1 ? (j) & 1 : HS == 2 ? (j) : (j) & 2)
#define JR(j) (HS == 0 ? 2 * (j) : HS == 1 ? 2 * ((j) & 2) : HS == 2 ? 0 : 4 * ((j) & 1))
#define NOX 0

__device__ __forceinline__ u32x4 mk_rsrc(const void* p, unsigned bytes) {
    const unsigned long long a = (unsigned long long)p;
    return u32x4{(unsigned)a, (unsigned)(a >> 32) & 0xffffu, bytes, 0x00020000u};
}
__device__ __forceinline__ __amdgpu_buffer_rsrc_t mk_brs(const void* p, unsigned bytes) { return __builtin_amdgcn_make_buffer_rsrc((void*)p, (short)0, bytes, 0x00020000); }
__device__ __forceinline__ void st16(const u32x4& d, __amdgpu_buffer_rsrc_t r, unsigned voff, unsigned soff) {
    __builtin_amdgcn_raw_buffer_store_b128(d, r, voff, soff, 16);
    asm volatile("s_nop 1" :: "v"(d) : "memory");
}
template <int IMM, bool SC1> __device__ __forceinline__ void dma(unsigned lds, unsigned voff, const u32x4& rs, unsigned soff) {
    if constexpr (SC1) asm volatile("s_add_u32 m0, %0, %1\n\ts_nop 0\n\tbuffer_load_dwordx4 %2, %3, %4 offen sc1 lds" :: "s"(lds), "n"(IMM), "v"(voff), "s"(rs), "s"(soff) : "memory", "scc");
    else asm volatile("s_add_u32 m0, %0, %1\n\ts_nop 0\n\tbuffer_load_dwordx4 %2, %3, %4 offen lds" :: "s"(lds), "n"(IMM), "v"(voff), "s"(rs), "s"(soff) : "memory", "scc");
}
template <int IMM, int I> __device__ __forceinline__ void dma4(unsigned lds, unsigned voff, const u32x4& rs, unsigned soff) {
    if constexpr (I == 0) dma<IMM, false>(lds, voff, rs, soff);
    else asm volatile("buffer_load_dwordx4 %0, %1, %2 offen offset:%3 lds" :: "v"(voff), "s"(rs), "s"(soff), "n"(I * 1024) : "memory");
}
template <int IMM> __device__ __forceinline__ void dma1(unsigned lds, unsigned voff, const u32x4& rs, unsigned soff) {
    asm volatile("s_add_u32 m0, %0, %1\n\ts_nop 0\n\tbuffer_load_dword %2, %3, %4 offen sc1 lds" :: "s"(lds), "n"(IMM), "v"(voff), "s"(rs), "s"(soff) : "memory", "scc");
}
__device__ __forceinline__ void flag_set(unsigned waddr, unsigned v) { asm volatile("ds_write_b32 %0, %1" :: "v"(waddr), "v"(v) : "memory"); }
__device__ __forceinline__ void flag_chk(unsigned& f, unsigned raddr, unsigned need) {
    unsigned cnt;
    asm volatile("v_cmp_le_u32 vcc, %3, %0\n\t"
                 "s_cmp_eq_u64 vcc, -1\n\t"
                 "s_cbranch_scc1 1f\n\t"
                 "s_mov_b32 %1, 0x100000\n"
                 "0:\n\t"
                 "ds_read_b32 %0, %2\n\t"
                 "s_sub_u32 %1, %1, 1\n\t"
                 "s_waitcnt lgkmcnt(0)\n\t"
                 "s_cbranch_scc1 1f\n\t"
                 "v_cmp_le_u32 vcc, %3, %0\n\t"
                 "s_cmp_eq_u64 vcc, -1\n\t"
                 "s_cbranch_scc0 0b\n"
                 "1:" : "+v"(f), "=&s"(cnt) : "v"(raddr), "s"(need) : "vcc", "scc", "memory");
}
template <int OFF, int SL = 0> __device__ __forceinline__ void zero_chk(unsigned& f, unsigned raddr) {
    unsigned cnt;
    asm volatile("v_cmp_ne_u32 vcc, 0, %0\n\t"
                 "s_cbranch_vccz 1f\n\t"
                 "s_mov_b32 %1, 0x100000\n"
                 "0:\n\t"
                 ".if %4\n\ts_sleep %4\n\t.endif\n\t"
                 "ds_read_b32 %0, %2 offset:%3\n\t"
                 "s_sub_u32 %1, %1, 1\n\t"
                 "s_waitcnt lgkmcnt(0)\n\t"
                 "s_cbranch_scc1 1f\n\t"
                 "v_cmp_ne_u32 vcc, 0, %0\n\t"
                 "s_cbranch_vccnz 0b\n"
                 "1:" : "+v"(f), "=&s"(cnt) : "v"(raddr), "n"(OFF), "n"(SL) : "vcc", "scc", "memory");
}
template <int IMM, class T> __device__ __forceinline__ void gld(T& d, unsigned voff, const u32x4& rs, unsigned soff) {
    if constexpr (sizeof(T) == 16) asm volatile("buffer_load_dwordx4 %0, %1, %2, %3 offen offset:%4" : "=v"(d) : "v"(voff), "s"(rs), "s"(soff), "n"(IMM) : "memory");
    else if constexpr (sizeof(T) == 8) asm volatile("buffer_load_dwordx2 %0, %1, %2, %3 offen offset:%4" : "=v"(d) : "v"(voff), "s"(rs), "s"(soff), "n"(IMM) : "memory");
    else asm volatile("buffer_load_ushort %0, %1, %2, %3 offen offset:%4" : "=v"(d) : "v"(voff), "s"(rs), "s"(soff), "n"(IMM) : "memory");
}
__device__ __forceinline__ void gld32(unsigned& d, unsigned voff, const u32x4& rs, unsigned soff) { asm volatile("buffer_load_dword %0, %1, %2, %3 offen" : "=v"(d) : "v"(voff), "s"(rs), "s"(soff) : "memory"); }
__device__ __forceinline__ void sld(unsigned& d, const void* p) { asm volatile("s_load_dword %0, %1, 0x0" : "=s"(d) : "s"(p) : "memory"); }
__device__ __forceinline__ void stouch(unsigned long long p) { asm volatile("s_load_dword s101, %0, 0x0\n\ts_load_dword s101, %0, 0x80\n\ts_load_dword s101, %0, 0x100" :: "s"(p) : "s101", "memory"); }
template <class T> __device__ __forceinline__ void ties(T& x) { asm volatile("" : "+s"(x)); }
template <int IMM, class T> __device__ __forceinline__ void gst(const T& d, unsigned voff, const u32x4& rs, unsigned soff) {
    if constexpr (sizeof(T) == 16) asm volatile("buffer_store_dwordx4 %0, %1, %2, %3 offen offset:%4 sc1" :: "v"(d), "v"(voff), "s"(rs), "s"(soff), "n"(IMM) : "memory");
    else asm volatile("buffer_store_dwordx2 %0, %1, %2, %3 offen offset:%4 sc1" :: "v"(d), "v"(voff), "s"(rs), "s"(soff), "n"(IMM) : "memory");
}
template <int SLQ> __device__ __forceinline__ void gate(unsigned raddr, unsigned need) {
    unsigned cnt, f;
    asm volatile("s_mov_b32 %1, 0x40000\n"
                 "0:\n\t"
                 "ds_read_b32 %0, %2\n\t"
                 "s_sub_u32 %1, %1, 1\n\t"
                 "s_waitcnt lgkmcnt(0)\n\t"
                 "s_cbranch_scc1 1f\n\t"
                 "v_cmp_le_u32 vcc, %3, %0\n\t"
                 "s_cmp_eq_u64 vcc, -1\n\t"
                 "s_cbranch_scc1 1f\n\t"
                 "s_sleep %4\n\t"
                 "s_branch 0b\n"
                 "1:" : "=&v"(f), "=&s"(cnt) : "v"(raddr), "s"(need), "n"(SLQ) : "vcc", "scc", "memory");
}
template <int SL = SLP, int AT_ = 0> __device__ __forceinline__ void tag_chk(unsigned& f, unsigned expect, unsigned voff, const u32x4& rs, unsigned soff) {
    unsigned cnt;
    asm volatile("v_cmp_eq_u32 vcc, %2, %0\n\t"
                 "s_cmp_eq_u64 vcc, -1\n\t"
                 "s_cbranch_scc1 1f\n\t"
                 "s_mov_b32 %1, 0x10000\n"
                 "0:\n\t"
                 ".if %7\n\tv_mov_b32 %0, 0\n\t.endif\n\t"
                 "s_sleep %6\n\t"
                 ".if %7\n\tbuffer_atomic_or %0, %3, %4, %5 offen sc0 sc1\n\t.else\n\tbuffer_load_dword %0, %3, %4, %5 offen sc1\n\t.endif\n\t"
                 "s_sub_u32 %1, %1, 1\n\t"
                 "s_waitcnt vmcnt(0)\n\t"
                 "s_cbranch_scc1 1f\n\t"
                 "v_cmp_eq_u32 vcc, %2, %0\n\t"
                 "s_cmp_eq_u64 vcc, -1\n\t"
                 "s_cbranch_scc0 0b\n"
                 "1:" : "+v"(f), "=&s"(cnt) : "v"(expect), "v"(voff), "s"(rs), "s"(soff), "n"(SL), "n"(AT_) : "vcc", "scc", "memory");
}

__device__ __forceinline__ unsigned race(unsigned w, const void* ws, unsigned long long rnd) {
    unsigned v;
    asm volatile("s_cmp_lt_u32 %1, 32\n\t"
                 "s_cbranch_scc0 1f\n\t"
                 "s_waitcnt lgkmcnt(0)\n\t"
                 "s_mov_b64 s[98:99], %3\n\t"
                 "s_lshl_b32 s96, s98, 7\n\t"
                 "s_lshl_b32 s97, %1, 13\n\t"
                 "s_andn2_b32 s96, s96, 0x2000\n\t"
                 "s_xor_b32 s96, s96, s97\n\t"
                 "s_and_b32 s96, s96, 0x7ff80\n\t"
                 "s_add_u32 s96, s96, %4\n\t"
                 "s_xor_b32 s97, s96, 0x2000\n\t"
                 "s_mov_b32 %0, s98\n\t"
                 "s_load_dword s99, %2, s96\n\t"
                 "s_load_dword s98, %2, s97\n\t"
                 "s_lshr_b32 s96, %1, 4\n\t"
                 "s_xor_b32 s96, s96, %1\n\t"
                 "s_waitcnt lgkmcnt(1)\n\t"
                 "s_sleep %5\n\t"
                 "s_cmp_lg_u32 %0, s98\n\t"
                 "s_addc_u32 %0, s96, 0\n\t"
                 "s_and_b32 %0, %0, 1\n"
                 "1:" : "=&s"(v) : "s"(w), "s"(ws), "s"(rnd), "n"(WS_P), "n"(RSL) : "s96", "s97", "s98", "s99", "scc", "memory");
    return v;
}
__device__ __forceinline__ void vote_chk(unsigned& f, unsigned expect, unsigned odd, unsigned raddr, unsigned lds, unsigned voff, const u32x4& rs, unsigned soff, unsigned& cnt) {
    unsigned t;
    asm volatile("s_mov_b32 %1, 0x10000\n\t"
                 "v_xor_b32 %2, %0, %3\n\t"
                 "v_cmp_gt_u32 vcc, %2, %4\n\t"
                 "s_cbranch_vccz 1f\n\t"
                 "s_mov_b32 m0, %6\n"
                 "0:\n\t"
                 "s_sleep 4\n\t"
                 "buffer_load_dword %7, %8, %9 offen sc1 lds\n\t"
                 "s_sub_u32 %1, %1, 1\n\t"
                 "s_waitcnt vmcnt(0)\n\t"
                 "ds_read_b32 %0, %5\n\t"
                 "s_waitcnt lgkmcnt(0)\n\t"
                 "s_cbranch_scc1 1f\n\t"
                 "v_xor_b32 %2, %0, %3\n\t"
                 "v_cmp_gt_u32 vcc, %2, %4\n\t"
                 "s_cbranch_vccnz 0b\n"
                 "1:" : "+v"(f), "=&s"(cnt), "=&v"(t) : "v"(expect), "v"(odd), "v"(raddr), "s"(lds), "v"(voff), "s"(rs), "s"(soff) : "vcc", "scc", "memory");
}
__device__ __forceinline__ unsigned vote_cnt(unsigned f, unsigned tag, unsigned up, unsigned& n) {
    unsigned r;
    asm volatile("s_xnor_b32 %0, %3, 1\n\tv_cmp_eq_u32 vcc, %0, %2\n\ts_bcnt1_i32_b64 %1, vcc\n\ts_cmp_gt_u32 %1, 16\n\ts_cselect_b32 %0, 0x2000, 0\n\ts_xor_b32 %0, %0, %4"
                 : "=&s"(r), "=&s"(n) : "v"(f), "s"(tag), "s"(up) : "vcc", "scc");
    return r;
}

template <int H> __device__ __forceinline__ void pp_ld(u32x4& d, unsigned voff, const u32x4& rs, unsigned soff) {
    if constexpr (H) asm volatile("buffer_load_dwordx4 v[244:247], %1, %2, %3 offen offset:1024 sc1" : "={v[244:247]}"(d) : "v"(voff), "s"(rs), "s"(soff) : "memory");
    else asm volatile("buffer_load_dwordx4 v[240:243], %1, %2, %3 offen sc1" : "={v[240:243]}"(d) : "v"(voff), "s"(rs), "s"(soff) : "memory");
}
__device__ __forceinline__ void pf_ld(unsigned& d, unsigned voff, const u32x4& rs, unsigned soff) { asm volatile("buffer_load_dword v248, %1, %2, %3 offen sc1" : "={v248}"(d) : "v"(voff), "s"(rs), "s"(soff) : "memory"); }
__device__ __forceinline__ void pf_keep(unsigned d) { asm volatile("" :: "{v248}"(d)); }
__device__ __forceinline__ void pf_zero(unsigned& d) { asm volatile("v_mov_b32 v248, 0" : "={v248}"(d)); }
template <int OFF> __device__ __forceinline__ void pf_at(unsigned& d, unsigned voff, const u32x4& rs, unsigned soff) {
    asm volatile("buffer_atomic_or v248, %1, %2, %3 offen offset:%4 sc0 sc1" : "+{v248}"(d) : "v"(voff), "s"(rs), "s"(soff), "n"(OFF) : "memory");
}
__device__ __forceinline__ void pp_sel(unsigned& so, unsigned& fail, unsigned d, unsigned tn, unsigned yes, unsigned no) {
    asm volatile("v_readlane_b32 %1, v248, 63\n\ts_and_b32 %1, %1, 0xf000f\n\ts_cmp_eq_u32 %1, %3\n\ts_cselect_b32 %0, %4, %5\n\ts_cselect_b32 %1, 0, -1"
                 : "=&s"(so), "=&s"(fail) : "{v248}"(d), "s"(tn), "s"(yes), "s"(no) : "scc");
}
#define PP_CHK(R, R1, R2, R3, OFF) \
    asm volatile("v_cmp_ne_u32 vcc, 0, %2\n\t" \
                 "s_cbranch_vccz 1f\n\t" \
                 "s_mov_b32 %1, 0x10000\n" \
                 "0:\n\t" \
                 ".if %9\n\t" \
                 "v_mov_b32 %2, 0\n\t" \
                 "s_sub_u32 %1, %1, 1\n\t" \
                 "s_cbranch_scc1 1f\n\t" \
                 "buffer_atomic_or %2, %8, %6, %7 offen offset:" OFF " sc0 sc1\n\t" \
                 "s_lshr_b32 vcc_lo, %4, 4\n\t" \
                 "s_waitcnt vmcnt(0)\n\t" \
                 "v_readlane_b32 %3, %2, 63\n\t" \
                 "s_xor_b32 %3, %3, vcc_lo\n\t" \
                 "s_and_b32 %3, %3, 0xf000f\n\t" \
                 "s_cbranch_scc1 0b\n\t" \
                 ".endif\n\t" \
                 "buffer_load_dwordx4 " R ", %5, %6, %7 offen offset:" OFF " sc1\n\t" \
                 "s_waitcnt vmcnt(0)\n\t" \
                 "s_and_b32 %3, %4, 0xf000f\n\t" \
                 "v_xor_b32 " R1 ", %3, " R1 "\n\t" \
                 "s_lshr_b32 %3, %4, 8\n\t" \
                 "s_and_b32 %3, %3, 0xf000f\n\t" \
                 "v_xor_b32 " R2 ", %3, " R2 "\n\t" \
                 "s_lshr_b32 %3, %4, 4\n\t" \
                 "s_and_b32 %3, %3, 0xf000f\n\t" \
                 "v_xor_b32 " R3 ", %3, " R3 "\n\t" \
                 "v_or3_b32 %2, " R1 ", " R2 ", " R3 "\n\t" \
                 "v_and_b32 %2, 0xf000f, %2\n\t" \
                 "s_sub_u32 %1, %1, 1\n\t" \
                 "s_cbranch_scc1 1f\n\t" \
                 "v_cmp_ne_u32 vcc, 0, %2\n\t" \
                 "s_cbranch_vccnz 0b\n" \
                 "1:" : "+{" R "}"(p), "=&s"(cnt), "+v"(bad), "=&s"(t) : "s"(tag), "v"(voff), "s"(rs), "s"(soff), "v"(vp), "n"(PB) : "vcc", "scc", "memory")
template <int H> __device__ __forceinline__ void pp_chk(u32x4& p, unsigned bad, unsigned tag, unsigned voff, const u32x4& rs, unsigned soff, unsigned vp) {
    unsigned cnt, t;
    if constexpr (H) PP_CHK("v[244:247]", "v245", "v246", "v247", "1024"); else PP_CHK("v[240:243]", "v241", "v242", "v243", "0");
}
template <int H> __device__ __forceinline__ void pp_tie(u32x4& p) { if constexpr (H) asm volatile("" : "+{v[244:247]}"(p)); else asm volatile("" : "+{v[240:243]}"(p)); }
__device__ __forceinline__ float amax3(float a, float b, float c) { float r; asm volatile("v_max3_f32 %0, |%1|, |%2|, |%3|" : "=v"(r) : "v"(a), "v"(b), "v"(c)); return r; }
__device__ __forceinline__ unsigned bfi_v(unsigned m, unsigned a, unsigned b) { unsigned r; asm volatile("v_bfi_b32 %0, %1, %2, %3" : "=v"(r) : "v"(m), "v"(a), "v"(b)); return r; }
__device__ __forceinline__ unsigned bfi_s(unsigned m, unsigned a, unsigned b) { unsigned r; asm volatile("v_bfi_b32 %0, %1, %2, %3" : "=v"(r) : "v"(m), "v"(a), "s"(b)); return r; }
__device__ __forceinline__ unsigned bfi_f(unsigned m, unsigned a, unsigned b) { unsigned r; asm volatile("v_bfi_b32 %0, %1, %2, %3" : "=v"(r) : "v"(m), "s"(a), "v"(b)); return r; }
template <int H> __device__ __forceinline__ float cvt16(unsigned w) {
    float r;
    if constexpr (H) asm volatile("v_cvt_f32_i32_sdwa %0, sext(%1) dst_sel:DWORD dst_unused:UNUSED_PAD src0_sel:WORD_1" : "=v"(r) : "v"(w));
    else asm volatile("v_cvt_f32_i32_sdwa %0, sext(%1) dst_sel:DWORD dst_unused:UNUSED_PAD src0_sel:WORD_0" : "=v"(r) : "v"(w));
    return r;
}
__device__ __forceinline__ float sel8(float a, float b, unsigned long long mask) {
    float r;
    asm volatile("s_mov_b64 vcc, %3\n\tv_cndmask_b32_dpp %0, %1, %2, vcc row_ror:8 row_mask:0xf bank_mask:0xf" : "=v"(r) : "v"(a), "v"(b), "s"(mask) : "vcc");
    return r;
}
__device__ __forceinline__ unsigned sel0(unsigned a, unsigned long long mask) { unsigned r; asm volatile("v_cndmask_b32_e64 %0, 0, %1, %2" : "=v"(r) : "v"(a), "s"(mask)); return r; }
constexpr unsigned long long diag_mask(int R) {
    unsigned long long m = 0;
    for (int l = 0; l < 64; ++l) if ((l >> 4) == ((l & 7) >> 1) && 2 * (l & 1) + ((l >> 3) & 1) == R) m |= 1ull << l;
    return m;
}
template <int IMM, int X, bool SC1> __device__ __forceinline__ void dma_x(unsigned& so, unsigned lds, unsigned voff, const u32x4& rs, unsigned base) {
    if constexpr (SC1) asm volatile("s_add_u32 m0, %1, %2\n\ts_xor_b32 %0, %5, %6\n\tbuffer_load_dwordx4 %3, %4, %0 offen sc1 lds" : "=&s"(so) : "s"(lds), "n"(IMM), "v"(voff), "s"(rs), "s"(base), "n"(X) : "memory", "scc");
    else asm volatile("s_add_u32 m0, %1, %2\n\ts_xor_b32 %0, %5, %6\n\tbuffer_load_dwordx4 %3, %4, %0 offen lds" : "=&s"(so) : "s"(lds), "n"(IMM), "v"(voff), "s"(rs), "s"(base), "n"(X) : "memory", "scc");
}
template <int OFF> __device__ __forceinline__ void dma_n(unsigned so, unsigned voff, const u32x4& rs) {
    asm volatile("buffer_load_dwordx4 %0, %1, %2 offen offset:%3 sc1 lds" :: "v"(voff), "s"(rs), "s"(so), "n"(OFF) : "memory");
}
template <int IMM, int OFF> __device__ __forceinline__ void dma_m(unsigned so, unsigned lds, unsigned voff, const u32x4& rs) {
    asm volatile("s_add_u32 m0, %4, %5\n\ts_nop 0\n\tbuffer_load_dwordx4 %0, %1, %2 offen offset:%3 sc1 lds" :: "v"(voff), "s"(rs), "s"(so), "n"(OFF), "s"(lds), "n"(IMM) : "memory", "scc");
}
__device__ __forceinline__ unsigned and_or(unsigned a, unsigned m, unsigned o) { unsigned r; asm("v_and_or_b32 %0, %1, %2, %3" : "=v"(r) : "v"(a), "v"(m), "s"(o)); return r; }

#define HOPS 1
#define HM 0x1ff221125B082ull
#define PG0 63
#define PG7 287
#define PG1 95
#define PG2 288
#define PG3 4
#define PG4 21
#define SQ 52
#define P18 -1
#define WS0 0
#define SLM 0x12234566ull
#define PGF 5
#define HQ 84
#define HW 4
constexpr int hop_nth(int n) { for (int k = 0; k < 62; ++k) if (((HM >> k) & 1) && n-- == 0) return k; return 63; }
constexpr int hop_next(int K) { int n = 0; for (int k = 0; k < K; ++k) n += (HM >> k) & 1; return hop_nth(n + HW); }
template <int K> __device__ __forceinline__ void hop() {
    if constexpr (HOPS && ((HM >> K) & 1)) asm volatile(".Lfkh%0:\n\ts_cbranch_execz .Lfkh%1" :: "n"(K), "n"(hop_next(K)));
}
template <int K> __device__ __forceinline__ void page() {
    if constexpr (HOPS && K >= 0 && ((HM >> (K & 63)) & 1)) asm volatile("s_branch 1f\n\t.p2align 12\n.Lfkh%0:\n\ts_cbranch_execz .Lfkh%1\n1:" :: "n"(K), "n"(hop_next(K)));
    else if constexpr (HOPS) asm volatile("s_branch 1f\n\t.p2align 12\n1:");
}
__device__ __forceinline__ void hop_all(int w) {
    if constexpr (HOPS) asm volatile("s_mov_b64 exec, 0\n\ts_cmp_eq_u32 %0, 0\n\ts_cbranch_scc1 .Lfkh%1\n\ts_cmp_eq_u32 %0, 1\n\ts_cbranch_scc1 .Lfkh%2\n\t"
                                     "s_cmp_eq_u32 %0, 2\n\ts_cbranch_scc1 .Lfkh%3\n\ts_branch .Lfkh%4\n.Lfkh63:\n\ts_mov_b64 exec, -1"
                                     :: "s"(w), "n"(hop_nth(0)), "n"(hop_nth(1)), "n"(hop_nth(2)), "n"(hop_nth(3)) : "scc");
}
#define NB 1
#define PFQ 0
#define CVR 3
#define SPR 3
#define ELP 8
#define PW 1
#define CPG 95
#define GQ -1
#define GE 3
#define KDL 0
#define GP 1
#define SG 507
#define KO 0
#define DP 0
#define B13 460
#define PRE 0
enum { OP_NONE, OP_A, OP_B, OP_FLAG, OP_SIG, OP_VOTE };
struct Op { int kind, a, b; };
constexpr Op g1_op(int G) {
    const int st = (G + 32) / 32 - 1, q = G - 32 * st, i = q >> 2, j = q & 3;
    if (i < 5) {
        if (st < 0) return {OP_NONE, 0, 0};
        if ((j & 1) == 0) return {OP_A, st * 8 + i + 3, j >> 1};
        if (q == FLQ && st < 15) return {OP_FLAG, 0, 0};
        if (NB && G == SG) return {OP_SIG, 2, 0};
        if (G == GCR) return {OP_VOTE, 0, 0};
        if (NB == 2 && G == 435) return {OP_SIG, 3, 0};
        return {OP_NONE, 0, 0};
    }
    if (st == 15) return NB && G == SG ? Op{OP_SIG, 2, 0} : Op{OP_NONE, 0, 0};
    const int n = (st + 1) * 8;
    switch (q) {
        case 20: return {OP_B, st + 1, 0}; case 21: return {OP_B, st + 1, 1}; case 22: return {OP_A, n, 0}; case 23: return {OP_B, st + 1, 2};
        case 24: return {OP_B, st + 1, 3}; case 25: return {OP_A, n, 1}; case 26: return {OP_A, n + 1, 0}; case 27: return {OP_A, n + 1, 1};
        case 28: return {OP_A, n + 2, 0}; case 30: return {OP_A, n + 2, 1};
    }
    return {OP_NONE, 0, 0};
}
constexpr int g1_need(int G) {
    const int st = G >> 5, j = G & 3;
    for (int h = G - 1; h >= -12; --h) {
        const Op o = g1_op(h);
        if (o.kind == OP_A && o.a == (G >> 2) && o.b == (j >> 1)) return h;
        if (o.kind == OP_B && o.a == st && o.b == j) return h;
        if (o.kind == OP_FLAG && (G & 31) == 20 && (h >> 5) == st) return h;
    }
    return -100;
}
constexpr int g1_wait(int G) {
    int n = -100, c = 0;
    for (int h = G; h < (G ? G + 4 : 2) && h < 512; ++h) if (g1_need(h) > n) n = g1_need(h);
    for (int h = n + 1; h < G; ++h) c += g1_op(h).kind != OP_NONE;
    return n < G ? c : -1;
}
#define KV0 3
#define KVN 0x2222
constexpr int kv_first(int k) { int n = 0; for (int i = KV0; i < k && i < KV0 + 8; ++i) n += (KVN >> (4 * (i - KV0))) & 15; return n > 8 ? 8 : n; }
constexpr int kv_last() { int k = KV0; while (kv_first(k + 1) < 8) ++k; return k; }
#define CQ -1
#define CTM 0
#define EC 2
#define ED 2
#define EB 0
#define KST 0
#define KFD 3
#define KPL 11
#define KPI 0
#define KSL SLP
#define CBQ 20
constexpr int KVL = kv_last(), KFL = KVL + KFD, KSD = KVS ? KCC : -1;
static_assert(KFL <= KPL && KPL <= 12 && KPL > KVL && (KPI & 3) == 0 && KTI != KCC && KTI != KFL && KTI < KPL && KVP >= 0 && KVP < KCQ && KCQ < KCC && KSD <= KVL && KCC <= KPL && GCR > 32 * KCC + 62, "");
#define ROT 0x814
#define KLP 13
#define KL0 8
#define KLS 4
constexpr int ld_pos(int j) { return j < KLP ? -100 : KL0 + (j - KLP) * KLS / 4; }
constexpr int ld_cnt(int k) { int c = 0; for (int j = KLP; j < 21; ++j) c += (ld_pos(j) + 8) / 8 - 1 == k; return c; }
constexpr int KLL = (ld_pos(20) + 8) / 8 - 1, KLI = ld_pos(20) - 8 * KLL;
static_assert(KLP == 21 || KLL < KV0, "");
constexpr int hv_cnt(int k0, int k1) {
    int c = 0;
    for (int k = k0; k < k1; ++k) c += 9 + (KST ? 4 * (k == KVL) : k < KSD ? 0 : kv_first(k + 1) / 2 - (k == KSD ? 0 : kv_first(k) / 2)) + (k == KFL) + (k == KPL) + (k == KCQ) + (k == KVP) + (TM && TM < 16 && k == KTI) + ld_cnt(k);
    return c;
}
#define ZM 1
#define CL 1
#define EP 0
constexpr unsigned long long lag0_mask(int w) {
    unsigned long long m = 0;
    for (int l = 0; l < 64; ++l) if ((l >> 4) == 2 + ((l >> 3) & 1) && 3 - ((l & 7) >> 1) == w) m |= 1ull << l;
    return m;
}
constexpr int cv_start(int n) { return n * 8 - n * (n - 1) / 2; }
constexpr int cv_n(int q) { int n = 0; while (cv_start(n + 1) <= q) ++n; return n; }
constexpr int cv_m(int q) { return 7 - cv_n(q) - (q - cv_start(cv_n(q))); }
enum { CO_NONE, CO_A, CO_B, CO_C };
constexpr Op cv_op(int G) {
    const int s = (G + 36) / 36 - 1, q = G - 36 * s;
    if (q >= 1 && q <= 6) return s >= 0 ? Op{CO_A, s * 8 + 5 + (q - 1) / 2, (q - 1) & 1} : Op{CO_NONE, 0, 0};
    if (q >= 11 && q <= 14) return s >= 0 && s >= CL && s - CL < 8 ? Op{CO_C, s - CL, q - 11} : Op{CO_NONE, 0, 0};
    if (s == 7) return {CO_NONE, 0, 0};
    for (int m = 0; m < 8; ++m) if (q == cv_start(7 - m)) return {CO_B, (s + 1) * 8 + m, 0};
    for (int n = 0; n < 5; ++n) if (q == cv_start(n + 1) + 1 || q == cv_start(n + 1) + 2) return {CO_A, (s + 1) * 8 + n, q - cv_start(n + 1) - 1};
    return {CO_NONE, 0, 0};
}
constexpr int cv_need(int G) {
    const int s = G / 36, q = G % 36;
    for (int h = G - 1; h >= -36; --h) {
        const Op o = cv_op(h);
        if (o.kind == CO_A && o.a == s * 8 + cv_n(q)) return h;
        if (o.kind == CO_B && o.a == s * 8 + cv_m(q)) return h;
        if (o.kind == CO_C && q == 24 && o.a == s - CL) return h;
    }
    return -100;
}
constexpr int cv_wait(int G) {
    const int n = cv_need(G);
    for (int h = G - 1; h >= 0 && h >= G - 40; --h) if (cv_need(h) >= n) return -1;
    int c = 0;
    for (int h = n + 1; h < G; ++h) c += cv_op(h).kind != CO_NONE;
    return c;
}
#define AT(n, m) ((n) * 8 + (m))
#define FM 0x326260
constexpr int fill_pos(int s, int i) {
    const int f = (FM >> (4 * (s - 2))) & 15, n = s == 2 ? 0 : 4 * s - 9;
    switch (f) {
        case 1: return AT(n + i, 5) - (s == 2 && i == 3) * 6;
        case 2: return AT(n + 1 + i / 2, 5 + 2 * (i & 1));
        case 3: return i < 2 ? AT(n, 5 + 2 * i) : AT(n + i - 1, 5);
        case 4: return AT(n + 2 + i / 2, 5 + 2 * (i & 1));
        case 5: return AT(n, 1 + 2 * i) + (s == 2 ? 4 : 0);
        case 6: return AT(n + 3 + i / 2, 5 + 2 * (i & 1));
        case 7: return AT(n + 3, 5 + 2 * i);
        case 8: return i < 2 ? AT(n + 1, 5 + 2 * i) : AT(n + i, 5);
        case 9: return AT(n + 1 + i, 1);
        case 10: return i < 2 ? AT(n, 1 + 2 * i) : AT(n + 3, 1 + 2 * i);
    }
    return AT(n + i / 2, 5 + 2 * (i & 1));
}
constexpr int g2_fill(int p) {
    for (int k = 8; k < 32; ++k) if (fill_pos(k >> 2, k & 3) == p) return k;
    return -1;
}
#define QX AT(2, 5)
#define QQ AT(3, 2)
#define QZ AT(11, 2)
#define QT AT(3, 4)
constexpr int P_X = QX, P_Q = QQ, P_Z = QZ, P_T = QT;
constexpr bool XL = false;
constexpr bool fill_m0(int k) {
    const int a = fill_pos(k >> 2, (k & 3) - 1), b = fill_pos(k >> 2, k & 3);
    return (P_Q + 2 >= a && P_Q < b) || (P_Z + 2 >= a && P_Z < b) || (P_X + 2 >= a && P_X < b);
}
#define DBG 0
#define XW 4
#define XW2 4
#define QPP0 AT(9, 4)
#define QL0 AT(21, 1)
#define QR0 AT(24, 6)
#define QF2 AT(28, 3)
#define QL1 AT(26, 1)
#define QR1 AT(29, 3)
#define QA AT(16, 4)
#define AD 0
#define D1 2
#define D0 1
constexpr int NS = 33, NR = 29;
constexpr int P_PP0 = QPP0, P_PP1 = AT(16, 4), P_L0 = QL0, P_R0 = QR0, P_F2 = QF2, P_L1 = QL1, P_R1 = QR1, P_ZC = AT(10, 6), P_A = QA;
#define QB 24
#define QW -2
constexpr int P_W = QW;
static_assert(P_W < AT(ZP, 4), "");
constexpr int P_B0 = P_L0 - QB, P_B1 = P_L1 - QB;
static_assert(P_B0 > P_ZC + 1 && P_B1 > P_L0 + 1, "");
constexpr int E_PP0 = P_PP0 + NS - 1, E_PP1 = P_PP1 + (NS - 1) / D1, E_R0 = P_R0 + (NR - 1) / D0;
constexpr int ZW = P_A >= AT(20, 4) ? 19 : 15;
static_assert(P_R0 + 14 / D0 >= AT(24, 3) && E_R0 < P_F2 && P_F2 >= AT(24, 4) && P_F2 + 8 <= AT(31, 0) && E_R0 < P_R1, "");
static_assert(P_PP0 >= AT(9, 2) && P_PP0 + 8 <= AT(15, 4) && E_PP0 < P_PP1 && P_A >= AT(16, 4), "");
constexpr bool a_free(int p) {
    return p >= P_A && (AD || !((p >= P_PP1 && p <= E_PP1) || (p >= P_R0 && p <= E_R0) || (p >= P_F2 && p < P_F2 + 16)));
}
constexpr int a_step(int p) {
    if (NOA || !a_free(p)) return -1;
    int c = 0;
    for (int h = P_A; h < p; ++h) c += a_free(h);
    return c <= 48 ? c : -1;
}
constexpr int a_pos(int s) { int p = P_A; while (a_step(p) != s) ++p; return p; }
constexpr int q_rd(int r) { int p = r < 2 ? AT(ZW, 6) + r : a_pos(12 * r - 16) + 1; while ((p & 7) < 4) ++p; return p; }
constexpr int q_tie(int r) { const int n = (q_rd(r) >> 3) + 1; return AT(n, (n & 3) == 3 ? 0 : 4); }
static_assert(NOA || (a_pos(48) < 256 && q_tie(2) <= a_pos(23) && q_tie(3) <= a_pos(35)), "");
constexpr int g2_vm(int p) {
    int c = g2_fill(p) >= 0;
    c += a_step(p) > 0 && a_step(p) % 12 == 0 && !(NOST & 1);
    if (!NOA) c += (p == P_T) + (p == P_Q || p == P_Q + 2) + (p == AT(ZP, 4)) + (p == P_Z || p == P_Z + 2);
    c += (p == P_X || p == P_X + 2) + (p == P_W);
    c += !(NOST & 2) && (p == E_PP0 || p == E_PP1);
    c += (p == P_L0 || p == P_L1) + (PB && (p == P_B0 || p == P_B1));
    c += !(NOST & 4) && (p == P_F2 + 11 || p == P_F2 + 15);
    return c;
}
constexpr int g2_lx(int n) {
    int c = 2 * (n == 7);
    if (n >= 0 && (n & 7) >= 6) c += 2;
    if (!NOA) { c += 4 * (n == ZW); for (int r = 0; r < 4; ++r) c += (q_rd(r) >> 3) == n; }
    return c;
}
constexpr int g2_tile(int n, int j) { return (n & 4) ? (n & 2) == 0 : j; }
constexpr int g2_unit(int n, int j) { return (n & 4) ? 4 + 2 * (n & 1) + j : n & 7; }
constexpr int imax(int a, int b) { return a > b ? a : b; }
constexpr int g2_need(int n) {
    int r = n == 3 ? -1 : fill_pos((n + 1) / 4, 3);
    if (n == 7 && !XL) r = imax(r, P_X + 2);
    if (n == ZW && !NOA) r = imax(r, imax(P_Q, P_Z) + 2);
    return r;
}
static_assert(P_Z > AT(11, 0) && P_T > AT(3, 0) && P_T < AT(ZP, 4) && (!XL || (P_X + 2 < P_R0 && P_R0 + 2 >= AT(24, 2) && P_R0 < AT(27, 0) && D0 == 1)), "");
constexpr bool rd1(int n) { return RD == 1 || (RD == 3 && (n & 3) == 2); }
constexpr int g2_allow(int e, int p) { int c = 0; for (int h = p + 1; h < e; ++h) c += g2_vm(h); return c; }

__global__ __launch_bounds__(512, 2) __attribute__((aligned(4096))) void k1(const uint16_t* __restrict__ x, const uint16_t* __restrict__ bre, const uint16_t* __restrict__ bim,
                                             const uint16_t* __restrict__ lre, const uint16_t* __restrict__ lim, const uint16_t* __restrict__ logdt, char* __restrict__ ws,
                                             const uint16_t* __restrict__ cre, const uint16_t* __restrict__ cim, const uint16_t* __restrict__ dskip,
                                             const uint16_t* __restrict__ ssmk, const uint16_t* __restrict__ ssmD, const uint16_t* __restrict__ qin,
                                             const uint16_t* __restrict__ kin, const uint16_t* __restrict__ vin, const uint16_t* __restrict__ sk,
                                             float* __restrict__ out TS_ARG) {
    unsigned long long pc0, rnd;
    asm volatile("s_getpc_b64 %0\n\ts_memtime %1" : "=s"(pc0), "=s"(rnd));
    TS_DECL TS(0);
    TL_DECL
    __shared__ __attribute__((aligned(1024))) char shm[LDS_TOTAL];
    const int tid = threadIdx.x, lane = tid & 63, wid = __builtin_amdgcn_readfirstlane(tid >> 6);
    const int b = blockIdx.x, xcd = b & 7, idx = b >> 3, hb = xcd & 1, tq = idx & 7;
    const int tm = (xcd >> 1) * 4 + (idx >> 3), tn = hb * 8 + tq;
    unsigned nr;
    const int tm2 = (xcd & 4) * 2 + tq, dt = (xcd & 2) * 2 + (idx >> 3), rot2 = tq + JR(idx >> 3);
    const unsigned lbase = (unsigned)(uintptr_t)shm;
    const int l15 = lane & 15, g4 = lane >> 4, lw = wid & 3, kg = wid >> 2;
    dma<U0 + 4096, false>(lbase + wid * 1024, lane * 16, mk_rsrc((const void*)(pc0 & ~255ull), CODE_KB * 1024), ((idx * 8 + wid) & 63) * 1024);
    const unsigned mk0 = lbase + POLL + lw * 256 + kg * (FULL - POLL + lw * (FST - 256));
    flag_set(mk0 + lane * 4, 1u);
    flag_set(lbase + HGO + lane * 4, 0u);
    if constexpr (NB) flag_set(lbase + DONE + lw * FST + lane * 4, 1u);
    unsigned vo[4];
    const bool xw = lw < 2;
#pragma unroll
    for (int i = 0; i < 4; ++i) {
        const int v = l15 ^ (i * 4 + g4);
        vo[i] = ((xw ? 64 * (i & 1) + 4 * (i >> 1) + (lane & 32) + 2 * (g4 & 1) : 8 * (i & 1) + 2 * g4) + (v >> 3)) * 2048 + (v & 7) * 16 - (i & 1) * 1024;
    }
    const int bw = lw >> 1;
    unsigned r0 = (tm * 128 + lw * 16 + bw * (tn * 64 + lw * 16 - 64 - tm * 128)) * 2048, rsz = 229376 - bw * 163840;
    const unsigned col = (tq + (idx >> 3)) & 1, grp = GS == 0 ? tq >> 2 : GS == 1 ? idx >> 4 : GS == 2 ? 0 : GS == 3 ? hb : (xcd >> 1) & 1;
    unsigned rp = grp * (16 - BL) * 128, mi = col * BL * 128, mo = mi >> grp;
    unsigned rot = (BL ? 0 : ROT >> 8) * col + ((ROT >> 4) & 15) * (xcd >> 1) + (ROT & 15) * hb, rst = 16384 << bw, lb = lbase + lw * 8192, oob = OOB;
    unsigned lb0 = lb + g1_buf(0) + kg * (g1_buf(1) - g1_buf(0)), so0 = BL ? (((kg * 128 + rp) ^ mo) + rot * 128) & 0x780 : ((kg + rot) & 15) * 128;
    if constexpr (BL) { rot *= 128; ties(rp); ties(mi); ties(mo); }
    unsigned co = l15 * 2 + (tn * 128 + lw * 32 + kg * 2048), vw = b * 4 + lw + 32 - 32 * kg;
    tie(vo[0]); tie(vo[1]); tie(vo[2]); tie(vo[3]); tie(oob); tie(co);
    ties(r0); ties(rsz); ties(rot); ties(rst); ties(lb); ties(lb0); ties(so0); ties(vw);
    sched();
    asm volatile("" :: "s"(x), "s"(bre), "s"(bim), "s"(lre), "s"(lim), "s"(logdt), "s"(cre), "s"(cim), "s"(dskip), "s"(ssmk), "s"(ssmD), "s"(qin),
                       "s"(kin), "s"(vin), "s"(sk), "s"(ws), "s"(out));
    const bool xv = fresh(bw) == 0;
    const u32x4 rs0 = mk_rsrc((const char*)(xv ? x : bre) + r0, rsz), rs1 = mk_rsrc((const char*)(xv ? x : bim) + r0 - 2048, rsz + 2048);
    if constexpr (SU) TS(3);
    if constexpr (AB) asm volatile("s_waitcnt lgkmcnt(0)\n\ts_cmp_eq_u32 %0, 0\n\ts_cbranch_scc1 1f\n\ts_barrier\n1:" :: "s"(kg) : "scc", "memory");
    unsigned c_dt, c_re, c_im;
    gld<0>(c_dt, co, mk_rsrc(logdt, 2048), 0);
    gld<0>(c_re, co, mk_rsrc(lre, 2048), 0);
    gld<0>(c_im, co, mk_rsrc(lim, 2048), 0);
    const unsigned vb = race(vw, ws, rnd);
    static_for<0, 8>([&](auto ic) { constexpr int i = CV(ic); dma4<(i & 4) * 1024, i & 3>(lb0, vo[i & 3], (i & 2) ? rs1 : rs0, so0 + (i >> 2) * rst); });
    wait_l<0>();
    if constexpr (SU) TS(4);
    if constexpr (AB) {
        dma1<0>(mk0 + kg * 120, oob, rs0, 0);
        asm volatile("s_cmp_lg_u32 %0, 0\n\ts_cbranch_scc1 1f\n\ts_barrier\n1:" :: "s"(kg) : "scc", "memory");
    } else {
        barrier();
        dma1<0>(mk0 + kg * 120, oob, rs0, 0);
    }
    if constexpr (SU) TS(5);
    sched();
    const unsigned a00 = (l15 >> 1) * 256 + ((((l15 & 1) * 8 + g4) ^ (l15 >> 1)) << 4);
    auto frag = [&](int par, int ks) { return (a00 ^ (ks * 64) ^ (par * 128)) + par * 2048; };
    bf16x8 cf[32];
    unsigned vc[4], cad[4];
    auto c_setup = [&]() {
#pragma unroll
        for (int i = 0; i < 4; ++i) { const int v = l15 ^ (i * 4 + g4); vc[i] = (4 * (2 * g4 + (v >> 3)) + 2 * (i & 1) + (i >> 1)) * 2048 + (v & 7) * 16 - i * 1024; }
    };
    const u32x4 rsc = mk_rsrc((kg ? cim : cre) + (size_t)dt * 131072, 262144);
    auto c_off = [&](int dg, int j) { return (unsigned)(dg * 65536 + hb * 1024 + ((rot2 + j) & 7) * 128); };
    const unsigned cst = lbase + (kg ? BUF : 2 * BUF) + lw * 8192;
    unsigned tag;
    const u32x4 rsf = mk_rsrc(ws + WS_F, 65536);
    const __amdgpu_buffer_rsrc_t rf = mk_brs(ws + WS_F, 65536);
    const int qx = (hb ? 0 : 2) ^ JQ(idx >> 3), wid_ = wid, b_ = b;

    if (wid < 4) {
        f32x4 acc[8][2];
        cf32 L, cc, M, M2, MG, NG, N4, N8, XC, ag;
        unsigned eo[4], ew, es, dna, ho, hfo, cb, fv; unsigned long long none[4]; u32x4 rse, rh, er;
        {
            unsigned aad[4], bad[4];
#pragma unroll
            for (int i = 0; i < 4; ++i) { aad[i] = lbase + frag(i & 1, i >> 1); bad[i] = aad[i] + 16384 + wid * 4096; }
            const unsigned fr = lbase + FULL + (lane & 3) * FST;
            sld(tag, ws + WS_F + F_EP);
            bf16x8 fa[4][2], fb[2][4];
            unsigned fl = 1, dz = 0, dw_ = lbase + DONE + wid * FST + lane * 4, vt = lbase + POLL + wid * 256 + lane * 4;
            tie(dz); tie(dw_); tie(vt);
            auto op = [&](auto gc) {
                constexpr int G = CV(gc);
                constexpr Op o = g1_op(G);
                if constexpr (o.kind == OP_A) {
                    constexpr int bo = g1_buf(o.a >> 3), hi = bo >= 65536 ? 65536 : 0, i = o.a & 7;
                    dsr128<bo - hi + (i >> 1) * 4096>(fa[i & 3][o.b], aad[(i & 1) + 2 * o.b] + hi);
                }
                if constexpr (o.kind == OP_B) { constexpr int bo = g1_buf(o.a), hi = bo >= 65536 ? 65536 : 0; dsr128<bo - hi>(fb[o.a & 1][o.b], bad[o.b] + hi); }
                if constexpr (o.kind == OP_FLAG) dsr32<(30 - (G >> 5)) * 4>(fl, fr);
                if constexpr (o.kind == OP_SIG) dsw32<o.a * 4>(dw_, dz);
                if constexpr (o.kind == OP_VOTE) dsr32<0>(vt, vt);
            };
            auto consts = [&]() {
                float ld, ar, ai;
                asm volatile("s_waitcnt vmcnt(9)\n\tv_lshlrev_b32 %0, 16, %3\n\tv_lshlrev_b32 %1, 16, %4\n\tv_lshlrev_b32 %2, 16, %5"
                             : "=&v"(ld), "=&v"(ar), "=&v"(ai) : "v"(c_dt), "v"(c_re), "v"(c_im) : "memory");
                if constexpr (SU) TS(6);
                const float dtv = rbf(expf(ld)), ea = expf(dtv * ar);
                float sn, cs;
                sincosf(dtv * ai, &sn, &cs);
                L = cf32{ea * cs, ea * sn};
                M = L;
#pragma unroll
                for (int e = 0; e < 5; ++e) M = cmul(M, M);
                M2 = cmul(M, M);
                const cf32 N = cmul(M2, M2), N2 = cmul(N, N), one = {1.f, 0.f};
                N4 = cmul(N2, N2); N8 = cmul(N4, N4);
                auto sel = [](bool c, cf32 a, cf32 b) { return cf32{c ? a.r : b.r, c ? a.i : b.i}; };
                MG = cmul(sel(g4 & 1, M, one), sel(g4 & 2, M2, one));
                NG = cmul(sel(g4 & 1, N, one), sel(g4 & 2, N2, one));
                const float nm = L.r - 1.f, den = ar * ar + ai * ai;
                cc = cf32{2.f * ((nm * ar + L.i * ai) / den), 2.f * ((L.i * ar - nm * ai) / den)};
                auto tc = [&](cf32& v) { tie(v.r); tie(v.i); };
                XC = cf32{g4 == 3 ? M.r : g4 == 2 ? 1.f : 0.f, g4 == 3 ? M.i : 0.f};
                tc(L); tc(cc); tc(M); tc(M2); tc(MG); tc(NG); tc(N4); tc(N8); tc(XC);
                sched();
            };
            if constexpr (CQ < 0) consts();
            if constexpr (SU) TS(10);
            __builtin_amdgcn_s_setprio(CP);
            zero_chk<0>(fl, lbase + POLL + (lane & 3) * 256);
            if constexpr (WS0 > 0) { fl = 1; zero_chk<(31 - WS0) * 4, 1>(fl, fr); }
            ties(tag);
            tag += 0x01010101u;
            TS(1);
            static_for<-12, 0>(op);
            static_for<0, 512>([&](auto gc) {
                constexpr int G = CV(gc), st = G >> 5, i = (G >> 2) & 7, j = G & 3;
                if constexpr ((G == 0 || j == 2) && G <= 506) { static_assert(g1_wait(G) >= 0, ""); wait_lr<G == 506 ? 0 : g1_wait(G)>(); }
                if constexpr (G == CQ) { asm volatile("s_waitcnt lgkmcnt(0)\n\ts_nop 7" ::: "memory"); consts(); }
                if constexpr (G == SQ) {
                    c_setup();
                    const int t0 = tm - 1 - g4;
                    static_for<0, 4>([&](auto ic) {
                        constexpr int i = CV(ic);
                        const int t = t0 - 4 * i;
                        cad[i] = lb + frag(i & 1, i >> 1);
                        eo[i] = ((((t ^ tm) & 8) << 10) | ((t & 7) << 8) | (l15 * 16) | (t & OOB)); none[i] = __builtin_amdgcn_ballot_w64(t < 0);
                        tie(vc[i]); tie(cad[i]); tie(eo[i]); ties(none[i]);
                    });
                    ew = (l15 * 16) | (g4 << 20); dna = lbase + DONE + (lane & 3) * FST; ho = g4 * 256 + ((l15 ^ (4 * (g4 & 1))) << 4); fv = lane << 20;
                    tie(ew); tie(dna); tie(ho); tie(fv);
                    sched();
                }
                if constexpr (G == HQ) hop_all(wid);
                {
                    constexpr int n = (SLM >> (4 * st)) & 15;
                    if constexpr (n > 0 && ((G & 31) + 1) * (n & 7) / 32 != (G & 31) * (n & 7) / 32) __builtin_amdgcn_s_sleep(1 + (n >> 3));
                }
                if constexpr ((G & 31) == 20 && st < 15) zero_chk<(30 - st) * 4>(fl, fr);
                if constexpr (((G & 31) == 30 && st < 13) || (NB < 2 && G == B13)) barrier();
                if constexpr ((G & 31) == 20) TL(st);
                mfma<st == 0 && j < 2>(acc[i][j & 1], fa[i & 3][j >> 1], fb[st & 1][j]);
                sched();
                op(gc);
                if constexpr (G == GCR + 16) { unsigned n; nr = vote_cnt(vt, tag, (xcd & 4) << 11, n); }
                if constexpr (G >= GCR + 18 && G < GCR + 22) { eo[G - GCR - 18] ^= nr; tie(eo[G - GCR - 18]); }
                if constexpr (G == GCR + 22) { rse = mk_rsrc(ws + WS_E + tn * 16384 + wid * 2048, 16384); ties(rse); }
                if constexpr (G == GCR + 24) { es = nr + (tm & 7) * 256; cb = wid * 65536 + hb * 1024; er = u32x4{0u, 0u, tag, ~tag}; ties(es); ties(cb); tie(er); }
                if constexpr (G == GCR + 26) { rh = mk_rsrc(ws + WS_H + (size_t)((tm & 7) * 16 + tn) * 65536 + (nr ^ ((tm & 2) << 12)) + wid * 2048, 65536); ties(rh); }
                if constexpr (G == GCR + 28) { hfo = F_H + nr + (((tm & 7) * 16 + tn) * 4 + wid) * 8; ties(hfo); }
                if constexpr (G == PG0) page<-1>();
                if constexpr (G == 95) hop<2>();
                if constexpr (G == 159) hop<4>();
                if constexpr (G == 207) hop<5>();
                if constexpr (G == PG7) page<7>();
                if constexpr (G == 319) hop<8>();
                if constexpr (G == 383) hop<10>();
                if constexpr (G == 431) hop<11>();
                if constexpr (G == 479) page<13>();
                if constexpr (G == 509) hop<15>();
                if constexpr (G == 489) hop<40>();
                if constexpr (G == 494) hop<41>();
                if constexpr (G == 499) hop<42>();
                if constexpr (G == 504) hop<43>();
                if constexpr (G >= 487) {
                    constexpr int k = G - 486;
                    if constexpr (k == 1) { asm volatile("s_nop 3"); ag = cf32{acc[0][0][0], acc[0][1][0]}; }
                    cf32 t;
                    asm volatile("v_fma_f32 %0, -%3, %5, %6\n\tv_fma_f32 %1, %3, %4, %7\n\tv_fmac_f32 %0, %2, %4\n\tv_fmac_f32 %1, %2, %5"
                                 : "=&v"(t.r), "=&v"(t.i) : "v"(L.r), "v"(L.i), "v"(ag.r), "v"(ag.i), "v"(acc[k >> 2][0][k & 3]), "v"(acc[k >> 2][1][k & 3]));
                    ag = t;
                }
                sched();
            });
        }
        __builtin_amdgcn_s_setprio(3);
        TL(16);
        TS(2);
#pragma unroll
        for (int k = 26; k < 32; ++k) ag = cfma(L, ag, cf32{acc[k >> 2][0][k & 3], acc[k >> 2][1][k & 3]});
        tie(ag.r); tie(ag.i); sched(); hop<44>(); sched();
        cf32 ev_, od, p01, p23;
        sw16(ag.r, ev_.r, od.r); sw16(ag.i, ev_.i, od.i);
        const cf32 pp = cfma(M, ev_, od);
        sw32(pp.r, p01.r, p23.r); sw32(pp.i, p01.i, p23.i);
        cf32 E = cfma(M2, p01, p23);

        er[0] = f2u(E.r); er[1] = f2u(E.i);
        gst<0>(er, ew, rse, es);
        sched();
        TL(17);
        TSX(3);
        hop<45>();
        sched();
        auto c_read = [&](auto rc) { static_for<0, 8>([&](auto ic) { constexpr int r = CV(rc), i = CV(ic); dsr128<(i >> 2) * 4096>(cf[r * 8 + i], cad[i & 3] + ((r & 1) ? 0 : 2 * BUF)); }); };
        auto c_tie = [&](auto rc) { static_for<0, 8>([&](auto ic) { tie(cf[CV(rc) * 8 + CV(ic)]); }); };
        auto c_unit = [&](auto uc) { static_for<0, 4>([&](auto ic) { constexpr int u = CV(uc), i = CV(ic); dma4<((u & 2) ? 0 : 2 * BUF) + (u & 1) * 4096, i>(lb, vc[i], rsc, cb + ((rot2 + u) & 7) * 128); }); };
        auto c_req = [&](auto rc) { c_unit(IC(CV(rc) * 2)); c_unit(IC(CV(rc) * 2 + 1)); };
        const float c2r = cc.r, c2i = cc.i;
        auto scale = [&](int i) {
#pragma unroll
            for (int j = 0; j < 4; ++j) {
                const float br = acc[i][0][j], bi = acc[i][1][j];
                acc[i][0][j] = fma_(c2r, br, -(c2i * bi)); acc[i][1][j] = fma_(-c2r, bi, -(c2i * br));
            }
            tie(acc[i][0]); tie(acc[i][1]);
        };
        auto scales = [&](auto ac, auto bc) {
            static_for<CV(ac), CV(bc)>([&](auto ic) {
                constexpr int i = CV(ic);
                scale(i);
                if constexpr (i & 1) { sched(); hop<i == 1 ? 46 : i == 3 ? 16 : i == 5 ? 47 : 48>(); sched(); }
            });
        };
        constexpr int CA = CVR == 0 ? 0 : CVR == 4 ? 1 : 2;
        if constexpr (NB) {
            unsigned dn = 1;
            scales(IC(0), IC(DP));
            if constexpr (CA) { zero_chk<8>(dn, dna); static_for<0, CA>(c_unit); }
            if constexpr (SPR != 3) __builtin_amdgcn_s_setprio(SPR);
            sched();
            scales(IC(DP), IC(ELP));
            if constexpr (!CA) zero_chk<8>(dn, dna);
        } else {
            scales(IC(0), IC(ELP));
            barrier();
            static_for<0, CA>(c_unit);
        }
        TL(18);
        u32x4 ev[4];
#pragma unroll
        for (int i = 0; i < 4; ++i) asm volatile("buffer_load_dwordx4 %0, %1, %2, 0 offen sc1" : "=v"(ev[i]) : "v"(eo[i]), "s"(rse) : "memory");
        asm volatile("" ::: "memory");
        sched();
        constexpr int CB = CVR == 0 ? 1 : CVR == 4 ? 2 : 4;
        static_for<CA, CB>(c_unit);
        scales(IC(ELP), IC(8));
        cf32 X = cfma(XC, p01, cf32{(g4 & 1) ? ev_.r : 0.f, (g4 & 1) ? ev_.i : 0.f});
        tie(X.r); tie(X.i);
        sched();
        hop<24>();
        if constexpr (SPR != 3) __builtin_amdgcn_s_setprio(3);
        if constexpr (CVR == 2) { wait_v<12>(); c_read(IC(0)); wait_l<0>(); c_tie(IC(0)); c_req(IC(2)); }
        wait_v<4 * (CB - CA) + 8 * (CVR == 2)>();
        TL(19);
        tie(ev[0]); tie(ev[1]); tie(ev[2]); tie(ev[3]);
        {
            const unsigned long long tag64 = (unsigned long long)tag | ((unsigned long long)~tag << 32);
#pragma unroll 1
            for (unsigned spins = 0; spins < SPIN_CAP; ++spins) {
                unsigned long long ok = ~0ull;
#pragma unroll
                for (int i = 0; i < 4; ++i) ok &= __builtin_amdgcn_ballot_w64(((unsigned long long)ev[i][2] | ((unsigned long long)ev[i][3] << 32)) == tag64) | none[i];
                TSV(11, spins);
                if (ok == ~0ull) break;
                asm volatile("buffer_load_dwordx4 %0, %4, %8, 0 offen sc1\n\tbuffer_load_dwordx4 %1, %5, %8, 0 offen sc1\n\tbuffer_load_dwordx4 %2, %6, %8, 0 offen sc1\n\t"
                             "buffer_load_dwordx4 %3, %7, %8, 0 offen sc1\n\ts_waitcnt vmcnt(0)" : "=&v"(ev[0]), "=&v"(ev[1]), "=&v"(ev[2]), "=&v"(ev[3])
                             : "v"(eo[0]), "v"(eo[1]), "v"(eo[2]), "v"(eo[3]), "s"(rse) : "memory");
            }
        }
        if constexpr (P18 < 0) page<18>();
        cf32 h;
        {
            auto e = [&](int i) { return cf32{u2f(ev[i][0]), u2f(ev[i][1])}; };
            cf32 c = cmul(NG, cfma(N8, cfma(N4, e(3), e(2)), cfma(N4, e(1), e(0)))), u, v;
            sw16(c.r, u.r, v.r); sw16(c.i, u.i, v.i);
            sw32(u.r + v.r, u.r, v.r); sw32(u.i + v.i, u.i, v.i);
            c = cfma(MG, cf32{u.r + v.r, u.i + v.i}, X);
            h = cf32{fma_(c2r, c.r, -(c2i * c.i)), fma_(-c2r, c.i, -(c2i * c.r))};
        }
        TSX(4);
        {
            const cf32 Lc = {L.r, -L.i};
            static_for<0, 4>([&](auto qc) {
                constexpr int q = CV(qc);
                if constexpr (q >= CB) c_unit(qc);
                if constexpr (q == 2) hop<20>();
                u32x4 wr, wi;
#pragma unroll
                for (int u = 0; u < 4; ++u) {
                    const int i = 2 * q + (u >> 1), j = 2 * (u & 1);
                    const cf32 h0 = cfma(Lc, h, cf32{acc[i][0][j], acc[i][1][j]});
                    h = cfma(Lc, h0, cf32{acc[i][0][j + 1], acc[i][1][j + 1]});
                    wr[u] = pack2(h0.r, h.r); wi[u] = pack2(h0.i, h.i);
                }
                if constexpr ((PRE == 2 || GQ >= 0) && q == GE) { tie(wr); tie(wi); dsw32<4>(lbase + DONE + wid * FST + lane * 4, 0u); }
                gst<0>(wr, ho, rh, q * 16384);
                gst<1024>(wi, ho, rh, q * 16384);
                if constexpr (q == P18) page<18>();
                asm volatile("s_nop 1" :: "v"(wr), "v"(wi));
                sched();
            });
        }
        TSX(10);
        auto flag = [&]() { gst<0>(u32x2{er[2], er[3]}, fv, rsf, hfo); TSX(5); };
        hop<22>();
        if constexpr (CVR == 2) {
            wait_v<16>();
            c_read(IC(1));
            wait_l<0>();
            c_req(IC(3));
            wait_v<8>();
            flag();
        } else {
            if constexpr (CVR == 1) wait_v<8>(); else { wait_v<0>(); flag(); }
            c_read(IC(0)); c_read(IC(1));
            wait_l<8>();
            c_req(IC(2));
            wait_l<0>();
            c_req(IC(3));
            if constexpr (CVR == 1) { wait_v<16>(); flag(); }
        }
        c_tie(IC(0)); c_tie(IC(1));
        static_for<16, 32>([&](auto ic) { undef(cf[CV(ic)]); });
    } else {
        const int ch = xcd * 128 + (idx >> 2) * 16 + lw * 4 + (idx & 3);
        unsigned dw, nx; u32x4 ad;
        auto c_unit = [&](auto uc) { static_for<0, 4>([&](auto ic) { constexpr int u = CV(uc), i = CV(ic); dma4<(u & 1) * 4096, i>(cst, vc[i], rsc, c_off(lw, u)); }); };
        constexpr int XV = 4 * (EC == 1) + 8 * (PFQ == 2);
        auto ctouch = [&](int r) { const unsigned j = (fresh(idx * 4 + lw) + r * 37) & 127; stouch((pc0 & ~255ull) + (j < CODE_KB * 8 / 3 ? j : j - 64) * 384); };
        {
            const unsigned fb = lbase + FULL + lw * FST;
            auto piece = [&](auto sc, auto ic) {
                constexpr int s = CV(sc), i = CV(ic);
                dma4<g1_buf(s) + (i & 4) * 1024, i & 3>(lb, vo[i & 3], (i & 2) ? rs1 : rs0, (BL ? (((s * 128 + rp) ^ (s >= BL && s < 16 - BL ? mi : mo)) + rot) & 0x780 : ((s + rot) & 15) * 128) + (i >> 2) * rst);
            };
            auto mark = [&](auto sc) { dma1<(31 - CV(sc)) * 4>(fb, oob, rs0, 0); };
            auto feed = [&](auto kc, auto ic) {
                constexpr int k = CV(kc), i = CV(ic);
                if constexpr (i < 4) piece(IC(k + 3), IC(i + 4));
                if constexpr (i == 3) mark(IC(k + 3));
                if constexpr (i >= 4 && k < 12) piece(IC(k + 4), IC(i - 4));
            };
            __builtin_amdgcn_s_setprio(HP);
            TS(7);
            sld(tag, ws + WS_F + F_EP);
            sld(dw, ssmD + (ch & ~1));
            const int sb = idx * 2 + (lw >> 1), kh = lw & 1;
            u32x2 kk[11], vv[8]; u32x4 w4[2], kd[4], ke[4], kvo[4];
            const unsigned ko = g4 * 16384 + l15 * 8, so = sb * 65536 + xcd * 256 + kh * 128;
            auto kv_ld = [&](auto jc) {
                constexpr int j = CV(jc), i = j < 13 ? j - 5 : j - 13;
                if constexpr (j < 2) gld<16 * j>(w4[j], l15 * 32, mk_rsrc(sk, 8192), xcd * 1024 + kh * 512);
                else if constexpr (j < 5) gld<0>(kk[j - 2], ko + so - 6144 + (j - 2) * 2048, mk_rsrc(kin, 4 << 20), 0);
                else if constexpr (j < 13) gld<(i & 1) * 2048>(kk[3 + i], ko, mk_rsrc(kin, 4 << 20), so + (i >> 1) * 4096);
                else gld<(i & 1) * 2048>(vv[i], ko, mk_rsrc(vin, 4 << 20), so + (i >> 1) * 4096);
            };
            {
                static_for<0, KLP>(kv_ld);
                dma1<DSK>(lbase, lane * 4, mk_rsrc(dskip, 2048), dt * 256);
                dma<U0, false>(lbase + kh * 1024, oob, rs0, 0);
            }
            static_for<4, 8>([&](auto ic) { feed(IC(-2), ic); });
            const __amdgpu_buffer_rsrc_t rk = mk_brs(ws + WS_KVT + (size_t)((xcd & 3) * 32 + kh * 16) * 32768, 524288);
            const unsigned K = lbase + KC + lw * KCH;
            unsigned kfo;
            const unsigned n0 = (xcd & 4) << 11;
            auto st = [&](int c) { st16(kvo[c], rk, (l15 >> 2) * 131072 + g4 * 64 + (l15 & 3) * 16, c * 32768 + (sb >> 5) * 16384 + (KVS ? nx : n0 ^ ((sb & 32) << 8)) + (sb & 31) * 256); };
            float kf[7], pa[4], pb[4];
            auto kv_step = [&](auto uc, auto jc) {
                constexpr int u = CV(uc), j = CV(jc), c = u >> 1, h = u & 1;
                auto el = [&](const u32x2& q) -> float { return (c & 1) ? bfhi(q[c >> 1]) : bflo(q[c >> 1]); };
                auto tap = [&](int t) { const unsigned w = w4[c >> 1][(c & 1) * 2 + (t >> 1)]; return (t & 1) ? bfhi(w) : bflo(w); };
                auto all = [&](auto f) {
#pragma unroll
                    for (int i = 0; i < 4; ++i) f(i);
                };
                if constexpr (j == 0) {
#pragma unroll
                    for (int i = 0; i < 7; ++i) { kf[i] = (h && i < 3) ? kf[i + 4] : el(kk[4 * h + i]); tie(kf[i]); }
                }
                if constexpr (EXACT) {
                    if constexpr (j == 1) { const float t = tap(3); all([&](int i) { pa[i] = kf[i] * t; }); all([&](int i) { pa[i] = rbf(pa[i]); tie(pa[i]); }); }
                    if constexpr (j == 2 || j == 4 || j == 6) { const float t = tap(3 - j / 2); all([&](int i) { pb[i] = kf[i + j / 2] * t; }); all([&](int i) { pb[i] = rbf(pb[i]); tie(pb[i]); }); }
                    if constexpr (j == 3 || j == 5 || j == 7) { all([&](int i) { pa[i] += pb[i]; }); all([&](int i) { pa[i] = rbf(pa[i]); tie(pa[i]); }); }
                } else {
                    if constexpr (j == 1) { const float t = tap(3); all([&](int i) { pa[i] = kf[i] * t; tie(pa[i]); }); }
                    if constexpr (j >= 2 && j <= 4) { const float t = tap(4 - j); all([&](int i) { pa[i] = fma_(kf[i + j - 1], t, pa[i]); tie(pa[i]); }); }
                    if constexpr (j == 5) all([&](int i) { pa[i] = rbf(pa[i]); tie(pa[i]); });
                }
                if constexpr (j == (EXACT ? 7 : 6)) all([&](int i) { pb[i] = el(vv[4 * h + i]); tie(pb[i]); });
                if constexpr (j == 7) {
                    all([&](int i) { pa[i] *= pb[i]; });
                    kvo[c][3 - 2 * h] = pack2(pa[1], pa[0]); kvo[c][2 - 2 * h] = pack2(pa[3], pa[2]);
                    if constexpr (h && !KST && u >= kv_first(KSD)) st(c);
                    else if constexpr (u == 7) all(st);
                    else tie(kvo[c]);
                }
            };
            static_for<-1, 13>([&](auto kc) {
                constexpr int k = CV(kc);
                if constexpr (k == PGF) page<1>();
                if constexpr (k == 2) hop<0>();
                if constexpr (k == 6) hop<6>();
                if constexpr (k == 8) hop<9>();
                if constexpr (k >= 0) barrier();
                if constexpr (k >= 0 && ((CTM >> k) & 1)) ctouch(k);
                if constexpr (k == KV0) {
                    wait_v<KLP == 21 ? 4 + hv_cnt(-1, k) : 9 - KLI - (KLI > 3) + hv_cnt(KLL + 1, k)>();
                    static_for<0, 11>([&](auto ic) { tie(kk[CV(ic)]); });
                    static_for<0, 8>([&](auto ic) { tie(vv[CV(ic)]); });
                    tie(w4[0]); tie(w4[1]); keep(c_dt); keep(c_re); keep(c_im);
                    TS(8);
                }
                if constexpr (k == KVP) {
                    wait_l<0>();
                    ties(tag); ties(dw);
                    tag += 0x01010101u;
                    __builtin_amdgcn_raw_buffer_store_b64(u32x2{tag, ~tag ^ vb}, rf, lane || vw >= 32 ? OOB : 0u, F_C + vw * 8, 16);
                }
                if constexpr (k == KCQ) dma1<POLL>(lbase + lw * 256, lane * 4, rsf, F_C);
                if constexpr (TM == 16 && k == KTI) stouch((unsigned long long)(ws + WS_E) + (TX ? (xcd * 128 + idx * 4 + lw) : b * 4 + lw) * 384);
                if constexpr (TM && TM < 16 && k == KTI) {
                    const unsigned wv = b * 4 + lw, a = lane < 32 ? ((TM & 1) ? wv * 4096 + lane * 128 : (TM & 4) && lane < 4 ? wv * 4096 + lane * 1024 + 896 : OOB) : lane < 35 && (TM & 2) ? (unsigned)(WS_E - WS_PP) + (wv * 3 + lane - 32) * 128 : OOB;
                    if constexpr (TM & 8) dma1<DUMP>(lbase + lw * 256, a, mk_rsrc(ws + WS_PP, WS_F + F_EP - WS_PP), 0);
                    else asm volatile("buffer_atomic_or %0, %1, %2, 0 offen sc1" :: "v"(0u), "v"(a), "s"(mk_rsrc(ws + WS_PP, WS_F + F_EP - WS_PP)) : "memory");
                }
                if constexpr (k == KCC) {
                    unsigned f, n, sp;
                    wait_v<hv_cnt(KCQ, k) - 1>();
                    dsr32<POLL - 131072>(f, lbase + 131072 + lw * 256 + lane * 4);
                    wait_l<0>();
                    vote_chk(f, (lane & 1) ? ~tag : tag, lane & 1, lbase + POLL + lw * 256 + lane * 4, lbase + POLL + lw * 256, lane * 4, rsf, F_C, sp);
                    nr = vote_cnt(f, tag, (xcd & 4) << 11, n);
                    nx = nr ^ ((xcd & 2) << 12);
                    kfo = F_K + (KVS ? nr : n0) + (((xcd & 3) * 2 + (idx >> 4)) * 64 + lw * 16) * 8;
                    TSV(11, n + 100 * (0x10000 - sp));
                }
                if constexpr (k == KFL) {
                    wait_v<hv_cnt(KVL + 1, k)>();
                    __builtin_amdgcn_raw_buffer_store_b64(u32x2{tag, ~tag}, rf, lane ? OOB : 0u, F_K + (KVS ? nr : n0) + (((xcd & 3) * 2 + kh) * 64 + sb) * 8, 16);
                    TSX(10);
                }
                if constexpr (k == 12) {
                    dma1<0>(K, oob, rs0, 0);
                    dma1<KCP>(K, oob, rs0, 0);
                }
                static_for<0, 8>([&](auto ic) {
                    constexpr int i = CV(ic), u0 = kv_first(k), nu = kv_first(k + 1) - u0;
                    static_for<KLP, 21>([&](auto jc) { if constexpr (ld_pos(CV(jc)) == 8 * k + i) kv_ld(jc); });
                    if constexpr (k == KPL && i == KPI) dma1<POLL>(lbase + wid * 256, (lane & 31) * 4, rsf, kfo);
                    feed(kc, ic);
                    if constexpr (k == 4 && i == 4) hop<3>();
                    if constexpr (!KST && k == KSD && (i & 1) && i / 2 < kv_first(KSD) / 2) st(i / 2);
                    if constexpr (nu == 1) kv_step(IC(u0), ic);
                    if constexpr (nu == 2) { kv_step(IC(u0 + i / 4), IC(2 * (i & 3))); kv_step(IC(u0 + i / 4), IC(2 * (i & 3) + 1)); }
                    if constexpr (!KDL && k == 12 && i >= 4) gld<(i - 4) * 1024>(kd[i - 4], lane * 16, mk_rsrc(ssmk, 4 << 20), ch * 4096);
                    if constexpr (k == 11 && i == 4) c_setup();
                    if constexpr (k == 10 && i >= 4) { cad[i - 4] = cst + frag(i & 1, (i >> 1) & 1); tie(cad[i - 4]); }
                    sched();
                });
            });
            page<12>();
            TS(1);
            if constexpr (EB == 2) barrier();
            unsigned pf;
            const unsigned fr = lbase + FULL + (lane & 3) * FST;
            wait_v<hv_cnt(KPL, 12) - (KPL < 12 ? 1 : 2) - (KPL == KFL) - KPI - (KPI > 3) + (KDL ? 7 : 11)>();
            dsr32<POLL - 131072>(pf, lbase + 131072 + wid * 256 + lane * 4);
            wait_l<0>();
            tag_chk<KSL>(pf, (lane & 1) ? ~tag : tag, (lane & 31) * 4, rsf, kfo);
            if constexpr (KDL) static_for<0, 4>([&](auto ic) { gld<CV(ic) * 1024>(kd[CV(ic)], lane * 16, mk_rsrc(ssmk, 4 << 20), ch * 4096); });
            auto b13 = [&]() { if constexpr (NB == 2) { unsigned f = 1; zero_chk<12, 1>(f, lbase + DONE + (lane & 3) * FST); } else if constexpr (NB < 2) barrier(); };
            if constexpr (EB == 1) b13();
            {
                const u32x4 rs = mk_rsrc(ws + WS_KVT + (size_t)((xcd & 3) * 32 + idx) * 32768, 32768);
                static_for<0, 4>([&](auto ic) { constexpr int i = CV(ic); dma<U0 + 2048 + i * 1024, true>(lbase + lw * 4096, (lane >> 2) * 64 + (((lane & 3) ^ g4) << 4), rs, (lw >> 1) * 16384 + (KVS ? nx : n0 ^ ((lw & 2) << 12)) + (lw & 1) * 4096 + i * 1024); });
            }
            mark(IC(16));
            auto warm = [&]() {
                const unsigned wo = g4 * 2048 + l15 * 16, dl = lbase + lw * 1024;
                const unsigned qo = (512 * (idx >> 3) + 128 * (idx & 1) + 32 * lw + 4 * ((idx >> 1) & 3)) * 2048 + xcd * 256, xo = (tm2 * 128 + 32 * lw + 8 * (qx & 2)) * 2048 + dt * 256;
                static_for<0, 4>([&](auto ic) { constexpr int i = CV(ic); dma<DUMP, false>(dl, wo, mk_rsrc(qin, 4 << 20), qo + ((i & 1) * 16 + (i >> 1) * 256) * 2048); });
                static_for<0, 4>([&](auto ic) { constexpr int i = CV(ic); dma<DUMP, false>(dl, wo, mk_rsrc(x, 4 << 20), xo + i * 8192); });
            };
            if constexpr (PFQ == 1) warm();
            if constexpr (EB == 0) b13();
            auto c_first = [&]() {
                if constexpr (CL <= 1) c_unit(IC(0));
                if constexpr (CL <= 0) c_unit(IC(1));
                if constexpr (EC == 1) static_for<0, 4>([&](auto ic) { constexpr int i = CV(ic); dma4<DUMP, i>(lbase, vc[i], mk_rsrc(cre + (size_t)dt * 131072, 262144), c_off(lw, 0)); });
                if constexpr (PFQ == 2) warm();
            };
            static_assert(NB != 3 || CL == 2, "");
            if constexpr (!KO) c_first();
            wait_v<5 + 8 * (PFQ == 1) + (KO ? 0 : 4 * (CL <= 1) + 4 * (CL <= 0) + XV)>();
            static_for<0, 4>([&](auto ic) { tie(kd[CV(ic)]); });
#pragma unroll
            for (int n = 0; n < 4; ++n) {
                const unsigned first = n ? __builtin_amdgcn_readlane(kd[n ? n - 1 : 0][3], 63) : 0u;
                const unsigned p = __builtin_amdgcn_update_dpp(first, kd[n][3], 0x138, 0xf, 0xf, false);
                ke[n] = u32x4{__builtin_amdgcn_alignbyte(kd[n][0], p, 2), __builtin_amdgcn_alignbyte(kd[n][1], kd[n][0], 2),
                              __builtin_amdgcn_alignbyte(kd[n][2], kd[n][1], 2), __builtin_amdgcn_alignbyte(kd[n][3], kd[n][2], 2)};
                tie(ke[n]);
            }
            if constexpr (ZM) {
                const unsigned dl = (ch & 1) ? dw >> 16 : dw & 0xffffu, dv = dl << ((~lane & 1) * 16);
                static_for<0, 4>([&](auto ic) { constexpr unsigned long long k = lag0_mask(CV(ic)); ad[CV(ic)] = sel0(dv, k); });
                tie(ad);
            }
            hop<14>();
            static_for<0, 4>([&](auto ic) { constexpr int i = CV(ic); dsw128<64 + i * 1024>(K + lane * 16, kd[i]); dsw128<KCP + 64 + i * 1024>(K + lane * 16, ke[i]); });
            if constexpr (KO) c_first();
            TS(9);
            static_for<0, -CL>([&](auto uc) {
                constexpr int u = CV(uc);
                wait_v<4 + (u < 2 ? XV : 0)>();
                static_for<0, 4>([&](auto ic) { constexpr int i = CV(ic); dsr128<(u & 1) * 4096>(cf[u * 4 + i], cad[i]); });
                wait_l<0>();
                static_for<0, 4>([&](auto ic) { tie(cf[u * 4 + CV(ic)]); });
                c_unit(IC(u + 2));
            });
            pf = 1;
            zero_chk<15 * 4>(pf, fr);
            if constexpr (PRE) { pf = 1; zero_chk<PRE == 1 ? 8 : 4>(pf, lbase + DONE + (lane & 3) * FST); }
            __builtin_amdgcn_s_setprio(0);
        }
        TS(2);
        TL(1);
        unsigned ka[4], ub[4];
#pragma unroll
        for (int i = 0; i < 4; ++i) {
            ka[i] = lbase + KC + lw * KCH + ((l15 & 1) ? 0 : KCP) + 84 + 2 * (l15 & 14) - 16 * g4 + i * 1024;
            const int e = 2 * l15 + g4 + 2;
            ub[i] = lbase + U0 + 64 * e + ((lw ^ (((e >> 2) + 3 - i) & 3)) << 4);
        }
        unsigned gad = lbase + DONE + lw * FST;
        if constexpr (GQ >= 0) tie(gad);
        f32x4 acc[8];
        bf16x4 FA[8][2]; bf16x8 FB[8];
        auto op = [&](auto gc) {
            constexpr Op o = cv_op(CV(gc));
            if constexpr (o.kind == CO_A) { constexpr int s = o.a >> 3, n = o.a & 7, d = (n & 1) * 128 + s * 16 + o.b * 2; dsr2<d, d + 1>(FA[n][o.b], ka[n >> 1]); }
            if constexpr (o.kind == CO_B) { constexpr int s = o.a >> 3, m = o.a & 7; dsr128<2048 * m + 1792 - 256 * s>(FB[m], ub[s & 3]); }
            if constexpr (o.kind == CO_C) dsr128<(o.a & 1) * 4096>(cf[o.a * 4 + o.b], cad[o.b]);
        };
        static_for<-36, 0>(op);
        static_for<0, 288>([&](auto gc) {
            constexpr int G = CV(gc), s = G / 36, q = G % 36, n = cv_n(q), m = cv_m(q);
            if constexpr (cv_wait(G) >= 0) wait_lr<cv_wait(G)>();
            if constexpr (q == 24 && s >= CL && s - CL < 8) static_for<0, 4>([&](auto ic) { tie(cf[(s - CL) * 4 + CV(ic)]); });
            if constexpr (ZM && G < 8) mfma<true>(acc[m], __builtin_bit_cast(bf16x8, ad), FB[m]);
            mfma<s == 0 && n == 0 && !ZM>(acc[n + m], cat(FA[n][0], FA[n][1]), FB[m]);
            sched();
            if constexpr (q == 11) TL(2 + 2 * s);
            if constexpr (q == 11 && s >= CL && s - CL < 8) wait_v<(s - CL < 7 ? 4 : 0) + (s - CL < (CL <= 1) + (CL <= 0) ? XV : 0) + 8 * (PFQ >= 3 && (s == PFQ - 2 || s == PFQ - 1))>();
            if constexpr (q == 11) TL(3 + 2 * s);
            op(gc);
            if constexpr (q == 30 && ((CTM >> (16 + s)) & 1)) ctouch(16 + s);
            if constexpr (G == 16) hop<17>();
            if constexpr (G == 66) hop<19>();
            if constexpr (G == PG1) page<21>();
            if constexpr (G == PG2) page<28>();
            if constexpr (G == 158) hop<23>();
            if constexpr (G == 209) hop<25>();
            if constexpr (G == 254) hop<27>();
            if constexpr (G == GQ) { unsigned f = 1; zero_chk<4, 1>(f, gad); }
            if constexpr (!NB && G == CBQ) barrier();
            if constexpr (NB == 3 && G == 23) { unsigned f = 1; zero_chk<8, 1>(f, lbase + DONE + (lane & 3) * FST); }
            if constexpr ((q == 24 || q == 25 || q == 29 || q == 34) && s + 2 - CL < 8) {
                constexpr int pc = q == 24 ? 0 : q == 25 ? 1 : q == 29 ? 2 : 3, u = s + 2 - CL;
                dma4<(u & 1) * 4096, pc>(cst, vc[pc], rsc, c_off(lw, u));
            }
            sched();
        });
        if constexpr (PG2 >= 288) page<28>();
        TL(18);
        TSX(3);
        {
            const __amdgpu_buffer_rsrc_t rz = mk_brs(ws + WS_ZT, 4 << 20);
            if constexpr (ZM) {
                asm volatile("s_nop 7");
#pragma unroll
                for (int jj = 0; jj < 4; ++jj) {
                    tie(acc[2 * jj]); tie(acc[2 * jj + 1]);
                    const f32x4 a = acc[2 * jj], c = acc[2 * jj + 1];
                    st16(u32x4{pack2(a[0], a[1]), pack2(a[2], a[3]), pack2(c[0], c[1]), pack2(c[2], c[3])}, rz, lane * 16 + jj * 1024, (ch & 510) * 8192 + nx + (ch & 1) * 4096);
                }
            } else {
            u32x2 kvr[8];
            const unsigned kva = lbase + U0 + 2048 + 128 * l15 + 64 * (g4 >> 1) + ((lw ^ ((l15 >> 1) & 3)) << 4) + 8 * (1 - (g4 & 1));
            static_for<0, 8>([&](auto ic) { dsr64<CV(ic) * 2048>(kvr[CV(ic)], kva); });
            wait_l<0>();
            const float Dd = (ch & 1) ? bfhi(dw) : bflo(dw);
#pragma unroll
            for (int jj = 0; jj < 4; ++jj) {
                u32x4 o;
#pragma unroll
                for (int u = 0; u < 2; ++u) {
                    const int j = 2 * jj + u;
                    tie(kvr[j]); tie(acc[j]);
                    const float kv3 = bflo(kvr[j][0]), kv2 = bfhi(kvr[j][0]), kv1 = bflo(kvr[j][1]), kv0 = bfhi(kvr[j][1]);
                    o[2 * u] = pack2(rbf(acc[j][0]) + rbf(kv0 * Dd), rbf(acc[j][1]) + rbf(kv1 * Dd));
                    o[2 * u + 1] = pack2(rbf(acc[j][2]) + rbf(kv2 * Dd), rbf(acc[j][3]) + rbf(kv3 * Dd));
                }
                st16(o, rz, lane * 16 + jj * 1024, (ch & 510) * 8192 + nx + (ch & 1) * 4096);
                if (jj == 1) { sched(); hop<29>(); sched(); }
            }
            }
            TS(8);
            hop<30>();
            if constexpr (CL > 0) {
                wait_v<4>();
                static_for<32 - 4 * CL, 32>([&](auto ic) { constexpr int i = CV(ic); dsr128<((i >> 2) & 1) * 4096>(cf[i], cad[i & 3]); });
                wait_l<0>();
                static_for<32 - 4 * CL, 32>([&](auto ic) { tie(cf[CV(ic)]); });
            }
        }
        TL(19);
        TSX(4);
    }
    {
        unsigned ln = lane, dsw_, pf;
        tie(ln);
        const int wid = fresh(wid_), lw = wid & 3, kg = wid >> 2, b = fresh(b_), xcd = b & 7, idx = b >> 3, tq = (idx & 7) + JR(idx >> 3);
        nr = fresh(__builtin_amdgcn_readfirstlane(nr));
        const unsigned nx = nr ^ ((xcd & 2) << 12), nh = nr ^ ((idx & 2) << 12), hp = (idx & 7) * 16 + (b & 1) * 8;
        const int tm2 = (xcd & 4) * 2 + (idx & 7), dt = (xcd & 2) * 2 + (idx >> 3), qx = ((~b & 1) * 2) ^ JQ(idx >> 3);
        const int ta = 512 * (idx >> 3) + 128 * (idx & 1) + 16 * wid + 4 * ((idx >> 1) & 3);
        const int tb = tm2 * 128 + 64 * kg;
        dsr32<DSK - 131072>(dsw_, lbase + 131072 + lw * 64 + (ln & 7) * 8 + ((ln >> 1) & 4));
        unsigned wl = lbase + wid * 4096, w2k = lbase + wid * 2048, hs0 = (hp + ((tq + (wid >> 1)) & 7)) * 65536 + nh + (wid & 1) * 4096 + qx * 16384;
        unsigned s_x = (tb + qx * 8) * 2048 + dt * 256 + lw * 64, s_q = ta * 2048 + xcd * 256, s_z = WS_ZT + (xcd & 3) * 1048576 + nx + idx * 128 + wid * 131072, zfo = WS_F + F_Z + (xcd & 2) * 8192 + nr + (xcd & 1) * 4096;
        unsigned ppo = PK ? WS_KVT + (xcd & 3) * 1048576 + idx * 32768 + kg * 16384 + (KVS ? nx : ((xcd ^ wid) & 4) << 11) + lw * 2048
                          : WS_PP + (xcd & 2) * 1048576 + idx * 65536 + (xcd & 1) * 32768 + kg * 16384 + nx + lw * 2048, s_a = ta * 8192 + xcd * 512, s_b = (tb + (2 ^ qx) * 8) * 8192 + 4096 + dt * 512 + lw * 128;
        const unsigned tagg = fresh(__builtin_amdgcn_readfirstlane(tag));
        u32x4 rsx = mk_rsrc(x, (NOX & 1) ? 0 : 4 << 20), rsq = mk_rsrc(qin, (NOX & 2) ? 0 : 4 << 20), rso = mk_rsrc(out, 16 << 20), rws = mk_rsrc(ws, 32 << 20);
        ties(wl); ties(w2k); ties(hs0); ties(s_x); ties(s_q); ties(s_z); ties(zfo); ties(ppo); ties(s_a); ties(s_b);
        ties(rsx); ties(rsq); ties(rso); ties(rws);
        const unsigned l31 = ln & 31, l5 = ln >> 5;
        unsigned l16 = ln * 16, ha[8];
        ha[0] = (lbase + kg * 1536 + l5 * 2048 + (ln & 2) * 128 + (ln & 1) * 8) | ((((ln >> 1) & 8) | ((ln >> 2) & 3) | ((ln & 2) << 1)) << 4);
        ha[1] = ha[0] ^ 64; ha[2] = ha[0] ^ 512; ha[3] = ha[0] ^ 576;
        static_for<0, 4>([&](auto ic) { ha[4 + CV(ic)] = ha[CV(ic)] + 65536; tie(ha[CV(ic)]); tie(ha[4 + CV(ic)]); });
        unsigned a_x = ((((ln >> 2) & 7) | (ln & 32)) << 11) | (l16 & 48), a_xr = (((l16 & 240) << 2) | (ln & 48)) + (w2k + ZST);
        unsigned a_q = ((ln & 48) << 7) | (l16 & 240), a_qr = (l5 << 10) + l31 * 8 + (w2k + QST), a_iw = w2k + IMG + l16, a_ir = a_iw ^ 8192;
        unsigned a_t = wid == 0 && ln == 0 ? idx * 128 : OOB, msk = 0xfff0fff0u;
        unsigned a_f = wid == 1 ? (ln >> 1) * 128 + (ln & 1) * 4 : OOB, expect = wid == 1 ? ((ln & 1) ? ~tagg : tagg) : 0u;
        unsigned a_z = (((ln ^ l5 ^ (wid * 4)) & 7) << 4) | ((ln & 48) << 10) | ((ln & 8) << 9), a_z2 = a_z ^ 32, a_zr = (l31 << 9) + (((((wid ^ ln) & 7) << 1) | l5) << 3) + (lbase + ZST);
        unsigned a_oa = (l5 << 21) | (l31 << 4), a_ob = ((((ln >> 2) & 4) | (ln & 32) | ((ln >> 3) & 1)) << 13) | (l16 & 112);
        unsigned a_p = PB ? (ln == 63 ? 1020u : OOB) : l16, a_w = ln < 16 ? ln * 128 : OOB;
        if constexpr (PB) tie(a_p);
        if constexpr (P_W >= -1) tie(a_w);
        if constexpr (P_W == -1) pf_ld(pf, a_w, rws, ppo);
        u32x2 tg2 = {tagg, ~tagg};
        tie(l16); tie(a_x); tie(a_xr); tie(a_q); tie(a_qr); tie(a_iw); tie(a_ir); tie(a_t); tie(msk); tie(a_f); tie(expect); tie(a_z); tie(a_z2); tie(a_zr);
        tie(a_oa); tie(a_ob); tie(tg2);
        wait_l<0>();
        tie(dsw_);
        u32x4 dg[2];
        static_for<0, 4>([&](auto ic) { constexpr unsigned long long k = diag_mask(CV(ic)); dg[0][CV(ic)] = sel0(dsw_ & 0xffffu, k); dg[1][CV(ic)] = sel0(dsw_ & 0xffff0000u, k); });
        tie(dg[0]); tie(dg[1]);
        unsigned fso, xso, ppi = ppo ^ (PK ? 1048576 : 32768), tn_[3] = {tagg & 0xf000fu, (tagg >> 8) & 0xf000fu, (tagg >> 4) & 0xf000fu}, psel = 0x05040100u;
        unsigned long long lo8 = 0x00ff00ff00ff00ffull, hi8 = ~lo8;
        ties(ppi); ties(tn_[0]); ties(tn_[1]); ties(tn_[2]); ties(psel); ties(lo8); ties(hi8);
        auto fill = [&](auto sc, auto pc) {
            constexpr int s = CV(sc), p = CV(pc);
            if constexpr (p == 0) dma_x<(2 * s % 3) * BUF, (s >> 1) * 16384 + (s & 1) * 262144, true>(fso, wl, l16, rws, hs0);
            else if constexpr (fill_m0(4 * s + p)) dma_m<(2 * s % 3) * BUF, p * 1024>(fso, wl, l16, rws);
            else dma_n<p * 1024>(fso, l16, rws);
        };
        bf16x4 hf[2][8];
        auto ldh = [&](auto nc, auto ic) {
            constexpr int n = CV(nc), i = CV(ic), j = (i >> 1) & 1, bo = (2 * (n >> 2) % 3) * BUF + (g2_unit(n, j) & 3) * 8192 + (i >> 2) * 4096, hi = bo >= 65536 ? 65536 : 0;
            dsr64tr<bo - hi>(hf[n & 1][i], ha[2 * g2_tile(n, j) + (i & 1) + (hi >> 14)]);
        };
        f32x4 yc[2][2][2];
        f32x4 im[2], ao, ob[2]; u32x4 pl[2], po;
        bf16x8 xf[2];
        u32x2 zv[4], qv[2];
        unsigned ck, ex, fail[2];
        float tq_[2], tz, sm[8], mx[2], cv[8];
        auto finB = [&](auto pc, auto jc) {
            constexpr int p = CV(pc), j = CV(jc);
            if constexpr (j < 8) { yc[p & 1][0][j & 1][j >> 1] += im[j & 1][j >> 1]; tie(yc[p & 1][0][j & 1]); }
            else {
                constexpr int i = (j - 8) >> 2, k = j & 3, dd = k & 1, h = k >> 1;
                ob[i][k] = sel8(yc[p & 1][0][dd][2 * i + 1 - h], yc[p & 1][0][dd][2 * i + h], h ? hi8 : lo8);
                if constexpr (k == 3 && (p == 3 || !(NOST & 4))) gst<0>(ob[i], a_ob, rso, s_b ^ ((p & 1) * 65536 + i * 16384));
                if constexpr (j == 12) keep(ob[0]);
            }
        };
        auto snd = [&](auto pc, auto jc) {
            constexpr int p = CV(pc), j = CV(jc);
            if constexpr (j < 8) { sm[j] = yc[p][0][j >> 2][j & 3] + im[j >> 2][j & 3]; tie(sm[j]); }
            if constexpr (j == 8) mx[0] = amax3(sm[0], sm[1], sm[2]);
            if constexpr (j == 9) mx[1] = amax3(sm[3], sm[4], sm[5]);
            if constexpr (j == 10) mx[0] = amax3(mx[0], sm[6], sm[7]);
            if constexpr (j == 11) mx[0] = amax3(mx[0], mx[1], mx[1]);
            if constexpr (j == 12) { ex = f2u(mx[0]) + 0x4000u; tie(ex); }
            if constexpr (j == 13) { ex &= 0x7f800000u; tie(ex); }
            if constexpr (j == 14) { mx[0] = u2f(ex + 0x04c00008u); tie(mx[0]); }
            if constexpr (j == 15) { ex >>= 23; tie(ex); }
            if constexpr (j >= 16 && j < 24) { sm[j - 16] += mx[0]; tie(sm[j - 16]); }
            if constexpr (j == 24) { ex |= ex << 12; tie(ex); }
            if constexpr (j >= 25 && j < 29) { po[j - 25] = __builtin_amdgcn_perm(f2u(sm[2 * (j - 25) + 1]), f2u(sm[2 * (j - 25)]), psel); tie(po); }
            if constexpr (j == 29) { po[0] = bfi_v(msk, po[0], ex); tie(po); }
            if constexpr (j >= 30 && j < NS) { po[j - 29] = bfi_s(msk, po[j - 29], tn_[j - 30]); if constexpr (j < NS - 1) tie(po); }
            if constexpr (j == NS - 1 && !(NOST & 2)) gst<p * 1024>(po, l16, rws, ppo);
        };
        auto rcv = [&](auto hc, auto jc) {
            constexpr int h = CV(hc), j = CV(jc);
            if constexpr (j < 3) { pl[h][j + 1] ^= tn_[j]; pp_tie<h>(pl[h]); }
            if constexpr (j == 3) { ck = pl[h][1] | pl[h][2] | pl[h][3]; tie(ck); }
            if constexpr (j == 4) { if constexpr (PB) ck = bfi_f(msk, fail[h], ck); else ck &= 0xf000fu; tie(ck); }
            if constexpr (j == 6 && !(NOST & 2)) pp_chk<h>(pl[h], ck, tagg, l16, rws, ppi, a_p);
            if constexpr (j == 7) { ex = pl[h][0] >> 12; tie(ex); }
            if constexpr (j == 8) ex = bfi_v(msk, ex, pl[h][0]);
            if constexpr (j == 9) { pl[h][0] &= msk; pp_tie<h>(pl[h]); }
            if constexpr (j == 10) { ex <<= 23; tie(ex); }
            if constexpr (j == 11) { ex &= 0x7f800000u; tie(ex); }
            if constexpr (j == 12) { mx[1] = u2f(ex) * 0x1p-14f; tie(mx[1]); }
            if constexpr (h == 0 && j >= 13) {
                constexpr int k = j - 13, a = (k + 1) / 2, e = k == 15 ? 7 : k / 2 - 1;
                if constexpr (k == 0 || ((k & 1) && k < 15)) cv[a] = cvt16<a & 1>(pl[h][a >> 1]);
                else { yc[0][0][e >> 2][e & 3] = fma_(cv[e], mx[1], yc[0][0][e >> 2][e & 3]); tie(yc[0][0][e >> 2]); }
            }
            if constexpr (h == 1 && j >= 13 && j < 21) cv[j - 13] = cvt16<(j - 13) & 1>(pl[h][(j - 13) >> 1]);
            if constexpr (h == 1 && j >= 21) { constexpr int e = j - 21; im[e >> 2][e & 3] = fma_(cv[e], mx[1], im[e >> 2][e & 3]); tie(im[e >> 2]); }
        };
        auto side = [&](auto pc) {
            constexpr int P = CV(pc), n = P >> 3, m = P & 7, p = n >> 3;
            if constexpr (P == P_X || P == P_X + 2) dma_x<ZST + (P != P_X) * 1024, (P != P_X) * 16384, false>(xso, w2k, a_x, rsx, s_x);
            if constexpr (P == P_W) pf_ld(pf, a_w, rws, ppo);
            if constexpr (!NOA) {
            if constexpr (P == P_Q || P == P_Q + 2) dma_x<QST + (P != P_Q) * 1024, (P != P_Q) * 524288, false>(xso, w2k, a_q, rsq, s_q);
            if constexpr (P == P_T) gst<0>(tg2, a_t, rws, zfo);
            if constexpr (P == AT(ZP, 4)) { if constexpr (P_W >= 0) pf_keep(pf); pf_ld(pf, a_f, rws, zfo); }
            if constexpr (P == P_ZC) { wait_v<g2_allow(P, AT(ZP, 4))>(); tag_chk(pf, expect, a_f, rws, zfo); }
            if constexpr (P == P_Z || P == P_Z + 2) dma_x<ZST + (P != P_Z) * 1024, (P != P_Z) * 65536, true>(xso, w2k, P == P_Z ? a_z : a_z2, rws, s_z);
            }
            if constexpr (P == AT(7, 5) || P == AT(7, 7)) dsr128<(m - 5) * 512>(xf[(m - 5) / 2], a_xr);
            if constexpr (P == AT(8, 4)) { tie(xf[0]); tie(xf[1]); }
            if constexpr (P == AT(8, 5) || P == AT(9, 3)) static_for<0, 2>([&](auto dc) { mfma<false>(yc[n & 1][0][CV(dc)], xf[n & 1], __builtin_bit_cast(bf16x8, dg[CV(dc)])); });
            if constexpr ((n & 7) == 6 && m == XW && !(DBG & 2)) dsw128<0>(a_iw, yc[p & 1][1][0]);
            if constexpr ((n & 7) == 6 && m == XW2 && !(DBG & 2)) dsw128<1024>(a_iw, yc[p & 1][1][1]);
            if constexpr ((n & 7) == 7 && (m & 5) == (n < 31 ? 4 : 0)) dsr128<(m & 2) * 512>(im[(m >> 1) & 1], a_ir);
            if constexpr (n >= 8 && (n & 7) == 0 && m == 4) { tie(im[0]); tie(im[1]); }
            if constexpr (P >= P_PP0 && P <= E_PP0) snd(IC(0), IC(P - P_PP0));
            if constexpr (P >= P_PP1 && P <= E_PP1) static_for<0, D1>([&](auto ic) { snd(IC(1), IC((P - P_PP1) * D1 + CV(ic))); });
            if constexpr (PB && (P == P_B0 - 1 || P == P_B1 - 1)) pf_zero(pf);
            if constexpr (PB && (P == P_B0 || P == P_B1)) pf_at<(P == P_B1) * 1024>(pf, a_p, rws, ppi);
            if constexpr (P == P_L0 || P == P_L1) {
                unsigned so = ppi;
                if constexpr (PB) { wait_v<g2_allow(P, P - QB) + (g2_fill(P) >= 0)>(); pp_sel(so, fail[P == P_L1], pf, tn_[2], ppi, ppo); }
                pp_ld<P == P_L1>(pl[P == P_L1], l16, rws, so);
            }
            if constexpr (P == P_R0 || P == P_R1) { wait_v<g2_allow(P, P == P_R0 ? imax(P_L0, XL ? P_X + 2 : 0) : P_L1)>(); pp_tie<P == P_R1>(pl[P == P_R1]); }
            if constexpr (P >= P_R0 && P <= E_R0) static_for<0, D0>([&](auto ic) { if constexpr ((P - P_R0) * D0 + CV(ic) < NR) rcv(IC(0), IC((P - P_R0) * D0 + CV(ic))); });
            if constexpr (P >= P_R1 && P < P_R1 + 21) rcv(IC(1), IC(P - P_R1));
            if constexpr (!NOA) {
            if constexpr (n == ZW && m >= 4) dsr64<(m - 4) * 128>(zv[m - 4], a_zr);
            static_for<0, 4>([&](auto rc) {
                constexpr int r = CV(rc);
                if constexpr (P == q_tie(r)) tie(qv[r & 1]);
                if constexpr (P == q_rd(r)) dsr64<r * 256>(qv[r & 1], a_qr);
            });
            if constexpr (P == AT(ZW + 1, 4)) { tie(zv[0]); tie(zv[1]); tie(zv[2]); tie(zv[3]); }
            }
            if constexpr (a_step(P) >= 0) {
                constexpr int s = a_step(P), e = s == 0 ? 0 : s % 3 == 2 ? (s + 1) / 3 : s % 3 == 1 ? s / 3 : s / 3 - 1, r = e >> 2, c = e & 3;
                if constexpr (s == 0 || (s % 3 == 2 && e < 16)) { tq_[e & 1] = (c & 1) ? bfhi(qv[r & 1][c >> 1]) : bflo(qv[r & 1][c >> 1]); tie(tq_[e & 1]); }
                else if constexpr (s % 3 == 1) { tz = (r & 1) ? bfhi(zv[c][r >> 1]) : bflo(zv[c][r >> 1]); tie(tz); }
                else if constexpr (s % 3 == 0) {
                    ao[c] = tq_[e & 1] * tz;
                    if constexpr (c == 3 && !(NOST & 1)) gst<0>(ao, a_oa, rso, s_a + r * 8192);
                    else tie(ao);
                }
            }
            if constexpr (P >= P_F2 && P < P_F2 + 16) finB(IC(2), IC(P - P_F2));
            if constexpr (a_step(P - 1) > 0 && a_step(P - 1) % 12 == 0) keep(ao);
            if constexpr (P == E_PP0 + 1 || P == E_PP1 + 1) keep(po);
            if constexpr (P == P_F2 + 16) keep(ob[1]);
        };
        {
            unsigned e_h = wid == 3 ? ((ln & 1) ? ~tagg : tagg) : 0u, hsl = lbase + lw * 8192, hso = (hp + ((tq + wid) & 7)) * 65536 + nh + qx * 16384;
            unsigned a_h = wid == 3 ? ln * 4 : OOB, s_h = WS_F + F_H + nr + hp * 32, ca2[4], a_g = lbase + HGO + ln * 4;
            static_for<0, 4>([&](auto ic) { ca2[CV(ic)] = cad[CV(ic)] + 2 * BUF; tie(ca2[CV(ic)]); });
            tie(e_h); tie(a_h); tie(a_g); ties(hsl); ties(hso); ties(s_h);
            sched();
            if (wid < 4) {
                auto look = [&]() { if constexpr (HA) pf_at<0>(pf, a_h, rws, s_h); else pf_ld(pf, a_h, rws, s_h); };
                if constexpr (HA) pf_zero(pf);
                if constexpr (PW == 0) { look(); wait_v<1>(); }
                if constexpr (PW == 1) { wait_v<0>(); look(); }
                if constexpr (PW == 2) { wait_v<8>(); look(); wait_v<1>(); }
                static_for<16, 32>([&](auto ic) { constexpr int i = CV(ic); dsr128u<((i >> 2) & 1) * 4096>(cf[i], (i & 8) ? cad[i & 3] : ca2[i & 3]); });
                wait_l<0>();
                static_for<16, 32>([&](auto ic) { tie(cf[CV(ic)]); });
                wait_v<0>();
                tag_chk<1, HA>(pf, e_h, a_h, rws, s_h);
                if (wid == 3) flag_set(a_g, 1u); else gate<1>(a_g, 1);
                TSX(6);
                hop<26>();
                static_for<0, 4>([&](auto ic) {
                    constexpr int i = CV(ic);
                    dma_x<(i >> 1) * 2 * BUF + (i & 1) * 4096, (i >> 1) * 262144 + (i & 1) * 4096, true>(fso, hsl, l16, rws, hso);
                    dma_n<1024>(fso, l16, rws); dma_n<2048>(fso, l16, rws); dma_n<3072>(fso, l16, rws);
                });
            }
        }
        wait_v<8>();
        if (wid < 4) TS(7);
        TG_DECL
        barrier();
        static_for<0, 8>([&](auto ic) { ldh(IC(0), ic); });
        static_for<0, 32>([&](auto nc) {
            constexpr int n = CV(nc), p = n >> 3;
            if constexpr (n == PG3) page<33>();
            if constexpr (n == PG4) page<37>();
            wait_lr<(n & 3) == 3 ? 0 : 4 + g2_lx(n - 1)>();
            if constexpr (n == 31) barrier();
            if constexpr ((n & 3) == 3 && n < 31) {
                constexpr int need = g2_need(n);
                TL(21 + n / 4 * 3);
                wait_v<g2_allow(8 * n, need)>();
                TL(22 + n / 4 * 3);
                barrier();
                TL(23 + n / 4 * 3);
            }
            if constexpr (n == 1) hop<31>();
            if constexpr (n == 4) hop<32>();
            if constexpr (n == 11) hop<34>();
            if constexpr (n == 15) hop<35>();
            if constexpr (n == 19) hop<36>();
            if constexpr (n == 27) hop<38>();
            if constexpr (n == 31) hop<39>();
            if constexpr (n == 0) TL(20);
            TG(20 + n);
            if constexpr (n == 31) TL(42);
            if constexpr (PRG) __builtin_amdgcn_s_setprio(n == 31 ? 0 : 3 - ((n + 1) & 3));
            sched();
            static_for<0, 8>([&](auto mc) {
                constexpr int m = CV(mc), ks = m >> 2, tt = (m >> 1) & 1, dd = m & 1, P = n * 8 + m;
                if constexpr (m == 4 && (n & 3) != 3) wait_lr<(rd1(n) ? 8 : 4) + ((n & 7) == 6 ? (XW < 4) + (XW2 < 4) : 0)>();
                mfma<(n & 7) == 0 && ks == 0>(yc[p & 1][(DBG & 1) ? tt : g2_tile(n, tt)][dd], cat(hf[n & 1][4 * ks + 2 * tt], hf[n & 1][4 * ks + 2 * tt + 1]), cf[g2_unit(n, tt) * 4 + 2 * ks + dd]);
                sched();
                if constexpr (n < 31 && rd1(n) && m < 4) { ldh(IC(n + 1), IC(2 * m)); ldh(IC(n + 1), IC(2 * m + 1)); }
                if constexpr (n < 31 && !rd1(n)) ldh(IC(n + 1), mc);
                if constexpr (g2_fill(P) >= 0) fill(IC(g2_fill(P) >> 2), IC(g2_fill(P) & 3));
                side(IC(P));
                sched();
            });
        });
        TG(52);
        if (wid >= 4) TSX(5); else TS(8);
        TG(53);
        static_for<256 - P_R1, 21>([&](auto jc) { rcv(IC(1), jc); });
        wait_l<0>();
        tie(im[0]); tie(im[1]);
        static_for<21, NR>([&](auto jc) { rcv(IC(1), jc); });
        asm volatile("s_nop 3");
        sched();
        static_for<0, 16>([&](auto jc) { finB(IC(3), jc); });
        if (b == 255 && wid == 0) gst<0>(u32x2{tagg, tagg}, l16 ? OOB : 0u, rws, WS_F + F_EP);
    }
}

}

#if defined(__HIP_DEVICE_COMPILE__)
#pragma clang attribute pop
#endif

extern "C" void kernel_launch(void* const* d_in, const int* in_sizes, int n_in, void* d_out, int out_size, void* d_ws, size_t ws_size,
                              hipStream_t stream) {
    (void)in_sizes; (void)n_in; (void)out_size; (void)ws_size;
    using namespace fk;
    const uint16_t* q = (const uint16_t*)d_in[0];
    const uint16_t* kin = (const uint16_t*)d_in[1];
    const uint16_t* vin = (const uint16_t*)d_in[2];
    const uint16_t* sk = (const uint16_t*)d_in[3];
    const uint16_t* ssmk = (const uint16_t*)d_in[4];
    const uint16_t* ssmD = (const uint16_t*)d_in[5];
    const uint16_t* x = (const uint16_t*)d_in[6];
    const uint16_t* lre = (const uint16_t*)d_in[7];
    const uint16_t* lim = (const uint16_t*)d_in[8];
    const uint16_t* bre = (const uint16_t*)d_in[9];
    const uint16_t* bim = (const uint16_t*)d_in[10];
    const uint16_t* cre = (const uint16_t*)d_in[11];
    const uint16_t* cim = (const uint16_t*)d_in[12];
    const uint16_t* logdt = (const uint16_t*)d_in[13];
    const uint16_t* dskip = (const uint16_t*)d_in[14];
#define WOFF 0
    char* ws = (char*)(((uintptr_t)d_ws + ((size_t)WOFF << 20) + 0x1fffff) & ~(uintptr_t)0x1fffff);
    hipLaunchKernelGGL(k1, dim3(256), dim3(512), 0, stream, x, bre, bim, lre, lim, logdt, ws, cre, cim, dskip, ssmk, ssmD, q, kin, vin, sk, (float*)d_out TS_PASS(0));
}
```

```cpp
#include <hip/hip_runtime.h>
#include <stdint.h>
#include <type_traits>

#if defined(__HIP_DEVICE_COMPILE__)
#pragma clang attribute push(__attribute__((target("no-packed-fp32-ops"))), apply_to = function)
#endif

namespace fk {

typedef __attribute__((ext_vector_type(8))) short bf16x8;
typedef __attribute__((ext_vector_type(4))) short bf16x4;
typedef __attribute__((ext_vector_type(4))) float f32x4;
typedef __attribute__((ext_vector_type(4))) unsigned u32x4;
typedef __attribute__((ext_vector_type(16))) unsigned u32x16;
typedef __attribute__((ext_vector_type(2))) unsigned u32x2;
typedef __attribute__((ext_vector_type(2))) __bf16 bf16x2_t;
typedef __attribute__((address_space(1))) unsigned gu32;
typedef __attribute__((address_space(1))) unsigned long long gu64;
#define RLX_AGENT __ATOMIC_RELAXED, __HIP_MEMORY_SCOPE_AGENT

__device__ __forceinline__ float u2f(unsigned v) { return __builtin_bit_cast(float, v); }
__device__ __forceinline__ unsigned f2u(float v) { return __builtin_bit_cast(unsigned, v); }
__device__ __forceinline__ bool wall(bool p) { return __builtin_amdgcn_ballot_w64(p) == ~0ull; }
__device__ __forceinline__ float bflo(unsigned w) { return u2f(w << 16); }
__device__ __forceinline__ float bfhi(unsigned w) { return u2f(w & 0xffff0000u); }
__device__ __forceinline__ unsigned pack2(float lo, float hi) {
    bf16x2_t v; v[0] = (__bf16)lo; v[1] = (__bf16)hi; return __builtin_bit_cast(unsigned, v);
}
__device__ __forceinline__ float rbf(float f) { float r; asm("v_cvt_pk_bf16_f32 %0, 0, %1" : "=v"(r) : "v"(f)); return r; }
__device__ __forceinline__ float fma_(float a, float b, float c) { return __builtin_fmaf(a, b, c); }

template <int I, int N, class F>
__device__ __forceinline__ void static_for(F&& f) {
    if constexpr (I + 1 == N) f(std::integral_constant<int, I>{});
    else if constexpr (I < N) { static_for<I, I + (N - I) / 2>(f); static_for<I + (N - I) / 2, N>(f); }
}
#define IC(v) std::integral_constant<int, (v)>{}
#define CV(c) decltype(c)::value

#define TS_ARG
#define TS_ARG0
#define TS_PASS(off)
#define TS_DECL
#define TS(i)
#define TSV(i, v)
#define TL_DECL
#define TL(i)
#define TS_FLUSH(slot)

#define TG_DECL
#define TG(i)
template <int N> __device__ __forceinline__ void wait_v() { asm volatile("s_waitcnt vmcnt(%0)" ::"n"(N > 63 ? 63 : N) : "memory"); }
template <int N> __device__ __forceinline__ void wait_l() { asm volatile("s_waitcnt lgkmcnt(%0)" ::"n"(N > 14 ? 14 : N) : "memory"); }
template <int N> __device__ __forceinline__ void wait_lr() { __builtin_amdgcn_s_waitcnt(0xc07f | ((N > 14 ? 14 : N) << 8)); }
template <class T> __device__ __forceinline__ void tie(T& x) { asm volatile("" : "+v"(x)); }
template <class T> __device__ __forceinline__ void undef(T& x) { asm volatile("" : "=v"(x)); }
template <int OFF, class T> __device__ __forceinline__ void dsr128u(T& d, unsigned a) { asm volatile("ds_read_b128 %0, %1 offset:%2" : "+v"(d) : "v"(a), "n"(OFF)); }
template <class T> __device__ __forceinline__ void keep(const T& x) { asm volatile("" :: "v"(x)); }
template <int OFF, class T> __device__ __forceinline__ void dsr128(T& d, unsigned a) { asm volatile("ds_read_b128 %0, %1 offset:%2" : "=v"(d) : "v"(a), "n"(OFF)); }
template <int OFF, class T> __device__ __forceinline__ void dsr64(T& d, unsigned a) { asm volatile("ds_read_b64 %0, %1 offset:%2" : "=v"(d) : "v"(a), "n"(OFF)); }
template <int OFF> __device__ __forceinline__ void dsr32(unsigned& d, unsigned a) { asm volatile("ds_read_b32 %0, %1 offset:%2" : "=v"(d) : "v"(a), "n"(OFF)); }
template <int O0, int O1, class T> __device__ __forceinline__ void dsr2(T& d, unsigned a) { asm volatile("ds_read2_b32 %0, %1 offset0:%2 offset1:%3" : "=v"(d) : "v"(a), "n"(O0), "n"(O1)); }
template <int OFF> __device__ __forceinline__ void dsr64tr(bf16x4& d, unsigned a) { asm volatile("ds_read_b64_tr_b16 %0, %1 offset:%2" : "=v"(d) : "v"(a), "n"(OFF)); }
template <int OFF, class T> __device__ __forceinline__ void dsw128(unsigned a, const T& v) { asm volatile("ds_write_b128 %0, %1 offset:%2" :: "v"(a), "v"(v), "n"(OFF) : "memory"); }
template <int OFF> __device__ __forceinline__ void dsw32(unsigned a, unsigned v) { asm volatile("ds_write_b32 %0, %1 offset:%2" :: "v"(a), "v"(v), "n"(OFF) : "memory"); }
__device__ __forceinline__ bf16x8 cat(const bf16x4& lo, const bf16x4& hi) { return __builtin_shufflevector(lo, hi, 0, 1, 2, 3, 4, 5, 6, 7); }
__device__ __forceinline__ void barrier() { asm volatile("" ::: "memory"); __builtin_amdgcn_s_barrier(); asm volatile("" ::: "memory"); }
__device__ __forceinline__ int fresh(int v) { asm volatile("" : "+s"(v)); return v; }
__device__ __forceinline__ void sched() { __builtin_amdgcn_sched_barrier(0); }
template <bool FIRST> __device__ __forceinline__ void mfma(f32x4& c, const bf16x8& a, const bf16x8& b) {
    if constexpr (FIRST) asm volatile("v_mfma_f32_16x16x32_bf16 %0, %1, %2, 0" : "=v"(c) : "v"(a), "v"(b));
    else asm volatile("v_mfma_f32_16x16x32_bf16 %0, %1, %2, %0" : "+v"(c) : "v"(a), "v"(b));
}
struct cf32 { float r, i; };
__device__ __forceinline__ cf32 cmul(cf32 a, cf32 b) { return cf32{fma_(a.r, b.r, -(a.i * b.i)), fma_(a.r, b.i, a.i * b.r)}; }
__device__ __forceinline__ cf32 cfma(cf32 a, cf32 b, cf32 c) { return cf32{fma_(a.r, b.r, fma_(-a.i, b.i, c.r)), fma_(a.r, b.i, fma_(a.i, b.r, c.i))}; }
__device__ __forceinline__ void sw16(float v, float& e, float& o) { const auto r = __builtin_amdgcn_permlane16_swap(f2u(v), f2u(v), false, false); e = u2f(r[0]); o = u2f(r[1]); }
__device__ __forceinline__ void sw32(float v, float& e, float& o) { const auto r = __builtin_amdgcn_permlane32_swap(f2u(v), f2u(v), false, false); e = u2f(r[0]); o = u2f(r[1]); }
constexpr unsigned SPIN_CAP = 1u << 16;
constexpr unsigned OOB = 0x40000000u;

constexpr size_t WS_H = 0;
constexpr size_t WS_KVT = (size_t)8 << 20;
constexpr size_t WS_ZT = (size_t)12 << 20;
constexpr size_t WS_PP = (size_t)16 << 20;
constexpr size_t WS_E = (size_t)24 << 20;
constexpr size_t WS_F = WS_E + (256 << 10);
constexpr int F_H = 0;
constexpr int F_K = 4096;
constexpr int F_Z = 16384;
constexpr int F_EP = F_Z + 32768;
constexpr int F_C = F_EP + 256;
constexpr unsigned WS_P = 25 << 20;
#define KP 0
#define SU 0
#define TSX(i) do { if constexpr (!SU) TS(i); } while (0)
#define AB 1
#define SW 1
#define BL 0
#define GS 0
#define LC 0
#define KCQ 2
#define KCC 4
#define KVP 0
#define RSL 1
#define PB 0
#define KTI 1
#define TM 0
#define TX 0
#define PK 1
#define HA 0
#define KVS 1
#define GCR 273
constexpr int LDS_TOTAL = 163840, BUF = 32768;
constexpr int KC = 98304, KCH = 8320, KCP = 4160;
constexpr int U0 = KC + 4 * KCH;
constexpr int FULL = U0 + 18432 + 8192, FST = 388;
constexpr int DONE = U0 + 18432, DUMP = DONE + 4096;
constexpr int HGO = FULL + 1792, DSK = HGO + 256, POLL = DSK + 256;
static_assert(POLL + 8 * 256 <= LDS_TOTAL && 4 * FST <= 1792, "lds");
constexpr int g1_buf(int s) { return ((0x0213 >> ((s & 3) * 4)) & 3) * BUF; }
constexpr int IMG = KC, QST = KC + 16384, ZST = U0, PLD = 2 * BUF;
#define CODE_KB 43
#define EXACT 0
#define HP 1
#define CP 2
#define FLQ 11
#define R01 3
#define ZP 7
#define SLP 8
#define STR_(x) #x
#define STR(x) STR_(x)
#define NOST 0
#define G2M 0
#define RD 2
#define PRG 1
#define OSC 16
#define NOA 0
#define PPH 0
#define HS 0
#define JQ(j) (HS == 0 ? 0 : HS == 1 ? (j) & 1 : HS == 2 ? (j) : (j) & 2)
#define JR(j) (HS == 0 ? 2 * (j) : HS == 1 ? 2 * ((j) & 2) : HS == 2 ? 0 : 4 * ((j) & 1))
#define NOX 0

__device__ __forceinline__ u32x4 mk_rsrc(const void* p, unsigned bytes) {
    const unsigned long long a = (unsigned long long)p;
    return u32x4{(unsigned)a, (unsigned)(a >> 32) & 0xffffu, bytes, 0x00020000u};
}
__device__ __forceinline__ __amdgpu_buffer_rsrc_t mk_brs(const void* p, unsigned bytes) { return __builtin_amdgcn_make_buffer_rsrc((void*)p, (short)0, bytes, 0x00020000); }
__device__ __forceinline__ void st16(const u32x4& d, __amdgpu_buffer_rsrc_t r, unsigned voff, unsigned soff) {
    __builtin_amdgcn_raw_buffer_store_b128(d, r, voff, soff, 16);
    asm volatile("s_nop 1" :: "v"(d) : "memory");
}
template <int IMM, bool SC1> __device__ __forceinline__ void dma(unsigned lds, unsigned voff, const u32x4& rs, unsigned soff) {
    if constexpr (SC1) asm volatile("s_add_u32 m0, %0, %1\n\ts_nop 0\n\tbuffer_load_dwordx4 %2, %3, %4 offen sc1 lds" :: "s"(lds), "n"(IMM), "v"(voff), "s"(rs), "s"(soff) : "memory", "scc");
    else asm volatile("s_add_u32 m0, %0, %1\n\ts_nop 0\n\tbuffer_load_dwordx4 %2, %3, %4 offen lds" :: "s"(lds), "n"(IMM), "v"(voff), "s"(rs), "s"(soff) : "memory", "scc");
}
template <int IMM, int I> __device__ __forceinline__ void dma4(unsigned lds, unsigned voff, const u32x4& rs, unsigned soff) {
    if constexpr (I == 0) dma<IMM, false>(lds, voff, rs, soff);
    else asm volatile("buffer_load_dwordx4 %0, %1, %2 offen offset:%3 lds" :: "v"(voff), "s"(rs), "s"(soff), "n"(I * 1024) : "memory");
}
template <int IMM> __device__ __forceinline__ void dma1(unsigned lds, unsigned voff, const u32x4& rs, unsigned soff) {
    asm volatile("s_add_u32 m0, %0, %1\n\ts_nop 0\n\tbuffer_load_dword %2, %3, %4 offen sc1 lds" :: "s"(lds), "n"(IMM), "v"(voff), "s"(rs), "s"(soff) : "memory", "scc");
}
__device__ __forceinline__ void flag_set(unsigned waddr, unsigned v) { asm volatile("ds_write_b32 %0, %1" :: "v"(waddr), "v"(v) : "memory"); }
__device__ __forceinline__ void flag_chk(unsigned& f, unsigned raddr, unsigned need) {
    unsigned cnt;
    asm volatile("v_cmp_le_u32 vcc, %3, %0\n\t"
                 "s_cmp_eq_u64 vcc, -1\n\t"
                 "s_cbranch_scc1 1f\n\t"
                 "s_mov_b32 %1, 0x100000\n"
                 "0:\n\t"
                 "ds_read_b32 %0, %2\n\t"
                 "s_sub_u32 %1, %1, 1\n\t"
                 "s_waitcnt lgkmcnt(0)\n\t"
                 "s_cbranch_scc1 1f\n\t"
                 "v_cmp_le_u32 vcc, %3, %0\n\t"
                 "s_cmp_eq_u64 vcc, -1\n\t"
                 "s_cbranch_scc0 0b\n"
                 "1:" : "+v"(f), "=&s"(cnt) : "v"(raddr), "s"(need) : "vcc", "scc", "memory");
}
template <int OFF, int SL = 0> __device__ __forceinline__ void zero_chk(unsigned& f, unsigned raddr) {
    unsigned cnt;
    asm volatile("v_cmp_ne_u32 vcc, 0, %0\n\t"
                 "s_cbranch_vccz 1f\n\t"
                 "s_mov_b32 %1, 0x100000\n"
                 "0:\n\t"
                 ".if %4\n\ts_sleep %4\n\t.endif\n\t"
                 "ds_read_b32 %0, %2 offset:%3\n\t"
                 "s_sub_u32 %1, %1, 1\n\t"
                 "s_waitcnt lgkmcnt(0)\n\t"
                 "s_cbranch_scc1 1f\n\t"
                 "v_cmp_ne_u32 vcc, 0, %0\n\t"
                 "s_cbranch_vccnz 0b\n"
                 "1:" : "+v"(f), "=&s"(cnt) : "v"(raddr), "n"(OFF), "n"(SL) : "vcc", "scc", "memory");
}
template <int IMM, class T> __device__ __forceinline__ void gld(T& d, unsigned voff, const u32x4& rs, unsigned soff) {
    if constexpr (sizeof(T) == 16) asm volatile("buffer_load_dwordx4 %0, %1, %2, %3 offen offset:%4" : "=v"(d) : "v"(voff), "s"(rs), "s"(soff), "n"(IMM) : "memory");
    else if constexpr (sizeof(T) == 8) asm volatile("buffer_load_dwordx2 %0, %1, %2, %3 offen offset:%4" : "=v"(d) : "v"(voff), "s"(rs), "s"(soff), "n"(IMM) : "memory");
    else asm volatile("buffer_load_ushort %0, %1, %2, %3 offen offset:%4" : "=v"(d) : "v"(voff), "s"(rs), "s"(soff), "n"(IMM) : "memory");
}
__device__ __forceinline__ void gld32(unsigned& d, unsigned voff, const u32x4& rs, unsigned soff) { asm volatile("buffer_load_dword %0, %1, %2, %3 offen" : "=v"(d) : "v"(voff), "s"(rs), "s"(soff) : "memory"); }
__device__ __forceinline__ void sld(unsigned& d, const void* p) { asm volatile("s_load_dword %0, %1, 0x0" : "=s"(d) : "s"(p) : "memory"); }
__device__ __forceinline__ void stouch(unsigned long long p) { asm volatile("s_load_dword s101, %0, 0x0\n\ts_load_dword s101, %0, 0x80\n\ts_load_dword s101, %0, 0x100" :: "s"(p) : "s101", "memory"); }
template <class T> __device__ __forceinline__ void ties(T& x) { asm volatile("" : "+s"(x)); }
template <int IMM, class T> __device__ __forceinline__ void gst(const T& d, unsigned voff, const u32x4& rs, unsigned soff) {
    if constexpr (sizeof(T) == 16) asm volatile("buffer_store_dwordx4 %0, %1, %2, %3 offen offset:%4 sc1" :: "v"(d), "v"(voff), "s"(rs), "s"(soff), "n"(IMM) : "memory");
    else asm volatile("buffer_store_dwordx2 %0, %1, %2, %3 offen offset:%4 sc1" :: "v"(d), "v"(voff), "s"(rs), "s"(soff), "n"(IMM) : "memory");
}
template <int SLQ> __device__ __forceinline__ void gate(unsigned raddr, unsigned need) {
    unsigned cnt, f;
    asm volatile("s_mov_b32 %1, 0x40000\n"
                 "0:\n\t"
                 "ds_read_b32 %0, %2\n\t"
                 "s_sub_u32 %1, %1, 1\n\t"
                 "s_waitcnt lgkmcnt(0)\n\t"
                 "s_cbranch_scc1 1f\n\t"
                 "v_cmp_le_u32 vcc, %3, %0\n\t"
                 "s_cmp_eq_u64 vcc, -1\n\t"
                 "s_cbranch_scc1 1f\n\t"
                 "s_sleep %4\n\t"
                 "s_branch 0b\n"
                 "1:" : "=&v"(f), "=&s"(cnt) : "v"(raddr), "s"(need), "n"(SLQ) : "vcc", "scc", "memory");
}
template <int SL = SLP, int AT_ = 0> __device__ __forceinline__ void tag_chk(unsigned& f, unsigned expect, unsigned voff, const u32x4& rs, unsigned soff) {
    unsigned cnt;
    asm volatile("v_cmp_eq_u32 vcc, %2, %0\n\t"
                 "s_cmp_eq_u64 vcc, -1\n\t"
                 "s_cbranch_scc1 1f\n\t"
                 "s_mov_b32 %1, 0x10000\n"
                 "0:\n\t"
                 ".if %7\n\tv_mov_b32 %0, 0\n\t.endif\n\t"
                 "s_sleep %6\n\t"
                 ".if %7\n\tbuffer_atomic_or %0, %3, %4, %5 offen sc0 sc1\n\t.else\n\tbuffer_load_dword %0, %3, %4, %5 offen sc1\n\t.endif\n\t"
                 "s_sub_u32 %1, %1, 1\n\t"
                 "s_waitcnt vmcnt(0)\n\t"
                 "s_cbranch_scc1 1f\n\t"
                 "v_cmp_eq_u32 vcc, %2, %0\n\t"
                 "s_cmp_eq_u64 vcc, -1\n\t"
                 "s_cbranch_scc0 0b\n"
                 "1:" : "+v"(f), "=&s"(cnt) : "v"(expect), "v"(voff), "s"(rs), "s"(soff), "n"(SL), "n"(AT_) : "vcc", "scc", "memory");
}

__device__ __forceinline__ unsigned race(unsigned w, const void* ws, unsigned long long rnd) {
    unsigned v;
    asm volatile("s_cmp_lt_u32 %1, 32\n\t"
                 "s_cbranch_scc0 1f\n\t"
                 "s_waitcnt lgkmcnt(0)\n\t"
                 "s_mov_b64 s[98:99], %3\n\t"
                 "s_lshl_b32 s96, s98, 7\n\t"
                 "s_lshl_b32 s97, %1, 13\n\t"
                 "s_andn2_b32 s96, s96, 0x2000\n\t"
                 "s_xor_b32 s96, s96, s97\n\t"
                 "s_and_b32 s96, s96, 0x7ff80\n\t"
                 "s_add_u32 s96, s96, %4\n\t"
                 "s_xor_b32 s97, s96, 0x2000\n\t"
                 "s_mov_b32 %0, s98\n\t"
                 "s_load_dword s99, %2, s96\n\t"
                 "s_load_dword s98, %2, s97\n\t"
                 "s_lshr_b32 s96, %1, 4\n\t"
                 "s_xor_b32 s96, s96, %1\n\t"
                 "s_waitcnt lgkmcnt(1)\n\t"
                 "s_sleep %5\n\t"
                 "s_cmp_lg_u32 %0, s98\n\t"
                 "s_addc_u32 %0, s96, 0\n\t"
                 "s_and_b32 %0, %0, 1\n"
                 "1:" : "=&s"(v) : "s"(w), "s"(ws), "s"(rnd), "n"(WS_P), "n"(RSL) : "s96", "s97", "s98", "s99", "scc", "memory");
    return v;
}
__device__ __forceinline__ void vote_chk(unsigned& f, unsigned expect, unsigned odd, unsigned raddr, unsigned lds, unsigned voff, const u32x4& rs, unsigned soff, unsigned& cnt) {
    unsigned t;
    asm volatile("s_mov_b32 %1, 0x10000\n\t"
                 "v_xor_b32 %2, %0, %3\n\t"
                 "v_cmp_gt_u32 vcc, %2, %4\n\t"
                 "s_cbranch_vccz 1f\n\t"
                 "s_mov_b32 m0, %6\n"
                 "0:\n\t"
                 "s_sleep 4\n\t"
                 "buffer_load_dword %7, %8, %9 offen sc1 lds\n\t"
                 "s_sub_u32 %1, %1, 1\n\t"
                 "s_waitcnt vmcnt(0)\n\t"
                 "ds_read_b32 %0, %5\n\t"
                 "s_waitcnt lgkmcnt(0)\n\t"
                 "s_cbranch_scc1 1f\n\t"
                 "v_xor_b32 %2, %0, %3\n\t"
                 "v_cmp_gt_u32 vcc, %2, %4\n\t"
                 "s_cbranch_vccnz 0b\n"
                 "1:" : "+v"(f), "=&s"(cnt), "=&v"(t) : "v"(expect), "v"(odd), "v"(raddr), "s"(lds), "v"(voff), "s"(rs), "s"(soff) : "vcc", "scc", "memory");
}
__device__ __forceinline__ unsigned vote_cnt(unsigned f, unsigned tag, unsigned up, unsigned& n) {
    unsigned r;
    asm volatile("s_xnor_b32 %0, %3, 1\n\tv_cmp_eq_u32 vcc, %0, %2\n\ts_bcnt1_i32_b64 %1, vcc\n\ts_cmp_gt_u32 %1, 16\n\ts_cselect_b32 %0, 0x2000, 0\n\ts_xor_b32 %0, %0, %4"
                 : "=&s"(r), "=&s"(n) : "v"(f), "s"(tag), "s"(up) : "vcc", "scc");
    return r;
}

template <int H> __device__ __forceinline__ void pp_ld(u32x4& d, unsigned voff, const u32x4& rs, unsigned soff) {
    if constexpr (H) asm volatile("buffer_load_dwordx4 v[244:247], %1, %2, %3 offen offset:1024 sc1" : "={v[244:247]}"(d) : "v"(voff), "s"(rs), "s"(soff) : "memory");
    else asm volatile("buffer_load_dwordx4 v[240:243], %1, %2, %3 offen sc1" : "={v[240:243]}"(d) : "v"(voff), "s"(rs), "s"(soff) : "memory");
}
__device__ __forceinline__ void pf_ld(unsigned& d, unsigned voff, const u32x4& rs, unsigned soff) { asm volatile("buffer_load_dword v248, %1, %2, %3 offen sc1" : "={v248}"(d) : "v"(voff), "s"(rs), "s"(soff) : "memory"); }
__device__ __forceinline__ void pf_keep(unsigned d) { asm volatile("" :: "{v248}"(d)); }
__device__ __forceinline__ void pf_zero(unsigned& d) { asm volatile("v_mov_b32 v248, 0" : "={v248}"(d)); }
template <int OFF> __device__ __forceinline__ void pf_at(unsigned& d, unsigned voff, const u32x4& rs, unsigned soff) {
    asm volatile("buffer_atomic_or v248, %1, %2, %3 offen offset:%4 sc0 sc1" : "+{v248}"(d) : "v"(voff), "s"(rs), "s"(soff), "n"(OFF) : "memory");
}
__device__ __forceinline__ void pp_sel(unsigned& so, unsigned& fail, unsigned d, unsigned tn, unsigned yes, unsigned no) {
    asm volatile("v_readlane_b32 %1, v248, 63\n\ts_and_b32 %1, %1, 0xf000f\n\ts_cmp_eq_u32 %1, %3\n\ts_cselect_b32 %0, %4, %5\n\ts_cselect_b32 %1, 0, -1"
                 : "=&s"(so), "=&s"(fail) : "{v248}"(d), "s"(tn), "s"(yes), "s"(no) : "scc");
}
#define PP_CHK(R, R1, R2, R3, OFF) \
    asm volatile("v_cmp_ne_u32 vcc, 0, %2\n\t" \
                 "s_cbranch_vccz 1f\n\t" \
                 "s_mov_b32 %1, 0x10000\n" \
                 "0:\n\t" \
                 ".if %9\n\t" \
                 "v_mov_b32 %2, 0\n\t" \
                 "s_sub_u32 %1, %1, 1\n\t" \
                 "s_cbranch_scc1 1f\n\t" \
                 "buffer_atomic_or %2, %8, %6, %7 offen offset:" OFF " sc0 sc1\n\t" \
                 "s_lshr_b32 vcc_lo, %4, 4\n\t" \
                 "s_waitcnt vmcnt(0)\n\t" \
                 "v_readlane_b32 %3, %2, 63\n\t" \
                 "s_xor_b32 %3, %3, vcc_lo\n\t" \
                 "s_and_b32 %3, %3, 0xf000f\n\t" \
                 "s_cbranch_scc1 0b\n\t" \
                 ".endif\n\t" \
                 "buffer_load_dwordx4 " R ", %5, %6, %7 offen offset:" OFF " sc1\n\t" \
                 "s_waitcnt vmcnt(0)\n\t" \
                 "s_and_b32 %3, %4, 0xf000f\n\t" \
                 "v_xor_b32 " R1 ", %3, " R1 "\n\t" \
                 "s_lshr_b32 %3, %4, 8\n\t" \
                 "s_and_b32 %3, %3, 0xf000f\n\t" \
                 "v_xor_b32 " R2 ", %3, " R2 "\n\t" \
                 "s_lshr_b32 %3, %4, 4\n\t" \
                 "s_and_b32 %3, %3, 0xf000f\n\t" \
                 "v_xor_b32 " R3 ", %3, " R3 "\n\t" \
                 "v_or3_b32 %2, " R1 ", " R2 ", " R3 "\n\t" \
                 "v_and_b32 %2, 0xf000f, %2\n\t" \
                 "s_sub_u32 %1, %1, 1\n\t" \
                 "s_cbranch_scc1 1f\n\t" \
                 "v_cmp_ne_u32 vcc, 0, %2\n\t" \
                 "s_cbranch_vccnz 0b\n" \
                 "1:" : "+{" R "}"(p), "=&s"(cnt), "+v"(bad), "=&s"(t) : "s"(tag), "v"(voff), "s"(rs), "s"(soff), "v"(vp), "n"(PB) : "vcc", "scc", "memory")
template <int H> __device__ __forceinline__ void pp_chk(u32x4& p, unsigned bad, unsigned tag, unsigned voff, const u32x4& rs, unsigned soff, unsigned vp) {
    unsigned cnt, t;
    if constexpr (H) PP_CHK("v[244:247]", "v245", "v246", "v247", "1024"); else PP_CHK("v[240:243]", "v241", "v242", "v243", "0");
}
template <int H> __device__ __forceinline__ void pp_tie(u32x4& p) { if constexpr (H) asm volatile("" : "+{v[244:247]}"(p)); else asm volatile("" : "+{v[240:243]}"(p)); }
__device__ __forceinline__ float amax3(float a, float b, float c) { float r; asm volatile("v_max3_f32 %0, |%1|, |%2|, |%3|" : "=v"(r) : "v"(a), "v"(b), "v"(c)); return r; }
__device__ __forceinline__ unsigned bfi_v(unsigned m, unsigned a, unsigned b) { unsigned r; asm volatile("v_bfi_b32 %0, %1, %2, %3" : "=v"(r) : "v"(m), "v"(a), "v"(b)); return r; }
__device__ __forceinline__ unsigned bfi_s(unsigned m, unsigned a, unsigned b) { unsigned r; asm volatile("v_bfi_b32 %0, %1, %2, %3" : "=v"(r) : "v"(m), "v"(a), "s"(b)); return r; }
__device__ __forceinline__ unsigned bfi_f(unsigned m, unsigned a, unsigned b) { unsigned r; asm volatile("v_bfi_b32 %0, %1, %2, %3" : "=v"(r) : "v"(m), "s"(a), "v"(b)); return r; }
template <int H> __device__ __forceinline__ float cvt16(unsigned w) {
    float r;
    if constexpr (H) asm volatile("v_cvt_f32_i32_sdwa %0, sext(%1) dst_sel:DWORD dst_unused:UNUSED_PAD src0_sel:WORD_1" : "=v"(r) : "v"(w));
    else asm volatile("v_cvt_f32_i32_sdwa %0, sext(%1) dst_sel:DWORD dst_unused:UNUSED_PAD src0_sel:WORD_0" : "=v"(r) : "v"(w));
    return r;
}
__device__ __forceinline__ float sel8(float a, float b, unsigned long long mask) {
    float r;
    asm volatile("s_mov_b64 vcc, %3\n\tv_cndmask_b32_dpp %0, %1, %2, vcc row_ror:8 row_mask:0xf bank_mask:0xf" : "=v"(r) : "v"(a), "v"(b), "s"(mask) : "vcc");
    return r;
}
__device__ __forceinline__ unsigned sel0(unsigned a, unsigned long long mask) { unsigned r; asm volatile("v_cndmask_b32_e64 %0, 0, %1, %2" : "=v"(r) : "v"(a), "s"(mask)); return r; }
constexpr unsigned long long diag_mask(int R) {
    unsigned long long m = 0;
    for (int l = 0; l < 64; ++l) if ((l >> 4) == ((l & 7) >> 1) && 2 * (l & 1) + ((l >> 3) & 1) == R) m |= 1ull << l;
    return m;
}
template <int IMM, int X, bool SC1> __device__ __forceinline__ void dma_x(unsigned& so, unsigned lds, unsigned voff, const u32x4& rs, unsigned base) {
    if constexpr (SC1) asm volatile("s_add_u32 m0, %1, %2\n\ts_xor_b32 %0, %5, %6\n\tbuffer_load_dwordx4 %3, %4, %0 offen sc1 lds" : "=&s"(so) : "s"(lds), "n"(IMM), "v"(voff), "s"(rs), "s"(base), "n"(X) : "memory", "scc");
    else asm volatile("s_add_u32 m0, %1, %2\n\ts_xor_b32 %0, %5, %6\n\tbuffer_load_dwordx4 %3, %4, %0 offen lds" : "=&s"(so) : "s"(lds), "n"(IMM), "v"(voff), "s"(rs), "s"(base), "n"(X) : "memory", "scc");
}
template <int OFF> __device__ __forceinline__ void dma_n(unsigned so, unsigned voff, const u32x4& rs) {
    asm volatile("buffer_load_dwordx4 %0, %1, %2 offen offset:%3 sc1 lds" :: "v"(voff), "s"(rs), "s"(so), "n"(OFF) : "memory");
}
template <int IMM, int OFF> __device__ __forceinline__ void dma_m(unsigned so, unsigned lds, unsigned voff, const u32x4& rs) {
    asm volatile("s_add_u32 m0, %4, %5\n\ts_nop 0\n\tbuffer_load_dwordx4 %0, %1, %2 offen offset:%3 sc1 lds" :: "v"(voff), "s"(rs), "s"(so), "n"(OFF), "s"(lds), "n"(IMM) : "memory", "scc");
}
__device__ __forceinline__ unsigned and_or(unsigned a, unsigned m, unsigned o) { unsigned r; asm("v_and_or_b32 %0, %1, %2, %3" : "=v"(r) : "v"(a), "v"(m), "s"(o)); return r; }

#define HOPS 1
#define HM 0x1ff221125B082ull
#define PG0 63
#define PG7 287
#define PG1 95
#define PG2 288
#define PG3 4
#define PG4 21
#define SQ 52
#define P18 -1
#define WS0 0
#define SLM 0x12234566ull
#define PGF 5
#define HQ 84
#define HW 4
constexpr int hop_nth(int n) { for (int k = 0; k < 62; ++k) if (((HM >> k) & 1) && n-- == 0) return k; return 63; }
constexpr int hop_next(int K) { int n = 0; for (int k = 0; k < K; ++k) n += (HM >> k) & 1; return hop_nth(n + HW); }
template <int K> __device__ __forceinline__ void hop() {
    if constexpr (HOPS && ((HM >> K) & 1)) asm volatile(".Lfkh%0:\n\ts_cbranch_execz .Lfkh%1" :: "n"(K), "n"(hop_next(K)));
}
template <int K> __device__ __forceinline__ void page() {
    if constexpr (HOPS && K >= 0 && ((HM >> (K & 63)) & 1)) asm volatile("s_branch 1f\n\t.p2align 12\n.Lfkh%0:\n\ts_cbranch_execz .Lfkh%1\n1:" :: "n"(K), "n"(hop_next(K)));
    else if constexpr (HOPS) asm volatile("s_branch 1f\n\t.p2align 12\n1:");
}
__device__ __forceinline__ void hop_all(int w) {
    if constexpr (HOPS) asm volatile("s_mov_b64 exec, 0\n\ts_cmp_eq_u32 %0, 0\n\ts_cbranch_scc1 .Lfkh%1\n\ts_cmp_eq_u32 %0, 1\n\ts_cbranch_scc1 .Lfkh%2\n\t"
                                     "s_cmp_eq_u32 %0, 2\n\ts_cbranch_scc1 .Lfkh%3\n\ts_branch .Lfkh%4\n.Lfkh63:\n\ts_mov_b64 exec, -1"
                                     :: "s"(w), "n"(hop_nth(0)), "n"(hop_nth(1)), "n"(hop_nth(2)), "n"(hop_nth(3)) : "scc");
}
#define NB 1
#define PFQ 0
#define CVR 3
#define SPR 3
#define ELP 8
#define PW 1
#define CPG 95
#define GQ -1
#define GE 3
#define KDL 0
#define GP 1
#define SG 507
#define KO 0
#define DP 0
#define B13 460
#define PRE 0
enum { OP_NONE, OP_A, OP_B, OP_FLAG, OP_SIG, OP_VOTE };
struct Op { int kind, a, b; };
constexpr Op g1_op(int G) {
    const int st = (G + 32) / 32 - 1, q = G - 32 * st, i = q >> 2, j = q & 3;
    if (i < 5) {
        if (st < 0) return {OP_NONE, 0, 0};
        if ((j & 1) == 0) return {OP_A, st * 8 + i + 3, j >> 1};
        if (q == FLQ && st < 15) return {OP_FLAG, 0, 0};
        if (NB && G == SG) return {OP_SIG, 2, 0};
        if (G == GCR) return {OP_VOTE, 0, 0};
        if (NB == 2 && G == 435) return {OP_SIG, 3, 0};
        return {OP_NONE, 0, 0};
    }
    if (st == 15) return NB && G == SG ? Op{OP_SIG, 2, 0} : Op{OP_NONE, 0, 0};
    const int n = (st + 1) * 8;
    switch (q) {
        case 20: return {OP_B, st + 1, 0}; case 21: return {OP_B, st + 1, 1}; case 22: return {OP_A, n, 0}; case 23: return {OP_B, st + 1, 2};
        case 24: return {OP_B, st + 1, 3}; case 25: return {OP_A, n, 1}; case 26: return {OP_A, n + 1, 0}; case 27: return {OP_A, n + 1, 1};
        case 28: return {OP_A, n + 2, 0}; case 30: return {OP_A, n + 2, 1};
    }
    return {OP_NONE, 0, 0};
}
constexpr int g1_need(int G) {
    const int st = G >> 5, j = G & 3;
    for (int h = G - 1; h >= -12; --h) {
        const Op o = g1_op(h);
        if (o.kind == OP_A && o.a == (G >> 2) && o.b == (j >> 1)) return h;
        if (o.kind == OP_B && o.a == st && o.b == j) return h;
        if (o.kind == OP_FLAG && (G & 31) == 20 && (h >> 5) == st) return h;
    }
    return -100;
}
constexpr int g1_wait(int G) {
    int n = -100, c = 0;
    for (int h = G; h < (G ? G + 4 : 2) && h < 512; ++h) if (g1_need(h) > n) n = g1_need(h);
    for (int h = n + 1; h < G; ++h) c += g1_op(h).kind != OP_NONE;
    return n < G ? c : -1;
}
#define KV0 3
#define KVN 0x2222
constexpr int kv_first(int k) { int n = 0; for (int i = KV0; i < k && i < KV0 + 8; ++i) n += (KVN >> (4 * (i - KV0))) & 15; return n > 8 ? 8 : n; }
constexpr int kv_last() { int k = KV0; while (kv_first(k + 1) < 8) ++k; return k; }
#define CQ -1
#define CTM 0
#define EC 2
#define ED 2
#define EB 0
#define KST 0
#define KFD 3
#define KPL 11
#define KPI 0
#define KSL SLP
#define CBQ 20
constexpr int KVL = kv_last(), KFL = KVL + KFD, KSD = KVS ? KCC : -1;
static_assert(KFL <= KPL && KPL <= 12 && KPL > KVL && (KPI & 3) == 0 && KTI != KCC && KTI != KFL && KTI < KPL && KVP >= 0 && KVP < KCQ && KCQ < KCC && KSD <= KVL && KCC <= KPL && GCR > 32 * KCC + 62, "");
#define ROT 0x814
#define KLP 13
#define KL0 8
#define KLS 4
constexpr int ld_pos(int j) { return j < KLP ? -100 : KL0 + (j - KLP) * KLS / 4; }
constexpr int ld_cnt(int k) { int c = 0; for (int j = KLP; j < 21; ++j) c += (ld_pos(j) + 8) / 8 - 1 == k; return c; }
constexpr int KLL = (ld_pos(20) + 8) / 8 - 1, KLI = ld_pos(20) - 8 * KLL;
static_assert(KLP == 21 || KLL < KV0, "");
constexpr int hv_cnt(int k0, int k1) {
    int c = 0;
    for (int k = k0; k < k1; ++k) c += 9 + (KST ? 4 * (k == KVL) : k < KSD ? 0 : kv_first(k + 1) / 2 - (k == KSD ? 0 : kv_first(k) / 2)) + (k == KFL) + (k == KPL) + (k == KCQ) + (k == KVP) + (TM && TM < 16 && k == KTI) + ld_cnt(k);
    return c;
}
#define ZM 1
#define CL 1
#define EP 0
constexpr unsigned long long lag0_mask(int w) {
    unsigned long long m = 0;
    for (int l = 0; l < 64; ++l) if ((l >> 4) == 2 + ((l >> 3) & 1) && 3 - ((l & 7) >> 1) == w) m |= 1ull << l;
    return m;
}
constexpr int cv_start(int n) { return n * 8 - n * (n - 1) / 2; }
constexpr int cv_n(int q) { int n = 0; while (cv_start(n + 1) <= q) ++n; return n; }
constexpr int cv_m(int q) { return 7 - cv_n(q) - (q - cv_start(cv_n(q))); }
enum { CO_NONE, CO_A, CO_B, CO_C };
constexpr Op cv_op(int G) {
    const int s = (G + 36) / 36 - 1, q = G - 36 * s;
    if (q >= 1 && q <= 6) return s >= 0 ? Op{CO_A, s * 8 + 5 + (q - 1) / 2, (q - 1) & 1} : Op{CO_NONE, 0, 0};
    if (q >= 11 && q <= 14) return s >= 0 && s >= CL && s - CL < 8 ? Op{CO_C, s - CL, q - 11} : Op{CO_NONE, 0, 0};
    if (s == 7) return {CO_NONE, 0, 0};
    for (int m = 0; m < 8; ++m) if (q == cv_start(7 - m)) return {CO_B, (s + 1) * 8 + m, 0};
    for (int n = 0; n < 5; ++n) if (q == cv_start(n + 1) + 1 || q == cv_start(n + 1) + 2) return {CO_A, (s + 1) * 8 + n, q - cv_start(n + 1) - 1};
    return {CO_NONE, 0, 0};
}
constexpr int cv_need(int G) {
    const int s = G / 36, q = G % 36;
    for (int h = G - 1; h >= -36; --h) {
        const Op o = cv_op(h);
        if (o.kind == CO_A && o.a == s * 8 + cv_n(q)) return h;
        if (o.kind == CO_B && o.a == s * 8 + cv_m(q)) return h;
        if (o.kind == CO_C && q == 24 && o.a == s - CL) return h;
    }
    return -100;
}
constexpr int cv_wait(int G) {
    const int n = cv_need(G);
    for (int h = G - 1; h >= 0 && h >= G - 40; --h) if (cv_need(h) >= n) return -1;
    int c = 0;
    for (int h = n + 1; h < G; ++h) c += cv_op(h).kind != CO_NONE;
    return c;
}
#define AT(n, m) ((n) * 8 + (m))
#define FM 0x326260
constexpr int fill_pos(int s, int i) {
    const int f = (FM >> (4 * (s - 2))) & 15, n = s == 2 ? 0 : 4 * s - 9;
    switch (f) {
        case 1: return AT(n + i, 5) - (s == 2 && i == 3) * 6;
        case 2: return AT(n + 1 + i / 2, 5 + 2 * (i & 1));
        case 3: return i < 2 ? AT(n, 5 + 2 * i) : AT(n + i - 1, 5);
        case 4: return AT(n + 2 + i / 2, 5 + 2 * (i & 1));
        case 5: return AT(n, 1 + 2 * i) + (s == 2 ? 4 : 0);
        case 6: return AT(n + 3 + i / 2, 5 + 2 * (i & 1));
        case 7: return AT(n + 3, 5 + 2 * i);
        case 8: return i < 2 ? AT(n + 1, 5 + 2 * i) : AT(n + i, 5);
        case 9: return AT(n + 1 + i, 1);
        case 10: return i < 2 ? AT(n, 1 + 2 * i) : AT(n + 3, 1 + 2 * i);
    }
    return AT(n + i / 2, 5 + 2 * (i & 1));
}
constexpr int g2_fill(int p) {
    for (int k = 8; k < 32; ++k) if (fill_pos(k >> 2, k & 3) == p) return k;
    return -1;
}
#define QX AT(2, 5)
#define QQ AT(3, 2)
#define QZ AT(11, 2)
#define QT AT(3, 4)
constexpr int P_X = QX, P_Q = QQ, P_Z = QZ, P_T = QT;
constexpr bool XL = false;
constexpr bool fill_m0(int k) {
    const int a = fill_pos(k >> 2, (k & 3) - 1), b = fill_pos(k >> 2, k & 3);
    return (P_Q + 2 >= a && P_Q < b) || (P_Z + 2 >= a && P_Z < b) || (P_X + 2 >= a && P_X < b);
}
#define DBG 0
#define XW 4
#define XW2 4
#define QPP0 AT(9, 4)
#define QL0 AT(21, 1)
#define QR0 AT(24, 6)
#define QF2 AT(28, 3)
#define QL1 AT(26, 1)
#define QR1 AT(29, 3)
#define QA AT(16, 4)
#define AD 0
#define D1 2
#define D0 1
constexpr int NS = 33, NR = 29;
constexpr int P_PP0 = QPP0, P_PP1 = AT(16, 4), P_L0 = QL0, P_R0 = QR0, P_F2 = QF2, P_L1 = QL1, P_R1 = QR1, P_ZC = AT(10, 6), P_A = QA;
#define QB 24
#define QW -2
constexpr int P_W = QW;
static_assert(P_W < AT(ZP, 4), "");
constexpr int P_B0 = P_L0 - QB, P_B1 = P_L1 - QB;
static_assert(P_B0 > P_ZC + 1 && P_B1 > P_L0 + 1, "");
constexpr int E_PP0 = P_PP0 + NS - 1, E_PP1 = P_PP1 + (NS - 1) / D1, E_R0 = P_R0 + (NR - 1) / D0;
constexpr int ZW = P_A >= AT(20, 4) ? 19 : 15;
static_assert(P_R0 + 14 / D0 >= AT(24, 3) && E_R0 < P_F2 && P_F2 >= AT(24, 4) && P_F2 + 8 <= AT(31, 0) && E_R0 < P_R1, "");
static_assert(P_PP0 >= AT(9, 2) && P_PP0 + 8 <= AT(15, 4) && E_PP0 < P_PP1 && P_A >= AT(16, 4), "");
constexpr bool a_free(int p) {
    return p >= P_A && (AD || !((p >= P_PP1 && p <= E_PP1) || (p >= P_R0 && p <= E_R0) || (p >= P_F2 && p < P_F2 + 16)));
}
constexpr int a_step(int p) {
    if (NOA || !a_free(p)) return -1;
    int c = 0;
    for (int h = P_A; h < p; ++h) c += a_free(h);
    return c <= 48 ? c : -1;
}
constexpr int a_pos(int s) { int p = P_A; while (a_step(p) != s) ++p; return p; }
constexpr int q_rd(int r) { int p = r < 2 ? AT(ZW, 6) + r : a_pos(12 * r - 16) + 1; while ((p & 7) < 4) ++p; return p; }
constexpr int q_tie(int r) { const int n = (q_rd(r) >> 3) + 1; return AT(n, (n & 3) == 3 ? 0 : 4); }
static_assert(NOA || (a_pos(48) < 256 && q_tie(2) <= a_pos(23) && q_tie(3) <= a_pos(35)), "");
constexpr int g2_vm(int p) {
    int c = g2_fill(p) >= 0;
    c += a_step(p) > 0 && a_step(p) % 12 == 0 && !(NOST & 1);
    if (!NOA) c += (p == P_T) + (p == P_Q || p == P_Q + 2) + (p == AT(ZP, 4)) + (p == P_Z || p == P_Z + 2);
    c += (p == P_X || p == P_X + 2) + (p == P_W);
    c += !(NOST & 2) && (p == E_PP0 || p == E_PP1);
    c += (p == P_L0 || p == P_L1) + (PB && (p == P_B0 || p == P_B1));
    c += !(NOST & 4) && (p == P_F2 + 11 || p == P_F2 + 15);
    return c;
}
constexpr int g2_lx(int n) {
    int c = 2 * (n == 7);
    if (n >= 0 && (n & 7) >= 6) c += 2;
    if (!NOA) { c += 4 * (n == ZW); for (int r = 0; r < 4; ++r) c += (q_rd(r) >> 3) == n; }
    return c;
}
constexpr int g2_tile(int n, int j) { return (n & 4) ? (n & 2) == 0 : j; }
constexpr int g2_unit(int n, int j) { return (n & 4) ? 4 + 2 * (n & 1) + j : n & 7; }
constexpr int imax(int a, int b) { return a > b ? a : b; }
constexpr int g2_need(int n) {
    int r = n == 3 ? -1 : fill_pos((n + 1) / 4, 3);
    if (n == 7 && !XL) r = imax(r, P_X + 2);
    if (n == ZW && !NOA) r = imax(r, imax(P_Q, P_Z) + 2);
    return r;
}
static_assert(P_Z > AT(11, 0) && P_T > AT(3, 0) && P_T < AT(ZP, 4) && (!XL || (P_X + 2 < P_R0 && P_R0 + 2 >= AT(24, 2) && P_R0 < AT(27, 0) && D0 == 1)), "");
constexpr bool rd1(int n) { return RD == 1 || (RD == 3 && (n & 3) == 2); }
constexpr int g2_allow(int e, int p) { int c = 0; for (int h = p + 1; h < e; ++h) c += g2_vm(h); return c; }

__global__ __launch_bounds__(512, 2) __attribute__((aligned(4096))) void k1(const uint16_t* __restrict__ x, const uint16_t* __restrict__ bre, const uint16_t* __restrict__ bim,
                                             const uint16_t* __restrict__ lre, const uint16_t* __restrict__ lim, const uint16_t* __restrict__ logdt, char* __restrict__ ws,
                                             const uint16_t* __restrict__ cre, const uint16_t* __restrict__ cim, const uint16_t* __restrict__ dskip,
                                             const uint16_t* __restrict__ ssmk, const uint16_t* __restrict__ ssmD, const uint16_t* __restrict__ qin,
                                             const uint16_t* __restrict__ kin, const uint16_t* __restrict__ vin, const uint16_t* __restrict__ sk,
                                             float* __restrict__ out TS_ARG) {
    unsigned long long pc0, rnd;
    asm volatile("s_getpc_b64 %0\n\ts_memtime %1" : "=s"(pc0), "=s"(rnd));
    TS_DECL TS(0);
    TL_DECL
    __shared__ __attribute__((aligned(1024))) char shm[LDS_TOTAL];
    const int tid = threadIdx.x, lane = tid & 63, wid = __builtin_amdgcn_readfirstlane(tid >> 6);
    const int b = blockIdx.x, xcd = b & 7, idx = b >> 3, hb = xcd & 1, tq = idx & 7;
    const int tm = (xcd >> 1) * 4 + (idx >> 3), tn = hb * 8 + tq;
    unsigned nr;
    const int tm2 = (xcd & 4) * 2 + tq, dt = (xcd & 2) * 2 + (idx >> 3), rot2 = tq + JR(idx >> 3);
    const unsigned lbase = (unsigned)(uintptr_t)shm;
    const int l15 = lane & 15, g4 = lane >> 4, lw = wid & 3, kg = wid >> 2;
    dma<U0 + 4096, false>(lbase + wid * 1024, lane * 16, mk_rsrc((const void*)(pc0 & ~255ull), CODE_KB * 1024), ((idx * 8 + wid) & 63) * 1024);
    const unsigned mk0 = lbase + POLL + lw * 256 + kg * (FULL - POLL + lw * (FST - 256));
    flag_set(mk0 + lane * 4, 1u);
    flag_set(lbase + HGO + lane * 4, 0u);
    if constexpr (NB) flag_set(lbase + DONE + lw * FST + lane * 4, 1u);
    unsigned vo[4];
    const bool xw = lw < 2;
#pragma unroll
    for (int i = 0; i < 4; ++i) {
        const int v = l15 ^ (i * 4 + g4);
        vo[i] = ((xw ? 64 * (i & 1) + 4 * (i >> 1) + (lane & 32) + 2 * (g4 & 1) : 8 * (i & 1) + 2 * g4) + (v >> 3)) * 2048 + (v & 7) * 16 - (i & 1) * 1024;
    }
    const int bw = lw >> 1;
    unsigned r0 = (tm * 128 + lw * 16 + bw * (tn * 64 + lw * 16 - 64 - tm * 128)) * 2048, rsz = 229376 - bw * 163840;
    const unsigned col = (tq + (idx >> 3)) & 1, grp = GS == 0 ? tq >> 2 : GS == 1 ? idx >> 4 : GS == 2 ? 0 : GS == 3 ? hb : (xcd >> 1) & 1;
    unsigned rp = grp * (16 - BL) * 128, mi = col * BL * 128, mo = mi >> grp;
    unsigned rot = (BL ? 0 : ROT >> 8) * col + ((ROT >> 4) & 15) * (xcd >> 1) + (ROT & 15) * hb, rst = 16384 << bw, lb = lbase + lw * 8192, oob = OOB;
    unsigned lb0 = lb + g1_buf(0) + kg * (g1_buf(1) - g1_buf(0)), so0 = BL ? (((kg * 128 + rp) ^ mo) + rot * 128) & 0x780 : ((kg + rot) & 15) * 128;
    if constexpr (BL) { rot *= 128; ties(rp); ties(mi); ties(mo); }
    unsigned co = l15 * 2 + (tn * 128 + lw * 32 + kg * 2048), vw = b * 4 + lw + 32 - 32 * kg;
    tie(vo[0]); tie(vo[1]); tie(vo[2]); tie(vo[3]); tie(oob); tie(co);
    ties(r0); ties(rsz); ties(rot); ties(rst); ties(lb); ties(lb0); ties(so0); ties(vw);
    sched();
    asm volatile("" :: "s"(x), "s"(bre), "s"(bim), "s"(lre), "s"(lim), "s"(logdt), "s"(cre), "s"(cim), "s"(dskip), "s"(ssmk), "s"(ssmD), "s"(qin),
                       "s"(kin), "s"(vin), "s"(sk), "s"(ws), "s"(out));
    const bool xv = fresh(bw) == 0;
    const u32x4 rs0 = mk_rsrc((const char*)(xv ? x : bre) + r0, rsz), rs1 = mk_rsrc((const char*)(xv ? x : bim) + r0 - 2048, rsz + 2048);
    if constexpr (SU) TS(3);
    if constexpr (AB) asm volatile("s_waitcnt lgkmcnt(0)\n\ts_cmp_eq_u32 %0, 0\n\ts_cbranch_scc1 1f\n\ts_barrier\n1:" :: "s"(kg) : "scc", "memory");
    unsigned c_dt, c_re, c_im;
    auto c_ld = [&]() {
        gld<0>(c_dt, co, mk_rsrc(logdt, 2048), 0);
        gld<0>(c_re, co, mk_rsrc(lre, 2048), 0);
        gld<0>(c_im, co, mk_rsrc(lim, 2048), 0);
    };
    if constexpr (!LC) c_ld();
    const unsigned vb = race(vw, ws, rnd);
    static_for<0, 8>([&](auto ic) { constexpr int i = CV(ic); dma4<(i & 4) * 1024, i & 3>(lb0, vo[i & 3], (i & 2) ? rs1 : rs0, so0 + (i >> 2) * rst); });
    wait_l<0>();
    if constexpr (SU) TS(4);
    if constexpr (AB) {
        dma1<0>(mk0 + kg * 120, oob, rs0, 0);
        asm volatile("s_cmp_lg_u32 %0, 0\n\ts_cbranch_scc1 1f\n\ts_barrier\n1:" :: "s"(kg) : "scc", "memory");
    } else {
        barrier();
        dma1<0>(mk0 + kg * 120, oob, rs0, 0);
    }
    if constexpr (SU) TS(5);
    sched();
    const unsigned a00 = (l15 >> 1) * 256 + ((((l15 & 1) * 8 + g4) ^ (l15 >> 1)) << 4);
    auto frag = [&](int par, int ks) { return (a00 ^ (ks * 64) ^ (par * 128)) + par * 2048; };
    bf16x8 cf[32];
    unsigned vc[4], cad[4];
    auto c_setup = [&]() {
#pragma unroll
        for (int i = 0; i < 4; ++i) { const int v = l15 ^ (i * 4 + g4); vc[i] = (4 * (2 * g4 + (v >> 3)) + 2 * (i & 1) + (i >> 1)) * 2048 + (v & 7) * 16 - i * 1024; }
    };
    const u32x4 rsc = mk_rsrc((kg ? cim : cre) + (size_t)dt * 131072, 262144);
    auto c_off = [&](int dg, int j) { return (unsigned)(dg * 65536 + hb * 1024 + ((rot2 + j) & 7) * 128); };
    const unsigned cst = lbase + (kg ? BUF : 2 * BUF) + lw * 8192;
    unsigned tag;
    const u32x4 rsf = mk_rsrc(ws + WS_F, 65536);
    const __amdgpu_buffer_rsrc_t rf = mk_brs(ws + WS_F, 65536);
    const int qx = (hb ? 0 : 2) ^ JQ(idx >> 3), wid_ = wid, b_ = b;

    if (wid < 4) {
        if constexpr (LC) c_ld();
        f32x4 acc[8][2];
        cf32 L, cc, M, M2, MG, NG, N4, N8, XC, ag;
        unsigned eo[4], ew, es, dna, ho, hfo, cb, fv; unsigned long long none[4]; u32x4 rse, rh, er;
        {
            unsigned aad[4], bad[4];
#pragma unroll
            for (int i = 0; i < 4; ++i) { aad[i] = lbase + frag(i & 1, i >> 1); bad[i] = aad[i] + 16384 + wid * 4096; }
            const unsigned fr = lbase + FULL + (lane & 3) * FST;
            sld(tag, ws + WS_F + F_EP);
            bf16x8 fa[4][2], fb[2][4];
            unsigned fl = 1, dz = 0, dw_ = lbase + DONE + wid * FST + lane * 4, vt = lbase + POLL + wid * 256 + lane * 4;
            tie(dz); tie(dw_); tie(vt);
            auto op = [&](auto gc) {
                constexpr int G = CV(gc);
                constexpr Op o = g1_op(G);
                if constexpr (o.kind == OP_A) {
                    constexpr int bo = g1_buf(o.a >> 3), hi = bo >= 65536 ? 65536 : 0, i = o.a & 7;
                    dsr128<bo - hi + (i >> 1) * 4096>(fa[i & 3][o.b], aad[(i & 1) + 2 * o.b] + hi);
                }
                if constexpr (o.kind == OP_B) { constexpr int bo = g1_buf(o.a), hi = bo >= 65536 ? 65536 : 0; dsr128<bo - hi>(fb[o.a & 1][o.b], bad[o.b] + hi); }
                if constexpr (o.kind == OP_FLAG) dsr32<(30 - (G >> 5)) * 4>(fl, fr);
                if constexpr (o.kind == OP_SIG) dsw32<o.a * 4>(dw_, dz);
                if constexpr (o.kind == OP_VOTE) dsr32<0>(vt, vt);
            };
            auto consts = [&]() {
                float ld, ar, ai;
                asm volatile("s_waitcnt vmcnt(%6)\n\tv_lshlrev_b32 %0, 16, %3\n\tv_lshlrev_b32 %1, 16, %4\n\tv_lshlrev_b32 %2, 16, %5"
                             : "=&v"(ld), "=&v"(ar), "=&v"(ai) : "v"(c_dt), "v"(c_re), "v"(c_im), "n"(LC ? 0 : 9) : "memory");
                if constexpr (SU) TS(6);
                const float dtv = rbf(expf(ld)), ea = expf(dtv * ar);
                float sn, cs;
                sincosf(dtv * ai, &sn, &cs);
                L = cf32{ea * cs, ea * sn};
                M = L;
#pragma unroll
                for (int e = 0; e < 5; ++e) M = cmul(M, M);
                M2 = cmul(M, M);
                const cf32 N = cmul(M2, M2), N2 = cmul(N, N), one = {1.f, 0.f};
                N4 = cmul(N2, N2); N8 = cmul(N4, N4);
                auto sel = [](bool c, cf32 a, cf32 b) { return cf32{c ? a.r : b.r, c ? a.i : b.i}; };
                MG = cmul(sel(g4 & 1, M, one), sel(g4 & 2, M2, one));
                NG = cmul(sel(g4 & 1, N, one), sel(g4 & 2, N2, one));
                const float nm = L.r - 1.f, den = ar * ar + ai * ai;
                cc = cf32{2.f * ((nm * ar + L.i * ai) / den), 2.f * ((L.i * ar - nm * ai) / den)};
                auto tc = [&](cf32& v) { tie(v.r); tie(v.i); };
                XC = cf32{g4 == 3 ? M.r : g4 == 2 ? 1.f : 0.f, g4 == 3 ? M.i : 0.f};
                tc(L); tc(cc); tc(M); tc(M2); tc(MG); tc(NG); tc(N4); tc(N8); tc(XC);
                sched();
            };
            if constexpr (CQ < 0) consts();
            if constexpr (SU) TS(10);
            __builtin_amdgcn_s_setprio(CP);
            zero_chk<0>(fl, lbase + POLL + (lane & 3) * 256);
            if constexpr (WS0 > 0) { fl = 1; zero_chk<(31 - WS0) * 4, 1>(fl, fr); }
            ties(tag);
            tag += 0x01010101u;
            TS(1);
            static_for<-12, 0>(op);
            static_for<0, 512>([&](auto gc) {
                constexpr int G = CV(gc), st = G >> 5, i = (G >> 2) & 7, j = G & 3;
                if constexpr ((G == 0 || j == 2) && G <= 506) { static_assert(g1_wait(G) >= 0, ""); wait_lr<G == 506 ? 0 : g1_wait(G)>(); }
                if constexpr (G == CQ) { asm volatile("s_waitcnt lgkmcnt(0)\n\ts_nop 7" ::: "memory"); consts(); }
                if constexpr (G == SQ) {
                    c_setup();
                    const int t0 = tm - 1 - g4;
                    static_for<0, 4>([&](auto ic) {
                        constexpr int i = CV(ic);
                        const int t = t0 - 4 * i;
                        cad[i] = lb + frag(i & 1, i >> 1);
                        eo[i] = ((((t ^ tm) & 8) << 10) | ((t & 7) << 8) | (l15 * 16) | (t & OOB)); none[i] = __builtin_amdgcn_ballot_w64(t < 0);
                        tie(vc[i]); tie(cad[i]); tie(eo[i]); ties(none[i]);
                    });
                    ew = (l15 * 16) | (g4 << 20); dna = lbase + DONE + (lane & 3) * FST; ho = g4 * 256 + ((l15 ^ (4 * (g4 & 1))) << 4); fv = lane << 20;
                    tie(ew); tie(dna); tie(ho); tie(fv);
                    sched();
                }
                if constexpr (G == HQ) hop_all(wid);
                {
                    constexpr int n = (SLM >> (4 * st)) & 15;
                    if constexpr (n > 0 && ((G & 31) + 1) * (n & 7) / 32 != (G & 31) * (n & 7) / 32) __builtin_amdgcn_s_sleep(1 + (n >> 3));
                }
                if constexpr ((G & 31) == 20 && st < 15) zero_chk<(30 - st) * 4>(fl, fr);
                if constexpr (((G & 31) == 30 && st < 13) || (NB < 2 && G == B13)) barrier();
                if constexpr ((G & 31) == 20) TL(st);
                mfma<st == 0 && j < 2>(acc[i][j & 1], fa[i & 3][j >> 1], fb[st & 1][j]);
                sched();
                op(gc);
                if constexpr (G == GCR + 16) { unsigned n; nr = vote_cnt(vt, tag, (xcd & 4) << 11, n); }
                if constexpr (G >= GCR + 18 && G < GCR + 22) { eo[G - GCR - 18] ^= nr; tie(eo[G - GCR - 18]); }
                if constexpr (G == GCR + 22) { rse = mk_rsrc(ws + WS_E + tn * 16384 + wid * 2048, 16384); ties(rse); }
                if constexpr (G == GCR + 24) { es = nr + (tm & 7) * 256; cb = wid * 65536 + hb * 1024; er = u32x4{0u, 0u, tag, ~tag}; ties(es); ties(cb); tie(er); }
                if constexpr (G == GCR + 26) { rh = mk_rsrc(ws + WS_H + (size_t)((tm & 7) * 16 + tn) * 65536 + (nr ^ ((tm & 2) << 12)) + wid * 2048, 65536); ties(rh); }
                if constexpr (G == GCR + 28) { hfo = F_H + nr + (((tm & 7) * 16 + tn) * 4 + wid) * 8; ties(hfo); }
                if constexpr (G == PG0) page<-1>();
                if constexpr (G == 95) hop<2>();
                if constexpr (G == 159) hop<4>();
                if constexpr (G == 207) hop<5>();
                if constexpr (G == PG7) page<7>();
                if constexpr (G == 319) hop<8>();
                if constexpr (G == 383) hop<10>();
                if constexpr (G == 431) hop<11>();
                if constexpr (G == 479) page<13>();
                if constexpr (G == 509) hop<15>();
                if constexpr (G == 489) hop<40>();
                if constexpr (G == 494) hop<41>();
                if constexpr (G == 499) hop<42>();
                if constexpr (G == 504) hop<43>();
                if constexpr (G >= 487) {
                    constexpr int k = G - 486;
                    if constexpr (k == 1) { asm volatile("s_nop 3"); ag = cf32{acc[0][0][0], acc[0][1][0]}; }
                    cf32 t;
                    asm volatile("v_fma_f32 %0, -%3, %5, %6\n\tv_fma_f32 %1, %3, %4, %7\n\tv_fmac_f32 %0, %2, %4\n\tv_fmac_f32 %1, %2, %5"
                                 : "=&v"(t.r), "=&v"(t.i) : "v"(L.r), "v"(L.i), "v"(ag.r), "v"(ag.i), "v"(acc[k >> 2][0][k & 3]), "v"(acc[k >> 2][1][k & 3]));
                    ag = t;
                }
                sched();
            });
        }
        __builtin_amdgcn_s_setprio(3);
        TL(16);
        TS(2);
#pragma unroll
        for (int k = 26; k < 32; ++k) ag = cfma(L, ag, cf32{acc[k >> 2][0][k & 3], acc[k >> 2][1][k & 3]});
        tie(ag.r); tie(ag.i); sched(); hop<44>(); sched();
        cf32 ev_, od, p01, p23;
        sw16(ag.r, ev_.r, od.r); sw16(ag.i, ev_.i, od.i);
        const cf32 pp = cfma(M, ev_, od);
        sw32(pp.r, p01.r, p23.r); sw32(pp.i, p01.i, p23.i);
        cf32 E = cfma(M2, p01, p23);

        er[0] = f2u(E.r); er[1] = f2u(E.i);
        gst<0>(er, ew, rse, es);
        sched();
        TL(17);
        TSX(3);
        hop<45>();
        sched();
        auto c_read = [&](auto rc) { static_for<0, 8>([&](auto ic) { constexpr int r = CV(rc), i = CV(ic); dsr128<(i >> 2) * 4096>(cf[r * 8 + i], cad[i & 3] + ((r & 1) ? 0 : 2 * BUF)); }); };
        auto c_tie = [&](auto rc) { static_for<0, 8>([&](auto ic) { tie(cf[CV(rc) * 8 + CV(ic)]); }); };
        auto c_unit = [&](auto uc) { static_for<0, 4>([&](auto ic) { constexpr int u = CV(uc), i = CV(ic); dma4<((u & 2) ? 0 : 2 * BUF) + (u & 1) * 4096, i>(lb, vc[i], rsc, cb + ((rot2 + u) & 7) * 128); }); };
        auto c_req = [&](auto rc) { c_unit(IC(CV(rc) * 2)); c_unit(IC(CV(rc) * 2 + 1)); };
        const float c2r = cc.r, c2i = cc.i;
        auto scale = [&](int i) {
#pragma unroll
            for (int j = 0; j < 4; ++j) {
                const float br = acc[i][0][j], bi = acc[i][1][j];
                acc[i][0][j] = fma_(c2r, br, -(c2i * bi)); acc[i][1][j] = fma_(-c2r, bi, -(c2i * br));
            }
            tie(acc[i][0]); tie(acc[i][1]);
        };
        auto scales = [&](auto ac, auto bc) {
            static_for<CV(ac), CV(bc)>([&](auto ic) {
                constexpr int i = CV(ic);
                scale(i);
                if constexpr (i & 1) { sched(); hop<i == 1 ? 46 : i == 3 ? 16 : i == 5 ? 47 : 48>(); sched(); }
            });
        };
        constexpr int CA = CVR == 0 ? 0 : CVR == 4 ? 1 : 2;
        if constexpr (NB) {
            unsigned dn = 1;
            scales(IC(0), IC(DP));
            if constexpr (CA) { zero_chk<8>(dn, dna); static_for<0, CA>(c_unit); }
            if constexpr (SPR != 3) __builtin_amdgcn_s_setprio(SPR);
            sched();
            scales(IC(DP), IC(ELP));
            if constexpr (!CA) zero_chk<8>(dn, dna);
        } else {
            scales(IC(0), IC(ELP));
            barrier();
            static_for<0, CA>(c_unit);
        }
        TL(18);
        u32x4 ev[4];
#pragma unroll
        for (int i = 0; i < 4; ++i) asm volatile("buffer_load_dwordx4 %0, %1, %2, 0 offen sc1" : "=v"(ev[i]) : "v"(eo[i]), "s"(rse) : "memory");
        asm volatile("" ::: "memory");
        sched();
        constexpr int CB = CVR == 0 ? 1 : CVR == 4 ? 2 : 4;
        static_for<CA, CB>(c_unit);
        scales(IC(ELP), IC(8));
        cf32 X = cfma(XC, p01, cf32{(g4 & 1) ? ev_.r : 0.f, (g4 & 1) ? ev_.i : 0.f});
        tie(X.r); tie(X.i);
        sched();
        hop<24>();
        if constexpr (SPR != 3) __builtin_amdgcn_s_setprio(3);
        if constexpr (CVR == 2) { wait_v<12>(); c_read(IC(0)); wait_l<0>(); c_tie(IC(0)); c_req(IC(2)); }
        wait_v<4 * (CB - CA) + 8 * (CVR == 2)>();
        TL(19);
        tie(ev[0]); tie(ev[1]); tie(ev[2]); tie(ev[3]);
        {
            const unsigned long long tag64 = (unsigned long long)tag | ((unsigned long long)~tag << 32);
#pragma unroll 1
            for (unsigned spins = 0; spins < SPIN_CAP; ++spins) {
                unsigned long long ok = ~0ull;
#pragma unroll
                for (int i = 0; i < 4; ++i) ok &= __builtin_amdgcn_ballot_w64(((unsigned long long)ev[i][2] | ((unsigned long long)ev[i][3] << 32)) == tag64) | none[i];
                TSV(11, spins);
                if (ok == ~0ull) break;
                asm volatile("buffer_load_dwordx4 %0, %4, %8, 0 offen sc1\n\tbuffer_load_dwordx4 %1, %5, %8, 0 offen sc1\n\tbuffer_load_dwordx4 %2, %6, %8, 0 offen sc1\n\t"
                             "buffer_load_dwordx4 %3, %7, %8, 0 offen sc1\n\ts_waitcnt vmcnt(0)" : "=&v"(ev[0]), "=&v"(ev[1]), "=&v"(ev[2]), "=&v"(ev[3])
                             : "v"(eo[0]), "v"(eo[1]), "v"(eo[2]), "v"(eo[3]), "s"(rse) : "memory");
            }
        }
        if constexpr (P18 < 0) page<18>();
        cf32 h;
        {
            auto e = [&](int i) { return cf32{u2f(ev[i][0]), u2f(ev[i][1])}; };
            cf32 c = cmul(NG, cfma(N8, cfma(N4, e(3), e(2)), cfma(N4, e(1), e(0)))), u, v;
            sw16(c.r, u.r, v.r); sw16(c.i, u.i, v.i);
            sw32(u.r + v.r, u.r, v.r); sw32(u.i + v.i, u.i, v.i);
            c = cfma(MG, cf32{u.r + v.r, u.i + v.i}, X);
            h = cf32{fma_(c2r, c.r, -(c2i * c.i)), fma_(-c2r, c.i, -(c2i * c.r))};
        }
        TSX(4);
        {
            const cf32 Lc = {L.r, -L.i};
            static_for<0, 4>([&](auto qc) {
                constexpr int q = CV(qc);
                if constexpr (q >= CB) c_unit(qc);
                if constexpr (q == 2) hop<20>();
                u32x4 wr, wi;
#pragma unroll
                for (int u = 0; u < 4; ++u) {
                    const int i = 2 * q + (u >> 1), j = 2 * (u & 1);
                    const cf32 h0 = cfma(Lc, h, cf32{acc[i][0][j], acc[i][1][j]});
                    h = cfma(Lc, h0, cf32{acc[i][0][j + 1], acc[i][1][j + 1]});
                    wr[u] = pack2(h0.r, h.r); wi[u] = pack2(h0.i, h.i);
                }
                if constexpr ((PRE == 2 || GQ >= 0) && q == GE) { tie(wr); tie(wi); dsw32<4>(lbase + DONE + wid * FST + lane * 4, 0u); }
                gst<0>(wr, ho, rh, q * 16384);
                gst<1024>(wi, ho, rh, q * 16384);
                if constexpr (q == P18) page<18>();
                asm volatile("s_nop 1" :: "v"(wr), "v"(wi));
                sched();
            });
        }
        TSX(10);
        auto flag = [&]() { gst<0>(u32x2{er[2], er[3]}, fv, rsf, hfo); TSX(5); };
        hop<22>();
        if constexpr (CVR == 2) {
            wait_v<16>();
            c_read(IC(1));
            wait_l<0>();
            c_req(IC(3));
            wait_v<8>();
            flag();
        } else {
            if constexpr (CVR == 1) wait_v<8>(); else { wait_v<0>(); flag(); }
            c_read(IC(0)); c_read(IC(1));
            wait_l<8>();
            c_req(IC(2));
            wait_l<0>();
            c_req(IC(3));
            if constexpr (CVR == 1) { wait_v<16>(); flag(); }
        }
        c_tie(IC(0)); c_tie(IC(1));
        static_for<16, 32>([&](auto ic) { undef(cf[CV(ic)]); });
    } else {
        const int ch = xcd * 128 + (idx >> 2) * 16 + lw * 4 + (idx & 3);
        unsigned dw, nx; u32x4 ad;
        auto c_unit = [&](auto uc) { static_for<0, 4>([&](auto ic) { constexpr int u = CV(uc), i = CV(ic); dma4<(u & 1) * 4096, i>(cst, vc[i], rsc, c_off(lw, u)); }); };
        constexpr int XV = 4 * (EC == 1) + 8 * (PFQ == 2);
        auto ctouch = [&](int r) { const unsigned j = (fresh(idx * 4 + lw) + r * 37) & 127; stouch((pc0 & ~255ull) + (j < CODE_KB * 8 / 3 ? j : j - 64) * 384); };
        {
            const unsigned fb = lbase + FULL + lw * FST;
            auto piece = [&](auto sc, auto ic) {
                constexpr int s = CV(sc), i = CV(ic);
                dma4<g1_buf(s) + (i & 4) * 1024, i & 3>(lb, vo[i & 3], (i & 2) ? rs1 : rs0, (BL ? (((s * 128 + rp) ^ (s >= BL && s < 16 - BL ? mi : mo)) + rot) & 0x780 : ((s + rot) & 15) * 128) + (i >> 2) * rst);
            };
            auto mark = [&](auto sc) { dma1<(31 - CV(sc)) * 4>(fb, oob, rs0, 0); };
            auto feed = [&](auto kc, auto ic) {
                constexpr int k = CV(kc), i = CV(ic);
                if constexpr (i < 4) piece(IC(k + 3), IC(i + 4));
                if constexpr (i == 3) mark(IC(k + 3));
                if constexpr (i >= 4 && k < 12) piece(IC(k + 4), IC(i - 4));
            };
            __builtin_amdgcn_s_setprio(HP);
            TS(7);
            sld(tag, ws + WS_F + F_EP);
            sld(dw, ssmD + (ch & ~1));
            const int sb = idx * 2 + (lw >> 1), kh = lw & 1;
            u32x2 kk[11], vv[8]; u32x4 w4[2], kd[4], ke[4], kvo[4];
            const unsigned ko = g4 * 16384 + l15 * 8, so = sb * 65536 + xcd * 256 + kh * 128;
            auto kv_ld = [&](auto jc) {
                constexpr int j = CV(jc), i = j < 13 ? j - 5 : j - 13;
                if constexpr (j < 2) gld<16 * j>(w4[j], l15 * 32, mk_rsrc(sk, 8192), xcd * 1024 + kh * 512);
                else if constexpr (j < 5) gld<0>(kk[j - 2], ko + so - 6144 + (j - 2) * 2048, mk_rsrc(kin, 4 << 20), 0);
                else if constexpr (j < 13) gld<(i & 1) * 2048>(kk[3 + i], ko, mk_rsrc(kin, 4 << 20), so + (i >> 1) * 4096);
                else gld<(i & 1) * 2048>(vv[i], ko, mk_rsrc(vin, 4 << 20), so + (i >> 1) * 4096);
            };
            {
                static_for<0, KLP>(kv_ld);
                dma1<DSK>(lbase, lane * 4, mk_rsrc(dskip, 2048), dt * 256);
                dma<U0, false>(lbase + kh * 1024, oob, rs0, 0);
            }
            static_for<4, 8>([&](auto ic) { feed(IC(-2), ic); });
            const __amdgpu_buffer_rsrc_t rk = mk_brs(ws + WS_KVT + (size_t)((xcd & 3) * 32 + kh * 16) * 32768, 524288);
            const unsigned K = lbase + KC + lw * KCH;
            unsigned kfo;
            const unsigned n0 = (xcd & 4) << 11;
            auto st = [&](int c) { st16(kvo[c], rk, (l15 >> 2) * 131072 + g4 * 64 + (l15 & 3) * 16, c * 32768 + (sb >> 5) * 16384 + (KVS ? nx : n0 ^ ((sb & 32) << 8)) + (sb & 31) * 256); };
            float kf[7], pa[4], pb[4];
            auto kv_step = [&](auto uc, auto jc) {
                constexpr int u = CV(uc), j = CV(jc), c = u >> 1, h = u & 1;
                auto el = [&](const u32x2& q) -> float { return (c & 1) ? bfhi(q[c >> 1]) : bflo(q[c >> 1]); };
                auto tap = [&](int t) { const unsigned w = w4[c >> 1][(c & 1) * 2 + (t >> 1)]; return (t & 1) ? bfhi(w) : bflo(w); };
                auto all = [&](auto f) {
#pragma unroll
                    for (int i = 0; i < 4; ++i) f(i);
                };
                if constexpr (j == 0) {
#pragma unroll
                    for (int i = 0; i < 7; ++i) { kf[i] = (h && i < 3) ? kf[i + 4] : el(kk[4 * h + i]); tie(kf[i]); }
                }
                if constexpr (EXACT) {
                    if constexpr (j == 1) { const float t = tap(3); all([&](int i) { pa[i] = kf[i] * t; }); all([&](int i) { pa[i] = rbf(pa[i]); tie(pa[i]); }); }
                    if constexpr (j == 2 || j == 4 || j == 6) { const float t = tap(3 - j / 2); all([&](int i) { pb[i] = kf[i + j / 2] * t; }); all([&](int i) { pb[i] = rbf(pb[i]); tie(pb[i]); }); }
                    if constexpr (j == 3 || j == 5 || j == 7) { all([&](int i) { pa[i] += pb[i]; }); all([&](int i) { pa[i] = rbf(pa[i]); tie(pa[i]); }); }
                } else {
                    if constexpr (j == 1) { const float t = tap(3); all([&](int i) { pa[i] = kf[i] * t; tie(pa[i]); }); }
                    if constexpr (j >= 2 && j <= 4) { const float t = tap(4 - j); all([&](int i) { pa[i] = fma_(kf[i + j - 1], t, pa[i]); tie(pa[i]); }); }
                    if constexpr (j == 5) all([&](int i) { pa[i] = rbf(pa[i]); tie(pa[i]); });
                }
                if constexpr (j == (EXACT ? 7 : 6)) all([&](int i) { pb[i] = el(vv[4 * h + i]); tie(pb[i]); });
                if constexpr (j == 7) {
                    all([&](int i) { pa[i] *= pb[i]; });
                    kvo[c][3 - 2 * h] = pack2(pa[1], pa[0]); kvo[c][2 - 2 * h] = pack2(pa[3], pa[2]);
                    if constexpr (h && !KST && u >= kv_first(KSD)) st(c);
                    else if constexpr (u == 7) all(st);
                    else tie(kvo[c]);
                }
            };
            static_for<-1, 13>([&](auto kc) {
                constexpr int k = CV(kc);
                if constexpr (k == PGF) page<1>();
                if constexpr (k == 2) hop<0>();
                if constexpr (k == 6) hop<6>();
                if constexpr (k == 8) hop<9>();
                if constexpr (k >= 0) barrier();
                if constexpr (k >= 0 && ((CTM >> k) & 1)) ctouch(k);
                if constexpr (k == KV0) {
                    wait_v<KLP == 21 ? 4 + hv_cnt(-1, k) : 9 - KLI - (KLI > 3) + hv_cnt(KLL + 1, k)>();
                    static_for<0, 11>([&](auto ic) { tie(kk[CV(ic)]); });
                    static_for<0, 8>([&](auto ic) { tie(vv[CV(ic)]); });
                    tie(w4[0]); tie(w4[1]); if constexpr (!LC) { keep(c_dt); keep(c_re); keep(c_im); }
                    TS(8);
                }
                if constexpr (k == KVP) {
                    wait_l<0>();
                    ties(tag); ties(dw);
                    tag += 0x01010101u;
                    __builtin_amdgcn_raw_buffer_store_b64(u32x2{tag, ~tag ^ vb}, rf, lane || vw >= 32 ? OOB : 0u, F_C + vw * 8, 16);
                }
                if constexpr (k == KCQ) dma1<POLL>(lbase + lw * 256, lane * 4, rsf, F_C);
                if constexpr (TM == 16 && k == KTI) stouch((unsigned long long)(ws + WS_E) + (TX ? (xcd * 128 + idx * 4 + lw) : b * 4 + lw) * 384);
                if constexpr (TM && TM < 16 && k == KTI) {
                    const unsigned wv = b * 4 + lw, a = lane < 32 ? ((TM & 1) ? wv * 4096 + lane * 128 : (TM & 4) && lane < 4 ? wv * 4096 + lane * 1024 + 896 : OOB) : lane < 35 && (TM & 2) ? (unsigned)(WS_E - WS_PP) + (wv * 3 + lane - 32) * 128 : OOB;
                    if constexpr (TM & 8) dma1<DUMP>(lbase + lw * 256, a, mk_rsrc(ws + WS_PP, WS_F + F_EP - WS_PP), 0);
                    else asm volatile("buffer_atomic_or %0, %1, %2, 0 offen sc1" :: "v"(0u), "v"(a), "s"(mk_rsrc(ws + WS_PP, WS_F + F_EP - WS_PP)) : "memory");
                }
                if constexpr (k == KCC) {
                    unsigned f, n, sp;
                    wait_v<hv_cnt(KCQ, k) - 1>();
                    dsr32<POLL - 131072>(f, lbase + 131072 + lw * 256 + lane * 4);
                    wait_l<0>();
                    vote_chk(f, (lane & 1) ? ~tag : tag, lane & 1, lbase + POLL + lw * 256 + lane * 4, lbase + POLL + lw * 256, lane * 4, rsf, F_C, sp);
                    nr = vote_cnt(f, tag, (xcd & 4) << 11, n);
                    nx = nr ^ ((xcd & 2) << 12);
                    kfo = F_K + (KVS ? nr : n0) + (((xcd & 3) * 2 + (idx >> 4)) * 64 + lw * 16) * 8;
                    TSV(11, n + 100 * (0x10000 - sp));
                }
                if constexpr (k == KFL) {
                    wait_v<hv_cnt(KVL + 1, k)>();
                    __builtin_amdgcn_raw_buffer_store_b64(u32x2{tag, ~tag}, rf, lane ? OOB : 0u, F_K + (KVS ? nr : n0) + (((xcd & 3) * 2 + kh) * 64 + sb) * 8, 16);
                    TSX(10);
                }
                if constexpr (k == 12) {
                    dma1<0>(K, oob, rs0, 0);
                    dma1<KCP>(K, oob, rs0, 0);
                }
                static_for<0, 8>([&](auto ic) {
                    constexpr int i = CV(ic), u0 = kv_first(k), nu = kv_first(k + 1) - u0;
                    static_for<KLP, 21>([&](auto jc) { if constexpr (ld_pos(CV(jc)) == 8 * k + i) kv_ld(jc); });
                    if constexpr (k == KPL && i == KPI) dma1<POLL>(lbase + wid * 256, (lane & 31) * 4, rsf, kfo);
                    feed(kc, ic);
                    if constexpr (k == 4 && i == 4) hop<3>();
                    if constexpr (!KST && k == KSD && (i & 1) && i / 2 < kv_first(KSD) / 2) st(i / 2);
                    if constexpr (nu == 1) kv_step(IC(u0), ic);
                    if constexpr (nu == 2) { kv_step(IC(u0 + i / 4), IC(2 * (i & 3))); kv_step(IC(u0 + i / 4), IC(2 * (i & 3) + 1)); }
                    if constexpr (!KDL && k == 12 && i >= 4) gld<(i - 4) * 1024>(kd[i - 4], lane * 16, mk_rsrc(ssmk, 4 << 20), ch * 4096);
                    if constexpr (k == 11 && i == 4) c_setup();
                    if constexpr (k == 10 && i >= 4) { cad[i - 4] = cst + frag(i & 1, (i >> 1) & 1); tie(cad[i - 4]); }
                    sched();
                });
            });
            page<12>();
            TS(1);
            if constexpr (EB == 2) barrier();
            unsigned pf;
            const unsigned fr = lbase + FULL + (lane & 3) * FST;
            wait_v<hv_cnt(KPL, 12) - (KPL < 12 ? 1 : 2) - (KPL == KFL) - KPI - (KPI > 3) + (KDL ? 7 : 11)>();
            dsr32<POLL - 131072>(pf, lbase + 131072 + wid * 256 + lane * 4);
            wait_l<0>();
            tag_chk<KSL>(pf, (lane & 1) ? ~tag : tag, (lane & 31) * 4, rsf, kfo);
            if constexpr (KDL) static_for<0, 4>([&](auto ic) { gld<CV(ic) * 1024>(kd[CV(ic)], lane * 16, mk_rsrc(ssmk, 4 << 20), ch * 4096); });
            auto b13 = [&]() { if constexpr (NB == 2) { unsigned f = 1; zero_chk<12, 1>(f, lbase + DONE + (lane & 3) * FST); } else if constexpr (NB < 2) barrier(); };
            if constexpr (EB == 1) b13();
            {
                const u32x4 rs = mk_rsrc(ws + WS_KVT + (size_t)((xcd & 3) * 32 + idx) * 32768, 32768);
                static_for<0, 4>([&](auto ic) { constexpr int i = CV(ic); dma<U0 + 2048 + i * 1024, true>(lbase + lw * 4096, (lane >> 2) * 64 + (((lane & 3) ^ g4) << 4), rs, (lw >> 1) * 16384 + (KVS ? nx : n0 ^ ((lw & 2) << 12)) + (lw & 1) * 4096 + i * 1024); });
            }
            mark(IC(16));
            auto warm = [&]() {
                const unsigned wo = g4 * 2048 + l15 * 16, dl = lbase + lw * 1024;
                const unsigned qo = (512 * (idx >> 3) + 128 * (idx & 1) + 32 * lw + 4 * ((idx >> 1) & 3)) * 2048 + xcd * 256, xo = (tm2 * 128 + 32 * lw + 8 * (qx & 2)) * 2048 + dt * 256;
                static_for<0, 4>([&](auto ic) { constexpr int i = CV(ic); dma<DUMP, false>(dl, wo, mk_rsrc(qin, 4 << 20), qo + ((i & 1) * 16 + (i >> 1) * 256) * 2048); });
                static_for<0, 4>([&](auto ic) { constexpr int i = CV(ic); dma<DUMP, false>(dl, wo, mk_rsrc(x, 4 << 20), xo + i * 8192); });
            };
            if constexpr (PFQ == 1) warm();
            if constexpr (EB == 0) b13();
            auto c_first = [&]() {
                if constexpr (CL <= 1) c_unit(IC(0));
                if constexpr (CL <= 0) c_unit(IC(1));
                if constexpr (EC == 1) static_for<0, 4>([&](auto ic) { constexpr int i = CV(ic); dma4<DUMP, i>(lbase, vc[i], mk_rsrc(cre + (size_t)dt * 131072, 262144), c_off(lw, 0)); });
                if constexpr (PFQ == 2) warm();
            };
            static_assert(NB != 3 || CL == 2, "");
            if constexpr (!KO) c_first();
            wait_v<5 + 8 * (PFQ == 1) + (KO ? 0 : 4 * (CL <= 1) + 4 * (CL <= 0) + XV)>();
            static_for<0, 4>([&](auto ic) { tie(kd[CV(ic)]); });
#pragma unroll
            for (int n = 0; n < 4; ++n) {
                const unsigned first = n ? __builtin_amdgcn_readlane(kd[n ? n - 1 : 0][3], 63) : 0u;
                const unsigned p = __builtin_amdgcn_update_dpp(first, kd[n][3], 0x138, 0xf, 0xf, false);
                ke[n] = u32x4{__builtin_amdgcn_alignbyte(kd[n][0], p, 2), __builtin_amdgcn_alignbyte(kd[n][1], kd[n][0], 2),
                              __builtin_amdgcn_alignbyte(kd[n][2], kd[n][1], 2), __builtin_amdgcn_alignbyte(kd[n][3], kd[n][2], 2)};
                tie(ke[n]);
            }
            if constexpr (ZM) {
                const unsigned dl = (ch & 1) ? dw >> 16 : dw & 0xffffu, dv = dl << ((~lane & 1) * 16);
                static_for<0, 4>([&](auto ic) { constexpr unsigned long long k = lag0_mask(CV(ic)); ad[CV(ic)] = sel0(dv, k); });
                tie(ad);
            }
            hop<14>();
            static_for<0, 4>([&](auto ic) { constexpr int i = CV(ic); dsw128<64 + i * 1024>(K + lane * 16, kd[i]); dsw128<KCP + 64 + i * 1024>(K + lane * 16, ke[i]); });
            if constexpr (KO) c_first();
            TS(9);
            static_for<0, -CL>([&](auto uc) {
                constexpr int u = CV(uc);
                wait_v<4 + (u < 2 ? XV : 0)>();
                static_for<0, 4>([&](auto ic) { constexpr int i = CV(ic); dsr128<(u & 1) * 4096>(cf[u * 4 + i], cad[i]); });
                wait_l<0>();
                static_for<0, 4>([&](auto ic) { tie(cf[u * 4 + CV(ic)]); });
                c_unit(IC(u + 2));
            });
            pf = 1;
            zero_chk<15 * 4>(pf, fr);
            if constexpr (PRE) { pf = 1; zero_chk<PRE == 1 ? 8 : 4>(pf, lbase + DONE + (lane & 3) * FST); }
            __builtin_amdgcn_s_setprio(0);
        }
        TS(2);
        TL(1);
        unsigned ka[4], ub[4];
#pragma unroll
        for (int i = 0; i < 4; ++i) {
            ka[i] = lbase + KC + lw * KCH + ((l15 & 1) ? 0 : KCP) + 84 + 2 * (l15 & 14) - 16 * g4 + i * 1024;
            const int e = 2 * l15 + g4 + 2;
            ub[i] = lbase + U0 + 64 * e + ((lw ^ (((e >> 2) + 3 - i) & 3)) << 4);
        }
        unsigned gad = lbase + DONE + lw * FST;
        if constexpr (GQ >= 0) tie(gad);
        f32x4 acc[8];
        bf16x4 FA[8][2]; bf16x8 FB[8];
        auto op = [&](auto gc) {
            constexpr Op o = cv_op(CV(gc));
            if constexpr (o.kind == CO_A) { constexpr int s = o.a >> 3, n = o.a & 7, d = (n & 1) * 128 + s * 16 + o.b * 2; dsr2<d, d + 1>(FA[n][o.b], ka[n >> 1]); }
            if constexpr (o.kind == CO_B) { constexpr int s = o.a >> 3, m = o.a & 7; dsr128<2048 * m + 1792 - 256 * s>(FB[m], ub[s & 3]); }
            if constexpr (o.kind == CO_C) dsr128<(o.a & 1) * 4096>(cf[o.a * 4 + o.b], cad[o.b]);
        };
        static_for<-36, 0>(op);
        static_for<0, 288>([&](auto gc) {
            constexpr int G = CV(gc), s = G / 36, q = G % 36, n = cv_n(q), m = cv_m(q);
            if constexpr (cv_wait(G) >= 0) wait_lr<cv_wait(G)>();
            if constexpr (q == 24 && s >= CL && s - CL < 8) static_for<0, 4>([&](auto ic) { tie(cf[(s - CL) * 4 + CV(ic)]); });
            if constexpr (ZM && G < 8) mfma<true>(acc[m], __builtin_bit_cast(bf16x8, ad), FB[m]);
            mfma<s == 0 && n == 0 && !ZM>(acc[n + m], cat(FA[n][0], FA[n][1]), FB[m]);
            sched();
            if constexpr (q == 11) TL(2 + 2 * s);
            if constexpr (q == 11 && s >= CL && s - CL < 8) wait_v<(s - CL < 7 ? 4 : 0) + (s - CL < (CL <= 1) + (CL <= 0) ? XV : 0) + 8 * (PFQ >= 3 && (s == PFQ - 2 || s == PFQ - 1))>();
            if constexpr (q == 11) TL(3 + 2 * s);
            op(gc);
            if constexpr (q == 30 && ((CTM >> (16 + s)) & 1)) ctouch(16 + s);
            if constexpr (G == 16) hop<17>();
            if constexpr (G == 66) hop<19>();
            if constexpr (G == PG1) page<21>();
            if constexpr (G == PG2) page<28>();
            if constexpr (G == 158) hop<23>();
            if constexpr (G == 209) hop<25>();
            if constexpr (G == 254) hop<27>();
            if constexpr (G == GQ) { unsigned f = 1; zero_chk<4, 1>(f, gad); }
            if constexpr (!NB && G == CBQ) barrier();
            if constexpr (NB == 3 && G == 23) { unsigned f = 1; zero_chk<8, 1>(f, lbase + DONE + (lane & 3) * FST); }
            if constexpr ((q == 24 || q == 25 || q == 29 || q == 34) && s + 2 - CL < 8) {
                constexpr int pc = q == 24 ? 0 : q == 25 ? 1 : q == 29 ? 2 : 3, u = s + 2 - CL;
                dma4<(u & 1) * 4096, pc>(cst, vc[pc], rsc, c_off(lw, u));
            }
            sched();
        });
        if constexpr (PG2 >= 288) page<28>();
        TL(18);
        TSX(3);
        {
            const __amdgpu_buffer_rsrc_t rz = mk_brs(ws + WS_ZT, 4 << 20);
            if constexpr (ZM) {
                asm volatile("s_nop 7");
#pragma unroll
                for (int jj = 0; jj < 4; ++jj) {
                    tie(acc[2 * jj]); tie(acc[2 * jj + 1]);
                    const f32x4 a = acc[2 * jj], c = acc[2 * jj + 1];
                    st16(u32x4{pack2(a[0], a[1]), pack2(a[2], a[3]), pack2(c[0], c[1]), pack2(c[2], c[3])}, rz, lane * 16 + jj * 1024, (ch & 510) * 8192 + nx + (ch & 1) * 4096);
                }
            } else {
            u32x2 kvr[8];
            const unsigned kva = lbase + U0 + 2048 + 128 * l15 + 64 * (g4 >> 1) + ((lw ^ ((l15 >> 1) & 3)) << 4) + 8 * (1 - (g4 & 1));
            static_for<0, 8>([&](auto ic) { dsr64<CV(ic) * 2048>(kvr[CV(ic)], kva); });
            wait_l<0>();
            const float Dd = (ch & 1) ? bfhi(dw) : bflo(dw);
#pragma unroll
            for (int jj = 0; jj < 4; ++jj) {
                u32x4 o;
#pragma unroll
                for (int u = 0; u < 2; ++u) {
                    const int j = 2 * jj + u;
                    tie(kvr[j]); tie(acc[j]);
                    const float kv3 = bflo(kvr[j][0]), kv2 = bfhi(kvr[j][0]), kv1 = bflo(kvr[j][1]), kv0 = bfhi(kvr[j][1]);
                    o[2 * u] = pack2(rbf(acc[j][0]) + rbf(kv0 * Dd), rbf(acc[j][1]) + rbf(kv1 * Dd));
                    o[2 * u + 1] = pack2(rbf(acc[j][2]) + rbf(kv2 * Dd), rbf(acc[j][3]) + rbf(kv3 * Dd));
                }
                st16(o, rz, lane * 16 + jj * 1024, (ch & 510) * 8192 + nx + (ch & 1) * 4096);
                if (jj == 1) { sched(); hop<29>(); sched(); }
            }
            }
            TS(8);
            hop<30>();
            if constexpr (CL > 0) {
                wait_v<4>();
                static_for<32 - 4 * CL, 32>([&](auto ic) { constexpr int i = CV(ic); dsr128<((i >> 2) & 1) * 4096>(cf[i], cad[i & 3]); });
                wait_l<0>();
                static_for<32 - 4 * CL, 32>([&](auto ic) { tie(cf[CV(ic)]); });
            }
        }
        TL(19);
        TSX(4);
    }
    {
        unsigned ln = lane, dsw_, pf;
        tie(ln);
        const int wid = fresh(wid_), lw = wid & 3, kg = wid >> 2, b = fresh(b_), xcd = b & 7, idx = b >> 3, tq = (idx & 7) + JR(idx >> 3);
        nr = fresh(__builtin_amdgcn_readfirstlane(nr));
        const unsigned nx = nr ^ ((xcd & 2) << 12), nh = nr ^ ((idx & 2) << 12), hp = (idx & 7) * 16 + (b & 1) * 8;
        const int tm2 = (xcd & 4) * 2 + (idx & 7), dt = (xcd & 2) * 2 + (idx >> 3), qx = ((~b & 1) * 2) ^ JQ(idx >> 3);
        const int ta = 512 * (idx >> 3) + 128 * (idx & 1) + 16 * wid + 4 * ((idx >> 1) & 3);
        const int tb = tm2 * 128 + 64 * kg;
        dsr32<DSK - 131072>(dsw_, lbase + 131072 + lw * 64 + (ln & 7) * 8 + ((ln >> 1) & 4));
        unsigned wl = lbase + wid * 4096, w2k = lbase + wid * 2048, hs0 = (hp + ((tq + (wid >> 1)) & 7)) * 65536 + nh + (wid & 1) * 4096 + qx * 16384;
        unsigned s_x = (tb + qx * 8) * 2048 + dt * 256 + lw * 64, s_q = ta * 2048 + xcd * 256, s_z = WS_ZT + (xcd & 3) * 1048576 + nx + idx * 128 + wid * 131072, zfo = WS_F + F_Z + (xcd & 2) * 8192 + nr + (xcd & 1) * 4096;
        unsigned ppo = PK ? WS_KVT + (xcd & 3) * 1048576 + idx * 32768 + kg * 16384 + (KVS ? nx : ((xcd ^ wid) & 4) << 11) + lw * 2048
                          : WS_PP + (xcd & 2) * 1048576 + idx * 65536 + (xcd & 1) * 32768 + kg * 16384 + nx + lw * 2048, s_a = ta * 8192 + xcd * 512, s_b = (tb + (2 ^ qx) * 8) * 8192 + 4096 + dt * 512 + lw * 128;
        const unsigned tagg = fresh(__builtin_amdgcn_readfirstlane(tag));
        u32x4 rsx = mk_rsrc(x, (NOX & 1) ? 0 : 4 << 20), rsq = mk_rsrc(qin, (NOX & 2) ? 0 : 4 << 20), rso = mk_rsrc(out, 16 << 20), rws = mk_rsrc(ws, 32 << 20);
        ties(wl); ties(w2k); ties(hs0); ties(s_x); ties(s_q); ties(s_z); ties(zfo); ties(ppo); ties(s_a); ties(s_b);
        ties(rsx); ties(rsq); ties(rso); ties(rws);
        const unsigned l31 = ln & 31, l5 = ln >> 5;
        unsigned l16 = ln * 16, ha[8];
        ha[0] = (lbase + kg * 1536 + l5 * 2048 + (ln & 2) * 128 + (ln & 1) * 8) | ((((ln >> 1) & 8) | ((ln >> 2) & 3) | ((ln & 2) << 1)) << 4);
        ha[1] = ha[0] ^ 64; ha[2] = ha[0] ^ 512; ha[3] = ha[0] ^ 576;
        static_for<0, 4>([&](auto ic) { ha[4 + CV(ic)] = ha[CV(ic)] + 65536; tie(ha[CV(ic)]); tie(ha[4 + CV(ic)]); });
        unsigned a_x = ((((ln >> 2) & 7) | (ln & 32)) << 11) | (l16 & 48), a_xr = (((l16 & 240) << 2) | (ln & 48)) + (w2k + ZST);
        unsigned a_q = ((ln & 48) << 7) | (l16 & 240), a_qr = (l5 << 10) + l31 * 8 + (w2k + QST), a_iw = w2k + IMG + l16, a_ir = a_iw ^ 8192;
        unsigned a_t = wid == 0 && ln == 0 ? idx * 128 : OOB, msk = 0xfff0fff0u;
        unsigned a_f = wid == 1 ? (ln >> 1) * 128 + (ln & 1) * 4 : OOB, expect = wid == 1 ? ((ln & 1) ? ~tagg : tagg) : 0u;
        unsigned a_z = (((ln ^ l5 ^ (wid * 4)) & 7) << 4) | ((ln & 48) << 10) | ((ln & 8) << 9), a_z2 = a_z ^ 32, a_zr = (l31 << 9) + (((((wid ^ ln) & 7) << 1) | l5) << 3) + (lbase + ZST);
        unsigned a_oa = (l5 << 21) | (l31 << 4), a_ob = ((((ln >> 2) & 4) | (ln & 32) | ((ln >> 3) & 1)) << 13) | (l16 & 112);
        unsigned a_p = PB ? (ln == 63 ? 1020u : OOB) : l16, a_w = ln < 16 ? ln * 128 : OOB;
        if constexpr (PB) tie(a_p);
        if constexpr (P_W >= -1) tie(a_w);
        if constexpr (P_W == -1) pf_ld(pf, a_w, rws, ppo);
        u32x2 tg2 = {tagg, ~tagg};
        tie(l16); tie(a_x); tie(a_xr); tie(a_q); tie(a_qr); tie(a_iw); tie(a_ir); tie(a_t); tie(msk); tie(a_f); tie(expect); tie(a_z); tie(a_z2); tie(a_zr);
        tie(a_oa); tie(a_ob); tie(tg2);
        wait_l<0>();
        tie(dsw_);
        u32x4 dg[2];
        static_for<0, 4>([&](auto ic) { constexpr unsigned long long k = diag_mask(CV(ic)); dg[0][CV(ic)] = sel0(dsw_ & 0xffffu, k); dg[1][CV(ic)] = sel0(dsw_ & 0xffff0000u, k); });
        tie(dg[0]); tie(dg[1]);
        unsigned fso, xso, ppi = ppo ^ (PK ? 1048576 : 32768), tn_[3] = {tagg & 0xf000fu, (tagg >> 8) & 0xf000fu, (tagg >> 4) & 0xf000fu}, psel = 0x05040100u;
        unsigned long long lo8 = 0x00ff00ff00ff00ffull, hi8 = ~lo8;
        ties(ppi); ties(tn_[0]); ties(tn_[1]); ties(tn_[2]); ties(psel); ties(lo8); ties(hi8);
        auto fill = [&](auto sc, auto pc) {
            constexpr int s = CV(sc), p = CV(pc);
            if constexpr (p == 0) dma_x<(2 * s % 3) * BUF, (s >> 1) * 16384 + (s & 1) * 262144, true>(fso, wl, l16, rws, hs0);
            else if constexpr (fill_m0(4 * s + p)) dma_m<(2 * s % 3) * BUF, p * 1024>(fso, wl, l16, rws);
            else dma_n<p * 1024>(fso, l16, rws);
        };
        bf16x4 hf[2][8];
        auto ldh = [&](auto nc, auto ic) {
            constexpr int n = CV(nc), i = CV(ic), j = (i >> 1) & 1, bo = (2 * (n >> 2) % 3) * BUF + (g2_unit(n, j) & 3) * 8192 + (i >> 2) * 4096, hi = bo >= 65536 ? 65536 : 0;
            dsr64tr<bo - hi>(hf[n & 1][i], ha[2 * g2_tile(n, j) + (i & 1) + (hi >> 14)]);
        };
        f32x4 yc[2][2][2];
        f32x4 im[2], ao, ob[2]; u32x4 pl[2], po;
        bf16x8 xf[2];
        u32x2 zv[4], qv[2];
        unsigned ck, ex, fail[2];
        float tq_[2], tz, sm[8], mx[2], cv[8];
        auto finB = [&](auto pc, auto jc) {
            constexpr int p = CV(pc), j = CV(jc);
            if constexpr (j < 8) { yc[p & 1][0][j & 1][j >> 1] += im[j & 1][j >> 1]; tie(yc[p & 1][0][j & 1]); }
            else {
                constexpr int i = (j - 8) >> 2, k = j & 3, dd = k & 1, h = k >> 1;
                ob[i][k] = sel8(yc[p & 1][0][dd][2 * i + 1 - h], yc[p & 1][0][dd][2 * i + h], h ? hi8 : lo8);
                if constexpr (k == 3 && (p == 3 || !(NOST & 4))) gst<0>(ob[i], a_ob, rso, s_b ^ ((p & 1) * 65536 + i * 16384));
                if constexpr (j == 12) keep(ob[0]);
            }
        };
        auto snd = [&](auto pc, auto jc) {
            constexpr int p = CV(pc), j = CV(jc);
            if constexpr (j < 8) { sm[j] = yc[p][0][j >> 2][j & 3] + im[j >> 2][j & 3]; tie(sm[j]); }
            if constexpr (j == 8) mx[0] = amax3(sm[0], sm[1], sm[2]);
            if constexpr (j == 9) mx[1] = amax3(sm[3], sm[4], sm[5]);
            if constexpr (j == 10) mx[0] = amax3(mx[0], sm[6], sm[7]);
            if constexpr (j == 11) mx[0] = amax3(mx[0], mx[1], mx[1]);
            if constexpr (j == 12) { ex = f2u(mx[0]) + 0x4000u; tie(ex); }
            if constexpr (j == 13) { ex &= 0x7f800000u; tie(ex); }
            if constexpr (j == 14) { mx[0] = u2f(ex + 0x04c00008u); tie(mx[0]); }
            if constexpr (j == 15) { ex >>= 23; tie(ex); }
            if constexpr (j >= 16 && j < 24) { sm[j - 16] += mx[0]; tie(sm[j - 16]); }
            if constexpr (j == 24) { ex |= ex << 12; tie(ex); }
            if constexpr (j >= 25 && j < 29) { po[j - 25] = __builtin_amdgcn_perm(f2u(sm[2 * (j - 25) + 1]), f2u(sm[2 * (j - 25)]), psel); tie(po); }
            if constexpr (j == 29) { po[0] = bfi_v(msk, po[0], ex); tie(po); }
            if constexpr (j >= 30 && j < NS) { po[j - 29] = bfi_s(msk, po[j - 29], tn_[j - 30]); if constexpr (j < NS - 1) tie(po); }
            if constexpr (j == NS - 1 && !(NOST & 2)) gst<p * 1024>(po, l16, rws, ppo);
        };
        auto rcv = [&](auto hc, auto jc) {
            constexpr int h = CV(hc), j = CV(jc);
            if constexpr (j < 3) { pl[h][j + 1] ^= tn_[j]; pp_tie<h>(pl[h]); }
            if constexpr (j == 3) { ck = pl[h][1] | pl[h][2] | pl[h][3]; tie(ck); }
            if constexpr (j == 4) { if constexpr (PB) ck = bfi_f(msk, fail[h], ck); else ck &= 0xf000fu; tie(ck); }
            if constexpr (j == 6 && !(NOST & 2)) pp_chk<h>(pl[h], ck, tagg, l16, rws, ppi, a_p);
            if constexpr (j == 7) { ex = pl[h][0] >> 12; tie(ex); }
            if constexpr (j == 8) ex = bfi_v(msk, ex, pl[h][0]);
            if constexpr (j == 9) { pl[h][0] &= msk; pp_tie<h>(pl[h]); }
            if constexpr (j == 10) { ex <<= 23; tie(ex); }
            if constexpr (j == 11) { ex &= 0x7f800000u; tie(ex); }
            if constexpr (j == 12) { mx[1] = u2f(ex) * 0x1p-14f; tie(mx[1]); }
            if constexpr (h == 0 && j >= 13) {
                constexpr int k = j - 13, a = (k + 1) / 2, e = k == 15 ? 7 : k / 2 - 1;
                if constexpr (k == 0 || ((k & 1) && k < 15)) cv[a] = cvt16<a & 1>(pl[h][a >> 1]);
                else { yc[0][0][e >> 2][e & 3] = fma_(cv[e], mx[1], yc[0][0][e >> 2][e & 3]); tie(yc[0][0][e >> 2]); }
            }
            if constexpr (h == 1 && j >= 13 && j < 21) cv[j - 13] = cvt16<(j - 13) & 1>(pl[h][(j - 13) >> 1]);
            if constexpr (h == 1 && j >= 21) { constexpr int e = j - 21; im[e >> 2][e & 3] = fma_(cv[e], mx[1], im[e >> 2][e & 3]); tie(im[e >> 2]); }
        };
        auto side = [&](auto pc) {
            constexpr int P = CV(pc), n = P >> 3, m = P & 7, p = n >> 3;
            if constexpr (P == P_X || P == P_X + 2) dma_x<ZST + (P != P_X) * 1024, (P != P_X) * 16384, false>(xso, w2k, a_x, rsx, s_x);
            if constexpr (P == P_W) pf_ld(pf, a_w, rws, ppo);
            if constexpr (!NOA) {
            if constexpr (P == P_Q || P == P_Q + 2) dma_x<QST + (P != P_Q) * 1024, (P != P_Q) * 524288, false>(xso, w2k, a_q, rsq, s_q);
            if constexpr (P == P_T) gst<0>(tg2, a_t, rws, zfo);
            if constexpr (P == AT(ZP, 4)) { if constexpr (P_W >= 0) pf_keep(pf); pf_ld(pf, a_f, rws, zfo); }
            if constexpr (P == P_ZC) { wait_v<g2_allow(P, AT(ZP, 4))>(); tag_chk(pf, expect, a_f, rws, zfo); }
            if constexpr (P == P_Z || P == P_Z + 2) dma_x<ZST + (P != P_Z) * 1024, (P != P_Z) * 65536, true>(xso, w2k, P == P_Z ? a_z : a_z2, rws, s_z);
            }
            if constexpr (P == AT(7, 5) || P == AT(7, 7)) dsr128<(m - 5) * 512>(xf[(m - 5) / 2], a_xr);
            if constexpr (P == AT(8, 4)) { tie(xf[0]); tie(xf[1]); }
            if constexpr (P == AT(8, 5) || P == AT(9, 3)) static_for<0, 2>([&](auto dc) { mfma<false>(yc[n & 1][0][CV(dc)], xf[n & 1], __builtin_bit_cast(bf16x8, dg[CV(dc)])); });
            if constexpr ((n & 7) == 6 && m == XW && !(DBG & 2)) dsw128<0>(a_iw, yc[p & 1][1][0]);
            if constexpr ((n & 7) == 6 && m == XW2 && !(DBG & 2)) dsw128<1024>(a_iw, yc[p & 1][1][1]);
            if constexpr ((n & 7) == 7 && (m & 5) == (n < 31 ? 4 : 0)) dsr128<(m & 2) * 512>(im[(m >> 1) & 1], a_ir);
            if constexpr (n >= 8 && (n & 7) == 0 && m == 4) { tie(im[0]); tie(im[1]); }
            if constexpr (P >= P_PP0 && P <= E_PP0) snd(IC(0), IC(P - P_PP0));
            if constexpr (P >= P_PP1 && P <= E_PP1) static_for<0, D1>([&](auto ic) { snd(IC(1), IC((P - P_PP1) * D1 + CV(ic))); });
            if constexpr (PB && (P == P_B0 - 1 || P == P_B1 - 1)) pf_zero(pf);
            if constexpr (PB && (P == P_B0 || P == P_B1)) pf_at<(P == P_B1) * 1024>(pf, a_p, rws, ppi);
            if constexpr (P == P_L0 || P == P_L1) {
                unsigned so = ppi;
                if constexpr (PB) { wait_v<g2_allow(P, P - QB) + (g2_fill(P) >= 0)>(); pp_sel(so, fail[P == P_L1], pf, tn_[2], ppi, ppo); }
                pp_ld<P == P_L1>(pl[P == P_L1], l16, rws, so);
            }
            if constexpr (P == P_R0 || P == P_R1) { wait_v<g2_allow(P, P == P_R0 ? imax(P_L0, XL ? P_X + 2 : 0) : P_L1)>(); pp_tie<P == P_R1>(pl[P == P_R1]); }
            if constexpr (P >= P_R0 && P <= E_R0) static_for<0, D0>([&](auto ic) { if constexpr ((P - P_R0) * D0 + CV(ic) < NR) rcv(IC(0), IC((P - P_R0) * D0 + CV(ic))); });
            if constexpr (P >= P_R1 && P < P_R1 + 21) rcv(IC(1), IC(P - P_R1));
            if constexpr (!NOA) {
            if constexpr (n == ZW && m >= 4) dsr64<(m - 4) * 128>(zv[m - 4], a_zr);
            static_for<0, 4>([&](auto rc) {
                constexpr int r = CV(rc);
                if constexpr (P == q_tie(r)) tie(qv[r & 1]);
                if constexpr (P == q_rd(r)) dsr64<r * 256>(qv[r & 1], a_qr);
            });
            if constexpr (P == AT(ZW + 1, 4)) { tie(zv[0]); tie(zv[1]); tie(zv[2]); tie(zv[3]); }
            }
            if constexpr (a_step(P) >= 0) {
                constexpr int s = a_step(P), e = s == 0 ? 0 : s % 3 == 2 ? (s + 1) / 3 : s % 3 == 1 ? s / 3 : s / 3 - 1, r = e >> 2, c = e & 3;
                if constexpr (s == 0 || (s % 3 == 2 && e < 16)) { tq_[e & 1] = (c & 1) ? bfhi(qv[r & 1][c >> 1]) : bflo(qv[r & 1][c >> 1]); tie(tq_[e & 1]); }
                else if constexpr (s % 3 == 1) { tz = (r & 1) ? bfhi(zv[c][r >> 1]) : bflo(zv[c][r >> 1]); tie(tz); }
                else if constexpr (s % 3 == 0) {
                    ao[c] = tq_[e & 1] * tz;
                    if constexpr (c == 3 && !(NOST & 1)) gst<0>(ao, a_oa, rso, s_a + r * 8192);
                    else tie(ao);
                }
            }
            if constexpr (P >= P_F2 && P < P_F2 + 16) finB(IC(2), IC(P - P_F2));
            if constexpr (a_step(P - 1) > 0 && a_step(P - 1) % 12 == 0) keep(ao);
            if constexpr (P == E_PP0 + 1 || P == E_PP1 + 1) keep(po);
            if constexpr (P == P_F2 + 16) keep(ob[1]);
        };
        {
            unsigned e_h = wid == 3 ? ((ln & 1) ? ~tagg : tagg) : 0u, hsl = lbase + lw * 8192, hso = (hp + ((tq + wid) & 7)) * 65536 + nh + qx * 16384;
            unsigned a_h = wid == 3 ? ln * 4 : OOB, s_h = WS_F + F_H + nr + hp * 32, ca2[4], a_g = lbase + HGO + ln * 4;
            static_for<0, 4>([&](auto ic) { ca2[CV(ic)] = cad[CV(ic)] + 2 * BUF; tie(ca2[CV(ic)]); });
            tie(e_h); tie(a_h); tie(a_g); ties(hsl); ties(hso); ties(s_h);
            sched();
            if (wid < 4) {
                auto look = [&]() { if constexpr (HA) pf_at<0>(pf, a_h, rws, s_h); else pf_ld(pf, a_h, rws, s_h); };
                if constexpr (HA) pf_zero(pf);
                if constexpr (PW == 0) { look(); wait_v<1>(); }
                if constexpr (PW == 1) { wait_v<0>(); look(); }
                if constexpr (PW == 2) { wait_v<8>(); look(); wait_v<1>(); }
                static_for<16, 32>([&](auto ic) { constexpr int i = CV(ic); dsr128u<((i >> 2) & 1) * 4096>(cf[i], (i & 8) ? cad[i & 3] : ca2[i & 3]); });
                wait_l<0>();
                static_for<16, 32>([&](auto ic) { tie(cf[CV(ic)]); });
                wait_v<0>();
                tag_chk<1, HA>(pf, e_h, a_h, rws, s_h);
                if (wid == 3) flag_set(a_g, 1u); else gate<1>(a_g, 1);
                TSX(6);
                hop<26>();
                static_for<0, 4>([&](auto ic) {
                    constexpr int i = CV(ic);
                    dma_x<(i >> 1) * 2 * BUF + (i & 1) * 4096, (i >> 1) * 262144 + (i & 1) * 4096, true>(fso, hsl, l16, rws, hso);
                    dma_n<1024>(fso, l16, rws); dma_n<2048>(fso, l16, rws); dma_n<3072>(fso, l16, rws);
                });
            }
        }
        wait_v<8>();
        if (wid < 4) TS(7);
        TG_DECL
        barrier();
        static_for<0, 8>([&](auto ic) { ldh(IC(0), ic); });
        static_for<0, 32>([&](auto nc) {
            constexpr int n = CV(nc), p = n >> 3;
            if constexpr (n == PG3) page<33>();
            if constexpr (n == PG4) page<37>();
            wait_lr<(n & 3) == 3 ? 0 : 4 + g2_lx(n - 1)>();
            if constexpr (n == 31) barrier();
            if constexpr ((n & 3) == 3 && n < 31) {
                constexpr int need = g2_need(n);
                TL(21 + n / 4 * 3);
                wait_v<g2_allow(8 * n, need)>();
                TL(22 + n / 4 * 3);
                barrier();
                TL(23 + n / 4 * 3);
            }
            if constexpr (n == 1) hop<31>();
            if constexpr (n == 4) hop<32>();
            if constexpr (n == 11) hop<34>();
            if constexpr (n == 15) hop<35>();
            if constexpr (n == 19) hop<36>();
            if constexpr (n == 27) hop<38>();
            if constexpr (n == 31) hop<39>();
            if constexpr (n == 0) TL(20);
            TG(20 + n);
            if constexpr (n == 31) TL(42);
            if constexpr (PRG) __builtin_amdgcn_s_setprio(n == 31 ? 0 : 3 - ((n + 1) & 3));
            sched();
            static_for<0, 8>([&](auto mc) {
                constexpr int m = CV(mc), ks = m >> 2, tt = (m >> 1) & 1, dd = m & 1, P = n * 8 + m;
                if constexpr (m == 4 && (n & 3) != 3) wait_lr<(rd1(n) ? 8 : 4) + ((n & 7) == 6 ? (XW < 4) + (XW2 < 4) : 0)>();
                mfma<(n & 7) == 0 && ks == 0>(yc[p & 1][(DBG & 1) ? tt : g2_tile(n, tt)][dd], cat(hf[n & 1][4 * ks + 2 * tt], hf[n & 1][4 * ks + 2 * tt + 1]), cf[g2_unit(n, tt) * 4 + 2 * ks + dd]);
                sched();
                if constexpr (n < 31 && rd1(n) && m < 4) { ldh(IC(n + 1), IC(2 * m)); ldh(IC(n + 1), IC(2 * m + 1)); }
                if constexpr (n < 31 && !rd1(n)) ldh(IC(n + 1), mc);
                if constexpr (g2_fill(P) >= 0) fill(IC(g2_fill(P) >> 2), IC(g2_fill(P) & 3));
                side(IC(P));
                sched();
            });
        });
        TG(52);
        if (wid >= 4) TSX(5); else TS(8);
        TG(53);
        static_for<256 - P_R1, 21>([&](auto jc) { rcv(IC(1), jc); });
        wait_l<0>();
        tie(im[0]); tie(im[1]);
        static_for<21, NR>([&](auto jc) { rcv(IC(1), jc); });
        asm volatile("s_nop 3");
        sched();
        static_for<0, 16>([&](auto jc) { finB(IC(3), jc); });
        if (b == 255 && wid == 0) gst<0>(u32x2{tagg, tagg}, l16 ? OOB : 0u, rws, WS_F + F_EP);
    }
}

}

#if defined(__HIP_DEVICE_COMPILE__)
#pragma clang attribute pop
#endif

extern "C" void kernel_launch(void* const* d_in, const int* in_sizes, int n_in, void* d_out, int out_size, void* d_ws, size_t ws_size,
                              hipStream_t stream) {
    (void)in_sizes; (void)n_in; (void)out_size; (void)ws_size;
    using namespace fk;
    const uint16_t* q = (const uint16_t*)d_in[0];
    const uint16_t* kin = (const uint16_t*)d_in[1];
    const uint16_t* vin = (const uint16_t*)d_in[2];
    const uint16_t* sk = (const uint16_t*)d_in[3];
    const uint16_t* ssmk = (const uint16_t*)d_in[4];
    const uint16_t* ssmD = (const uint16_t*)d_in[5];
    const uint16_t* x = (const uint16_t*)d_in[6];
    const uint16_t* lre = (const uint16_t*)d_in[7];
    const uint16_t* lim = (const uint16_t*)d_in[8];
    const uint16_t* bre = (const uint16_t*)d_in[9];
    const uint16_t* bim = (const uint16_t*)d_in[10];
    const uint16_t* cre = (const uint16_t*)d_in[11];
    const uint16_t* cim = (const uint16_t*)d_in[12];
    const uint16_t* logdt = (const uint16_t*)d_in[13];
    const uint16_t* dskip = (const uint16_t*)d_in[14];
#define WOFF 0
    char* ws = (char*)(((uintptr_t)d_ws + ((size_t)WOFF << 20) + 0x1fffff) & ~(uintptr_t)0x1fffff);
    hipLaunchKernelGGL(k1, dim3(256), dim3(512), 0, stream, x, bre, bim, lre, lim, logdt, ws, cre, cim, dskip, ssmk, ssmD, q, kin, vin, sk, (float*)d_out TS_PASS(0));
}
```

```cpp
#include <hip/hip_runtime.h>
#include <stdint.h>
#include <type_traits>

#if defined(__HIP_DEVICE_COMPILE__)
#pragma clang attribute push(__attribute__((target("no-packed-fp32-ops"))), apply_to = function)
#endif

namespace fk {

typedef __attribute__((ext_vector_type(8))) short bf16x8;
typedef __attribute__((ext_vector_type(4))) short bf16x4;
typedef __attribute__((ext_vector_type(4))) float f32x4;
typedef __attribute__((ext_vector_type(4))) unsigned u32x4;
typedef __attribute__((ext_vector_type(16))) unsigned u32x16;
typedef __attribute__((ext_vector_type(2))) unsigned u32x2;
typedef __attribute__((ext_vector_type(2))) __bf16 bf16x2_t;
typedef __attribute__((address_space(1))) unsigned gu32;
typedef __attribute__((address_space(1))) unsigned long long gu64;
#define RLX_AGENT __ATOMIC_RELAXED, __HIP_MEMORY_SCOPE_AGENT

__device__ __forceinline__ float u2f(unsigned v) { return __builtin_bit_cast(float, v); }
__device__ __forceinline__ unsigned f2u(float v) { return __builtin_bit_cast(unsigned, v); }
__device__ __forceinline__ bool wall(bool p) { return __builtin_amdgcn_ballot_w64(p) == ~0ull; }
__device__ __forceinline__ float bflo(unsigned w) { return u2f(w << 16); }
__device__ __forceinline__ float bfhi(unsigned w) { return u2f(w & 0xffff0000u); }
__device__ __forceinline__ unsigned pack2(float lo, float hi) {
    bf16x2_t v; v[0] = (__bf16)lo; v[1] = (__bf16)hi; return __builtin_bit_cast(unsigned, v);
}
__device__ __forceinline__ float rbf(float f) { float r; asm("v_cvt_pk_bf16_f32 %0, 0, %1" : "=v"(r) : "v"(f)); return r; }
__device__ __forceinline__ float fma_(float a, float b, float c) { return __builtin_fmaf(a, b, c); }

template <int I, int N, class F>
__device__ __forceinline__ void static_for(F&& f) {
    if constexpr (I + 1 == N) f(std::integral_constant<int, I>{});
    else if constexpr (I < N) { static_for<I, I + (N - I) / 2>(f); static_for<I + (N - I) / 2, N>(f); }
}
#define IC(v) std::integral_constant<int, (v)>{}
#define CV(c) decltype(c)::value

#define TS_ARG
#define TS_ARG0
#define TS_PASS(off)
#define TS_DECL
#define TS(i)
#define TSV(i, v)
#define TL_DECL
#define TL(i)
#define TS_FLUSH(slot)

#define TG_DECL
#define TG(i)
template <int N> __device__ __forceinline__ void wait_v() { asm volatile("s_waitcnt vmcnt(%0)" ::"n"(N > 63 ? 63 : N) : "memory"); }
template <int N> __device__ __forceinline__ void wait_l() { asm volatile("s_waitcnt lgkmcnt(%0)" ::"n"(N > 14 ? 14 : N) : "memory"); }
template <int N> __device__ __forceinline__ void wait_lr() { __builtin_amdgcn_s_waitcnt(0xc07f | ((N > 14 ? 14 : N) << 8)); }
template <class T> __device__ __forceinline__ void tie(T& x) { asm volatile("" : "+v"(x)); }
template <class T> __device__ __forceinline__ void undef(T& x) { asm volatile("" : "=v"(x)); }
template <int OFF, class T> __device__ __forceinline__ void dsr128u(T& d, unsigned a) { asm volatile("ds_read_b128 %0, %1 offset:%2" : "+v"(d) : "v"(a), "n"(OFF)); }
template <class T> __device__ __forceinline__ void keep(const T& x) { asm volatile("" :: "v"(x)); }
template <int OFF, class T> __device__ __forceinline__ void dsr128(T& d, unsigned a) { asm volatile("ds_read_b128 %0, %1 offset:%2" : "=v"(d) : "v"(a), "n"(OFF)); }
template <int OFF, class T> __device__ __forceinline__ void dsr64(T& d, unsigned a) { asm volatile("ds_read_b64 %0, %1 offset:%2" : "=v"(d) : "v"(a), "n"(OFF)); }
template <int OFF> __device__ __forceinline__ void dsr32(unsigned& d, unsigned a) { asm volatile("ds_read_b32 %0, %1 offset:%2" : "=v"(d) : "v"(a), "n"(OFF)); }
template <int O0, int O1, class T> __device__ __forceinline__ void dsr2(T& d, unsigned a) { asm volatile("ds_read2_b32 %0, %1 offset0:%2 offset1:%3" : "=v"(d) : "v"(a), "n"(O0), "n"(O1)); }
template <int OFF> __device__ __forceinline__ void dsr64tr(bf16x4& d, unsigned a) { asm volatile("ds_read_b64_tr_b16 %0, %1 offset:%2" : "=v"(d) : "v"(a), "n"(OFF)); }
template <int OFF, class T> __device__ __forceinline__ void dsw128(unsigned a, const T& v) { asm volatile("ds_write_b128 %0, %1 offset:%2" :: "v"(a), "v"(v), "n"(OFF) : "memory"); }
template <int OFF> __device__ __forceinline__ void dsw32(unsigned a, unsigned v) { asm volatile("ds_write_b32 %0, %1 offset:%2" :: "v"(a), "v"(v), "n"(OFF) : "memory"); }
__device__ __forceinline__ bf16x8 cat(const bf16x4& lo, const bf16x4& hi) { return __builtin_shufflevector(lo, hi, 0, 1, 2, 3, 4, 5, 6, 7); }
__device__ __forceinline__ void barrier() { asm volatile("" ::: "memory"); __builtin_amdgcn_s_barrier(); asm volatile("" ::: "memory"); }
__device__ __forceinline__ int fresh(int v) { asm volatile("" : "+s"(v)); return v; }
__device__ __forceinline__ void sched() { __builtin_amdgcn_sched_barrier(0); }
template <bool FIRST> __device__ __forceinline__ void mfma(f32x4& c, const bf16x8& a, const bf16x8& b) {
    if constexpr (FIRST) asm volatile("v_mfma_f32_16x16x32_bf16 %0, %1, %2, 0" : "=v"(c) : "v"(a), "v"(b));
    else asm volatile("v_mfma_f32_16x16x32_bf16 %0, %1, %2, %0" : "+v"(c) : "v"(a), "v"(b));
}
struct cf32 { float r, i; };
__device__ __forceinline__ cf32 cmul(cf32 a, cf32 b) { return cf32{fma_(a.r, b.r, -(a.i * b.i)), fma_(a.r, b.i, a.i * b.r)}; }
__device__ __forceinline__ cf32 cfma(cf32 a, cf32 b, cf32 c) { return cf32{fma_(a.r, b.r, fma_(-a.i, b.i, c.r)), fma_(a.r, b.i, fma_(a.i, b.r, c.i))}; }
__device__ __forceinline__ void sw16(float v, float& e, float& o) { const auto r = __builtin_amdgcn_permlane16_swap(f2u(v), f2u(v), false, false); e = u2f(r[0]); o = u2f(r[1]); }
__device__ __forceinline__ void sw32(float v, float& e, float& o) { const auto r = __builtin_amdgcn_permlane32_swap(f2u(v), f2u(v), false, false); e = u2f(r[0]); o = u2f(r[1]); }
constexpr unsigned SPIN_CAP = 1u << 16;
constexpr unsigned OOB = 0x40000000u;

constexpr size_t WS_H = 0;
constexpr size_t WS_KVT = (size_t)8 << 20;
constexpr size_t WS_ZT = (size_t)12 << 20;
constexpr size_t WS_PP = (size_t)16 << 20;
constexpr size_t WS_E = (size_t)24 << 20;
constexpr size_t WS_F = WS_E + (256 << 10);
constexpr int F_H = 0;
constexpr int F_K = 4096;
constexpr int F_Z = 16384;
constexpr int F_EP = F_Z + 32768;
constexpr int F_C = F_EP + 256;
constexpr unsigned WS_P = 25 << 20;
#define KP 0
#define SU 0
#define TSX(i) do { if constexpr (!SU) TS(i); } while (0)
#define AB 1
#define SW 1
#define BL 0
#define GS 0
#define LC 0
#define CR 1
#define CD 2
#define SU0 190
#define SUG 3
#define SK0 7
#define SK1 12
#define PGI 2
#define SV0 0
#define KCQ 2
#define KCC 4
#define KVP 0
#define RSL 1
#define PB 0
#define KTI 1
#define TM 0
#define TX 0
#define PK 1
#define HA 0
#define KVS 1
#define GCR 273
constexpr int LDS_TOTAL = 163840, BUF = 32768;
constexpr int KC = 98304, KCH = 8320, KCP = 4160;
constexpr int U0 = KC + 4 * KCH;
constexpr int FULL = U0 + 18432 + 8192, FST = 388;
constexpr int DONE = U0 + 18432, DUMP = DONE + 4096;
constexpr int HGO = FULL + 1792, DSK = HGO + 256, POLL = DSK + 256;
static_assert(POLL + 8 * 256 <= LDS_TOTAL && 4 * FST <= 1792, "lds");
constexpr int g1_buf(int s) { return ((0x0213 >> ((s & 3) * 4)) & 3) * BUF; }
constexpr int IMG = KC, QST = KC + 16384, ZST = U0, PLD = 2 * BUF;
#define CODE_KB 43
#define EXACT 0
#define HP 1
#define CP 2
#define FLQ 11
#define R01 3
#define ZP 7
#define SLP 8
#define STR_(x) #x
#define STR(x) STR_(x)
#define NOST 0
#define G2M 0
#define RD 2
#define PRG 1
#define OSC 16
#define NOA 0
#define PPH 0
#define HS 0
#define JQ(j) (HS == 0 ? 0 : HS == 1 ? (j) & 1 : HS == 2 ? (j) : (j) & 2)
#define JR(j) (HS == 0 ? 2 * (j) : HS == 1 ? 2 * ((j) & 2) : HS == 2 ? 0 : 4 * ((j) & 1))
#define NOX 0

__device__ __forceinline__ u32x4 mk_rsrc(const void* p, unsigned bytes) {
    const unsigned long long a = (unsigned long long)p;
    return u32x4{(unsigned)a, (unsigned)(a >> 32) & 0xffffu, bytes, 0x00020000u};
}
__device__ __forceinline__ __amdgpu_buffer_rsrc_t mk_brs(const void* p, unsigned bytes) { return __builtin_amdgcn_make_buffer_rsrc((void*)p, (short)0, bytes, 0x00020000); }
__device__ __forceinline__ void st16(const u32x4& d, __amdgpu_buffer_rsrc_t r, unsigned voff, unsigned soff) {
    __builtin_amdgcn_raw_buffer_store_b128(d, r, voff, soff, 16);
    asm volatile("s_nop 1" :: "v"(d) : "memory");
}
template <int IMM, bool SC1> __device__ __forceinline__ void dma(unsigned lds, unsigned voff, const u32x4& rs, unsigned soff) {
    if constexpr (SC1) asm volatile("s_add_u32 m0, %0, %1\n\ts_nop 0\n\tbuffer_load_dwordx4 %2, %3, %4 offen sc1 lds" :: "s"(lds), "n"(IMM), "v"(voff), "s"(rs), "s"(soff) : "memory", "scc");
    else asm volatile("s_add_u32 m0, %0, %1\n\ts_nop 0\n\tbuffer_load_dwordx4 %2, %3, %4 offen lds" :: "s"(lds), "n"(IMM), "v"(voff), "s"(rs), "s"(soff) : "memory", "scc");
}
template <int IMM, int I> __device__ __forceinline__ void dma4(unsigned lds, unsigned voff, const u32x4& rs, unsigned soff) {
    if constexpr (I == 0) dma<IMM, false>(lds, voff, rs, soff);
    else asm volatile("buffer_load_dwordx4 %0, %1, %2 offen offset:%3 lds" :: "v"(voff), "s"(rs), "s"(soff), "n"(I * 1024) : "memory");
}
template <int IMM> __device__ __forceinline__ void dma1(unsigned lds, unsigned voff, const u32x4& rs, unsigned soff) {
    asm volatile("s_add_u32 m0, %0, %1\n\ts_nop 0\n\tbuffer_load_dword %2, %3, %4 offen sc1 lds" :: "s"(lds), "n"(IMM), "v"(voff), "s"(rs), "s"(soff) : "memory", "scc");
}
__device__ __forceinline__ void flag_set(unsigned waddr, unsigned v) { asm volatile("ds_write_b32 %0, %1" :: "v"(waddr), "v"(v) : "memory"); }
__device__ __forceinline__ void flag_chk(unsigned& f, unsigned raddr, unsigned need) {
    unsigned cnt;
    asm volatile("v_cmp_le_u32 vcc, %3, %0\n\t"
                 "s_cmp_eq_u64 vcc, -1\n\t"
                 "s_cbranch_scc1 1f\n\t"
                 "s_mov_b32 %1, 0x100000\n"
                 "0:\n\t"
                 "ds_read_b32 %0, %2\n\t"
                 "s_sub_u32 %1, %1, 1\n\t"
                 "s_waitcnt lgkmcnt(0)\n\t"
                 "s_cbranch_scc1 1f\n\t"
                 "v_cmp_le_u32 vcc, %3, %0\n\t"
                 "s_cmp_eq_u64 vcc, -1\n\t"
                 "s_cbranch_scc0 0b\n"
                 "1:" : "+v"(f), "=&s"(cnt) : "v"(raddr), "s"(need) : "vcc", "scc", "memory");
}
template <int OFF, int SL = 0> __device__ __forceinline__ void zero_chk(unsigned& f, unsigned raddr) {
    unsigned cnt;
    asm volatile("v_cmp_ne_u32 vcc, 0, %0\n\t"
                 "s_cbranch_vccz 1f\n\t"
                 "s_mov_b32 %1, 0x100000\n"
                 "0:\n\t"
                 ".if %4\n\ts_sleep %4\n\t.endif\n\t"
                 "ds_read_b32 %0, %2 offset:%3\n\t"
                 "s_sub_u32 %1, %1, 1\n\t"
                 "s_waitcnt lgkmcnt(0)\n\t"
                 "s_cbranch_scc1 1f\n\t"
                 "v_cmp_ne_u32 vcc, 0, %0\n\t"
                 "s_cbranch_vccnz 0b\n"
                 "1:" : "+v"(f), "=&s"(cnt) : "v"(raddr), "n"(OFF), "n"(SL) : "vcc", "scc", "memory");
}
template <int IMM, class T> __device__ __forceinline__ void gld(T& d, unsigned voff, const u32x4& rs, unsigned soff) {
    if constexpr (sizeof(T) == 16) asm volatile("buffer_load_dwordx4 %0, %1, %2, %3 offen offset:%4" : "=v"(d) : "v"(voff), "s"(rs), "s"(soff), "n"(IMM) : "memory");
    else if constexpr (sizeof(T) == 8) asm volatile("buffer_load_dwordx2 %0, %1, %2, %3 offen offset:%4" : "=v"(d) : "v"(voff), "s"(rs), "s"(soff), "n"(IMM) : "memory");
    else asm volatile("buffer_load_ushort %0, %1, %2, %3 offen offset:%4" : "=v"(d) : "v"(voff), "s"(rs), "s"(soff), "n"(IMM) : "memory");
}
__device__ __forceinline__ void gld32(unsigned& d, unsigned voff, const u32x4& rs, unsigned soff) { asm volatile("buffer_load_dword %0, %1, %2, %3 offen" : "=v"(d) : "v"(voff), "s"(rs), "s"(soff) : "memory"); }
__device__ __forceinline__ void sld(unsigned& d, const void* p) { asm volatile("s_load_dword %0, %1, 0x0" : "=s"(d) : "s"(p) : "memory"); }
__device__ __forceinline__ void stouch(unsigned long long p) { asm volatile("s_load_dword s101, %0, 0x0\n\ts_load_dword s101, %0, 0x80\n\ts_load_dword s101, %0, 0x100" :: "s"(p) : "s101", "memory"); }
template <class T> __device__ __forceinline__ void ties(T& x) { asm volatile("" : "+s"(x)); }
template <int IMM, class T> __device__ __forceinline__ void gst(const T& d, unsigned voff, const u32x4& rs, unsigned soff) {
    if constexpr (sizeof(T) == 16) asm volatile("buffer_store_dwordx4 %0, %1, %2, %3 offen offset:%4 sc1" :: "v"(d), "v"(voff), "s"(rs), "s"(soff), "n"(IMM) : "memory");
    else asm volatile("buffer_store_dwordx2 %0, %1, %2, %3 offen offset:%4 sc1" :: "v"(d), "v"(voff), "s"(rs), "s"(soff), "n"(IMM) : "memory");
}
template <int SLQ> __device__ __forceinline__ void gate(unsigned raddr, unsigned need) {
    unsigned cnt, f;
    asm volatile("s_mov_b32 %1, 0x40000\n"
                 "0:\n\t"
                 "ds_read_b32 %0, %2\n\t"
                 "s_sub_u32 %1, %1, 1\n\t"
                 "s_waitcnt lgkmcnt(0)\n\t"
                 "s_cbranch_scc1 1f\n\t"
                 "v_cmp_le_u32 vcc, %3, %0\n\t"
                 "s_cmp_eq_u64 vcc, -1\n\t"
                 "s_cbranch_scc1 1f\n\t"
                 "s_sleep %4\n\t"
                 "s_branch 0b\n"
                 "1:" : "=&v"(f), "=&s"(cnt) : "v"(raddr), "s"(need), "n"(SLQ) : "vcc", "scc", "memory");
}
template <int SL = SLP, int AT_ = 0> __device__ __forceinline__ void tag_chk(unsigned& f, unsigned expect, unsigned voff, const u32x4& rs, unsigned soff) {
    unsigned cnt;
    asm volatile("v_cmp_eq_u32 vcc, %2, %0\n\t"
                 "s_cmp_eq_u64 vcc, -1\n\t"
                 "s_cbranch_scc1 1f\n\t"
                 "s_mov_b32 %1, 0x10000\n"
                 "0:\n\t"
                 ".if %7\n\tv_mov_b32 %0, 0\n\t.endif\n\t"
                 "s_sleep %6\n\t"
                 ".if %7\n\tbuffer_atomic_or %0, %3, %4, %5 offen sc0 sc1\n\t.else\n\tbuffer_load_dword %0, %3, %4, %5 offen sc1\n\t.endif\n\t"
                 "s_sub_u32 %1, %1, 1\n\t"
                 "s_waitcnt vmcnt(0)\n\t"
                 "s_cbranch_scc1 1f\n\t"
                 "v_cmp_eq_u32 vcc, %2, %0\n\t"
                 "s_cmp_eq_u64 vcc, -1\n\t"
                 "s_cbranch_scc0 0b\n"
                 "1:" : "+v"(f), "=&s"(cnt) : "v"(expect), "v"(voff), "s"(rs), "s"(soff), "n"(SL), "n"(AT_) : "vcc", "scc", "memory");
}

__device__ __forceinline__ unsigned race(unsigned w, const void* ws, unsigned long long rnd) {
    unsigned v;
    asm volatile("s_cmp_lt_u32 %1, 32\n\t"
                 "s_cbranch_scc0 1f\n\t"
                 "s_waitcnt lgkmcnt(0)\n\t"
                 "s_mov_b64 s[98:99], %3\n\t"
                 "s_lshl_b32 s96, s98, 7\n\t"
                 "s_lshl_b32 s97, %1, 13\n\t"
                 "s_andn2_b32 s96, s96, 0x2000\n\t"
                 "s_xor_b32 s96, s96, s97\n\t"
                 "s_and_b32 s96, s96, 0x7ff80\n\t"
                 "s_add_u32 s96, s96, %4\n\t"
                 "s_xor_b32 s97, s96, 0x2000\n\t"
                 "s_mov_b32 %0, s98\n\t"
                 "s_load_dword s99, %2, s96\n\t"
                 "s_load_dword s98, %2, s97\n\t"
                 "s_lshr_b32 s96, %1, 4\n\t"
                 "s_xor_b32 s96, s96, %1\n\t"
                 "s_waitcnt lgkmcnt(1)\n\t"
                 "s_sleep %5\n\t"
                 "s_cmp_lg_u32 %0, s98\n\t"
                 "s_addc_u32 %0, s96, 0\n\t"
                 "s_and_b32 %0, %0, 1\n"
                 "1:" : "=&s"(v) : "s"(w), "s"(ws), "s"(rnd), "n"(WS_P), "n"(RSL) : "s96", "s97", "s98", "s99", "scc", "memory");
    return v;
}
__device__ __forceinline__ void vote_chk(unsigned& f, unsigned expect, unsigned odd, unsigned raddr, unsigned lds, unsigned voff, const u32x4& rs, unsigned soff, unsigned& cnt) {
    unsigned t;
    asm volatile("s_mov_b32 %1, 0x10000\n\t"
                 "v_xor_b32 %2, %0, %3\n\t"
                 "v_cmp_gt_u32 vcc, %2, %4\n\t"
                 "s_cbranch_vccz 1f\n\t"
                 "s_mov_b32 m0, %6\n"
                 "0:\n\t"
                 "s_sleep 4\n\t"
                 "buffer_load_dword %7, %8, %9 offen sc1 lds\n\t"
                 "s_sub_u32 %1, %1, 1\n\t"
                 "s_waitcnt vmcnt(0)\n\t"
                 "ds_read_b32 %0, %5\n\t"
                 "s_waitcnt lgkmcnt(0)\n\t"
                 "s_cbranch_scc1 1f\n\t"
                 "v_xor_b32 %2, %0, %3\n\t"
                 "v_cmp_gt_u32 vcc, %2, %4\n\t"
                 "s_cbranch_vccnz 0b\n"
                 "1:" : "+v"(f), "=&s"(cnt), "=&v"(t) : "v"(expect), "v"(odd), "v"(raddr), "s"(lds), "v"(voff), "s"(rs), "s"(soff) : "vcc", "scc", "memory");
}
__device__ __forceinline__ unsigned vote_cnt(unsigned f, unsigned tag, unsigned up, unsigned& n) {
    unsigned r;
    asm volatile("s_xnor_b32 %0, %3, 1\n\tv_cmp_eq_u32 vcc, %0, %2\n\ts_bcnt1_i32_b64 %1, vcc\n\ts_cmp_gt_u32 %1, 16\n\ts_cselect_b32 %0, 0x2000, 0\n\ts_xor_b32 %0, %0, %4"
                 : "=&s"(r), "=&s"(n) : "v"(f), "s"(tag), "s"(up) : "vcc", "scc");
    return r;
}

template <int H> __device__ __forceinline__ void pp_ld(u32x4& d, unsigned voff, const u32x4& rs, unsigned soff) {
    if constexpr (H) asm volatile("buffer_load_dwordx4 v[244:247], %1, %2, %3 offen offset:1024 sc1" : "={v[244:247]}"(d) : "v"(voff), "s"(rs), "s"(soff) : "memory");
    else asm volatile("buffer_load_dwordx4 v[240:243], %1, %2, %3 offen sc1" : "={v[240:243]}"(d) : "v"(voff), "s"(rs), "s"(soff) : "memory");
}
__device__ __forceinline__ void pf_ld(unsigned& d, unsigned voff, const u32x4& rs, unsigned soff) { asm volatile("buffer_load_dword v248, %1, %2, %3 offen sc1" : "={v248}"(d) : "v"(voff), "s"(rs), "s"(soff) : "memory"); }
__device__ __forceinline__ void pf_keep(unsigned d) { asm volatile("" :: "{v248}"(d)); }
__device__ __forceinline__ void pf_zero(unsigned& d) { asm volatile("v_mov_b32 v248, 0" : "={v248}"(d)); }
template <int OFF> __device__ __forceinline__ void pf_at(unsigned& d, unsigned voff, const u32x4& rs, unsigned soff) {
    asm volatile("buffer_atomic_or v248, %1, %2, %3 offen offset:%4 sc0 sc1" : "+{v248}"(d) : "v"(voff), "s"(rs), "s"(soff), "n"(OFF) : "memory");
}
__device__ __forceinline__ void pp_sel(unsigned& so, unsigned& fail, unsigned d, unsigned tn, unsigned yes, unsigned no) {
    asm volatile("v_readlane_b32 %1, v248, 63\n\ts_and_b32 %1, %1, 0xf000f\n\ts_cmp_eq_u32 %1, %3\n\ts_cselect_b32 %0, %4, %5\n\ts_cselect_b32 %1, 0, -1"
                 : "=&s"(so), "=&s"(fail) : "{v248}"(d), "s"(tn), "s"(yes), "s"(no) : "scc");
}
#define PP_CHK(R, R1, R2, R3, OFF) \
    asm volatile("v_cmp_ne_u32 vcc, 0, %2\n\t" \
                 "s_cbranch_vccz 1f\n\t" \
                 "s_mov_b32 %1, 0x10000\n" \
                 "0:\n\t" \
                 ".if %9\n\t" \
                 "v_mov_b32 %2, 0\n\t" \
                 "s_sub_u32 %1, %1, 1\n\t" \
                 "s_cbranch_scc1 1f\n\t" \
                 "buffer_atomic_or %2, %8, %6, %7 offen offset:" OFF " sc0 sc1\n\t" \
                 "s_lshr_b32 vcc_lo, %4, 4\n\t" \
                 "s_waitcnt vmcnt(0)\n\t" \
                 "v_readlane_b32 %3, %2, 63\n\t" \
                 "s_xor_b32 %3, %3, vcc_lo\n\t" \
                 "s_and_b32 %3, %3, 0xf000f\n\t" \
                 "s_cbranch_scc1 0b\n\t" \
                 ".endif\n\t" \
                 "buffer_load_dwordx4 " R ", %5, %6, %7 offen offset:" OFF " sc1\n\t" \
                 "s_waitcnt vmcnt(0)\n\t" \
                 "s_and_b32 %3, %4, 0xf000f\n\t" \
                 "v_xor_b32 " R1 ", %3, " R1 "\n\t" \
                 "s_lshr_b32 %3, %4, 8\n\t" \
                 "s_and_b32 %3, %3, 0xf000f\n\t" \
                 "v_xor_b32 " R2 ", %3, " R2 "\n\t" \
                 "s_lshr_b32 %3, %4, 4\n\t" \
                 "s_and_b32 %3, %3, 0xf000f\n\t" \
                 "v_xor_b32 " R3 ", %3, " R3 "\n\t" \
                 "v_or3_b32 %2, " R1 ", " R2 ", " R3 "\n\t" \
                 "v_and_b32 %2, 0xf000f, %2\n\t" \
                 "s_sub_u32 %1, %1, 1\n\t" \
                 "s_cbranch_scc1 1f\n\t" \
                 "v_cmp_ne_u32 vcc, 0, %2\n\t" \
                 "s_cbranch_vccnz 0b\n" \
                 "1:" : "+{" R "}"(p), "=&s"(cnt), "+v"(bad), "=&s"(t) : "s"(tag), "v"(voff), "s"(rs), "s"(soff), "v"(vp), "n"(PB) : "vcc", "scc", "memory")
template <int H> __device__ __forceinline__ void pp_chk(u32x4& p, unsigned bad, unsigned tag, unsigned voff, const u32x4& rs, unsigned soff, unsigned vp) {
    unsigned cnt, t;
    if constexpr (H) PP_CHK("v[244:247]", "v245", "v246", "v247", "1024"); else PP_CHK("v[240:243]", "v241", "v242", "v243", "0");
}
template <int H> __device__ __forceinline__ void pp_tie(u32x4& p) { if constexpr (H) asm volatile("" : "+{v[244:247]}"(p)); else asm volatile("" : "+{v[240:243]}"(p)); }
__device__ __forceinline__ float amax3(float a, float b, float c) { float r; asm volatile("v_max3_f32 %0, |%1|, |%2|, |%3|" : "=v"(r) : "v"(a), "v"(b), "v"(c)); return r; }
__device__ __forceinline__ unsigned bfi_v(unsigned m, unsigned a, unsigned b) { unsigned r; asm volatile("v_bfi_b32 %0, %1, %2, %3" : "=v"(r) : "v"(m), "v"(a), "v"(b)); return r; }
__device__ __forceinline__ unsigned bfi_s(unsigned m, unsigned a, unsigned b) { unsigned r; asm volatile("v_bfi_b32 %0, %1, %2, %3" : "=v"(r) : "v"(m), "v"(a), "s"(b)); return r; }
__device__ __forceinline__ unsigned bfi_f(unsigned m, unsigned a, unsigned b) { unsigned r; asm volatile("v_bfi_b32 %0, %1, %2, %3" : "=v"(r) : "v"(m), "s"(a), "v"(b)); return r; }
template <int H> __device__ __forceinline__ float cvt16(unsigned w) {
    float r;
    if constexpr (H) asm volatile("v_cvt_f32_i32_sdwa %0, sext(%1) dst_sel:DWORD dst_unused:UNUSED_PAD src0_sel:WORD_1" : "=v"(r) : "v"(w));
    else asm volatile("v_cvt_f32_i32_sdwa %0, sext(%1) dst_sel:DWORD dst_unused:UNUSED_PAD src0_sel:WORD_0" : "=v"(r) : "v"(w));
    return r;
}
__device__ __forceinline__ float sel8(float a, float b, unsigned long long mask) {
    float r;
    asm volatile("s_mov_b64 vcc, %3\n\tv_cndmask_b32_dpp %0, %1, %2, vcc row_ror:8 row_mask:0xf bank_mask:0xf" : "=v"(r) : "v"(a), "v"(b), "s"(mask) : "vcc");
    return r;
}
__device__ __forceinline__ unsigned sel0(unsigned a, unsigned long long mask) { unsigned r; asm volatile("v_cndmask_b32_e64 %0, 0, %1, %2" : "=v"(r) : "v"(a), "s"(mask)); return r; }
constexpr unsigned long long diag_mask(int R) {
    unsigned long long m = 0;
    for (int l = 0; l < 64; ++l) if ((l >> 4) == ((l & 7) >> 1) && 2 * (l & 1) + ((l >> 3) & 1) == R) m |= 1ull << l;
    return m;
}
template <int IMM, int X, bool SC1> __device__ __forceinline__ void dma_x(unsigned& so, unsigned lds, unsigned voff, const u32x4& rs, unsigned base) {
    if constexpr (SC1) asm volatile("s_add_u32 m0, %1, %2\n\ts_xor_b32 %0, %5, %6\n\tbuffer_load_dwordx4 %3, %4, %0 offen sc1 lds" : "=&s"(so) : "s"(lds), "n"(IMM), "v"(voff), "s"(rs), "s"(base), "n"(X) : "memory", "scc");
    else asm volatile("s_add_u32 m0, %1, %2\n\ts_xor_b32 %0, %5, %6\n\tbuffer_load_dwordx4 %3, %4, %0 offen lds" : "=&s"(so) : "s"(lds), "n"(IMM), "v"(voff), "s"(rs), "s"(base), "n"(X) : "memory", "scc");
}
template <int OFF> __device__ __forceinline__ void dma_n(unsigned so, unsigned voff, const u32x4& rs) {
    asm volatile("buffer_load_dwordx4 %0, %1, %2 offen offset:%3 sc1 lds" :: "v"(voff), "s"(rs), "s"(so), "n"(OFF) : "memory");
}
template <int IMM, int OFF> __device__ __forceinline__ void dma_m(unsigned so, unsigned lds, unsigned voff, const u32x4& rs) {
    asm volatile("s_add_u32 m0, %4, %5\n\ts_nop 0\n\tbuffer_load_dwordx4 %0, %1, %2 offen offset:%3 sc1 lds" :: "v"(voff), "s"(rs), "s"(so), "n"(OFF), "s"(lds), "n"(IMM) : "memory", "scc");
}
__device__ __forceinline__ unsigned and_or(unsigned a, unsigned m, unsigned o) { unsigned r; asm("v_and_or_b32 %0, %1, %2, %3" : "=v"(r) : "v"(a), "v"(m), "s"(o)); return r; }

#define HOPS 1
#define HM 0x1ff221125B082ull
#define PG0 63
#define PG7 287
#define PG1 95
#define PG2 288
#define PG3 4
#define PG4 21
#define SQ 52
#define P18 -1
#define WS0 0
#define SLM 0x12234566ull
#define PGF 4
#define HQ 84
#define HW 4
constexpr int hop_nth(int n) { for (int k = 0; k < 62; ++k) if (((HM >> k) & 1) && n-- == 0) return k; return 63; }
constexpr int hop_next(int K) { int n = 0; for (int k = 0; k < K; ++k) n += (HM >> k) & 1; return hop_nth(n + HW); }
template <int K> __device__ __forceinline__ void hop() {
    if constexpr (HOPS && ((HM >> K) & 1)) asm volatile(".Lfkh%0:\n\ts_cbranch_execz .Lfkh%1" :: "n"(K), "n"(hop_next(K)));
}
template <int K> __device__ __forceinline__ void page() {
    if constexpr (HOPS && K >= 0 && ((HM >> (K & 63)) & 1)) asm volatile("s_branch 1f\n\t.p2align 12\n.Lfkh%0:\n\ts_cbranch_execz .Lfkh%1\n1:" :: "n"(K), "n"(hop_next(K)));
    else if constexpr (HOPS) asm volatile("s_branch 1f\n\t.p2align 12\n1:");
}
__device__ __forceinline__ void hop_all(int w) {
    if constexpr (HOPS) asm volatile("s_mov_b64 exec, 0\n\ts_cmp_eq_u32 %0, 0\n\ts_cbranch_scc1 .Lfkh%1\n\ts_cmp_eq_u32 %0, 1\n\ts_cbranch_scc1 .Lfkh%2\n\t"
                                     "s_cmp_eq_u32 %0, 2\n\ts_cbranch_scc1 .Lfkh%3\n\ts_branch .Lfkh%4\n.Lfkh63:\n\ts_mov_b64 exec, -1"
                                     :: "s"(w), "n"(hop_nth(0)), "n"(hop_nth(1)), "n"(hop_nth(2)), "n"(hop_nth(3)) : "scc");
}
#define NB 1
#define PFQ 0
#define CVR 3
#define SPR 3
#define ELP 8
#define PW 0
#define CPG 95
#define GQ 108
#define GE 3
#define KDL 0
#define GP 1
#define SG 507
#define KO 0
#define DP 0
#define B13 460
#define PRE 0
enum { OP_NONE, OP_A, OP_B, OP_FLAG, OP_SIG, OP_VOTE };
struct Op { int kind, a, b; };
constexpr Op g1_op(int G) {
    const int st = (G + 32) / 32 - 1, q = G - 32 * st, i = q >> 2, j = q & 3;
    if (i < 5) {
        if (st < 0) return {OP_NONE, 0, 0};
        if ((j & 1) == 0) return {OP_A, st * 8 + i + 3, j >> 1};
        if (q == FLQ && st < 15) return {OP_FLAG, 0, 0};
        if (NB && G == SG) return {OP_SIG, 2, 0};
        if (G == GCR) return {OP_VOTE, 0, 0};
        if (NB == 2 && G == 435) return {OP_SIG, 3, 0};
        return {OP_NONE, 0, 0};
    }
    if (st == 15) return NB && G == SG ? Op{OP_SIG, 2, 0} : Op{OP_NONE, 0, 0};
    const int n = (st + 1) * 8;
    switch (q) {
        case 20: return {OP_B, st + 1, 0}; case 21: return {OP_B, st + 1, 1}; case 22: return {OP_A, n, 0}; case 23: return {OP_B, st + 1, 2};
        case 24: return {OP_B, st + 1, 3}; case 25: return {OP_A, n, 1}; case 26: return {OP_A, n + 1, 0}; case 27: return {OP_A, n + 1, 1};
        case 28: return {OP_A, n + 2, 0}; case 30: return {OP_A, n + 2, 1};
    }
    return {OP_NONE, 0, 0};
}
constexpr int g1_need(int G) {
    const int st = G >> 5, j = G & 3;
    for (int h = G - 1; h >= -12; --h) {
        const Op o = g1_op(h);
        if (o.kind == OP_A && o.a == (G >> 2) && o.b == (j >> 1)) return h;
        if (o.kind == OP_B && o.a == st && o.b == j) return h;
        if (o.kind == OP_FLAG && (G & 31) == 20 && (h >> 5) == st) return h;
    }
    return -100;
}
constexpr int g1_wait(int G) {
    int n = -100, c = 0;
    for (int h = G; h < (G ? G + 4 : 2) && h < 512; ++h) if (g1_need(h) > n) n = g1_need(h);
    for (int h = n + 1; h < G; ++h) c += g1_op(h).kind != OP_NONE;
    return n < G ? c : -1;
}
#define KV0 3
#define KVN 0x2222
constexpr int kv_first(int k) { int n = 0; for (int i = KV0; i < k && i < KV0 + 8; ++i) n += (KVN >> (4 * (i - KV0))) & 15; return n > 8 ? 8 : n; }
constexpr int kv_last() { int k = KV0; while (kv_first(k + 1) < 8) ++k; return k; }
#define CQ -1
#define CTM 0
#define EC 2
#define ED 2
#define EB 0
#define KST 0
#define KFD 3
#define KPL 11
#define KPI 0
#define KSL SLP
#define CBQ 20
constexpr int KVL = kv_last(), KFL = KVL + KFD, KSD = KVS ? KCC : -1;
static_assert(KFL <= KPL && KPL <= 12 && KPL > KVL && (KPI & 3) == 0 && KTI != KCC && KTI != KFL && KTI < KPL && KVP >= 0 && KVP < KCQ && KCQ < KCC && KSD <= KVL && KCC <= KPL && GCR > 32 * KCC + 62, "");
#define ROT 0x814
#define KLP 13
#define KL0 8
#define KLS 4
constexpr int ld_pos(int j) { return j < KLP ? -100 : KL0 + (j - KLP) * KLS / 4; }
constexpr int ld_cnt(int k) { int c = 0; for (int j = KLP; j < 21; ++j) c += (ld_pos(j) + 8) / 8 - 1 == k; return c; }
constexpr int KLL = (ld_pos(20) + 8) / 8 - 1, KLI = ld_pos(20) - 8 * KLL;
static_assert(KLP == 21 || KLL < KV0, "");
constexpr int hv_cnt(int k0, int k1) {
    int c = 0;
    for (int k = k0; k < k1; ++k) c += 9 + (KST ? 4 * (k == KVL) : k < KSD ? 0 : kv_first(k + 1) / 2 - (k == KSD ? 0 : kv_first(k) / 2)) + (k == KFL) + (k == KPL) + (k == KCQ) + (k == KVP) + (TM && TM < 16 && k == KTI) + ld_cnt(k);
    return c;
}
#define ZM 1
#define CL 1
#define EP 0
constexpr unsigned long long lag0_mask(int w) {
    unsigned long long m = 0;
    for (int l = 0; l < 64; ++l) if ((l >> 4) == 2 + ((l >> 3) & 1) && 3 - ((l & 7) >> 1) == w) m |= 1ull << l;
    return m;
}
constexpr int cv_start(int n) { return n * 8 - n * (n - 1) / 2; }
constexpr int cv_n(int q) { int n = 0; while (cv_start(n + 1) <= q) ++n; return n; }
constexpr int cv_m(int q) { return 7 - cv_n(q) - (q - cv_start(cv_n(q))); }
enum { CO_NONE, CO_A, CO_B, CO_C };
constexpr Op cv_op(int G) {
    const int s = (G + 36) / 36 - 1, q = G - 36 * s;
    if (q >= 1 && q <= 6) return s >= 0 ? Op{CO_A, s * 8 + 5 + (q - 1) / 2, (q - 1) & 1} : Op{CO_NONE, 0, 0};
    if (q >= 11 && q <= 14) return s >= 0 && s >= CL && s - CL < 8 ? Op{CO_C, s - CL, q - 11} : Op{CO_NONE, 0, 0};
    if (s == 7) return {CO_NONE, 0, 0};
    for (int m = 0; m < 8; ++m) if (q == cv_start(7 - m)) return {CO_B, (s + 1) * 8 + m, 0};
    for (int n = 0; n < 5; ++n) if (q == cv_start(n + 1) + 1 || q == cv_start(n + 1) + 2) return {CO_A, (s + 1) * 8 + n, q - cv_start(n + 1) - 1};
    return {CO_NONE, 0, 0};
}
constexpr int cv_need(int G) {
    const int s = G / 36, q = G % 36;
    for (int h = G - 1; h >= -36; --h) {
        const Op o = cv_op(h);
        if (o.kind == CO_A && o.a == s * 8 + cv_n(q)) return h;
        if (o.kind == CO_B && o.a == s * 8 + cv_m(q)) return h;
        if (o.kind == CO_C && q == 24 && o.a == s - CL) return h;
    }
    return -100;
}
constexpr int cv_wait(int G) {
    const int n = cv_need(G);
    for (int h = G - 1; h >= 0 && h >= G - 40; --h) if (cv_need(h) >= n) return -1;
    int c = 0;
    for (int h = n + 1; h < G; ++h) c += cv_op(h).kind != CO_NONE;
    return c;
}
#define AT(n, m) ((n) * 8 + (m))
#define FM 0x326260
constexpr int fill_pos(int s, int i) {
    const int f = (FM >> (4 * (s - 2))) & 15, n = s == 2 ? 0 : 4 * s - 9;
    switch (f) {
        case 1: return AT(n + i, 5) - (s == 2 && i == 3) * 6;
        case 2: return AT(n + 1 + i / 2, 5 + 2 * (i & 1));
        case 3: return i < 2 ? AT(n, 5 + 2 * i) : AT(n + i - 1, 5);
        case 4: return AT(n + 2 + i / 2, 5 + 2 * (i & 1));
        case 5: return AT(n, 1 + 2 * i) + (s == 2 ? 4 : 0);
        case 6: return AT(n + 3 + i / 2, 5 + 2 * (i & 1));
        case 7: return AT(n + 3, 5 + 2 * i);
        case 8: return i < 2 ? AT(n + 1, 5 + 2 * i) : AT(n + i, 5);
        case 9: return AT(n + 1 + i, 1);
        case 10: return i < 2 ? AT(n, 1 + 2 * i) : AT(n + 3, 1 + 2 * i);
    }
    return AT(n + i / 2, 5 + 2 * (i & 1));
}
constexpr int g2_fill(int p) {
    for (int k = 8; k < 32; ++k) if (fill_pos(k >> 2, k & 3) == p) return k;
    return -1;
}
#define QX AT(2, 5)
#define QQ AT(3, 2)
#define QZ AT(11, 2)
#define QT AT(3, 4)
constexpr int P_X = QX, P_Q = QQ, P_Z = QZ, P_T = QT;
constexpr bool XL = false;
constexpr bool fill_m0(int k) {
    const int a = fill_pos(k >> 2, (k & 3) - 1), b = fill_pos(k >> 2, k & 3);
    return (P_Q + 2 >= a && P_Q < b) || (P_Z + 2 >= a && P_Z < b) || (P_X + 2 >= a && P_X < b);
}
#define DBG 0
#define XW 4
#define XW2 4
#define QPP0 AT(9, 4)
#define QL0 AT(21, 1)
#define QR0 AT(24, 6)
#define QF2 AT(28, 3)
#define QL1 AT(26, 1)
#define QR1 AT(29, 3)
#define QA AT(16, 4)
#define AD 0
#define D1 2
#define D0 1
constexpr int NS = 33, NR = 29;
constexpr int P_PP0 = QPP0, P_PP1 = AT(16, 4), P_L0 = QL0, P_R0 = QR0, P_F2 = QF2, P_L1 = QL1, P_R1 = QR1, P_ZC = AT(10, 6), P_A = QA;
#define QB 24
#define QW -2
constexpr int P_W = QW;
static_assert(P_W < AT(ZP, 4), "");
constexpr int P_B0 = P_L0 - QB, P_B1 = P_L1 - QB;
static_assert(P_B0 > P_ZC + 1 && P_B1 > P_L0 + 1, "");
constexpr int E_PP0 = P_PP0 + NS - 1, E_PP1 = P_PP1 + (NS - 1) / D1, E_R0 = P_R0 + (NR - 1) / D0;
constexpr int ZW = P_A >= AT(20, 4) ? 19 : 15;
static_assert(P_R0 + 14 / D0 >= AT(24, 3) && E_R0 < P_F2 && P_F2 >= AT(24, 4) && P_F2 + 8 <= AT(31, 0) && E_R0 < P_R1, "");
static_assert(P_PP0 >= AT(9, 2) && P_PP0 + 8 <= AT(15, 4) && E_PP0 < P_PP1 && P_A >= AT(16, 4), "");
constexpr bool a_free(int p) {
    return p >= P_A && (AD || !((p >= P_PP1 && p <= E_PP1) || (p >= P_R0 && p <= E_R0) || (p >= P_F2 && p < P_F2 + 16)));
}
constexpr int a_step(int p) {
    if (NOA || !a_free(p)) return -1;
    int c = 0;
    for (int h = P_A; h < p; ++h) c += a_free(h);
    return c <= 48 ? c : -1;
}
constexpr int a_pos(int s) { int p = P_A; while (a_step(p) != s) ++p; return p; }
constexpr int q_rd(int r) { int p = r < 2 ? AT(ZW, 6) + r : a_pos(12 * r - 16) + 1; while ((p & 7) < 4) ++p; return p; }
constexpr int q_tie(int r) { const int n = (q_rd(r) >> 3) + 1; return AT(n, (n & 3) == 3 ? 0 : 4); }
static_assert(NOA || (a_pos(48) < 256 && q_tie(2) <= a_pos(23) && q_tie(3) <= a_pos(35)), "");
constexpr int g2_vm(int p) {
    int c = g2_fill(p) >= 0;
    c += a_step(p) > 0 && a_step(p) % 12 == 0 && !(NOST & 1);
    if (!NOA) c += (p == P_T) + (p == P_Q || p == P_Q + 2) + (p == AT(ZP, 4)) + (p == P_Z || p == P_Z + 2);
    c += (p == P_X || p == P_X + 2) + (p == P_W);
    c += !(NOST & 2) && (p == E_PP0 || p == E_PP1);
    c += (p == P_L0 || p == P_L1) + (PB && (p == P_B0 || p == P_B1));
    c += !(NOST & 4) && (p == P_F2 + 11 || p == P_F2 + 15);
    return c;
}
constexpr int g2_lx(int n) {
    int c = 2 * (n == 7);
    if (n >= 0 && (n & 7) >= 6) c += 2;
    if (!NOA) { c += 4 * (n == ZW); for (int r = 0; r < 4; ++r) c += (q_rd(r) >> 3) == n; }
    return c;
}
constexpr int g2_tile(int n, int j) { return (n & 4) ? (n & 2) == 0 : j; }
constexpr int g2_unit(int n, int j) { return (n & 4) ? 4 + 2 * (n & 1) + j : n & 7; }
constexpr int imin(int a, int b) { return a < b ? a : b; }
constexpr int imax(int a, int b) { return a > b ? a : b; }
constexpr int g2_need(int n) {
    int r = n == 3 ? -1 : fill_pos((n + 1) / 4, 3);
    if (n == 7 && !XL) r = imax(r, P_X + 2);
    if (n == ZW && !NOA) r = imax(r, imax(P_Q, P_Z) + 2);
    return r;
}
static_assert(P_Z > AT(11, 0) && P_T > AT(3, 0) && P_T < AT(ZP, 4) && (!XL || (P_X + 2 < P_R0 && P_R0 + 2 >= AT(24, 2) && P_R0 < AT(27, 0) && D0 == 1)), "");
constexpr bool rd1(int n) { return RD == 1 || (RD == 3 && (n & 3) == 2); }
constexpr int g2_allow(int e, int p) { int c = 0; for (int h = p + 1; h < e; ++h) c += g2_vm(h); return c; }

__global__ __launch_bounds__(512, 2) __attribute__((aligned(4096))) void k1(const uint16_t* __restrict__ x, const uint16_t* __restrict__ bre, const uint16_t* __restrict__ bim,
                                             const uint16_t* __restrict__ lre, const uint16_t* __restrict__ lim, const uint16_t* __restrict__ logdt, char* __restrict__ ws,
                                             const uint16_t* __restrict__ cre, const uint16_t* __restrict__ cim, const uint16_t* __restrict__ dskip,
                                             const uint16_t* __restrict__ ssmk, const uint16_t* __restrict__ ssmD, const uint16_t* __restrict__ qin,
                                             const uint16_t* __restrict__ kin, const uint16_t* __restrict__ vin, const uint16_t* __restrict__ sk,
                                             float* __restrict__ out TS_ARG) {
    unsigned long long pc0, rnd;
    asm volatile("s_getpc_b64 %0\n\ts_memtime %1" : "=s"(pc0), "=s"(rnd));
    TS_DECL TS(0);
    TL_DECL
    __shared__ __attribute__((aligned(1024))) char shm[LDS_TOTAL];
    const int tid = threadIdx.x, lane = tid & 63, wid = __builtin_amdgcn_readfirstlane(tid >> 6);
    const int b = blockIdx.x, xcd = b & 7, idx = b >> 3, hb = xcd & 1, tq = idx & 7;
    const int tm = (xcd >> 1) * 4 + (idx >> 3), tn = hb * 8 + tq;
    unsigned nr;
    const int tm2 = (xcd & 4) * 2 + tq, dt = (xcd & 2) * 2 + (idx >> 3), rot2 = tq + JR(idx >> 3);
    const unsigned lbase = (unsigned)(uintptr_t)shm;
    const int l15 = lane & 15, g4 = lane >> 4, lw = wid & 3, kg = wid >> 2;
    dma<U0 + 4096, false>(lbase + wid * 1024, CR ? ((lane * 16) & (1023 >> CR)) | ((lane >> (6 - CR)) << 20) : lane * 16, mk_rsrc((const void*)(pc0 & ~255ull), CODE_KB * 1024), ((idx * 8 + wid) & ((64 << CR) - 1)) * (1024 >> CR));
    const unsigned mk0 = lbase + POLL + lw * 256 + kg * (FULL - POLL + lw * (FST - 256));
    flag_set(mk0 + lane * 4, 1u);
    flag_set(lbase + HGO + lane * 4, 0u);
    if constexpr (NB) flag_set(lbase + DONE + lw * FST + lane * 4, 1u);
    unsigned vo[4];
    const bool xw = lw < 2;
#pragma unroll
    for (int i = 0; i < 4; ++i) {
        const int v = l15 ^ (i * 4 + g4);
        vo[i] = ((xw ? 64 * (i & 1) + 4 * (i >> 1) + (lane & 32) + 2 * (g4 & 1) : 8 * (i & 1) + 2 * g4) + (v >> 3)) * 2048 + (v & 7) * 16 - (i & 1) * 1024;
    }
    const int bw = lw >> 1;
    unsigned r0 = (tm * 128 + lw * 16 + bw * (tn * 64 + lw * 16 - 64 - tm * 128)) * 2048, rsz = 229376 - bw * 163840;
    const unsigned col = (tq + (idx >> 3)) & 1, grp = GS == 0 ? tq >> 2 : GS == 1 ? idx >> 4 : GS == 2 ? 0 : GS == 3 ? hb : (xcd >> 1) & 1;
    unsigned rp = grp * (16 - BL) * 128, mi = col * BL * 128, mo = mi >> grp;
    unsigned rot = (BL ? 0 : ROT >> 8) * col + ((ROT >> 4) & 15) * (xcd >> 1) + (ROT & 15) * hb, rst = 16384 << bw, lb = lbase + lw * 8192, oob = OOB;
    unsigned lb0 = lb + g1_buf(0) + kg * (g1_buf(1) - g1_buf(0)), so0 = BL ? (((kg * 128 + rp) ^ mo) + rot * 128) & 0x780 : ((kg + rot) & 15) * 128;
    if constexpr (BL) { rot *= 128; ties(rp); ties(mi); ties(mo); }
    unsigned co = l15 * 2 + (tn * 128 + lw * 32 + kg * 2048), vw = b * 4 + lw + 32 - 32 * kg;
    tie(vo[0]); tie(vo[1]); tie(vo[2]); tie(vo[3]); tie(oob); tie(co);
    ties(r0); ties(rsz); ties(rot); ties(rst); ties(lb); ties(lb0); ties(so0); ties(vw);
    sched();
    asm volatile("" :: "s"(x), "s"(bre), "s"(bim), "s"(lre), "s"(lim), "s"(logdt), "s"(cre), "s"(cim), "s"(dskip), "s"(ssmk), "s"(ssmD), "s"(qin),
                       "s"(kin), "s"(vin), "s"(sk), "s"(ws), "s"(out));
    const bool xv = fresh(bw) == 0;
    const u32x4 rs0 = mk_rsrc((const char*)(xv ? x : bre) + r0, rsz), rs1 = mk_rsrc((const char*)(xv ? x : bim) + r0 - 2048, rsz + 2048);
    if constexpr (SU) TS(3);
    if constexpr (AB) asm volatile("s_waitcnt lgkmcnt(0)\n\ts_cmp_eq_u32 %0, 0\n\ts_cbranch_scc1 1f\n\ts_barrier\n1:" :: "s"(kg) : "scc", "memory");
    unsigned c_dt, c_re, c_im;
    auto c_ld = [&]() {
        gld<0>(c_dt, co, mk_rsrc(logdt, 2048), 0);
        gld<0>(c_re, co, mk_rsrc(lre, 2048), 0);
        gld<0>(c_im, co, mk_rsrc(lim, 2048), 0);
    };
    if constexpr (!LC) c_ld();
    const unsigned vb = race(vw, ws, rnd);
    static_for<0, 8>([&](auto ic) { constexpr int i = CV(ic); dma4<(i & 4) * 1024, i & 3>(lb0, vo[i & 3], (i & 2) ? rs1 : rs0, so0 + (i >> 2) * rst); });
    wait_l<0>();
    if constexpr (SU) TS(4);
    if constexpr (AB) {
        dma1<0>(mk0 + kg * 120, oob, rs0, 0);
        asm volatile("s_cmp_lg_u32 %0, 0\n\ts_cbranch_scc1 1f\n\ts_barrier\n1:" :: "s"(kg) : "scc", "memory");
    } else {
        barrier();
        dma1<0>(mk0 + kg * 120, oob, rs0, 0);
    }
    if constexpr (SU) TS(5);
    sched();
    const unsigned a00 = (l15 >> 1) * 256 + ((((l15 & 1) * 8 + g4) ^ (l15 >> 1)) << 4);
    auto frag = [&](int par, int ks) { return (a00 ^ (ks * 64) ^ (par * 128)) + par * 2048; };
    bf16x8 cf[32];
    unsigned vc[4], cad[4];
    auto c_setup = [&]() {
#pragma unroll
        for (int i = 0; i < 4; ++i) { const int v = l15 ^ (i * 4 + g4); vc[i] = (4 * (2 * g4 + (v >> 3)) + 2 * (i & 1) + (i >> 1)) * 2048 + (v & 7) * 16 - i * 1024; }
    };
    const u32x4 rsc = mk_rsrc((kg ? cim : cre) + (size_t)dt * 131072, 262144);
    auto c_off = [&](int dg, int j) { return (unsigned)(dg * 65536 + hb * 1024 + ((rot2 + j) & 7) * 128); };
    const unsigned cst = lbase + (kg ? BUF : 2 * BUF) + lw * 8192;
    unsigned tag;
    const u32x4 rsf = mk_rsrc(ws + WS_F, 65536);
    const __amdgpu_buffer_rsrc_t rf = mk_brs(ws + WS_F, 65536);
    const int qx = (hb ? 0 : 2) ^ JQ(idx >> 3), wid_ = wid, b_ = b;

    unsigned wl, w2k, hs0, s_x, s_q, s_z, zfo, ppo, s_a, s_b, tagg, ppi, tn_[3], psel, lastw, dsw_;
    unsigned l16, ha[8], a_x, a_xr, a_q, a_qr, a_iw, a_ir, a_t, msk, a_f, expect, a_z, a_z2, a_zr, a_oa, a_ob;
    unsigned long long lo8, hi8;
    u32x4 rsx, rsq, rso, rws, dg[2]; u32x2 tg2;
    constexpr int SU_S = 21, SU_D = 39, SU_N = 44;
    auto su = [&](auto kc, auto cc, int lw, int b, unsigned ln) {
        constexpr int kg = CV(kc), c = CV(cc);
        const int wid = 4 * kg + lw, xcd = b & 7, idx = b >> 3, tq = (idx & 7) + JR(idx >> 3);
        const unsigned nx = nr ^ ((xcd & 2) << 12), nh = nr ^ ((idx & 2) << 12), hp = (idx & 7) * 16 + (b & 1) * 8;
        const int tm2 = (xcd & 4) * 2 + (idx & 7), dt = (xcd & 2) * 2 + (idx >> 3), qx = ((~b & 1) * 2) ^ JQ(idx >> 3);
        const int ta = 512 * (idx >> 3) + 128 * (idx & 1) + 16 * wid + 4 * ((idx >> 1) & 3);
        const int tb = tm2 * 128 + 64 * kg;
        const unsigned l31 = ln & 31, l5 = ln >> 5;
        auto S = [](auto& v, auto e) { v = e; ties(v); };
        auto V = [](auto& v, auto e) { v = e; tie(v); };
        if constexpr (c == 0) { S(wl, lbase + wid * 4096); S(w2k, lbase + wid * 2048); }
        if constexpr (c == 1) S(hs0, (hp + ((tq + (wid >> 1)) & 7)) * 65536);
        if constexpr (c == 2) S(hs0, hs0 + nh + (wid & 1) * 4096);
        if constexpr (c == 3) S(hs0, hs0 + qx * 16384);
        if constexpr (c == 4) S(s_x, (tb + qx * 8) * 2048);
        if constexpr (c == 5) S(s_x, s_x + dt * 256 + lw * 64);
        if constexpr (c == 6) S(s_q, ta * 2048);
        if constexpr (c == 7) { S(s_a, s_q * 4 + xcd * 512); S(s_q, s_q + xcd * 256); }
        if constexpr (c == 8) S(s_z, (xcd & 3) * 1048576 + nx);
        if constexpr (c == 9) S(ppo, s_z + idx * 32768 + (WS_KVT + kg * 16384) + lw * 2048);
        if constexpr (c == 10) S(s_z, s_z + WS_ZT + idx * 128 + wid * 131072);
        if constexpr (c == 11) S(zfo, WS_F + F_Z + (xcd & 2) * 8192 + nr + (xcd & 1) * 4096);
        if constexpr (c == 12) S(s_b, (tb + (2 ^ qx) * 8) * 8192 + 4096);
        if constexpr (c == 13) S(s_b, s_b + dt * 512 + lw * 128);
        if constexpr (c == 14) S(rsx, mk_rsrc(x, (NOX & 1) ? 0 : 4 << 20));
        if constexpr (c == 15) S(rsq, mk_rsrc(qin, (NOX & 2) ? 0 : 4 << 20));
        if constexpr (c == 16) S(rso, mk_rsrc(out, 16 << 20));
        if constexpr (c == 17) S(rws, mk_rsrc(ws, 32 << 20));
        if constexpr (c == 18) { S(tagg, tag); S(ppi, ppo ^ 1048576); S(tn_[0], tag & 0xf000fu); S(lastw, kg ? 1 : (b ^ 255) | lw); }
        if constexpr (c == 19) { S(tn_[1], (tag >> 8) & 0xf000fu); S(tn_[2], (tag >> 4) & 0xf000fu); }
        if constexpr (c == 20) { S(psel, 0x05040100u); S(lo8, 0x00ff00ff00ff00ffull); S(hi8, ~0x00ff00ff00ff00ffull); }
        if constexpr (c == 21) { V(l16, ln * 16); V(dsw_, lbase + 131072 + lw * 64 + (ln & 7) * 8 + ((ln >> 1) & 4)); }
        if constexpr (c == 22) V(ha[0], lbase + kg * 1536 + l5 * 2048 + (ln & 2) * 128 + (ln & 1) * 8);
        if constexpr (c == 23) V(ha[1], ((ln >> 1) & 8) | ((ln >> 2) & 3));
        if constexpr (c == 24) V(ha[0], ha[0] | ((ha[1] | ((ln & 2) << 1)) << 4));
        if constexpr (c == 25) { V(ha[1], ha[0] ^ 64); V(ha[2], ha[0] ^ 512); V(ha[3], ha[0] ^ 576); }
        if constexpr (c == 26) static_for<0, 4>([&](auto ic) { V(ha[4 + CV(ic)], ha[CV(ic)] + 65536); });
        if constexpr (c == 27) V(a_x, ((((ln >> 2) & 7) | (ln & 32)) << 11) | (l16 & 48));
        if constexpr (c == 28) V(a_xr, (((l16 & 240) << 2) | (ln & 48)) + (w2k + ZST));
        if constexpr (c == 29) { dsr32<DSK - 131072>(dsw_, dsw_); V(a_q, ((ln & 48) << 7) | (l16 & 240)); }
        if constexpr (c == 30) V(a_qr, (l5 << 10) + l31 * 8 + (w2k + QST));
        if constexpr (c == 31) { V(a_iw, w2k + IMG + l16); V(a_ir, a_iw ^ 8192); V(msk, 0xfff0fff0u); }
        if constexpr (c == 32) {
            V(a_t, wid == 0 && ln == 0 ? idx * 128 : OOB);
            V(a_f, wid == 1 ? (ln >> 1) * 128 + (ln & 1) * 4 : OOB); V(expect, wid == 1 ? ((ln & 1) ? ~tagg : tagg) : 0u);
        }
        if constexpr (c == 33) V(a_z, ((ln ^ l5 ^ (wid * 4)) & 7) << 4);
        if constexpr (c == 34) V(a_z, a_z | ((ln & 48) << 10) | ((ln & 8) << 9));
        if constexpr (c == 35) V(a_zr, ((((wid ^ ln) & 7) << 1) | l5) << 3);
        if constexpr (c == 36) { V(a_zr, a_zr + (l31 << 9) + (lbase + ZST)); V(a_oa, (l5 << 21) | (l31 << 4)); }
        if constexpr (c == 37) V(a_ob, ((ln >> 2) & 4) | (ln & 32) | ((ln >> 3) & 1));
        if constexpr (c == 38) V(a_ob, (a_ob << 13) | (l16 & 112));
        if constexpr (c == SU_D) tie(dsw_);
        if constexpr (c == SU_D + 4) { V(tg2, u32x2{tagg, ~tagg}); V(a_z2, a_z ^ 32); }
        if constexpr (c >= SU_D && c < SU_D + 4) {
            constexpr unsigned long long k = diag_mask(c - SU_D);
            dg[0][c - SU_D] = sel0(dsw_ & 0xffffu, k); dg[1][c - SU_D] = sel0(dsw_ & 0xffff0000u, k);
            if constexpr (c == SU_D + 3) { tie(dg[0]); tie(dg[1]); }
        }
    };

    if (wid < 4) {
        if constexpr (LC) c_ld();
        f32x4 acc[8][2];
        cf32 L, cc, M, M2, MG, NG, N4, N8, XC, ag;
        unsigned eo[4], ew, es, dna, ho, hfo, cb, fv; unsigned long long none[4]; u32x4 rse, rh, er;
        {
            unsigned aad[4], bad[4];
#pragma unroll
            for (int i = 0; i < 4; ++i) { aad[i] = lbase + frag(i & 1, i >> 1); bad[i] = aad[i] + 16384 + wid * 4096; }
            const unsigned fr = lbase + FULL + (lane & 3) * FST;
            sld(tag, ws + WS_F + F_EP);
            bf16x8 fa[4][2], fb[2][4];
            unsigned fl = 1, dz = 0, dw_ = lbase + DONE + wid * FST + lane * 4, vt = lbase + POLL + wid * 256 + lane * 4;
            tie(dz); tie(dw_); tie(vt);
            auto op = [&](auto gc) {
                constexpr int G = CV(gc);
                constexpr Op o = g1_op(G);
                if constexpr (o.kind == OP_A) {
                    constexpr int bo = g1_buf(o.a >> 3), hi = bo >= 65536 ? 65536 : 0, i = o.a & 7;
                    dsr128<bo - hi + (i >> 1) * 4096>(fa[i & 3][o.b], aad[(i & 1) + 2 * o.b] + hi);
                }
                if constexpr (o.kind == OP_B) { constexpr int bo = g1_buf(o.a), hi = bo >= 65536 ? 65536 : 0; dsr128<bo - hi>(fb[o.a & 1][o.b], bad[o.b] + hi); }
                if constexpr (o.kind == OP_FLAG) dsr32<(30 - (G >> 5)) * 4>(fl, fr);
                if constexpr (o.kind == OP_SIG) dsw32<o.a * 4>(dw_, dz);
                if constexpr (o.kind == OP_VOTE) dsr32<0>(vt, vt);
            };
            auto consts = [&]() {
                float ld, ar, ai;
                asm volatile("s_waitcnt vmcnt(%6)\n\tv_lshlrev_b32 %0, 16, %3\n\tv_lshlrev_b32 %1, 16, %4\n\tv_lshlrev_b32 %2, 16, %5"
                             : "=&v"(ld), "=&v"(ar), "=&v"(ai) : "v"(c_dt), "v"(c_re), "v"(c_im), "n"(LC ? 0 : 9) : "memory");
                if constexpr (SU) TS(6);
                const float dtv = rbf(expf(ld)), ea = expf(dtv * ar);
                float sn, cs;
                sincosf(dtv * ai, &sn, &cs);
                L = cf32{ea * cs, ea * sn};
                M = L;
#pragma unroll
                for (int e = 0; e < 5; ++e) M = cmul(M, M);
                M2 = cmul(M, M);
                const cf32 N = cmul(M2, M2), N2 = cmul(N, N), one = {1.f, 0.f};
                N4 = cmul(N2, N2); N8 = cmul(N4, N4);
                auto sel = [](bool c, cf32 a, cf32 b) { return cf32{c ? a.r : b.r, c ? a.i : b.i}; };
                MG = cmul(sel(g4 & 1, M, one), sel(g4 & 2, M2, one));
                NG = cmul(sel(g4 & 1, N, one), sel(g4 & 2, N2, one));
                const float nm = L.r - 1.f, den = ar * ar + ai * ai;
                cc = cf32{2.f * ((nm * ar + L.i * ai) / den), 2.f * ((L.i * ar - nm * ai) / den)};
                auto tc = [&](cf32& v) { tie(v.r); tie(v.i); };
                XC = cf32{g4 == 3 ? M.r : g4 == 2 ? 1.f : 0.f, g4 == 3 ? M.i : 0.f};
                tc(L); tc(cc); tc(M); tc(M2); tc(MG); tc(NG); tc(N4); tc(N8); tc(XC);
                sched();
            };
            if constexpr (CQ < 0) consts();
            if constexpr (SU) TS(10);
            __builtin_amdgcn_s_setprio(CP);
            zero_chk<0>(fl, lbase + POLL + (lane & 3) * 256);
            if constexpr (WS0 > 0) { fl = 1; zero_chk<(31 - WS0) * 4, 1>(fl, fr); }
            ties(tag);
            tag += 0x01010101u;
            TS(1);
            static_for<-12, 0>(op);
            static_for<0, 512>([&](auto gc) {
                constexpr int G = CV(gc), st = G >> 5, i = (G >> 2) & 7, j = G & 3;
                if constexpr ((G == 0 || j == 2) && G <= 506) { static_assert(g1_wait(G) >= 0, ""); wait_lr<G == 506 ? 0 : g1_wait(G)>(); }
                if constexpr (G == CQ) { asm volatile("s_waitcnt lgkmcnt(0)\n\ts_nop 7" ::: "memory"); consts(); }
                if constexpr (G == SQ) {
                    c_setup();
                    const int t0 = tm - 1 - g4;
                    static_for<0, 4>([&](auto ic) {
                        constexpr int i = CV(ic);
                        const int t = t0 - 4 * i;
                        cad[i] = lb + frag(i & 1, i >> 1);
                        eo[i] = ((((t ^ tm) & 8) << 10) | ((t & 7) << 8) | (l15 * 16) | (t & OOB)); none[i] = __builtin_amdgcn_ballot_w64(t < 0);
                        tie(vc[i]); tie(cad[i]); tie(eo[i]); ties(none[i]);
                    });
                    ew = (l15 * 16) | (g4 << 20); dna = lbase + DONE + (lane & 3) * FST; ho = g4 * 256 + ((l15 ^ (4 * (g4 & 1))) << 4); fv = lane << 20;
                    tie(ew); tie(dna); tie(ho); tie(fv);
                    sched();
                }
                if constexpr (G == HQ) hop_all(wid);
                {
                    constexpr int n = (SLM >> (4 * st)) & 15;
                    if constexpr (n > 0 && ((G & 31) + 1) * (n & 7) / 32 != (G & 31) * (n & 7) / 32) __builtin_amdgcn_s_sleep(1 + (n >> 3));
                }
                if constexpr ((G & 31) == 20 && st < 15) zero_chk<(30 - st) * 4>(fl, fr);
                if constexpr (((G & 31) == 30 && st < 13) || (NB < 2 && G == B13)) barrier();
                if constexpr ((G & 31) == 20) TL(st);
                mfma<st == 0 && j < 2>(acc[i][j & 1], fa[i & 3][j >> 1], fb[st & 1][j]);
                sched();
                op(gc);
                if constexpr (G == GCR + 16) { unsigned n; nr = vote_cnt(vt, tag, (xcd & 4) << 11, n); }
                if constexpr (G >= GCR + 18 && G < GCR + 22) { eo[G - GCR - 18] ^= nr; tie(eo[G - GCR - 18]); }
                if constexpr (G == GCR + 22) { rse = mk_rsrc(ws + WS_E + tn * 16384 + wid * 2048, 16384); ties(rse); }
                if constexpr (G == GCR + 24) { es = nr + (tm & 7) * 256; cb = wid * 65536 + hb * 1024; er = u32x4{0u, 0u, tag, ~tag}; ties(es); ties(cb); tie(er); }
                if constexpr (G == GCR + 26) { rh = mk_rsrc(ws + WS_H + (size_t)((tm & 7) * 16 + tn) * 65536 + (nr ^ ((tm & 2) << 12)) + wid * 2048, 65536); ties(rh); }
                if constexpr (G == GCR + 28) { hfo = F_H + nr + (((tm & 7) * 16 + tn) * 4 + wid) * 8; ties(hfo); }
                if constexpr (G == PG0) page<-1>();
                if constexpr (G == 95) hop<2>();
                if constexpr (G == 159) hop<4>();
                if constexpr (G == 207) hop<5>();
                if constexpr (G == PG7) page<7>();
                if constexpr (G == 319) hop<8>();
                if constexpr (G == 383) hop<10>();
                if constexpr (G == 431) hop<11>();
                if constexpr (G == 479) page<13>();
                if constexpr (G == 509) hop<15>();
                if constexpr (G == 489) hop<40>();
                if constexpr (G == 494) hop<41>();
                if constexpr (G == 499) hop<42>();
                if constexpr (G == 504) hop<43>();
                if constexpr (G >= 487) {
                    constexpr int k = G - 486;
                    if constexpr (k == 1) { asm volatile("s_nop 3"); ag = cf32{acc[0][0][0], acc[0][1][0]}; }
                    cf32 t;
                    asm volatile("v_fma_f32 %0, -%3, %5, %6\n\tv_fma_f32 %1, %3, %4, %7\n\tv_fmac_f32 %0, %2, %4\n\tv_fmac_f32 %1, %2, %5"
                                 : "=&v"(t.r), "=&v"(t.i) : "v"(L.r), "v"(L.i), "v"(ag.r), "v"(ag.i), "v"(acc[k >> 2][0][k & 3]), "v"(acc[k >> 2][1][k & 3]));
                    ag = t;
                }
                sched();
            });
        }
        __builtin_amdgcn_s_setprio(3);
        TL(16);
        TS(2);
#pragma unroll
        for (int k = 26; k < 32; ++k) ag = cfma(L, ag, cf32{acc[k >> 2][0][k & 3], acc[k >> 2][1][k & 3]});
        tie(ag.r); tie(ag.i); sched(); hop<44>(); sched();
        cf32 ev_, od, p01, p23;
        sw16(ag.r, ev_.r, od.r); sw16(ag.i, ev_.i, od.i);
        const cf32 pp = cfma(M, ev_, od);
        sw32(pp.r, p01.r, p23.r); sw32(pp.i, p01.i, p23.i);
        cf32 E = cfma(M2, p01, p23);

        er[0] = f2u(E.r); er[1] = f2u(E.i);
        gst<0>(er, ew, rse, es);
        sched();
        TSX(3);
        hop<45>();
        sched();
        auto c_read = [&](auto rc) { static_for<0, 8>([&](auto ic) { constexpr int r = CV(rc), i = CV(ic); dsr128<(i >> 2) * 4096>(cf[r * 8 + i], cad[i & 3] + ((r & 1) ? 0 : 2 * BUF)); }); };
        auto c_tie = [&](auto rc) { static_for<0, 8>([&](auto ic) { tie(cf[CV(rc) * 8 + CV(ic)]); }); };
        auto c_unit = [&](auto uc) { static_for<0, 4>([&](auto ic) { constexpr int u = CV(uc), i = CV(ic); dma4<((u & 2) ? 0 : 2 * BUF) + (u & 1) * 4096, i>(lb, vc[i], rsc, cb + ((rot2 + u) & 7) * 128); }); };
        auto c_req = [&](auto rc) { c_unit(IC(CV(rc) * 2)); c_unit(IC(CV(rc) * 2 + 1)); };
        const float c2r = cc.r, c2i = cc.i;
        auto scale = [&](int i) {
#pragma unroll
            for (int j = 0; j < 4; ++j) {
                const float br = acc[i][0][j], bi = acc[i][1][j];
                acc[i][0][j] = fma_(c2r, br, -(c2i * bi)); acc[i][1][j] = fma_(-c2r, bi, -(c2i * br));
            }
            tie(acc[i][0]); tie(acc[i][1]);
        };
        auto scales = [&](auto ac, auto bc) {
            static_for<CV(ac), CV(bc)>([&](auto ic) {
                constexpr int i = CV(ic);
                scale(i);
                if constexpr (i & 1) { sched(); hop<i == 1 ? 46 : i == 3 ? 16 : i == 5 ? 47 : 48>(); sched(); }
            });
        };
        constexpr int CA = CVR == 0 ? 0 : CVR == 4 ? 1 : 2;
        if constexpr (NB) {
            unsigned dn = 1;
            scales(IC(0), IC(DP));
            if constexpr (CA) { zero_chk<8>(dn, dna); static_for<0, CA>(c_unit); }
            if constexpr (SPR != 3) __builtin_amdgcn_s_setprio(SPR);
            TL(17);
            sched();
            scales(IC(DP), IC(ELP));
            if constexpr (!CA) zero_chk<8>(dn, dna);
        } else {
            scales(IC(0), IC(ELP));
            barrier();
            static_for<0, CA>(c_unit);
        }
        TL(18);
        u32x4 ev[4];
#pragma unroll
        for (int i = 0; i < 4; ++i) asm volatile("buffer_load_dwordx4 %0, %1, %2, 0 offen sc1" : "=v"(ev[i]) : "v"(eo[i]), "s"(rse) : "memory");
        asm volatile("" ::: "memory");
        sched();
        constexpr int CB = CVR == 0 ? 1 : CVR == 4 ? 2 : 4;
        static_for<CA, CB>(c_unit);
        scales(IC(ELP), IC(8));
        cf32 X = cfma(XC, p01, cf32{(g4 & 1) ? ev_.r : 0.f, (g4 & 1) ? ev_.i : 0.f});
        tie(X.r); tie(X.i);
        sched();
        hop<24>();
        if constexpr (SPR != 3) __builtin_amdgcn_s_setprio(3);
        if constexpr (CVR == 2 || CVR == 5) { wait_v<12>(); c_read(IC(0)); wait_l<0>(); c_tie(IC(0)); c_req(IC(2)); }
        wait_v<4 * (CB - CA) + 8 * (CVR == 2 || CVR == 5)>();
        TL(19);
        tie(ev[0]); tie(ev[1]); tie(ev[2]); tie(ev[3]);
        {
            const unsigned long long tag64 = (unsigned long long)tag | ((unsigned long long)~tag << 32);
#pragma unroll 1
            for (unsigned spins = 0; spins < SPIN_CAP; ++spins) {
                unsigned long long ok = ~0ull;
#pragma unroll
                for (int i = 0; i < 4; ++i) ok &= __builtin_amdgcn_ballot_w64(((unsigned long long)ev[i][2] | ((unsigned long long)ev[i][3] << 32)) == tag64) | none[i];
                TSV(11, spins);
                if (ok == ~0ull) break;
                asm volatile("buffer_load_dwordx4 %0, %4, %8, 0 offen sc1\n\tbuffer_load_dwordx4 %1, %5, %8, 0 offen sc1\n\tbuffer_load_dwordx4 %2, %6, %8, 0 offen sc1\n\t"
                             "buffer_load_dwordx4 %3, %7, %8, 0 offen sc1\n\ts_waitcnt vmcnt(0)" : "=&v"(ev[0]), "=&v"(ev[1]), "=&v"(ev[2]), "=&v"(ev[3])
                             : "v"(eo[0]), "v"(eo[1]), "v"(eo[2]), "v"(eo[3]), "s"(rse) : "memory");
            }
        }
        if constexpr (P18 < 0) page<18>();
        cf32 h;
        {
            auto e = [&](int i) { return cf32{u2f(ev[i][0]), u2f(ev[i][1])}; };
            cf32 c = cmul(NG, cfma(N8, cfma(N4, e(3), e(2)), cfma(N4, e(1), e(0)))), u, v;
            sw16(c.r, u.r, v.r); sw16(c.i, u.i, v.i);
            sw32(u.r + v.r, u.r, v.r); sw32(u.i + v.i, u.i, v.i);
            c = cfma(MG, cf32{u.r + v.r, u.i + v.i}, X);
            h = cf32{fma_(c2r, c.r, -(c2i * c.i)), fma_(-c2r, c.i, -(c2i * c.r))};
        }
        TSX(4);
        {
            const cf32 Lc = {L.r, -L.i};
            static_for<0, 4>([&](auto qc) {
                constexpr int q = CV(qc);
                if constexpr (q >= CB) c_unit(qc);
                if constexpr (q == 2) hop<20>();
                u32x4 wr, wi;
#pragma unroll
                for (int u = 0; u < 4; ++u) {
                    const int i = 2 * q + (u >> 1), j = 2 * (u & 1);
                    const cf32 h0 = cfma(Lc, h, cf32{acc[i][0][j], acc[i][1][j]});
                    h = cfma(Lc, h0, cf32{acc[i][0][j + 1], acc[i][1][j + 1]});
                    wr[u] = pack2(h0.r, h.r); wi[u] = pack2(h0.i, h.i);
                }
                if constexpr ((PRE == 2 || GQ >= 0) && q == GE) { tie(wr); tie(wi); dsw32<4>(lbase + DONE + wid * FST + lane * 4, 0u); }
                gst<0>(wr, ho, rh, q * 16384);
                gst<1024>(wi, ho, rh, q * 16384);
                if constexpr (q == P18) page<18>();
                asm volatile("s_nop 1" :: "v"(wr), "v"(wi));
                sched();
            });
        }
        TSX(10);
        auto flag = [&]() { gst<0>(u32x2{er[2], er[3]}, fv, rsf, hfo); TSX(5); };
        hop<22>();
        if constexpr (CVR == 5) {
            wait_v<16>();
            c_read(IC(1));
            wait_v<0>();
            flag();
            wait_l<0>();
            static_for<6, imax(6, 8 - CD)>(c_unit);
        } else if constexpr (CVR == 2) {
            wait_v<16>();
            c_read(IC(1));
            wait_l<0>();
            c_req(IC(3));
            wait_v<8>();
            flag();
        } else {
            if constexpr (CVR == 1) wait_v<8>(); else { wait_v<0>(); flag(); }
            c_read(IC(0)); c_read(IC(1));
            wait_l<8>();
            static_for<4, imin(6, 8 - CD)>(c_unit);
            wait_l<0>();
            static_for<6, imax(6, 8 - CD)>(c_unit);
            if constexpr (CVR == 1) { wait_v<16>(); flag(); }
        }
        c_tie(IC(0)); c_tie(IC(1));
        {
            unsigned ln = lane, pf, fso;
            tie(ln);
            const int wid = fresh(wid_), lw = wid, b = fresh(b_), idx = b >> 3, tq = (idx & 7) + JR(idx >> 3), qx = ((~b & 1) * 2) ^ JQ(idx >> 3);
            const unsigned nh = nr ^ ((idx & 2) << 12), hp = (idx & 7) * 16 + (b & 1) * 8;
            static_for<0, SU_D>([&](auto cc) { su(IC(0), cc, wid, b, ln); });
            wait_l<0>();
            static_for<SU_D, SU_N>([&](auto cc) { su(IC(0), cc, wid, b, ln); });
            unsigned e_h = wid == 3 ? ((ln & 1) ? ~tagg : tagg) : 0u, hsl = lbase + lw * 8192, hso = (hp + ((tq + wid) & 7)) * 65536 + nh + qx * 16384;
            unsigned a_h = wid == 3 ? ln * 4 : OOB, s_h = WS_F + F_H + nr + hp * 32, ca2[4], a_g = lbase + HGO + ln * 4;
            static_for<0, 4>([&](auto ic) { ca2[CV(ic)] = cad[CV(ic)] + 2 * BUF; tie(ca2[CV(ic)]); });
            tie(e_h); tie(a_h); tie(a_g); ties(hsl); ties(hso); ties(s_h);
                        sched();
            {
                auto look = [&]() { if constexpr (HA) pf_at<0>(pf, a_h, rws, s_h); else pf_ld(pf, a_h, rws, s_h); };
                static_for<8 - CD, 8>(c_unit);
                if constexpr (HA) pf_zero(pf);
                if constexpr (PW == 0) { look(); wait_v<1>(); }
                if constexpr (PW == 1) { wait_v<0>(); look(); }
                if constexpr (PW == 2) { wait_v<8>(); look(); wait_v<1>(); }
                static_for<16, 32>([&](auto ic) { constexpr int i = CV(ic); dsr128<((i >> 2) & 1) * 4096>(cf[i], (i & 8) ? cad[i & 3] : ca2[i & 3]); });
                wait_l<0>();
                static_for<16, 32>([&](auto ic) { tie(cf[CV(ic)]); });
                wait_v<0>();
                tag_chk<1, HA>(pf, e_h, a_h, rws, s_h);
                if (wid == 3) flag_set(a_g, 1u); else gate<1>(a_g, 1);
                TSX(6);
                hop<26>();
                static_for<0, 4>([&](auto ic) {
                    constexpr int i = CV(ic);
                    dma_x<(i >> 1) * 2 * BUF + (i & 1) * 4096, (i >> 1) * 262144 + (i & 1) * 4096, true>(fso, hsl, l16, rws, hso);
                    dma_n<1024>(fso, l16, rws); dma_n<2048>(fso, l16, rws); dma_n<3072>(fso, l16, rws);
                });
            }
        }
    } else {
        const int ch = xcd * 128 + (idx >> 2) * 16 + lw * 4 + (idx & 3);
        unsigned ln_ = lane; int lw_ = lw, bb_ = b;
        unsigned dw, nx; u32x4 ad;
        auto c_unit = [&](auto uc) { static_for<0, 4>([&](auto ic) { constexpr int u = CV(uc), i = CV(ic); dma4<(u & 1) * 4096, i>(cst, vc[i], rsc, c_off(lw, u)); }); };
        constexpr int XV = 4 * (EC == 1) + 8 * (PFQ == 2);
        auto ctouch = [&](int r) { const unsigned j = (fresh(idx * 4 + lw) + r * 37) & 127; stouch((pc0 & ~255ull) + (j < CODE_KB * 8 / 3 ? j : j - 64) * 384); };
        {
            const unsigned fb = lbase + FULL + lw * FST;
            auto piece = [&](auto sc, auto ic) {
                constexpr int s = CV(sc), i = CV(ic);
                dma4<g1_buf(s) + (i & 4) * 1024, i & 3>(lb, vo[i & 3], (i & 2) ? rs1 : rs0, (BL ? (((s * 128 + rp) ^ (s >= BL && s < 16 - BL ? mi : mo)) + rot) & 0x780 : ((s + rot) & 15) * 128) + (i >> 2) * rst);
            };
            auto mark = [&](auto sc) { dma1<(31 - CV(sc)) * 4>(fb, oob, rs0, 0); };
            auto feed = [&](auto kc, auto ic) {
                constexpr int k = CV(kc), i = CV(ic);
                if constexpr (i < 4) piece(IC(k + 3), IC(i + 4));
                if constexpr (i == 3) mark(IC(k + 3));
                if constexpr (i >= 4 && k < 12) piece(IC(k + 4), IC(i - 4));
            };
            __builtin_amdgcn_s_setprio(HP);
            TS(7);
            sld(tag, ws + WS_F + F_EP);
            sld(dw, ssmD + (ch & ~1));
            const int sb = idx * 2 + (lw >> 1), kh = lw & 1;
            u32x2 kk[11], vv[8]; u32x4 w4[2], kd[4], ke[4], kvo[4];
            const unsigned ko = g4 * 16384 + l15 * 8, so = sb * 65536 + xcd * 256 + kh * 128;
            auto kv_ld = [&](auto jc) {
                constexpr int j = CV(jc), i = j < 13 ? j - 5 : j - 13;
                if constexpr (j < 2) gld<16 * j>(w4[j], l15 * 32, mk_rsrc(sk, 8192), xcd * 1024 + kh * 512);
                else if constexpr (j < 5) gld<0>(kk[j - 2], ko + so - 6144 + (j - 2) * 2048, mk_rsrc(kin, 4 << 20), 0);
                else if constexpr (j < 13) gld<(i & 1) * 2048>(kk[3 + i], ko, mk_rsrc(kin, 4 << 20), so + (i >> 1) * 4096);
                else gld<(i & 1) * 2048>(vv[i], ko, mk_rsrc(vin, 4 << 20), so + (i >> 1) * 4096);
            };
            {
                static_for<0, KLP>(kv_ld);
                dma1<DSK>(lbase, lane * 4, mk_rsrc(dskip, 2048), dt * 256);
                dma<U0, false>(lbase + kh * 1024, oob, rs0, 0);
            }
            static_for<4, 8>([&](auto ic) { feed(IC(-2), ic); });
            const __amdgpu_buffer_rsrc_t rk = mk_brs(ws + WS_KVT + (size_t)((xcd & 3) * 32 + kh * 16) * 32768, 524288);
            const unsigned K = lbase + KC + lw * KCH;
            unsigned kfo;
            const unsigned n0 = (xcd & 4) << 11;
            auto st = [&](int c) { st16(kvo[c], rk, (l15 >> 2) * 131072 + g4 * 64 + (l15 & 3) * 16, c * 32768 + (sb >> 5) * 16384 + (KVS ? nx : n0 ^ ((sb & 32) << 8)) + (sb & 31) * 256); };
            float kf[7], pa[4], pb[4];
            auto kv_step = [&](auto uc, auto jc) {
                constexpr int u = CV(uc), j = CV(jc), c = u >> 1, h = u & 1;
                auto el = [&](const u32x2& q) -> float { return (c & 1) ? bfhi(q[c >> 1]) : bflo(q[c >> 1]); };
                auto tap = [&](int t) { const unsigned w = w4[c >> 1][(c & 1) * 2 + (t >> 1)]; return (t & 1) ? bfhi(w) : bflo(w); };
                auto all = [&](auto f) {
#pragma unroll
                    for (int i = 0; i < 4; ++i) f(i);
                };
                if constexpr (j == 0) {
#pragma unroll
                    for (int i = 0; i < 7; ++i) { kf[i] = (h && i < 3) ? kf[i + 4] : el(kk[4 * h + i]); tie(kf[i]); }
                }
                if constexpr (EXACT) {
                    if constexpr (j == 1) { const float t = tap(3); all([&](int i) { pa[i] = kf[i] * t; }); all([&](int i) { pa[i] = rbf(pa[i]); tie(pa[i]); }); }
                    if constexpr (j == 2 || j == 4 || j == 6) { const float t = tap(3 - j / 2); all([&](int i) { pb[i] = kf[i + j / 2] * t; }); all([&](int i) { pb[i] = rbf(pb[i]); tie(pb[i]); }); }
                    if constexpr (j == 3 || j == 5 || j == 7) { all([&](int i) { pa[i] += pb[i]; }); all([&](int i) { pa[i] = rbf(pa[i]); tie(pa[i]); }); }
                } else {
                    if constexpr (j == 1) { const float t = tap(3); all([&](int i) { pa[i] = kf[i] * t; tie(pa[i]); }); }
                    if constexpr (j >= 2 && j <= 4) { const float t = tap(4 - j); all([&](int i) { pa[i] = fma_(kf[i + j - 1], t, pa[i]); tie(pa[i]); }); }
                    if constexpr (j == 5) all([&](int i) { pa[i] = rbf(pa[i]); tie(pa[i]); });
                }
                if constexpr (j == (EXACT ? 7 : 6)) all([&](int i) { pb[i] = el(vv[4 * h + i]); tie(pb[i]); });
                if constexpr (j == 7) {
                    all([&](int i) { pa[i] *= pb[i]; });
                    kvo[c][3 - 2 * h] = pack2(pa[1], pa[0]); kvo[c][2 - 2 * h] = pack2(pa[3], pa[2]);
                    if constexpr (h && !KST && u >= kv_first(KSD)) st(c);
                    else if constexpr (u == 7) all(st);
                    else tie(kvo[c]);
                }
            };
            static_for<-1, 13>([&](auto kc) {
                constexpr int k = CV(kc);
                if constexpr (k == PGF && PGI == 0) page<1>();
                if constexpr (k == 2) hop<0>();
                if constexpr (k == 6) hop<6>();
                if constexpr (k == 8) hop<9>();
                if constexpr (k >= 0) barrier();
                if constexpr (k >= 0 && ((CTM >> k) & 1)) ctouch(k);
                if constexpr (k == KV0) {
                    wait_v<KLP == 21 ? 4 + hv_cnt(-1, k) : 9 - KLI - (KLI > 3) + hv_cnt(KLL + 1, k)>();
                    static_for<0, 11>([&](auto ic) { tie(kk[CV(ic)]); });
                    static_for<0, 8>([&](auto ic) { tie(vv[CV(ic)]); });
                    tie(w4[0]); tie(w4[1]); if constexpr (!LC) { keep(c_dt); keep(c_re); keep(c_im); }
                    TS(8);
                }
                if constexpr (k == KVP) {
                    wait_l<0>();
                    ties(tag); ties(dw);
                    tag += 0x01010101u;
                    __builtin_amdgcn_raw_buffer_store_b64(u32x2{tag, ~tag ^ vb}, rf, lane || vw >= 32 ? OOB : 0u, F_C + vw * 8, 16);
                }
                if constexpr (k == KCQ) dma1<POLL>(lbase + lw * 256, lane * 4, rsf, F_C);
                if constexpr (TM == 16 && k == KTI) stouch((unsigned long long)(ws + WS_E) + (TX ? (xcd * 128 + idx * 4 + lw) : b * 4 + lw) * 384);
                if constexpr (TM && TM < 16 && k == KTI) {
                    const unsigned wv = b * 4 + lw, a = lane < 32 ? ((TM & 1) ? wv * 4096 + lane * 128 : (TM & 4) && lane < 4 ? wv * 4096 + lane * 1024 + 896 : OOB) : lane < 35 && (TM & 2) ? (unsigned)(WS_E - WS_PP) + (wv * 3 + lane - 32) * 128 : OOB;
                    if constexpr (TM & 8) dma1<DUMP>(lbase + lw * 256, a, mk_rsrc(ws + WS_PP, WS_F + F_EP - WS_PP), 0);
                    else asm volatile("buffer_atomic_or %0, %1, %2, 0 offen sc1" :: "v"(0u), "v"(a), "s"(mk_rsrc(ws + WS_PP, WS_F + F_EP - WS_PP)) : "memory");
                }
                if constexpr (k == KCC) {
                    unsigned f, n, sp;
                    wait_v<hv_cnt(KCQ, k) - 1>();
                    dsr32<POLL - 131072>(f, lbase + 131072 + lw * 256 + lane * 4);
                    wait_l<0>();
                    vote_chk(f, (lane & 1) ? ~tag : tag, lane & 1, lbase + POLL + lw * 256 + lane * 4, lbase + POLL + lw * 256, lane * 4, rsf, F_C, sp);
                    nr = vote_cnt(f, tag, (xcd & 4) << 11, n);
                    nx = nr ^ ((xcd & 2) << 12);
                    kfo = F_K + (KVS ? nr : n0) + (((xcd & 3) * 2 + (idx >> 4)) * 64 + lw * 16) * 8;
                    TSV(11, n + 100 * (0x10000 - sp));
                }
                if constexpr (k == KFL) {
                    wait_v<hv_cnt(KVL + 1, k)>();
                    __builtin_amdgcn_raw_buffer_store_b64(u32x2{tag, ~tag}, rf, lane ? OOB : 0u, F_K + (KVS ? nr : n0) + (((xcd & 3) * 2 + kh) * 64 + sb) * 8, 16);
                    TSX(10);
                }
                if constexpr (k == 12) {
                    dma1<0>(K, oob, rs0, 0);
                    dma1<KCP>(K, oob, rs0, 0);
                }
                static_for<0, 8>([&](auto ic) {
                    constexpr int i = CV(ic), u0 = kv_first(k), nu = kv_first(k + 1) - u0;
                    static_for<KLP, 21>([&](auto jc) { if constexpr (ld_pos(CV(jc)) == 8 * k + i) kv_ld(jc); });
                    if constexpr (k == KPL && i == KPI) dma1<POLL>(lbase + wid * 256, (lane & 31) * 4, rsf, kfo);
                    if constexpr (k == PGF && PGI && i == PGI) page<1>();
                    feed(kc, ic);
                    if constexpr (k == 4 && i == 4) hop<3>();
                    if constexpr (!KST && k == KSD && (i & 1) && i / 2 < kv_first(KSD) / 2) st(i / 2);
                    if constexpr (nu == 1) kv_step(IC(u0), ic);
                    if constexpr (nu == 2) { kv_step(IC(u0 + i / 4), IC(2 * (i & 3))); kv_step(IC(u0 + i / 4), IC(2 * (i & 3) + 1)); }
                    if constexpr (!KDL && k == 12 && i >= 4) gld<(i - 4) * 1024>(kd[i - 4], lane * 16, mk_rsrc(ssmk, 4 << 20), ch * 4096);
                    if constexpr (k == 11 && i == 4) c_setup();
                    if constexpr (k == 10 && i >= 4) { cad[i - 4] = cst + frag(i & 1, (i >> 1) & 1); tie(cad[i - 4]); }
                    sched();
                });
                if constexpr (SUG == 3) {
                    auto go = [&](auto cc) { constexpr int c = CV(cc); if constexpr (c != 25 && c != 26 && (c < 29 || c > 32)) su(IC(1), cc, lw_, bb_, ln_); };
                    if constexpr (k == SV0) { ties(lw_); ties(bb_); tie(ln_); go(IC(0)); }
                    if constexpr (k >= SV0 && k < SV0 + 2) static_for<k == SV0 ? SU_S : 33, k == SV0 ? 33 : SU_D>(go);
                    if constexpr (k == SK0) { su(IC(1), IC(29), lw_, bb_, ln_); su(IC(1), IC(30), lw_, bb_, ln_); wait_l<0>(); tie(dsw_); }
                    if constexpr (k >= SK0 && k < SK1) static_for<1 + (k - SK0) * (SU_S - 1) / (SK1 - SK0), 1 + (k + 1 - SK0) * (SU_S - 1) / (SK1 - SK0)>(go);
                    sched();
                } else
                if constexpr (SUG && k >= SK0 && k < SK1) {
                    constexpr int nn = SUG == 1 ? SU_S : SU_D;
                    if constexpr (k == SK0) { ties(lw_); ties(bb_); tie(ln_); }
                    static_for<(k - SK0) * nn / (SK1 - SK0), (k + 1 - SK0) * nn / (SK1 - SK0)>([&](auto cc) {
                        constexpr int c = CV(cc);
                        if constexpr (c != 25 && c != 26 && c != 31 && c != 32) su(IC(1), cc, lw_, bb_, ln_);
                        if constexpr (c == 30) { wait_l<0>(); tie(dsw_); }
                    });
                    sched();
                }
            });
            page<12>();
            TS(1);
            if constexpr (EB == 2) barrier();
            unsigned pf;
            const unsigned fr = lbase + FULL + (lane & 3) * FST;
            wait_v<hv_cnt(KPL, 12) - (KPL < 12 ? 1 : 2) - (KPL == KFL) - KPI - (KPI > 3) + (KDL ? 7 : 11)>();
            dsr32<POLL - 131072>(pf, lbase + 131072 + wid * 256 + lane * 4);
            wait_l<0>();
            tag_chk<KSL>(pf, (lane & 1) ? ~tag : tag, (lane & 31) * 4, rsf, kfo);
            if constexpr (KDL) static_for<0, 4>([&](auto ic) { gld<CV(ic) * 1024>(kd[CV(ic)], lane * 16, mk_rsrc(ssmk, 4 << 20), ch * 4096); });
            auto b13 = [&]() { if constexpr (NB == 2) { unsigned f = 1; zero_chk<12, 1>(f, lbase + DONE + (lane & 3) * FST); } else if constexpr (NB < 2) barrier(); };
            if constexpr (EB == 1) b13();
            {
                const u32x4 rs = mk_rsrc(ws + WS_KVT + (size_t)((xcd & 3) * 32 + idx) * 32768, 32768);
                static_for<0, 4>([&](auto ic) { constexpr int i = CV(ic); dma<U0 + 2048 + i * 1024, true>(lbase + lw * 4096, (lane >> 2) * 64 + (((lane & 3) ^ g4) << 4), rs, (lw >> 1) * 16384 + (KVS ? nx : n0 ^ ((lw & 2) << 12)) + (lw & 1) * 4096 + i * 1024); });
            }
            mark(IC(16));
            auto warm = [&]() {
                const unsigned wo = g4 * 2048 + l15 * 16, dl = lbase + lw * 1024;
                const unsigned qo = (512 * (idx >> 3) + 128 * (idx & 1) + 32 * lw + 4 * ((idx >> 1) & 3)) * 2048 + xcd * 256, xo = (tm2 * 128 + 32 * lw + 8 * (qx & 2)) * 2048 + dt * 256;
                static_for<0, 4>([&](auto ic) { constexpr int i = CV(ic); dma<DUMP, false>(dl, wo, mk_rsrc(qin, 4 << 20), qo + ((i & 1) * 16 + (i >> 1) * 256) * 2048); });
                static_for<0, 4>([&](auto ic) { constexpr int i = CV(ic); dma<DUMP, false>(dl, wo, mk_rsrc(x, 4 << 20), xo + i * 8192); });
            };
            if constexpr (PFQ == 1) warm();
            if constexpr (EB == 0) b13();
            auto c_first = [&]() {
                if constexpr (CL <= 1) c_unit(IC(0));
                if constexpr (CL <= 0) c_unit(IC(1));
                if constexpr (EC == 1) static_for<0, 4>([&](auto ic) { constexpr int i = CV(ic); dma4<DUMP, i>(lbase, vc[i], mk_rsrc(cre + (size_t)dt * 131072, 262144), c_off(lw, 0)); });
                if constexpr (PFQ == 2) warm();
            };
            static_assert(NB != 3 || CL == 2, "");
            if constexpr (!KO) c_first();
            wait_v<5 + 8 * (PFQ == 1) + (KO ? 0 : 4 * (CL <= 1) + 4 * (CL <= 0) + XV)>();
            static_for<0, 4>([&](auto ic) { tie(kd[CV(ic)]); });
#pragma unroll
            for (int n = 0; n < 4; ++n) {
                const unsigned first = n ? __builtin_amdgcn_readlane(kd[n ? n - 1 : 0][3], 63) : 0u;
                const unsigned p = __builtin_amdgcn_update_dpp(first, kd[n][3], 0x138, 0xf, 0xf, false);
                ke[n] = u32x4{__builtin_amdgcn_alignbyte(kd[n][0], p, 2), __builtin_amdgcn_alignbyte(kd[n][1], kd[n][0], 2),
                              __builtin_amdgcn_alignbyte(kd[n][2], kd[n][1], 2), __builtin_amdgcn_alignbyte(kd[n][3], kd[n][2], 2)};
                tie(ke[n]);
            }
            if constexpr (ZM) {
                const unsigned dl = (ch & 1) ? dw >> 16 : dw & 0xffffu, dv = dl << ((~lane & 1) * 16);
                static_for<0, 4>([&](auto ic) { constexpr unsigned long long k = lag0_mask(CV(ic)); ad[CV(ic)] = sel0(dv, k); });
                tie(ad);
            }
            hop<14>();
            static_for<0, 4>([&](auto ic) { constexpr int i = CV(ic); dsw128<64 + i * 1024>(K + lane * 16, kd[i]); dsw128<KCP + 64 + i * 1024>(K + lane * 16, ke[i]); });
            if constexpr (KO) c_first();
            TS(9);
            static_for<0, -CL>([&](auto uc) {
                constexpr int u = CV(uc);
                wait_v<4 + (u < 2 ? XV : 0)>();
                static_for<0, 4>([&](auto ic) { constexpr int i = CV(ic); dsr128<(u & 1) * 4096>(cf[u * 4 + i], cad[i]); });
                wait_l<0>();
                static_for<0, 4>([&](auto ic) { tie(cf[u * 4 + CV(ic)]); });
                c_unit(IC(u + 2));
            });
            pf = 1;
            zero_chk<15 * 4>(pf, fr);
            if constexpr (PRE) { pf = 1; zero_chk<PRE == 1 ? 8 : 4>(pf, lbase + DONE + (lane & 3) * FST); }
            __builtin_amdgcn_s_setprio(0);
        }
        TS(2);
        TL(1);
        unsigned ka[4], ub[4];
#pragma unroll
        for (int i = 0; i < 4; ++i) {
            ka[i] = lbase + KC + lw * KCH + ((l15 & 1) ? 0 : KCP) + 84 + 2 * (l15 & 14) - 16 * g4 + i * 1024;
            const int e = 2 * l15 + g4 + 2;
            ub[i] = lbase + U0 + 64 * e + ((lw ^ (((e >> 2) + 3 - i) & 3)) << 4);
        }
        unsigned gad = lbase + DONE + lw * FST;
        if constexpr (GQ >= 0) tie(gad);
        f32x4 acc[8];
        bf16x4 FA[8][2]; bf16x8 FB[8];
        auto op = [&](auto gc) {
            constexpr Op o = cv_op(CV(gc));
            if constexpr (o.kind == CO_A) { constexpr int s = o.a >> 3, n = o.a & 7, d = (n & 1) * 128 + s * 16 + o.b * 2; dsr2<d, d + 1>(FA[n][o.b], ka[n >> 1]); }
            if constexpr (o.kind == CO_B) { constexpr int s = o.a >> 3, m = o.a & 7; dsr128<2048 * m + 1792 - 256 * s>(FB[m], ub[s & 3]); }
            if constexpr (o.kind == CO_C) dsr128<(o.a & 1) * 4096>(cf[o.a * 4 + o.b], cad[o.b]);
        };
        static_for<-36, 0>(op);
        static_for<0, 288>([&](auto gc) {
            constexpr int G = CV(gc), s = G / 36, q = G % 36, n = cv_n(q), m = cv_m(q);
            if constexpr (cv_wait(G) >= 0) wait_lr<cv_wait(G)>();
            if constexpr (q == 24 && s >= CL && s - CL < 8) static_for<0, 4>([&](auto ic) { tie(cf[(s - CL) * 4 + CV(ic)]); });
            if constexpr (ZM && G < 8) mfma<true>(acc[m], __builtin_bit_cast(bf16x8, ad), FB[m]);
            mfma<s == 0 && n == 0 && !ZM>(acc[n + m], cat(FA[n][0], FA[n][1]), FB[m]);
            sched();
            if constexpr (q == 11) TL(2 + 2 * s);
            if constexpr (q == 11 && s >= CL && s - CL < 8) wait_v<(s - CL < 7 ? 4 : 0) + (s - CL < (CL <= 1) + (CL <= 0) ? XV : 0) + 8 * (PFQ >= 3 && (s == PFQ - 2 || s == PFQ - 1))>();
            if constexpr (q == 11) TL(3 + 2 * s);
            op(gc);
            if constexpr (q == 30 && ((CTM >> (16 + s)) & 1)) ctouch(16 + s);
            if constexpr (!SUG && G == SU0) { ties(lw_); ties(bb_); }
            if constexpr (!SUG && G >= SU0 && G < SU0 + 2 * SU_S && !((G - SU0) & 1)) su(IC(1), IC((G - SU0) / 2), lw_, bb_, ln_);
            if constexpr (SUG < 2 && G == 259) tie(ln_);
            if constexpr (SUG < 2 && G >= 259 && G < 259 + SU_N - SU_S) su(IC(1), IC(G - 259 + SU_S), lw_, bb_, ln_);
            if constexpr (SUG >= 2 && G >= 270 && G < 288 && !(G & 1)) { constexpr int j = (G - 270) / 2; su(IC(1), IC(j < 2 ? 25 + j : j < 4 ? 29 + j : SU_D - 4 + j), lw_, bb_, ln_); }
            if constexpr (G == 16) hop<17>();
            if constexpr (G == 66) hop<19>();
            if constexpr (G == PG1) page<21>();
            if constexpr (G == PG2) page<28>();
            if constexpr (G == 158) hop<23>();
            if constexpr (G == 209) hop<25>();
            if constexpr (G == 254) hop<27>();
            if constexpr (G == GQ) { unsigned f = 1; zero_chk<4, 1>(f, gad); }
            if constexpr (!NB && G == CBQ) barrier();
            if constexpr (NB == 3 && G == 23) { unsigned f = 1; zero_chk<8, 1>(f, lbase + DONE + (lane & 3) * FST); }
            if constexpr ((q == 24 || q == 25 || q == 29 || q == 34) && s + 2 - CL < 8) {
                constexpr int pc = q == 24 ? 0 : q == 25 ? 1 : q == 29 ? 2 : 3, u = s + 2 - CL;
                dma4<(u & 1) * 4096, pc>(cst, vc[pc], rsc, c_off(lw, u));
            }
            sched();
        });
        if constexpr (PG2 >= 288) page<28>();
        TL(18);
        TSX(3);
        {
            const __amdgpu_buffer_rsrc_t rz = mk_brs(ws + WS_ZT, 4 << 20);
            if constexpr (ZM) {
                asm volatile("s_nop 7");
#pragma unroll
                for (int jj = 0; jj < 4; ++jj) {
                    tie(acc[2 * jj]); tie(acc[2 * jj + 1]);
                    const f32x4 a = acc[2 * jj], c = acc[2 * jj + 1];
                    st16(u32x4{pack2(a[0], a[1]), pack2(a[2], a[3]), pack2(c[0], c[1]), pack2(c[2], c[3])}, rz, lane * 16 + jj * 1024, (ch & 510) * 8192 + nx + (ch & 1) * 4096);
                }
            } else {
            u32x2 kvr[8];
            const unsigned kva = lbase + U0 + 2048 + 128 * l15 + 64 * (g4 >> 1) + ((lw ^ ((l15 >> 1) & 3)) << 4) + 8 * (1 - (g4 & 1));
            static_for<0, 8>([&](auto ic) { dsr64<CV(ic) * 2048>(kvr[CV(ic)], kva); });
            wait_l<0>();
            const float Dd = (ch & 1) ? bfhi(dw) : bflo(dw);
#pragma unroll
            for (int jj = 0; jj < 4; ++jj) {
                u32x4 o;
#pragma unroll
                for (int u = 0; u < 2; ++u) {
                    const int j = 2 * jj + u;
                    tie(kvr[j]); tie(acc[j]);
                    const float kv3 = bflo(kvr[j][0]), kv2 = bfhi(kvr[j][0]), kv1 = bflo(kvr[j][1]), kv0 = bfhi(kvr[j][1]);
                    o[2 * u] = pack2(rbf(acc[j][0]) + rbf(kv0 * Dd), rbf(acc[j][1]) + rbf(kv1 * Dd));
                    o[2 * u + 1] = pack2(rbf(acc[j][2]) + rbf(kv2 * Dd), rbf(acc[j][3]) + rbf(kv3 * Dd));
                }
                st16(o, rz, lane * 16 + jj * 1024, (ch & 510) * 8192 + nx + (ch & 1) * 4096);
                if (jj == 1) { sched(); hop<29>(); sched(); }
            }
            }
            TS(8);
            hop<30>();
            if constexpr (CL > 0) {
                wait_v<4>();
                static_for<32 - 4 * CL, 32>([&](auto ic) { constexpr int i = CV(ic); dsr128<((i >> 2) & 1) * 4096>(cf[i], cad[i & 3]); });
                wait_l<0>();
                static_for<32 - 4 * CL, 32>([&](auto ic) { tie(cf[CV(ic)]); });
            }
        }
        TL(19);
        TSX(4);
    }
    {
        unsigned pf, fso, xso;
        const int wid = fresh(wid_);
        static_assert(!PB && PK && KVS && P_W < -1, "");
        unsigned& a_p = l16; unsigned& a_w = l16;
        auto fill = [&](auto sc, auto pc) {
            constexpr int s = CV(sc), p = CV(pc);
            if constexpr (p == 0) dma_x<(2 * s % 3) * BUF, (s >> 1) * 16384 + (s & 1) * 262144, true>(fso, wl, l16, rws, hs0);
            else if constexpr (fill_m0(4 * s + p)) dma_m<(2 * s % 3) * BUF, p * 1024>(fso, wl, l16, rws);
            else dma_n<p * 1024>(fso, l16, rws);
        };
        bf16x4 hf[2][8];
        auto ldh = [&](auto nc, auto ic) {
            constexpr int n = CV(nc), i = CV(ic), j = (i >> 1) & 1, bo = (2 * (n >> 2) % 3) * BUF + (g2_unit(n, j) & 3) * 8192 + (i >> 2) * 4096, hi = bo >= 65536 ? 65536 : 0;
            dsr64tr<bo - hi>(hf[n & 1][i], ha[2 * g2_tile(n, j) + (i & 1) + (hi >> 14)]);
        };
        f32x4 yc[2][2][2];
        f32x4 im[2], ao, ob[2]; u32x4 pl[2], po;
        bf16x8 xf[2];
        u32x2 zv[4], qv[2];
        unsigned ck, ex, fail[2];
        float tq_[2], tz, sm[8], mx[2], cv[8];
        auto finB = [&](auto pc, auto jc) {
            constexpr int p = CV(pc), j = CV(jc);
            if constexpr (j < 8) { yc[p & 1][0][j & 1][j >> 1] += im[j & 1][j >> 1]; tie(yc[p & 1][0][j & 1]); }
            else {
                constexpr int i = (j - 8) >> 2, k = j & 3, dd = k & 1, h = k >> 1;
                ob[i][k] = sel8(yc[p & 1][0][dd][2 * i + 1 - h], yc[p & 1][0][dd][2 * i + h], h ? hi8 : lo8);
                if constexpr (k == 3 && (p == 3 || !(NOST & 4))) gst<0>(ob[i], a_ob, rso, s_b ^ ((p & 1) * 65536 + i * 16384));
                if constexpr (j == 12) keep(ob[0]);
            }
        };
        auto snd = [&](auto pc, auto jc) {
            constexpr int p = CV(pc), j = CV(jc);
            if constexpr (j < 8) { sm[j] = yc[p][0][j >> 2][j & 3] + im[j >> 2][j & 3]; tie(sm[j]); }
            if constexpr (j == 8) mx[0] = amax3(sm[0], sm[1], sm[2]);
            if constexpr (j == 9) mx[1] = amax3(sm[3], sm[4], sm[5]);
            if constexpr (j == 10) mx[0] = amax3(mx[0], sm[6], sm[7]);
            if constexpr (j == 11) mx[0] = amax3(mx[0], mx[1], mx[1]);
            if constexpr (j == 12) { ex = f2u(mx[0]) + 0x4000u; tie(ex); }
            if constexpr (j == 13) { ex &= 0x7f800000u; tie(ex); }
            if constexpr (j == 14) { mx[0] = u2f(ex + 0x04c00008u); tie(mx[0]); }
            if constexpr (j == 15) { ex >>= 23; tie(ex); }
            if constexpr (j >= 16 && j < 24) { sm[j - 16] += mx[0]; tie(sm[j - 16]); }
            if constexpr (j == 24) { ex |= ex << 12; tie(ex); }
            if constexpr (j >= 25 && j < 29) { po[j - 25] = __builtin_amdgcn_perm(f2u(sm[2 * (j - 25) + 1]), f2u(sm[2 * (j - 25)]), psel); tie(po); }
            if constexpr (j == 29) { po[0] = bfi_v(msk, po[0], ex); tie(po); }
            if constexpr (j >= 30 && j < NS) { po[j - 29] = bfi_s(msk, po[j - 29], tn_[j - 30]); if constexpr (j < NS - 1) tie(po); }
            if constexpr (j == NS - 1 && !(NOST & 2)) gst<p * 1024>(po, l16, rws, ppo);
        };
        auto rcv = [&](auto hc, auto jc) {
            constexpr int h = CV(hc), j = CV(jc);
            if constexpr (j < 3) { pl[h][j + 1] ^= tn_[j]; pp_tie<h>(pl[h]); }
            if constexpr (j == 3) { ck = pl[h][1] | pl[h][2] | pl[h][3]; tie(ck); }
            if constexpr (j == 4) { if constexpr (PB) ck = bfi_f(msk, fail[h], ck); else ck &= 0xf000fu; tie(ck); }
            if constexpr (j == 6 && !(NOST & 2)) pp_chk<h>(pl[h], ck, tagg, l16, rws, ppi, a_p);
            if constexpr (j == 7) { ex = pl[h][0] >> 12; tie(ex); }
            if constexpr (j == 8) ex = bfi_v(msk, ex, pl[h][0]);
            if constexpr (j == 9) { pl[h][0] &= msk; pp_tie<h>(pl[h]); }
            if constexpr (j == 10) { ex <<= 23; tie(ex); }
            if constexpr (j == 11) { ex &= 0x7f800000u; tie(ex); }
            if constexpr (j == 12) { mx[1] = u2f(ex) * 0x1p-14f; tie(mx[1]); }
            if constexpr (h == 0 && j >= 13) {
                constexpr int k = j - 13, a = (k + 1) / 2, e = k == 15 ? 7 : k / 2 - 1;
                if constexpr (k == 0 || ((k & 1) && k < 15)) cv[a] = cvt16<a & 1>(pl[h][a >> 1]);
                else { yc[0][0][e >> 2][e & 3] = fma_(cv[e], mx[1], yc[0][0][e >> 2][e & 3]); tie(yc[0][0][e >> 2]); }
            }
            if constexpr (h == 1 && j >= 13 && j < 21) cv[j - 13] = cvt16<(j - 13) & 1>(pl[h][(j - 13) >> 1]);
            if constexpr (h == 1 && j >= 21) { constexpr int e = j - 21; im[e >> 2][e & 3] = fma_(cv[e], mx[1], im[e >> 2][e & 3]); tie(im[e >> 2]); }
        };
        auto side = [&](auto pc) {
            constexpr int P = CV(pc), n = P >> 3, m = P & 7, p = n >> 3;
            if constexpr (P == P_X || P == P_X + 2) dma_x<ZST + (P != P_X) * 1024, (P != P_X) * 16384, false>(xso, w2k, a_x, rsx, s_x);
            if constexpr (P == P_W) pf_ld(pf, a_w, rws, ppo);
            if constexpr (!NOA) {
            if constexpr (P == P_Q || P == P_Q + 2) dma_x<QST + (P != P_Q) * 1024, (P != P_Q) * 524288, false>(xso, w2k, a_q, rsq, s_q);
            if constexpr (P == P_T) gst<0>(tg2, a_t, rws, zfo);
            if constexpr (P == AT(ZP, 4)) { if constexpr (P_W >= 0) pf_keep(pf); pf_ld(pf, a_f, rws, zfo); }
            if constexpr (P == P_ZC) { wait_v<g2_allow(P, AT(ZP, 4))>(); tag_chk(pf, expect, a_f, rws, zfo); }
            if constexpr (P == P_Z || P == P_Z + 2) dma_x<ZST + (P != P_Z) * 1024, (P != P_Z) * 65536, true>(xso, w2k, P == P_Z ? a_z : a_z2, rws, s_z);
            }
            if constexpr (P == AT(7, 5) || P == AT(7, 7)) dsr128<(m - 5) * 512>(xf[(m - 5) / 2], a_xr);
            if constexpr (P == AT(8, 4)) { tie(xf[0]); tie(xf[1]); }
            if constexpr (P == AT(8, 5) || P == AT(9, 3)) static_for<0, 2>([&](auto dc) { mfma<false>(yc[n & 1][0][CV(dc)], xf[n & 1], __builtin_bit_cast(bf16x8, dg[CV(dc)])); });
            if constexpr ((n & 7) == 6 && m == XW && !(DBG & 2)) dsw128<0>(a_iw, yc[p & 1][1][0]);
            if constexpr ((n & 7) == 6 && m == XW2 && !(DBG & 2)) dsw128<1024>(a_iw, yc[p & 1][1][1]);
            if constexpr ((n & 7) == 7 && (m & 5) == (n < 31 ? 4 : 0)) dsr128<(m & 2) * 512>(im[(m >> 1) & 1], a_ir);
            if constexpr (n >= 8 && (n & 7) == 0 && m == 4) { tie(im[0]); tie(im[1]); }
            if constexpr (P >= P_PP0 && P <= E_PP0) snd(IC(0), IC(P - P_PP0));
            if constexpr (P >= P_PP1 && P <= E_PP1) static_for<0, D1>([&](auto ic) { snd(IC(1), IC((P - P_PP1) * D1 + CV(ic))); });
            if constexpr (PB && (P == P_B0 - 1 || P == P_B1 - 1)) pf_zero(pf);
            if constexpr (PB && (P == P_B0 || P == P_B1)) pf_at<(P == P_B1) * 1024>(pf, a_p, rws, ppi);
            if constexpr (P == P_L0 || P == P_L1) {
                unsigned so = ppi;
                if constexpr (PB) { wait_v<g2_allow(P, P - QB) + (g2_fill(P) >= 0)>(); pp_sel(so, fail[P == P_L1], pf, tn_[2], ppi, ppo); }
                pp_ld<P == P_L1>(pl[P == P_L1], l16, rws, so);
            }
            if constexpr (P == P_R0 || P == P_R1) { wait_v<g2_allow(P, P == P_R0 ? imax(P_L0, XL ? P_X + 2 : 0) : P_L1)>(); pp_tie<P == P_R1>(pl[P == P_R1]); }
            if constexpr (P >= P_R0 && P <= E_R0) static_for<0, D0>([&](auto ic) { if constexpr ((P - P_R0) * D0 + CV(ic) < NR) rcv(IC(0), IC((P - P_R0) * D0 + CV(ic))); });
            if constexpr (P >= P_R1 && P < P_R1 + 21) rcv(IC(1), IC(P - P_R1));
            if constexpr (!NOA) {
            if constexpr (n == ZW && m >= 4) dsr64<(m - 4) * 128>(zv[m - 4], a_zr);
            static_for<0, 4>([&](auto rc) {
                constexpr int r = CV(rc);
                if constexpr (P == q_tie(r)) tie(qv[r & 1]);
                if constexpr (P == q_rd(r)) dsr64<r * 256>(qv[r & 1], a_qr);
            });
            if constexpr (P == AT(ZW + 1, 4)) { tie(zv[0]); tie(zv[1]); tie(zv[2]); tie(zv[3]); }
            }
            if constexpr (a_step(P) >= 0) {
                constexpr int s = a_step(P), e = s == 0 ? 0 : s % 3 == 2 ? (s + 1) / 3 : s % 3 == 1 ? s / 3 : s / 3 - 1, r = e >> 2, c = e & 3;
                if constexpr (s == 0 || (s % 3 == 2 && e < 16)) { tq_[e & 1] = (c & 1) ? bfhi(qv[r & 1][c >> 1]) : bflo(qv[r & 1][c >> 1]); tie(tq_[e & 1]); }
                else if constexpr (s % 3 == 1) { tz = (r & 1) ? bfhi(zv[c][r >> 1]) : bflo(zv[c][r >> 1]); tie(tz); }
                else if constexpr (s % 3 == 0) {
                    ao[c] = tq_[e & 1] * tz;
                    if constexpr (c == 3 && !(NOST & 1)) gst<0>(ao, a_oa, rso, s_a + r * 8192);
                    else tie(ao);
                }
            }
            if constexpr (P >= P_F2 && P < P_F2 + 16) finB(IC(2), IC(P - P_F2));
            if constexpr (a_step(P - 1) > 0 && a_step(P - 1) % 12 == 0) keep(ao);
            if constexpr (P == E_PP0 + 1 || P == E_PP1 + 1) keep(po);
            if constexpr (P == P_F2 + 16) keep(ob[1]);
        };
        wait_v<8>();
        if (wid < 4) TS(7);
        TG_DECL
        barrier();
        static_for<0, 8>([&](auto ic) { ldh(IC(0), ic); });
        static_for<0, 32>([&](auto nc) {
            constexpr int n = CV(nc), p = n >> 3;
            if constexpr (n == PG3) page<33>();
            if constexpr (n == PG4) page<37>();
            wait_lr<(n & 3) == 3 ? 0 : 4 + g2_lx(n - 1)>();
            if constexpr (n == 31) barrier();
            if constexpr ((n & 3) == 3 && n < 31) {
                constexpr int need = g2_need(n);
                TL(21 + n / 4 * 3);
                wait_v<g2_allow(8 * n, need)>();
                TL(22 + n / 4 * 3);
                barrier();
                TL(23 + n / 4 * 3);
            }
            if constexpr (n == 1) hop<31>();
            if constexpr (n == 4) hop<32>();
            if constexpr (n == 11) hop<34>();
            if constexpr (n == 15) hop<35>();
            if constexpr (n == 19) hop<36>();
            if constexpr (n == 27) hop<38>();
            if constexpr (n == 31) hop<39>();
            if constexpr (n == 0) TL(20);
            TG(20 + n);
            if constexpr (n == 31) TL(42);
            if constexpr (PRG) __builtin_amdgcn_s_setprio(n == 31 ? 0 : 3 - ((n + 1) & 3));
            sched();
            static_for<0, 8>([&](auto mc) {
                constexpr int m = CV(mc), ks = m >> 2, tt = (m >> 1) & 1, dd = m & 1, P = n * 8 + m;
                if constexpr (m == 4 && (n & 3) != 3) wait_lr<(rd1(n) ? 8 : 4) + ((n & 7) == 6 ? (XW < 4) + (XW2 < 4) : 0)>();
                mfma<(n & 7) == 0 && ks == 0>(yc[p & 1][(DBG & 1) ? tt : g2_tile(n, tt)][dd], cat(hf[n & 1][4 * ks + 2 * tt], hf[n & 1][4 * ks + 2 * tt + 1]), cf[g2_unit(n, tt) * 4 + 2 * ks + dd]);
                sched();
                if constexpr (n < 31 && rd1(n) && m < 4) { ldh(IC(n + 1), IC(2 * m)); ldh(IC(n + 1), IC(2 * m + 1)); }
                if constexpr (n < 31 && !rd1(n)) ldh(IC(n + 1), mc);
                if constexpr (g2_fill(P) >= 0) fill(IC(g2_fill(P) >> 2), IC(g2_fill(P) & 3));
                side(IC(P));
                sched();
            });
        });
        TG(52);
        if (wid >= 4) TSX(5); else TS(8);
        TG(53);
        static_for<256 - P_R1, 21>([&](auto jc) { rcv(IC(1), jc); });
        wait_l<0>();
        tie(im[0]); tie(im[1]);
        static_for<21, NR>([&](auto jc) { rcv(IC(1), jc); });
        asm volatile("s_nop 3");
        sched();
        static_for<0, 16>([&](auto jc) { finB(IC(3), jc); });
        if (lastw == 0) gst<0>(u32x2{tagg, tagg}, l16 ? OOB : 0u, rws, WS_F + F_EP);
    }
}

}

#if defined(__HIP_DEVICE_COMPILE__)
#pragma clang attribute pop
#endif

extern "C" void kernel_launch(void* const* d_in, const int* in_sizes, int n_in, void* d_out, int out_size, void* d_ws, size_t ws_size,
                              hipStream_t stream) {
    (void)in_sizes; (void)n_in; (void)out_size; (void)ws_size;
    using namespace fk;
    const uint16_t* q = (const uint16_t*)d_in[0];
    const uint16_t* kin = (const uint16_t*)d_in[1];
    const uint16_t* vin = (const uint16_t*)d_in[2];
    const uint16_t* sk = (const uint16_t*)d_in[3];
    const uint16_t* ssmk = (const uint16_t*)d_in[4];
    const uint16_t* ssmD = (const uint16_t*)d_in[5];
    const uint16_t* x = (const uint16_t*)d_in[6];
    const uint16_t* lre = (const uint16_t*)d_in[7];
    const uint16_t* lim = (const uint16_t*)d_in[8];
    const uint16_t* bre = (const uint16_t*)d_in[9];
    const uint16_t* bim = (const uint16_t*)d_in[10];
    const uint16_t* cre = (const uint16_t*)d_in[11];
    const uint16_t* cim = (const uint16_t*)d_in[12];
    const uint16_t* logdt = (const uint16_t*)d_in[13];
    const uint16_t* dskip = (const uint16_t*)d_in[14];
#define WOFF 0
    char* ws = (char*)(((uintptr_t)d_ws + ((size_t)WOFF << 20) + 0x1fffff) & ~(uintptr_t)0x1fffff);
    hipLaunchKernelGGL(k1, dim3(256), dim3(512), 0, stream, x, bre, bim, lre, lim, logdt, ws, cre, cim, dskip, ssmk, ssmD, q, kin, vin, sk, (float*)d_out TS_PASS(0));
}
```

```cpp
#include <hip/hip_runtime.h>
#include <stdint.h>
#include <type_traits>

#if defined(__HIP_DEVICE_COMPILE__)
#pragma clang attribute push(__attribute__((target("no-packed-fp32-ops"))), apply_to = function)
#endif

namespace fk {

typedef __attribute__((ext_vector_type(8))) short bf16x8;
typedef __attribute__((ext_vector_type(4))) short bf16x4;
typedef __attribute__((ext_vector_type(4))) float f32x4;
typedef __attribute__((ext_vector_type(4))) unsigned u32x4;
typedef __attribute__((ext_vector_type(16))) unsigned u32x16;
typedef __attribute__((ext_vector_type(2))) unsigned u32x2;
typedef __attribute__((ext_vector_type(2))) __bf16 bf16x2_t;
typedef __attribute__((address_space(1))) unsigned gu32;
typedef __attribute__((address_space(1))) unsigned long long gu64;
#define RLX_AGENT __ATOMIC_RELAXED, __HIP_MEMORY_SCOPE_AGENT

__device__ __forceinline__ float u2f(unsigned v) { return __builtin_bit_cast(float, v); }
__device__ __forceinline__ unsigned f2u(float v) { return __builtin_bit_cast(unsigned, v); }
__device__ __forceinline__ bool wall(bool p) { return __builtin_amdgcn_ballot_w64(p) == ~0ull; }
__device__ __forceinline__ float bflo(unsigned w) { return u2f(w << 16); }
__device__ __forceinline__ float bfhi(unsigned w) { return u2f(w & 0xffff0000u); }
__device__ __forceinline__ unsigned pack2(float lo, float hi) {
    bf16x2_t v; v[0] = (__bf16)lo; v[1] = (__bf16)hi; return __builtin_bit_cast(unsigned, v);
}
__device__ __forceinline__ float rbf(float f) { float r; asm("v_cvt_pk_bf16_f32 %0, 0, %1" : "=v"(r) : "v"(f)); return r; }
__device__ __forceinline__ float fma_(float a, float b, float c) { return __builtin_fmaf(a, b, c); }

template <int I, int N, class F>
__device__ __forceinline__ void static_for(F&& f) {
    if constexpr (I + 1 == N) f(std::integral_constant<int, I>{});
    else if constexpr (I < N) { static_for<I, I + (N - I) / 2>(f); static_for<I + (N - I) / 2, N>(f); }
}
#define IC(v) std::integral_constant<int, (v)>{}
#define CV(c) decltype(c)::value

#define TS_ARG
#define TS_ARG0
#define TS_PASS(off)
#define TS_DECL
#define TS(i)
#define TSV(i, v)
#define TL_DECL
#define TL(i)
#define TS_FLUSH(slot)

#define TG_DECL
#define TG(i)
template <int N> __device__ __forceinline__ void wait_v() { asm volatile("s_waitcnt vmcnt(%0)" ::"n"(N > 63 ? 63 : N) : "memory"); }
template <int N> __device__ __forceinline__ void wait_l() { asm volatile("s_waitcnt lgkmcnt(%0)" ::"n"(N > 14 ? 14 : N) : "memory"); }
template <int N> __device__ __forceinline__ void wait_lr() { __builtin_amdgcn_s_waitcnt(0xc07f | ((N > 14 ? 14 : N) << 8)); }
template <class T> __device__ __forceinline__ void tie(T& x) { asm volatile("" : "+v"(x)); }
template <class T> __device__ __forceinline__ void undef(T& x) { asm volatile("" : "=v"(x)); }
template <int OFF, class T> __device__ __forceinline__ void dsr128u(T& d, unsigned a) { asm volatile("ds_read_b128 %0, %1 offset:%2" : "+v"(d) : "v"(a), "n"(OFF)); }
template <class T> __device__ __forceinline__ void keep(const T& x) { asm volatile("" :: "v"(x)); }
template <int OFF, class T> __device__ __forceinline__ void dsr128(T& d, unsigned a) { asm volatile("ds_read_b128 %0, %1 offset:%2" : "=v"(d) : "v"(a), "n"(OFF)); }
template <int OFF, class T> __device__ __forceinline__ void dsr64(T& d, unsigned a) { asm volatile("ds_read_b64 %0, %1 offset:%2" : "=v"(d) : "v"(a), "n"(OFF)); }
template <int OFF> __device__ __forceinline__ void dsr32(unsigned& d, unsigned a) { asm volatile("ds_read_b32 %0, %1 offset:%2" : "=v"(d) : "v"(a), "n"(OFF)); }
template <int O0, int O1, class T> __device__ __forceinline__ void dsr2(T& d, unsigned a) { asm volatile("ds_read2_b32 %0, %1 offset0:%2 offset1:%3" : "=v"(d) : "v"(a), "n"(O0), "n"(O1)); }
template <int OFF> __device__ __forceinline__ void dsr64tr(bf16x4& d, unsigned a) { asm volatile("ds_read_b64_tr_b16 %0, %1 offset:%2" : "=v"(d) : "v"(a), "n"(OFF)); }
template <int OFF, class T> __device__ __forceinline__ void dsw128(unsigned a, const T& v) { asm volatile("ds_write_b128 %0, %1 offset:%2" :: "v"(a), "v"(v), "n"(OFF) : "memory"); }
template <int OFF> __device__ __forceinline__ void dsw32(unsigned a, unsigned v) { asm volatile("ds_write_b32 %0, %1 offset:%2" :: "v"(a), "v"(v), "n"(OFF) : "memory"); }
__device__ __forceinline__ bf16x8 cat(const bf16x4& lo, const bf16x4& hi) { return __builtin_shufflevector(lo, hi, 0, 1, 2, 3, 4, 5, 6, 7); }
__device__ __forceinline__ void barrier() { asm volatile("" ::: "memory"); __builtin_amdgcn_s_barrier(); asm volatile("" ::: "memory"); }
__device__ __forceinline__ int fresh(int v) { asm volatile("" : "+s"(v)); return v; }
__device__ __forceinline__ void sched() { __builtin_amdgcn_sched_barrier(0); }
template <bool FIRST> __device__ __forceinline__ void mfma(f32x4& c, const bf16x8& a, const bf16x8& b) {
    if constexpr (FIRST) asm volatile("v_mfma_f32_16x16x32_bf16 %0, %1, %2, 0" : "=v"(c) : "v"(a), "v"(b));
    else asm volatile("v_mfma_f32_16x16x32_bf16 %0, %1, %2, %0" : "+v"(c) : "v"(a), "v"(b));
}
struct cf32 { float r, i; };
__device__ __forceinline__ cf32 cmul(cf32 a, cf32 b) { return cf32{fma_(a.r, b.r, -(a.i * b.i)), fma_(a.r, b.i, a.i * b.r)}; }
__device__ __forceinline__ cf32 cfma(cf32 a, cf32 b, cf32 c) { return cf32{fma_(a.r, b.r, fma_(-a.i, b.i, c.r)), fma_(a.r, b.i, fma_(a.i, b.r, c.i))}; }
__device__ __forceinline__ void sw16(float v, float& e, float& o) { const auto r = __builtin_amdgcn_permlane16_swap(f2u(v), f2u(v), false, false); e = u2f(r[0]); o = u2f(r[1]); }
__device__ __forceinline__ void sw32(float v, float& e, float& o) { const auto r = __builtin_amdgcn_permlane32_swap(f2u(v), f2u(v), false, false); e = u2f(r[0]); o = u2f(r[1]); }
constexpr unsigned SPIN_CAP = 1u << 16;
constexpr unsigned OOB = 0x40000000u;

constexpr size_t WS_H = 0;
constexpr size_t WS_KVT = (size_t)8 << 20;
constexpr size_t WS_ZT = (size_t)12 << 20;
constexpr size_t WS_PP = (size_t)16 << 20;
constexpr size_t WS_E = (size_t)24 << 20;
constexpr size_t WS_F = WS_E + (256 << 10);
constexpr int F_H = 0;
constexpr int F_K = 4096;
constexpr int F_Z = 16384;
constexpr int F_EP = F_Z + 32768;
constexpr int F_C = F_EP + 256;
constexpr unsigned WS_P = 25 << 20;
#define KP 0
#define SU 0
#define TSX(i) do { if constexpr (!SU) TS(i); } while (0)
#define AB 1
#define SW 1
#define BL 0
#define GS 0
#define LC 0
#define CR 1
#define CD 2
#define SU0 190
#define SUG 3
#define SK0 7
#define SK1 12
#define PGI 2
#define SV0 0
#define CT 1
#define KCQ 2
#define KCC 4
#define KVP 0
#define RSL 1
#define PB 0
#define KTI 1
#define TM 0
#define TX 0
#define PK 1
#define HA 0
#define KVS 1
#define GCR 273
constexpr int LDS_TOTAL = 163840, BUF = 32768;
constexpr int KC = 98304, KCH = 8320, KCP = 4160;
constexpr int U0 = KC + 4 * KCH;
constexpr int FULL = U0 + 18432 + 8192, FST = 388;
constexpr int DONE = U0 + 18432, DUMP = DONE + 4096;
constexpr int HGO = FULL + 1792, DSK = HGO + 256, POLL = DSK + 256;
static_assert(POLL + 8 * 256 <= LDS_TOTAL && 4 * FST <= 1792, "lds");
constexpr int g1_buf(int s) { return ((0x0213 >> ((s & 3) * 4)) & 3) * BUF; }
constexpr int IMG = KC, QST = KC + 16384, ZST = U0, PLD = 2 * BUF;
#define CODE_KB 43
#define EXACT 0
#define HP 1
#define CP 2
#define FLQ 11
#define R01 3
#define ZP 7
#define SLP 8
#define STR_(x) #x
#define STR(x) STR_(x)
#define NOST 0
#define G2M 0
#define RD 2
#define PRG 1
#define OSC 16
#define NOA 0
#define PPH 0
#define HS 0
#define JQ(j) (HS == 0 ? 0 : HS == 1 ? (j) & 1 : HS == 2 ? (j) : (j) & 2)
#define JR(j) (HS == 0 ? 2 * (j) : HS == 1 ? 2 * ((j) & 2) : HS == 2 ? 0 : 4 * ((j) & 1))
#define NOX 0

__device__ __forceinline__ u32x4 mk_rsrc(const void* p, unsigned bytes) {
    const unsigned long long a = (unsigned long long)p;
    return u32x4{(unsigned)a, (unsigned)(a >> 32) & 0xffffu, bytes, 0x00020000u};
}
__device__ __forceinline__ __amdgpu_buffer_rsrc_t mk_brs(const void* p, unsigned bytes) { return __builtin_amdgcn_make_buffer_rsrc((void*)p, (short)0, bytes, 0x00020000); }
__device__ __forceinline__ void st16(const u32x4& d, __amdgpu_buffer_rsrc_t r, unsigned voff, unsigned soff) {
    __builtin_amdgcn_raw_buffer_store_b128(d, r, voff, soff, 16);
    asm volatile("s_nop 1" :: "v"(d) : "memory");
}
template <int IMM, bool SC1> __device__ __forceinline__ void dma(unsigned lds, unsigned voff, const u32x4& rs, unsigned soff) {
    if constexpr (SC1) asm volatile("s_add_u32 m0, %0, %1\n\ts_nop 0\n\tbuffer_load_dwordx4 %2, %3, %4 offen sc1 lds" :: "s"(lds), "n"(IMM), "v"(voff), "s"(rs), "s"(soff) : "memory", "scc");
    else asm volatile("s_add_u32 m0, %0, %1\n\ts_nop 0\n\tbuffer_load_dwordx4 %2, %3, %4 offen lds" :: "s"(lds), "n"(IMM), "v"(voff), "s"(rs), "s"(soff) : "memory", "scc");
}
template <int IMM, int I> __device__ __forceinline__ void dma4(unsigned lds, unsigned voff, const u32x4& rs, unsigned soff) {
    if constexpr (I == 0) dma<IMM, false>(lds, voff, rs, soff);
    else asm volatile("buffer_load_dwordx4 %0, %1, %2 offen offset:%3 lds" :: "v"(voff), "s"(rs), "s"(soff), "n"(I * 1024) : "memory");
}
template <int IMM> __device__ __forceinline__ void dma1(unsigned lds, unsigned voff, const u32x4& rs, unsigned soff) {
    asm volatile("s_add_u32 m0, %0, %1\n\ts_nop 0\n\tbuffer_load_dword %2, %3, %4 offen sc1 lds" :: "s"(lds), "n"(IMM), "v"(voff), "s"(rs), "s"(soff) : "memory", "scc");
}
__device__ __forceinline__ void flag_set(unsigned waddr, unsigned v) { asm volatile("ds_write_b32 %0, %1" :: "v"(waddr), "v"(v) : "memory"); }
__device__ __forceinline__ void flag_chk(unsigned& f, unsigned raddr, unsigned need) {
    unsigned cnt;
    asm volatile("v_cmp_le_u32 vcc, %3, %0\n\t"
                 "s_cmp_eq_u64 vcc, -1\n\t"
                 "s_cbranch_scc1 1f\n\t"
                 "s_mov_b32 %1, 0x100000\n"
                 "0:\n\t"
                 "ds_read_b32 %0, %2\n\t"
                 "s_sub_u32 %1, %1, 1\n\t"
                 "s_waitcnt lgkmcnt(0)\n\t"
                 "s_cbranch_scc1 1f\n\t"
                 "v_cmp_le_u32 vcc, %3, %0\n\t"
                 "s_cmp_eq_u64 vcc, -1\n\t"
                 "s_cbranch_scc0 0b\n"
                 "1:" : "+v"(f), "=&s"(cnt) : "v"(raddr), "s"(need) : "vcc", "scc", "memory");
}
template <int OFF, int SL = 0> __device__ __forceinline__ void zero_chk(unsigned& f, unsigned raddr) {
    unsigned cnt;
    asm volatile("v_cmp_ne_u32 vcc, 0, %0\n\t"
                 "s_cbranch_vccz 1f\n\t"
                 "s_mov_b32 %1, 0x100000\n"
                 "0:\n\t"
                 ".if %4\n\ts_sleep %4\n\t.endif\n\t"
                 "ds_read_b32 %0, %2 offset:%3\n\t"
                 "s_sub_u32 %1, %1, 1\n\t"
                 "s_waitcnt lgkmcnt(0)\n\t"
                 "s_cbranch_scc1 1f\n\t"
                 "v_cmp_ne_u32 vcc, 0, %0\n\t"
                 "s_cbranch_vccnz 0b\n"
                 "1:" : "+v"(f), "=&s"(cnt) : "v"(raddr), "n"(OFF), "n"(SL) : "vcc", "scc", "memory");
}
template <int IMM, class T> __device__ __forceinline__ void gld(T& d, unsigned voff, const u32x4& rs, unsigned soff) {
    if constexpr (sizeof(T) == 16) asm volatile("buffer_load_dwordx4 %0, %1, %2, %3 offen offset:%4" : "=v"(d) : "v"(voff), "s"(rs), "s"(soff), "n"(IMM) : "memory");
    else if constexpr (sizeof(T) == 8) asm volatile("buffer_load_dwordx2 %0, %1, %2, %3 offen offset:%4" : "=v"(d) : "v"(voff), "s"(rs), "s"(soff), "n"(IMM) : "memory");
    else asm volatile("buffer_load_ushort %0, %1, %2, %3 offen offset:%4" : "=v"(d) : "v"(voff), "s"(rs), "s"(soff), "n"(IMM) : "memory");
}
__device__ __forceinline__ void gld32(unsigned& d, unsigned voff, const u32x4& rs, unsigned soff) { asm volatile("buffer_load_dword %0, %1, %2, %3 offen" : "=v"(d) : "v"(voff), "s"(rs), "s"(soff) : "memory"); }
__device__ __forceinline__ void sld(unsigned& d, const void* p) { asm volatile("s_load_dword %0, %1, 0x0" : "=s"(d) : "s"(p) : "memory"); }
__device__ __forceinline__ void stouch(unsigned long long p) { asm volatile("s_load_dword s101, %0, 0x0\n\ts_load_dword s101, %0, 0x80\n\ts_load_dword s101, %0, 0x100" :: "s"(p) : "s101", "memory"); }
template <class T> __device__ __forceinline__ void ties(T& x) { asm volatile("" : "+s"(x)); }
template <int IMM, class T> __device__ __forceinline__ void gst(const T& d, unsigned voff, const u32x4& rs, unsigned soff) {
    if constexpr (sizeof(T) == 16) asm volatile("buffer_store_dwordx4 %0, %1, %2, %3 offen offset:%4 sc1" :: "v"(d), "v"(voff), "s"(rs), "s"(soff), "n"(IMM) : "memory");
    else asm volatile("buffer_store_dwordx2 %0, %1, %2, %3 offen offset:%4 sc1" :: "v"(d), "v"(voff), "s"(rs), "s"(soff), "n"(IMM) : "memory");
}
template <int SLQ> __device__ __forceinline__ void gate(unsigned raddr, unsigned need) {
    unsigned cnt, f;
    asm volatile("s_mov_b32 %1, 0x40000\n"
                 "0:\n\t"
                 "ds_read_b32 %0, %2\n\t"
                 "s_sub_u32 %1, %1, 1\n\t"
                 "s_waitcnt lgkmcnt(0)\n\t"
                 "s_cbranch_scc1 1f\n\t"
                 "v_cmp_le_u32 vcc, %3, %0\n\t"
                 "s_cmp_eq_u64 vcc, -1\n\t"
                 "s_cbranch_scc1 1f\n\t"
                 "s_sleep %4\n\t"
                 "s_branch 0b\n"
                 "1:" : "=&v"(f), "=&s"(cnt) : "v"(raddr), "s"(need), "n"(SLQ) : "vcc", "scc", "memory");
}
template <int SL = SLP, int AT_ = 0> __device__ __forceinline__ void tag_chk(unsigned& f, unsigned expect, unsigned voff, const u32x4& rs, unsigned soff) {
    unsigned cnt;
    asm volatile("v_cmp_eq_u32 vcc, %2, %0\n\t"
                 "s_cmp_eq_u64 vcc, -1\n\t"
                 "s_cbranch_scc1 1f\n\t"
                 "s_mov_b32 %1, 0x10000\n"
                 "0:\n\t"
                 ".if %7\n\tv_mov_b32 %0, 0\n\t.endif\n\t"
                 "s_sleep %6\n\t"
                 ".if %7\n\tbuffer_atomic_or %0, %3, %4, %5 offen sc0 sc1\n\t.else\n\tbuffer_load_dword %0, %3, %4, %5 offen sc1\n\t.endif\n\t"
                 "s_sub_u32 %1, %1, 1\n\t"
                 "s_waitcnt vmcnt(0)\n\t"
                 "s_cbranch_scc1 1f\n\t"
                 "v_cmp_eq_u32 vcc, %2, %0\n\t"
                 "s_cmp_eq_u64 vcc, -1\n\t"
                 "s_cbranch_scc0 0b\n"
                 "1:" : "+v"(f), "=&s"(cnt) : "v"(expect), "v"(voff), "s"(rs), "s"(soff), "n"(SL), "n"(AT_) : "vcc", "scc", "memory");
}

__device__ __forceinline__ unsigned race(unsigned w, const void* ws, unsigned long long rnd) {
    unsigned v;
    asm volatile("s_cmp_lt_u32 %1, 32\n\t"
                 "s_cbranch_scc0 1f\n\t"
                 "s_waitcnt lgkmcnt(0)\n\t"
                 "s_mov_b64 s[98:99], %3\n\t"
                 "s_lshl_b32 s96, s98, 7\n\t"
                 "s_lshl_b32 s97, %1, 13\n\t"
                 "s_andn2_b32 s96, s96, 0x2000\n\t"
                 "s_xor_b32 s96, s96, s97\n\t"
                 "s_and_b32 s96, s96, 0x7ff80\n\t"
                 "s_add_u32 s96, s96, %4\n\t"
                 "s_xor_b32 s97, s96, 0x2000\n\t"
                 "s_mov_b32 %0, s98\n\t"
                 "s_load_dword s99, %2, s96\n\t"
                 "s_load_dword s98, %2, s97\n\t"
                 "s_lshr_b32 s96, %1, 4\n\t"
                 "s_xor_b32 s96, s96, %1\n\t"
                 "s_waitcnt lgkmcnt(1)\n\t"
                 "s_sleep %5\n\t"
                 "s_cmp_lg_u32 %0, s98\n\t"
                 "s_addc_u32 %0, s96, 0\n\t"
                 "s_and_b32 %0, %0, 1\n"
                 "1:" : "=&s"(v) : "s"(w), "s"(ws), "s"(rnd), "n"(WS_P), "n"(RSL) : "s96", "s97", "s98", "s99", "scc", "memory");
    return v;
}
__device__ __forceinline__ void vote_chk(unsigned& f, unsigned expect, unsigned odd, unsigned raddr, unsigned lds, unsigned voff, const u32x4& rs, unsigned soff, unsigned& cnt) {
    unsigned t;
    asm volatile("s_mov_b32 %1, 0x10000\n\t"
                 "v_xor_b32 %2, %0, %3\n\t"
                 "v_cmp_gt_u32 vcc, %2, %4\n\t"
                 "s_cbranch_vccz 1f\n\t"
                 "s_mov_b32 m0, %6\n"
                 "0:\n\t"
                 "s_sleep 4\n\t"
                 "buffer_load_dword %7, %8, %9 offen sc1 lds\n\t"
                 "s_sub_u32 %1, %1, 1\n\t"
                 "s_waitcnt vmcnt(0)\n\t"
                 "ds_read_b32 %0, %5\n\t"
                 "s_waitcnt lgkmcnt(0)\n\t"
                 "s_cbranch_scc1 1f\n\t"
                 "v_xor_b32 %2, %0, %3\n\t"
                 "v_cmp_gt_u32 vcc, %2, %4\n\t"
                 "s_cbranch_vccnz 0b\n"
                 "1:" : "+v"(f), "=&s"(cnt), "=&v"(t) : "v"(expect), "v"(odd), "v"(raddr), "s"(lds), "v"(voff), "s"(rs), "s"(soff) : "vcc", "scc", "memory");
}
__device__ __forceinline__ unsigned vote_cnt(unsigned f, unsigned tag, unsigned up, unsigned& n) {
    unsigned r;
    asm volatile("s_xnor_b32 %0, %3, 1\n\tv_cmp_eq_u32 vcc, %0, %2\n\ts_bcnt1_i32_b64 %1, vcc\n\ts_cmp_gt_u32 %1, 16\n\ts_cselect_b32 %0, 0x2000, 0\n\ts_xor_b32 %0, %0, %4"
                 : "=&s"(r), "=&s"(n) : "v"(f), "s"(tag), "s"(up) : "vcc", "scc");
    return r;
}

template <int H> __device__ __forceinline__ void pp_ld(u32x4& d, unsigned voff, const u32x4& rs, unsigned soff) {
    if constexpr (H) asm volatile("buffer_load_dwordx4 v[244:247], %1, %2, %3 offen offset:1024 sc1" : "={v[244:247]}"(d) : "v"(voff), "s"(rs), "s"(soff) : "memory");
    else asm volatile("buffer_load_dwordx4 v[240:243], %1, %2, %3 offen sc1" : "={v[240:243]}"(d) : "v"(voff), "s"(rs), "s"(soff) : "memory");
}
__device__ __forceinline__ void pf_ld(unsigned& d, unsigned voff, const u32x4& rs, unsigned soff) { asm volatile("buffer_load_dword v248, %1, %2, %3 offen sc1" : "={v248}"(d) : "v"(voff), "s"(rs), "s"(soff) : "memory"); }
__device__ __forceinline__ void pf_keep(unsigned d) { asm volatile("" :: "{v248}"(d)); }
__device__ __forceinline__ void pf_zero(unsigned& d) { asm volatile("v_mov_b32 v248, 0" : "={v248}"(d)); }
template <int OFF> __device__ __forceinline__ void pf_at(unsigned& d, unsigned voff, const u32x4& rs, unsigned soff) {
    asm volatile("buffer_atomic_or v248, %1, %2, %3 offen offset:%4 sc0 sc1" : "+{v248}"(d) : "v"(voff), "s"(rs), "s"(soff), "n"(OFF) : "memory");
}
__device__ __forceinline__ void pp_sel(unsigned& so, unsigned& fail, unsigned d, unsigned tn, unsigned yes, unsigned no) {
    asm volatile("v_readlane_b32 %1, v248, 63\n\ts_and_b32 %1, %1, 0xf000f\n\ts_cmp_eq_u32 %1, %3\n\ts_cselect_b32 %0, %4, %5\n\ts_cselect_b32 %1, 0, -1"
                 : "=&s"(so), "=&s"(fail) : "{v248}"(d), "s"(tn), "s"(yes), "s"(no) : "scc");
}
#define PP_CHK(R, R1, R2, R3, OFF) \
    asm volatile("v_cmp_ne_u32 vcc, 0, %2\n\t" \
                 "s_cbranch_vccz 1f\n\t" \
                 "s_mov_b32 %1, 0x10000\n" \
                 "0:\n\t" \
                 ".if %9\n\t" \
                 "v_mov_b32 %2, 0\n\t" \
                 "s_sub_u32 %1, %1, 1\n\t" \
                 "s_cbranch_scc1 1f\n\t" \
                 "buffer_atomic_or %2, %8, %6, %7 offen offset:" OFF " sc0 sc1\n\t" \
                 "s_lshr_b32 vcc_lo, %4, 4\n\t" \
                 "s_waitcnt vmcnt(0)\n\t" \
                 "v_readlane_b32 %3, %2, 63\n\t" \
                 "s_xor_b32 %3, %3, vcc_lo\n\t" \
                 "s_and_b32 %3, %3, 0xf000f\n\t" \
                 "s_cbranch_scc1 0b\n\t" \
                 ".endif\n\t" \
                 "buffer_load_dwordx4 " R ", %5, %6, %7 offen offset:" OFF " sc1\n\t" \
                 "s_waitcnt vmcnt(0)\n\t" \
                 "s_and_b32 %3, %4, 0xf000f\n\t" \
                 "v_xor_b32 " R1 ", %3, " R1 "\n\t" \
                 "s_lshr_b32 %3, %4, 8\n\t" \
                 "s_and_b32 %3, %3, 0xf000f\n\t" \
                 "v_xor_b32 " R2 ", %3, " R2 "\n\t" \
                 "s_lshr_b32 %3, %4, 4\n\t" \
                 "s_and_b32 %3, %3, 0xf000f\n\t" \
                 "v_xor_b32 " R3 ", %3, " R3 "\n\t" \
                 "v_or3_b32 %2, " R1 ", " R2 ", " R3 "\n\t" \
                 "v_and_b32 %2, 0xf000f, %2\n\t" \
                 "s_sub_u32 %1, %1, 1\n\t" \
                 "s_cbranch_scc1 1f\n\t" \
                 "v_cmp_ne_u32 vcc, 0, %2\n\t" \
                 "s_cbranch_vccnz 0b\n" \
                 "1:" : "+{" R "}"(p), "=&s"(cnt), "+v"(bad), "=&s"(t) : "s"(tag), "v"(voff), "s"(rs), "s"(soff), "v"(vp), "n"(PB) : "vcc", "scc", "memory")
template <int H> __device__ __forceinline__ void pp_chk(u32x4& p, unsigned bad, unsigned tag, unsigned voff, const u32x4& rs, unsigned soff, unsigned vp) {
    unsigned cnt, t;
    if constexpr (H) PP_CHK("v[244:247]", "v245", "v246", "v247", "1024"); else PP_CHK("v[240:243]", "v241", "v242", "v243", "0");
}
template <int H> __device__ __forceinline__ void pp_tie(u32x4& p) { if constexpr (H) asm volatile("" : "+{v[244:247]}"(p)); else asm volatile("" : "+{v[240:243]}"(p)); }
__device__ __forceinline__ float amax3(float a, float b, float c) { float r; asm volatile("v_max3_f32 %0, |%1|, |%2|, |%3|" : "=v"(r) : "v"(a), "v"(b), "v"(c)); return r; }
__device__ __forceinline__ unsigned bfi_v(unsigned m, unsigned a, unsigned b) { unsigned r; asm volatile("v_bfi_b32 %0, %1, %2, %3" : "=v"(r) : "v"(m), "v"(a), "v"(b)); return r; }
__device__ __forceinline__ unsigned bfi_s(unsigned m, unsigned a, unsigned b) { unsigned r; asm volatile("v_bfi_b32 %0, %1, %2, %3" : "=v"(r) : "v"(m), "v"(a), "s"(b)); return r; }
__device__ __forceinline__ unsigned bfi_f(unsigned m, unsigned a, unsigned b) { unsigned r; asm volatile("v_bfi_b32 %0, %1, %2, %3" : "=v"(r) : "v"(m), "s"(a), "v"(b)); return r; }
template <int H> __device__ __forceinline__ float cvt16(unsigned w) {
    float r;
    if constexpr (H) asm volatile("v_cvt_f32_i32_sdwa %0, sext(%1) dst_sel:DWORD dst_unused:UNUSED_PAD src0_sel:WORD_1" : "=v"(r) : "v"(w));
    else asm volatile("v_cvt_f32_i32_sdwa %0, sext(%1) dst_sel:DWORD dst_unused:UNUSED_PAD src0_sel:WORD_0" : "=v"(r) : "v"(w));
    return r;
}
__device__ __forceinline__ float sel8(float a, float b, unsigned long long mask) {
    float r;
    asm volatile("s_mov_b64 vcc, %3\n\tv_cndmask_b32_dpp %0, %1, %2, vcc row_ror:8 row_mask:0xf bank_mask:0xf" : "=v"(r) : "v"(a), "v"(b), "s"(mask) : "vcc");
    return r;
}
__device__ __forceinline__ unsigned sel0(unsigned a, unsigned long long mask) { unsigned r; asm volatile("v_cndmask_b32_e64 %0, 0, %1, %2" : "=v"(r) : "v"(a), "s"(mask)); return r; }
constexpr unsigned long long diag_mask(int R) {
    unsigned long long m = 0;
    for (int l = 0; l < 64; ++l) if ((l >> 4) == ((l & 7) >> 1) && 2 * (l & 1) + ((l >> 3) & 1) == R) m |= 1ull << l;
    return m;
}
template <int IMM, int X, bool SC1> __device__ __forceinline__ void dma_x(unsigned& so, unsigned lds, unsigned voff, const u32x4& rs, unsigned base) {
    if constexpr (SC1) asm volatile("s_add_u32 m0, %1, %2\n\ts_xor_b32 %0, %5, %6\n\tbuffer_load_dwordx4 %3, %4, %0 offen sc1 lds" : "=&s"(so) : "s"(lds), "n"(IMM), "v"(voff), "s"(rs), "s"(base), "n"(X) : "memory", "scc");
    else asm volatile("s_add_u32 m0, %1, %2\n\ts_xor_b32 %0, %5, %6\n\tbuffer_load_dwordx4 %3, %4, %0 offen lds" : "=&s"(so) : "s"(lds), "n"(IMM), "v"(voff), "s"(rs), "s"(base), "n"(X) : "memory", "scc");
}
template <int OFF> __device__ __forceinline__ void dma_n(unsigned so, unsigned voff, const u32x4& rs) {
    asm volatile("buffer_load_dwordx4 %0, %1, %2 offen offset:%3 sc1 lds" :: "v"(voff), "s"(rs), "s"(so), "n"(OFF) : "memory");
}
template <int IMM, int OFF> __device__ __forceinline__ void dma_m(unsigned so, unsigned lds, unsigned voff, const u32x4& rs) {
    asm volatile("s_add_u32 m0, %4, %5\n\ts_nop 0\n\tbuffer_load_dwordx4 %0, %1, %2 offen offset:%3 sc1 lds" :: "v"(voff), "s"(rs), "s"(so), "n"(OFF), "s"(lds), "n"(IMM) : "memory", "scc");
}
__device__ __forceinline__ unsigned and_or(unsigned a, unsigned m, unsigned o) { unsigned r; asm("v_and_or_b32 %0, %1, %2, %3" : "=v"(r) : "v"(a), "v"(m), "s"(o)); return r; }

#define HOPS 1
#define HM 0x1ff221125B082ull
#define PG0 63
#define PG7 287
#define PG1 110
#define PG2 288
#define PG3 4
#define PG4 21
#define SQ 52
#define P18 -1
#define WS0 0
#define SLM 0x1122334445566ull
#define PGF 4
#define HQ 84
#define HW 4
constexpr int hop_nth(int n) { for (int k = 0; k < 62; ++k) if (((HM >> k) & 1) && n-- == 0) return k; return 63; }
constexpr int hop_next(int K) { int n = 0; for (int k = 0; k < K; ++k) n += (HM >> k) & 1; return hop_nth(n + HW); }
template <int K> __device__ __forceinline__ void hop() {
    if constexpr (HOPS && ((HM >> K) & 1)) asm volatile(".Lfkh%0:\n\ts_cbranch_execz .Lfkh%1" :: "n"(K), "n"(hop_next(K)));
}
template <int K> __device__ __forceinline__ void page() {
    if constexpr (HOPS && K >= 0 && ((HM >> (K & 63)) & 1)) asm volatile("s_branch 1f\n\t.p2align 12\n.Lfkh%0:\n\ts_cbranch_execz .Lfkh%1\n1:" :: "n"(K), "n"(hop_next(K)));
    else if constexpr (HOPS) asm volatile("s_branch 1f\n\t.p2align 12\n1:");
}
__device__ __forceinline__ void hop_all(int w) {
    if constexpr (HOPS) asm volatile("s_mov_b64 exec, 0\n\ts_cmp_eq_u32 %0, 0\n\ts_cbranch_scc1 .Lfkh%1\n\ts_cmp_eq_u32 %0, 1\n\ts_cbranch_scc1 .Lfkh%2\n\t"
                                     "s_cmp_eq_u32 %0, 2\n\ts_cbranch_scc1 .Lfkh%3\n\ts_branch .Lfkh%4\n.Lfkh63:\n\ts_mov_b64 exec, -1"
                                     :: "s"(w), "n"(hop_nth(0)), "n"(hop_nth(1)), "n"(hop_nth(2)), "n"(hop_nth(3)) : "scc");
}
#define NB 1
#define PFQ 0
#define CVR 3
#define SPR 3
#define ELP 8
#define PW 0
#define CPG 95
#define GQ 98
#define GE 3
#define KDL 0
#define GP 1
#define SG 507
#define KO 0
#define DP 0
#define B13 460
#define PRE 0
enum { OP_NONE, OP_A, OP_B, OP_FLAG, OP_SIG, OP_VOTE };
struct Op { int kind, a, b; };
constexpr Op g1_op(int G) {
    const int st = (G + 32) / 32 - 1, q = G - 32 * st, i = q >> 2, j = q & 3;
    if (i < 5) {
        if (st < 0) return {OP_NONE, 0, 0};
        if ((j & 1) == 0) return {OP_A, st * 8 + i + 3, j >> 1};
        if (q == FLQ && st < 15) return {OP_FLAG, 0, 0};
        if (NB && G == SG) return {OP_SIG, 2, 0};
        if (G == GCR) return {OP_VOTE, 0, 0};
        if (NB == 2 && G == 435) return {OP_SIG, 3, 0};
        return {OP_NONE, 0, 0};
    }
    if (st == 15) return NB && G == SG ? Op{OP_SIG, 2, 0} : Op{OP_NONE, 0, 0};
    const int n = (st + 1) * 8;
    switch (q) {
        case 20: return {OP_B, st + 1, 0}; case 21: return {OP_B, st + 1, 1}; case 22: return {OP_A, n, 0}; case 23: return {OP_B, st + 1, 2};
        case 24: return {OP_B, st + 1, 3}; case 25: return {OP_A, n, 1}; case 26: return {OP_A, n + 1, 0}; case 27: return {OP_A, n + 1, 1};
        case 28: return {OP_A, n + 2, 0}; case 30: return {OP_A, n + 2, 1};
    }
    return {OP_NONE, 0, 0};
}
constexpr int g1_need(int G) {
    const int st = G >> 5, j = G & 3;
    for (int h = G - 1; h >= -12; --h) {
        const Op o = g1_op(h);
        if (o.kind == OP_A && o.a == (G >> 2) && o.b == (j >> 1)) return h;
        if (o.kind == OP_B && o.a == st && o.b == j) return h;
        if (o.kind == OP_FLAG && (G & 31) == 20 && (h >> 5) == st) return h;
    }
    return -100;
}
constexpr int g1_wait(int G) {
    int n = -100, c = 0;
    for (int h = G; h < (G ? G + 4 : 2) && h < 512; ++h) if (g1_need(h) > n) n = g1_need(h);
    for (int h = n + 1; h < G; ++h) c += g1_op(h).kind != OP_NONE;
    return n < G ? c : -1;
}
#define KV0 3
#define KVN 0x2222
constexpr int kv_first(int k) { int n = 0; for (int i = KV0; i < k && i < KV0 + 8; ++i) n += (KVN >> (4 * (i - KV0))) & 15; return n > 8 ? 8 : n; }
constexpr int kv_last() { int k = KV0; while (kv_first(k + 1) < 8) ++k; return k; }
#define CQ -1
#define CTM 0
#define EC 2
#define ED 2
#define EB 0
#define KST 0
#define KFD 3
#define KPL 11
#define KPI 0
#define KSL SLP
#define CBQ 20
constexpr int KVL = kv_last(), KFL = KVL + KFD, KSD = KVS ? KCC : -1;
static_assert(KFL <= KPL && KPL <= 12 && KPL > KVL && (KPI & 3) == 0 && KTI != KCC && KTI != KFL && KTI < KPL && KVP >= 0 && KVP < KCQ && KCQ < KCC && KSD <= KVL && KCC <= KPL && GCR > 32 * KCC + 62, "");
#define ROT 0x814
#define KLP 13
#define KL0 8
#define KLS 4
constexpr int ld_pos(int j) { return j < KLP ? -100 : KL0 + (j - KLP) * KLS / 4; }
constexpr int ld_cnt(int k) { int c = 0; for (int j = KLP; j < 21; ++j) c += (ld_pos(j) + 8) / 8 - 1 == k; return c; }
constexpr int KLL = (ld_pos(20) + 8) / 8 - 1, KLI = ld_pos(20) - 8 * KLL;
static_assert(KLP == 21 || KLL < KV0, "");
constexpr int hv_cnt(int k0, int k1) {
    int c = 0;
    for (int k = k0; k < k1; ++k) c += 9 + (KST ? 4 * (k == KVL) : k < KSD ? 0 : kv_first(k + 1) / 2 - (k == KSD ? 0 : kv_first(k) / 2)) + (k == KFL) + (k == KPL) + (k == KCQ) + (k == KVP) + (TM && TM < 16 && k == KTI) + ld_cnt(k);
    return c;
}
#define ZM 1
#define CL 1
#define EP 0
constexpr unsigned long long lag0_mask(int w) {
    unsigned long long m = 0;
    for (int l = 0; l < 64; ++l) if ((l >> 4) == 2 + ((l >> 3) & 1) && 3 - ((l & 7) >> 1) == w) m |= 1ull << l;
    return m;
}
constexpr int cv_start(int n) { return n * 8 - n * (n - 1) / 2; }
constexpr int cv_n(int q) { int n = 0; while (cv_start(n + 1) <= q) ++n; return n; }
constexpr int cv_m(int q) { return 7 - cv_n(q) - (q - cv_start(cv_n(q))); }
enum { CO_NONE, CO_A, CO_B, CO_C };
constexpr Op cv_op(int G) {
    const int s = (G + 36) / 36 - 1, q = G - 36 * s;
    if (q >= 1 && q <= 6) return s >= 0 ? Op{CO_A, s * 8 + 5 + (q - 1) / 2, (q - 1) & 1} : Op{CO_NONE, 0, 0};
    if (q >= 11 && q <= 14) return s >= 0 && s >= CL && s - CL < 8 ? Op{CO_C, s - CL, q - 11} : Op{CO_NONE, 0, 0};
    if (s == 7) return {CO_NONE, 0, 0};
    for (int m = 0; m < 8; ++m) if (q == cv_start(7 - m)) return {CO_B, (s + 1) * 8 + m, 0};
    for (int n = 0; n < 5; ++n) if (q == cv_start(n + 1) + 1 || q == cv_start(n + 1) + 2) return {CO_A, (s + 1) * 8 + n, q - cv_start(n + 1) - 1};
    return {CO_NONE, 0, 0};
}
constexpr int cv_need(int G) {
    const int s = G / 36, q = G % 36;
    for (int h = G - 1; h >= -36; --h) {
        const Op o = cv_op(h);
        if (o.kind == CO_A && o.a == s * 8 + cv_n(q)) return h;
        if (o.kind == CO_B && o.a == s * 8 + cv_m(q)) return h;
        if (o.kind == CO_C && q == 24 && o.a == s - CL) return h;
    }
    return -100;
}
constexpr int cv_wait(int G) {
    const int n = cv_need(G);
    for (int h = G - 1; h >= 0 && h >= G - 40; --h) if (cv_need(h) >= n) return -1;
    int c = 0;
    for (int h = n + 1; h < G; ++h) c += cv_op(h).kind != CO_NONE;
    return c;
}
#define AT(n, m) ((n) * 8 + (m))
#define FM 0x326260
constexpr int fill_pos(int s, int i) {
    const int f = (FM >> (4 * (s - 2))) & 15, n = s == 2 ? 0 : 4 * s - 9;
    switch (f) {
        case 1: return AT(n + i, 5) - (s == 2 && i == 3) * 6;
        case 2: return AT(n + 1 + i / 2, 5 + 2 * (i & 1));
        case 3: return i < 2 ? AT(n, 5 + 2 * i) : AT(n + i - 1, 5);
        case 4: return AT(n + 2 + i / 2, 5 + 2 * (i & 1));
        case 5: return AT(n, 1 + 2 * i) + (s == 2 ? 4 : 0);
        case 6: return AT(n + 3 + i / 2, 5 + 2 * (i & 1));
        case 7: return AT(n + 3, 5 + 2 * i);
        case 8: return i < 2 ? AT(n + 1, 5 + 2 * i) : AT(n + i, 5);
        case 9: return AT(n + 1 + i, 1);
        case 10: return i < 2 ? AT(n, 1 + 2 * i) : AT(n + 3, 1 + 2 * i);
    }
    return AT(n + i / 2, 5 + 2 * (i & 1));
}
constexpr int g2_fill(int p) {
    for (int k = 8; k < 32; ++k) if (fill_pos(k >> 2, k & 3) == p) return k;
    return -1;
}
#define QX AT(2, 5)
#define QQ AT(3, 2)
#define QZ AT(11, 2)
#define QT AT(3, 4)
constexpr int P_X = QX, P_Q = QQ, P_Z = QZ, P_T = QT;
constexpr bool XL = false;
constexpr bool fill_m0(int k) {
    const int a = fill_pos(k >> 2, (k & 3) - 1), b = fill_pos(k >> 2, k & 3);
    return (P_Q + 2 >= a && P_Q < b) || (P_Z + 2 >= a && P_Z < b) || (P_X + 2 >= a && P_X < b);
}
#define DBG 0
#define XW 4
#define XW2 4
#define QPP0 AT(9, 4)
#define QL0 AT(21, 1)
#define QR0 AT(24, 6)
#define QF2 AT(28, 3)
#define QL1 AT(26, 1)
#define QR1 AT(29, 3)
#define QA AT(16, 4)
#define AD 0
#define D1 2
#define D0 1
constexpr int NS = 33, NR = 29;
constexpr int P_PP0 = QPP0, P_PP1 = AT(16, 4), P_L0 = QL0, P_R0 = QR0, P_F2 = QF2, P_L1 = QL1, P_R1 = QR1, P_ZC = AT(10, 6), P_A = QA;
#define QB 24
#define QW -2
constexpr int P_W = QW;
static_assert(P_W < AT(ZP, 4), "");
constexpr int P_B0 = P_L0 - QB, P_B1 = P_L1 - QB;
static_assert(P_B0 > P_ZC + 1 && P_B1 > P_L0 + 1, "");
constexpr int E_PP0 = P_PP0 + NS - 1, E_PP1 = P_PP1 + (NS - 1) / D1, E_R0 = P_R0 + (NR - 1) / D0;
constexpr int ZW = P_A >= AT(20, 4) ? 19 : 15;
static_assert(P_R0 + 14 / D0 >= AT(24, 3) && E_R0 < P_F2 && P_F2 >= AT(24, 4) && P_F2 + 8 <= AT(31, 0) && E_R0 < P_R1, "");
static_assert(P_PP0 >= AT(9, 2) && P_PP0 + 8 <= AT(15, 4) && E_PP0 < P_PP1 && P_A >= AT(16, 4), "");
constexpr bool a_free(int p) {
    return p >= P_A && (AD || !((p >= P_PP1 && p <= E_PP1) || (p >= P_R0 && p <= E_R0) || (p >= P_F2 && p < P_F2 + 16)));
}
constexpr int a_step(int p) {
    if (NOA || !a_free(p)) return -1;
    int c = 0;
    for (int h = P_A; h < p; ++h) c += a_free(h);
    return c <= 48 ? c : -1;
}
constexpr int a_pos(int s) { int p = P_A; while (a_step(p) != s) ++p; return p; }
constexpr int q_rd(int r) { int p = r < 2 ? AT(ZW, 6) + r : a_pos(12 * r - 16) + 1; while ((p & 7) < 4) ++p; return p; }
constexpr int q_tie(int r) { const int n = (q_rd(r) >> 3) + 1; return AT(n, (n & 3) == 3 ? 0 : 4); }
static_assert(NOA || (a_pos(48) < 256 && q_tie(2) <= a_pos(23) && q_tie(3) <= a_pos(35)), "");
constexpr int g2_vm(int p) {
    int c = g2_fill(p) >= 0;
    c += a_step(p) > 0 && a_step(p) % 12 == 0 && !(NOST & 1);
    if (!NOA) c += (p == P_T) + (p == P_Q || p == P_Q + 2) + (p == AT(ZP, 4)) + (p == P_Z || p == P_Z + 2);
    c += (p == P_X || p == P_X + 2) + (p == P_W);
    c += !(NOST & 2) && (p == E_PP0 || p == E_PP1);
    c += (p == P_L0 || p == P_L1) + (PB && (p == P_B0 || p == P_B1));
    c += !(NOST & 4) && (p == P_F2 + 11 || p == P_F2 + 15);
    return c;
}
constexpr int g2_lx(int n) {
    int c = 2 * (n == 7);
    if (n >= 0 && (n & 7) >= 6) c += 2;
    if (!NOA) { c += 4 * (n == ZW); for (int r = 0; r < 4; ++r) c += (q_rd(r) >> 3) == n; }
    return c;
}
constexpr int g2_tile(int n, int j) { return (n & 4) ? (n & 2) == 0 : j; }
constexpr int g2_unit(int n, int j) { return (n & 4) ? 4 + 2 * (n & 1) + j : n & 7; }
constexpr int imin(int a, int b) { return a < b ? a : b; }
constexpr int imax(int a, int b) { return a > b ? a : b; }
constexpr int g2_need(int n) {
    int r = n == 3 ? -1 : fill_pos((n + 1) / 4, 3);
    if (n == 7 && !XL) r = imax(r, P_X + 2);
    if (n == ZW && !NOA) r = imax(r, imax(P_Q, P_Z) + 2);
    return r;
}
static_assert(P_Z > AT(11, 0) && P_T > AT(3, 0) && P_T < AT(ZP, 4) && (!XL || (P_X + 2 < P_R0 && P_R0 + 2 >= AT(24, 2) && P_R0 < AT(27, 0) && D0 == 1)), "");
constexpr bool rd1(int n) { return RD == 1 || (RD == 3 && (n & 3) == 2); }
constexpr int g2_allow(int e, int p) { int c = 0; for (int h = p + 1; h < e; ++h) c += g2_vm(h); return c; }

__global__ __launch_bounds__(512, 2) __attribute__((aligned(4096))) void k1(const uint16_t* __restrict__ x, const uint16_t* __restrict__ bre, const uint16_t* __restrict__ bim,
                                             const uint16_t* __restrict__ lre, const uint16_t* __restrict__ lim, const uint16_t* __restrict__ logdt, char* __restrict__ ws,
                                             const uint16_t* __restrict__ cre, const uint16_t* __restrict__ cim, const uint16_t* __restrict__ dskip,
                                             const uint16_t* __restrict__ ssmk, const uint16_t* __restrict__ ssmD, const uint16_t* __restrict__ qin,
                                             const uint16_t* __restrict__ kin, const uint16_t* __restrict__ vin, const uint16_t* __restrict__ sk,
                                             float* __restrict__ out TS_ARG) {
    unsigned long long pc0, rnd;
    asm volatile("s_getpc_b64 %0\n\ts_memtime %1" : "=s"(pc0), "=s"(rnd));
    TS_DECL TS(0);
    TL_DECL
    __shared__ __attribute__((aligned(1024))) char shm[LDS_TOTAL];
    const int tid = threadIdx.x, lane = tid & 63, wid = __builtin_amdgcn_readfirstlane(tid >> 6);
    const int b = blockIdx.x, xcd = b & 7, idx = b >> 3, hb = xcd & 1, tq = idx & 7;
    const int tm = (xcd >> 1) * 4 + (idx >> 3), tn = hb * 8 + tq;
    unsigned nr;
    const int tm2 = (xcd & 4) * 2 + tq, dt = (xcd & 2) * 2 + (idx >> 3), rot2 = tq + JR(idx >> 3);
    const unsigned lbase = (unsigned)(uintptr_t)shm;
    const int l15 = lane & 15, g4 = lane >> 4, lw = wid & 3, kg = wid >> 2;
    dma<U0 + 4096, false>(lbase + wid * 1024, CR ? ((lane * 16) & (1023 >> CR)) | ((lane >> (6 - CR)) << 20) : lane * 16, mk_rsrc((const void*)(pc0 & ~255ull), CODE_KB * 1024), ((idx * 8 + wid) & ((64 << CR) - 1)) * (1024 >> CR));
    const unsigned mk0 = lbase + POLL + lw * 256 + kg * (FULL - POLL + lw * (FST - 256));
    flag_set(mk0 + lane * 4, 1u);
    flag_set(lbase + HGO + lane * 4, 0u);
    if constexpr (NB) flag_set(lbase + DONE + lw * FST + lane * 4, 1u);
    unsigned vo[4];
    const bool xw = lw < 2;
#pragma unroll
    for (int i = 0; i < 4; ++i) {
        const int v = l15 ^ (i * 4 + g4);
        vo[i] = ((xw ? 64 * (i & 1) + 4 * (i >> 1) + (lane & 32) + 2 * (g4 & 1) : 8 * (i & 1) + 2 * g4) + (v >> 3)) * 2048 + (v & 7) * 16 - (i & 1) * 1024;
    }
    const int bw = lw >> 1;
    unsigned r0 = (tm * 128 + lw * 16 + bw * (tn * 64 + lw * 16 - 64 - tm * 128)) * 2048, rsz = 229376 - bw * 163840;
    const unsigned col = (tq + (idx >> 3)) & 1, grp = GS == 0 ? tq >> 2 : GS == 1 ? idx >> 4 : GS == 2 ? 0 : GS == 3 ? hb : (xcd >> 1) & 1;
    unsigned rp = grp * (16 - BL) * 128, mi = col * BL * 128, mo = mi >> grp;
    unsigned rot = (BL ? 0 : ROT >> 8) * col + ((ROT >> 4) & 15) * (xcd >> 1) + (ROT & 15) * hb, rst = 16384 << bw, lb = lbase + lw * 8192, oob = OOB;
    unsigned lb0 = lb + g1_buf(0) + kg * (g1_buf(1) - g1_buf(0)), so0 = BL ? (((kg * 128 + rp) ^ mo) + rot * 128) & 0x780 : ((kg + rot) & 15) * 128;
    if constexpr (BL) { rot *= 128; ties(rp); ties(mi); ties(mo); }
    unsigned co = l15 * 2 + (tn * 128 + lw * 32 + kg * 2048), vw = b * 4 + lw + 32 - 32 * kg;
    tie(vo[0]); tie(vo[1]); tie(vo[2]); tie(vo[3]); tie(oob); tie(co);
    ties(r0); ties(rsz); ties(rot); ties(rst); ties(lb); ties(lb0); ties(so0); ties(vw);
    sched();
    asm volatile("" :: "s"(x), "s"(bre), "s"(bim), "s"(lre), "s"(lim), "s"(logdt), "s"(cre), "s"(cim), "s"(dskip), "s"(ssmk), "s"(ssmD), "s"(qin),
                       "s"(kin), "s"(vin), "s"(sk), "s"(ws), "s"(out));
    const bool xv = fresh(bw) == 0;
    const u32x4 rs0 = mk_rsrc((const char*)(xv ? x : bre) + r0, rsz), rs1 = mk_rsrc((const char*)(xv ? x : bim) + r0 - 2048, rsz + 2048);
    if constexpr (SU) TS(3);
    if constexpr (AB) asm volatile("s_waitcnt lgkmcnt(0)\n\ts_cmp_eq_u32 %0, 0\n\ts_cbranch_scc1 1f\n\ts_barrier\n1:" :: "s"(kg) : "scc", "memory");
    unsigned c_dt, c_re, c_im;
    auto c_ld = [&]() {
        gld<0>(c_dt, co, mk_rsrc(logdt, 2048), 0);
        gld<0>(c_re, co, mk_rsrc(lre, 2048), 0);
        gld<0>(c_im, co, mk_rsrc(lim, 2048), 0);
    };
    if constexpr (!LC) c_ld();
    const unsigned vb = race(vw, ws, rnd);
    static_for<0, 8>([&](auto ic) { constexpr int i = CV(ic); dma4<(i & 4) * 1024, i & 3>(lb0, vo[i & 3], (i & 2) ? rs1 : rs0, so0 + (i >> 2) * rst); });
    wait_l<0>();
    if constexpr (SU) TS(4);
    if constexpr (AB) {
        dma1<0>(mk0 + kg * 120, oob, rs0, 0);
        asm volatile("s_cmp_lg_u32 %0, 0\n\ts_cbranch_scc1 1f\n\ts_barrier\n1:" :: "s"(kg) : "scc", "memory");
    } else {
        barrier();
        dma1<0>(mk0 + kg * 120, oob, rs0, 0);
    }
    if constexpr (SU) TS(5);
    sched();
    const unsigned a00 = (l15 >> 1) * 256 + ((((l15 & 1) * 8 + g4) ^ (l15 >> 1)) << 4);
    auto frag = [&](int par, int ks) { return (a00 ^ (ks * 64) ^ (par * 128)) + par * 2048; };
    bf16x8 cf[32];
    unsigned vc[4], cad[4];
    auto c_setup = [&]() {
#pragma unroll
        for (int i = 0; i < 4; ++i) { const int v = l15 ^ (i * 4 + g4); vc[i] = (4 * (2 * g4 + (v >> 3)) + 2 * (i & 1) + (i >> 1)) * 2048 + (v & 7) * 16 - i * 1024; }
    };
    const u32x4 rsc = mk_rsrc((kg ? cim : cre) + (size_t)dt * 131072, 262144);
    auto c_off = [&](int dg, int j) { return (unsigned)(dg * 65536 + hb * 1024 + ((rot2 + j) & 7) * 128); };
    const unsigned cst = lbase + (kg ? BUF : 2 * BUF) + lw * 8192;
    unsigned tag;
    const u32x4 rsf = mk_rsrc(ws + WS_F, 65536);
    const __amdgpu_buffer_rsrc_t rf = mk_brs(ws + WS_F, 65536);
    const int qx = (hb ? 0 : 2) ^ JQ(idx >> 3), wid_ = wid, b_ = b;

    unsigned wl, w2k, hs0, s_x, s_q, s_z, zfo, ppo, s_a, s_b, tagg, ppi, tn_[3], psel, lastw, dsw_;
    unsigned l16, ha[8], a_x, a_xr, a_q, a_qr, a_iw, a_ir, a_t, msk, a_f, expect, a_z, a_z2, a_zr, a_oa, a_ob;
    unsigned long long lo8, hi8;
    u32x4 rsx, rsq, rso, rws, dg[2]; u32x2 tg2;
    constexpr int SU_S = 21, SU_D = 39, SU_N = 44;
    auto su = [&](auto kc, auto cc, int lw, int b, unsigned ln) {
        constexpr int kg = CV(kc), c = CV(cc);
        const int wid = 4 * kg + lw, xcd = b & 7, idx = b >> 3, tq = (idx & 7) + JR(idx >> 3);
        const unsigned nx = nr ^ ((xcd & 2) << 12), nh = nr ^ ((idx & 2) << 12), hp = (idx & 7) * 16 + (b & 1) * 8;
        const int tm2 = (xcd & 4) * 2 + (idx & 7), dt = (xcd & 2) * 2 + (idx >> 3), qx = ((~b & 1) * 2) ^ JQ(idx >> 3);
        const int ta = 512 * (idx >> 3) + 128 * (idx & 1) + 16 * wid + 4 * ((idx >> 1) & 3);
        const int tb = tm2 * 128 + 64 * kg;
        const unsigned l31 = ln & 31, l5 = ln >> 5;
        auto S = [](auto& v, auto e) { v = e; ties(v); };
        auto V = [](auto& v, auto e) { v = e; tie(v); };
        if constexpr (c == 0) { S(wl, lbase + wid * 4096); S(w2k, lbase + wid * 2048); }
        if constexpr (c == 1) S(hs0, (hp + ((tq + (wid >> 1)) & 7)) * 65536);
        if constexpr (c == 2) S(hs0, hs0 + nh + (wid & 1) * 4096);
        if constexpr (c == 3) S(hs0, hs0 + qx * 16384);
        if constexpr (c == 4) S(s_x, (tb + qx * 8) * 2048);
        if constexpr (c == 5) S(s_x, s_x + dt * 256 + lw * 64);
        if constexpr (c == 6) S(s_q, ta * 2048);
        if constexpr (c == 7) { S(s_a, s_q * 4 + xcd * 512); S(s_q, s_q + xcd * 256); }
        if constexpr (c == 8) S(s_z, (xcd & 3) * 1048576 + nx);
        if constexpr (c == 9) S(ppo, s_z + idx * 32768 + (WS_KVT + kg * 16384) + lw * 2048);
        if constexpr (c == 10) S(s_z, s_z + WS_ZT + idx * 128 + wid * 131072);
        if constexpr (c == 11) S(zfo, WS_F + F_Z + (xcd & 2) * 8192 + nr + (xcd & 1) * 4096);
        if constexpr (c == 12) S(s_b, (tb + (2 ^ qx) * 8) * 8192 + 4096);
        if constexpr (c == 13) S(s_b, s_b + dt * 512 + lw * 128);
        if constexpr (c == 14) S(rsx, mk_rsrc(x, (NOX & 1) ? 0 : 4 << 20));
        if constexpr (c == 15) S(rsq, mk_rsrc(qin, (NOX & 2) ? 0 : 4 << 20));
        if constexpr (c == 16) S(rso, mk_rsrc(out, 16 << 20));
        if constexpr (c == 17) S(rws, mk_rsrc(ws, 32 << 20));
        if constexpr (c == 18) { S(tagg, tag); S(ppi, ppo ^ 1048576); S(tn_[0], tag & 0xf000fu); S(lastw, kg ? 1 : (b ^ 255) | lw); }
        if constexpr (c == 19) { S(tn_[1], (tag >> 8) & 0xf000fu); S(tn_[2], (tag >> 4) & 0xf000fu); }
        if constexpr (c == 20) { S(psel, 0x05040100u); S(lo8, 0x00ff00ff00ff00ffull); S(hi8, ~0x00ff00ff00ff00ffull); }
        if constexpr (c == 21) { V(l16, ln * 16); V(dsw_, lbase + 131072 + lw * 64 + (ln & 7) * 8 + ((ln >> 1) & 4)); }
        if constexpr (c == 22) V(ha[0], lbase + kg * 1536 + l5 * 2048 + (ln & 2) * 128 + (ln & 1) * 8);
        if constexpr (c == 23) V(ha[1], ((ln >> 1) & 8) | ((ln >> 2) & 3));
        if constexpr (c == 24) V(ha[0], ha[0] | ((ha[1] | ((ln & 2) << 1)) << 4));
        if constexpr (c == 25) { V(ha[1], ha[0] ^ 64); V(ha[2], ha[0] ^ 512); V(ha[3], ha[0] ^ 576); }
        if constexpr (c == 26) static_for<0, 4>([&](auto ic) { V(ha[4 + CV(ic)], ha[CV(ic)] + 65536); });
        if constexpr (c == 27) V(a_x, ((((ln >> 2) & 7) | (ln & 32)) << 11) | (l16 & 48));
        if constexpr (c == 28) V(a_xr, (((l16 & 240) << 2) | (ln & 48)) + (w2k + ZST));
        if constexpr (c == 29) { dsr32<DSK - 131072>(dsw_, dsw_); V(a_q, ((ln & 48) << 7) | (l16 & 240)); }
        if constexpr (c == 30) V(a_qr, (l5 << 10) + l31 * 8 + (w2k + QST));
        if constexpr (c == 31) { V(a_iw, w2k + IMG + l16); V(a_ir, a_iw ^ 8192); V(msk, 0xfff0fff0u); }
        if constexpr (c == 32) {
            V(a_t, wid == 0 && ln == 0 ? idx * 128 : OOB);
            V(a_f, wid == 1 ? (ln >> 1) * 128 + (ln & 1) * 4 : OOB); V(expect, wid == 1 ? ((ln & 1) ? ~tagg : tagg) : 0u);
        }
        if constexpr (c == 33) V(a_z, ((ln ^ l5 ^ (wid * 4)) & 7) << 4);
        if constexpr (c == 34) V(a_z, a_z | ((ln & 48) << 10) | ((ln & 8) << 9));
        if constexpr (c == 35) V(a_zr, ((((wid ^ ln) & 7) << 1) | l5) << 3);
        if constexpr (c == 36) { V(a_zr, a_zr + (l31 << 9) + (lbase + ZST)); V(a_oa, (l5 << 21) | (l31 << 4)); }
        if constexpr (c == 37) V(a_ob, ((ln >> 2) & 4) | (ln & 32) | ((ln >> 3) & 1));
        if constexpr (c == 38) V(a_ob, (a_ob << 13) | (l16 & 112));
        if constexpr (c == SU_D) tie(dsw_);
        if constexpr (c == SU_D + 4) { V(tg2, u32x2{tagg, ~tagg}); V(a_z2, a_z ^ 32); }
        if constexpr (c >= SU_D && c < SU_D + 4) {
            constexpr unsigned long long k = diag_mask(c - SU_D);
            dg[0][c - SU_D] = sel0(dsw_ & 0xffffu, k); dg[1][c - SU_D] = sel0(dsw_ & 0xffff0000u, k);
            if constexpr (c == SU_D + 3) { tie(dg[0]); tie(dg[1]); }
        }
    };

    if (wid < 4) {
        if constexpr (LC) c_ld();
        f32x4 acc[8][2];
        cf32 L, cc, M, M2, MG, NG, N4, N8, XC, ag;
        unsigned eo[4], ew, es, dna, ho, hfo, cb, fv; unsigned long long none[4]; u32x4 rse, rh, er;
        {
            unsigned aad[4], bad[4];
#pragma unroll
            for (int i = 0; i < 4; ++i) { aad[i] = lbase + frag(i & 1, i >> 1); bad[i] = aad[i] + 16384 + wid * 4096; }
            const unsigned fr = lbase + FULL + (lane & 3) * FST;
            sld(tag, ws + WS_F + F_EP);
            bf16x8 fa[4][2], fb[2][4];
            unsigned fl = 1, dz = 0, dw_ = lbase + DONE + wid * FST + lane * 4, vt = lbase + POLL + wid * 256 + lane * 4;
            tie(dz); tie(dw_); tie(vt);
            auto op = [&](auto gc) {
                constexpr int G = CV(gc);
                constexpr Op o = g1_op(G);
                if constexpr (o.kind == OP_A) {
                    constexpr int bo = g1_buf(o.a >> 3), hi = bo >= 65536 ? 65536 : 0, i = o.a & 7;
                    dsr128<bo - hi + (i >> 1) * 4096>(fa[i & 3][o.b], aad[(i & 1) + 2 * o.b] + hi);
                }
                if constexpr (o.kind == OP_B) { constexpr int bo = g1_buf(o.a), hi = bo >= 65536 ? 65536 : 0; dsr128<bo - hi>(fb[o.a & 1][o.b], bad[o.b] + hi); }
                if constexpr (o.kind == OP_FLAG) dsr32<(30 - (G >> 5)) * 4>(fl, fr);
                if constexpr (o.kind == OP_SIG) dsw32<o.a * 4>(dw_, dz);
                if constexpr (o.kind == OP_VOTE) dsr32<0>(vt, vt);
            };
            auto consts = [&]() {
                float ld, ar, ai;
                asm volatile("s_waitcnt vmcnt(%6)\n\tv_lshlrev_b32 %0, 16, %3\n\tv_lshlrev_b32 %1, 16, %4\n\tv_lshlrev_b32 %2, 16, %5"
                             : "=&v"(ld), "=&v"(ar), "=&v"(ai) : "v"(c_dt), "v"(c_re), "v"(c_im), "n"(LC ? 0 : 9) : "memory");
                if constexpr (SU) TS(6);
                const float dtv = rbf(expf(ld)), ea = expf(dtv * ar);
                float sn, cs;
                sincosf(dtv * ai, &sn, &cs);
                L = cf32{ea * cs, ea * sn};
                M = L;
#pragma unroll
                for (int e = 0; e < 5; ++e) M = cmul(M, M);
                M2 = cmul(M, M);
                const cf32 N = cmul(M2, M2), N2 = cmul(N, N), one = {1.f, 0.f};
                N4 = cmul(N2, N2); N8 = cmul(N4, N4);
                auto sel = [](bool c, cf32 a, cf32 b) { return cf32{c ? a.r : b.r, c ? a.i : b.i}; };
                MG = cmul(sel(g4 & 1, M, one), sel(g4 & 2, M2, one));
                NG = cmul(sel(g4 & 1, N, one), sel(g4 & 2, N2, one));
                const float nm = L.r - 1.f, den = ar * ar + ai * ai;
                cc = cf32{2.f * ((nm * ar + L.i * ai) / den), 2.f * ((L.i * ar - nm * ai) / den)};
                auto tc = [&](cf32& v) { tie(v.r); tie(v.i); };
                XC = cf32{g4 == 3 ? M.r : g4 == 2 ? 1.f : 0.f, g4 == 3 ? M.i : 0.f};
                tc(L); tc(cc); tc(M); tc(M2); tc(MG); tc(NG); tc(N4); tc(N8); tc(XC);
                sched();
            };
            if constexpr (CQ < 0) consts();
            if constexpr (SU) TS(10);
            __builtin_amdgcn_s_setprio(CP);
            zero_chk<0>(fl, lbase + POLL + (lane & 3) * 256);
            if constexpr (WS0 > 0) { fl = 1; zero_chk<(31 - WS0) * 4, 1>(fl, fr); }
            ties(tag);
            tag += 0x01010101u;
            TS(1);
            static_for<-12, 0>(op);
            static_for<0, 512>([&](auto gc) {
                constexpr int G = CV(gc), st = G >> 5, i = (G >> 2) & 7, j = G & 3;
                if constexpr ((G == 0 || j == 2) && G <= 506) { static_assert(g1_wait(G) >= 0, ""); wait_lr<G == 506 ? 0 : g1_wait(G)>(); }
                if constexpr (G == CQ) { asm volatile("s_waitcnt lgkmcnt(0)\n\ts_nop 7" ::: "memory"); consts(); }
                if constexpr (G == SQ) {
                    c_setup();
                    const int t0 = tm - 1 - g4;
                    static_for<0, 4>([&](auto ic) {
                        constexpr int i = CV(ic);
                        const int t = t0 - 4 * i;
                        cad[i] = lb + frag(i & 1, i >> 1);
                        eo[i] = ((((t ^ tm) & 8) << 10) | ((t & 7) << 8) | (l15 * 16) | (t & OOB)); none[i] = __builtin_amdgcn_ballot_w64(t < 0);
                        tie(vc[i]); tie(cad[i]); tie(eo[i]); ties(none[i]);
                    });
                    ew = (l15 * 16) | (g4 << 20); dna = lbase + DONE + (lane & 3) * FST; ho = g4 * 256 + ((l15 ^ (4 * (g4 & 1))) << 4); fv = lane << 20;
                    tie(ew); tie(dna); tie(ho); tie(fv);
                    sched();
                }
                if constexpr (G == HQ) hop_all(wid);
                {
                    constexpr int n = (SLM >> (4 * st)) & 15;
                    if constexpr (n > 0 && ((G & 31) + 1) * (n & 7) / 32 != (G & 31) * (n & 7) / 32) __builtin_amdgcn_s_sleep(1 + (n >> 3));
                }
                if constexpr ((G & 31) == 20 && st < 15) zero_chk<(30 - st) * 4>(fl, fr);
                if constexpr (((G & 31) == 30 && st < 13) || (NB < 2 && G == B13)) barrier();
                if constexpr ((G & 31) == 20) TL(st);
                mfma<st == 0 && j < 2>(acc[i][j & 1], fa[i & 3][j >> 1], fb[st & 1][j]);
                sched();
                op(gc);
                if constexpr (G == GCR + 16) { unsigned n; nr = vote_cnt(vt, tag, (xcd & 4) << 11, n); }
                if constexpr (G >= GCR + 18 && G < GCR + 22) { eo[G - GCR - 18] ^= nr; tie(eo[G - GCR - 18]); }
                if constexpr (G == GCR + 22) { rse = mk_rsrc(ws + WS_E + tn * 16384 + wid * 2048, 16384); ties(rse); }
                if constexpr (G == GCR + 24) { es = nr + (tm & 7) * 256; cb = wid * 65536 + hb * 1024; er = u32x4{0u, 0u, tag, ~tag}; ties(es); ties(cb); tie(er); }
                if constexpr (G == GCR + 26) { rh = mk_rsrc(ws + WS_H + (size_t)((tm & 7) * 16 + tn) * 65536 + (nr ^ ((tm & 2) << 12)) + wid * 2048, 65536); ties(rh); }
                if constexpr (G == GCR + 28) { hfo = F_H + nr + (((tm & 7) * 16 + tn) * 4 + wid) * 8; ties(hfo); }
                if constexpr (G == PG0) page<-1>();
                if constexpr (G == 95) hop<2>();
                if constexpr (G == 159) hop<4>();
                if constexpr (G == 207) hop<5>();
                if constexpr (G == PG7) page<7>();
                if constexpr (G == 319) hop<8>();
                if constexpr (G == 383) hop<10>();
                if constexpr (G == 431) hop<11>();
                if constexpr (G == 479) page<13>();
                if constexpr (G == 509) hop<15>();
                if constexpr (G == 489) hop<40>();
                if constexpr (G == 494) hop<41>();
                if constexpr (G == 499) hop<42>();
                if constexpr (G == 504) hop<43>();
                if constexpr (G >= 487) {
                    constexpr int k = G - 486;
                    if constexpr (k == 1) { asm volatile("s_nop 3"); ag = cf32{acc[0][0][0], acc[0][1][0]}; }
                    cf32 t;
                    asm volatile("v_fma_f32 %0, -%3, %5, %6\n\tv_fma_f32 %1, %3, %4, %7\n\tv_fmac_f32 %0, %2, %4\n\tv_fmac_f32 %1, %2, %5"
                                 : "=&v"(t.r), "=&v"(t.i) : "v"(L.r), "v"(L.i), "v"(ag.r), "v"(ag.i), "v"(acc[k >> 2][0][k & 3]), "v"(acc[k >> 2][1][k & 3]));
                    ag = t;
                }
                sched();
            });
        }
        __builtin_amdgcn_s_setprio(3);
        TL(16);
        TS(2);
#pragma unroll
        for (int k = 26; k < 32; ++k) ag = cfma(L, ag, cf32{acc[k >> 2][0][k & 3], acc[k >> 2][1][k & 3]});
        tie(ag.r); tie(ag.i); sched(); hop<44>(); sched();
        cf32 ev_, od, p01, p23;
        sw16(ag.r, ev_.r, od.r); sw16(ag.i, ev_.i, od.i);
        const cf32 pp = cfma(M, ev_, od);
        sw32(pp.r, p01.r, p23.r); sw32(pp.i, p01.i, p23.i);
        cf32 E = cfma(M2, p01, p23);

        er[0] = f2u(E.r); er[1] = f2u(E.i);
        gst<0>(er, ew, rse, es);
        sched();
        TSX(3);
        hop<45>();
        sched();
        auto c_read = [&](auto rc) { static_for<0, 8>([&](auto ic) { constexpr int r = CV(rc), i = CV(ic); dsr128<(i >> 2) * 4096>(cf[r * 8 + i], cad[i & 3] + ((r & 1) ? 0 : 2 * BUF)); }); };
        auto c_tie = [&](auto rc) { static_for<0, 8>([&](auto ic) { tie(cf[CV(rc) * 8 + CV(ic)]); }); };
        auto c_unit = [&](auto uc) { static_for<0, 4>([&](auto ic) { constexpr int u = CV(uc), i = CV(ic); dma4<((u & 2) ? 0 : 2 * BUF) + (u & 1) * 4096, i>(lb, vc[i], rsc, cb + ((rot2 + u) & 7) * 128); }); };
        auto c_req = [&](auto rc) { c_unit(IC(CV(rc) * 2)); c_unit(IC(CV(rc) * 2 + 1)); };
        const float c2r = cc.r, c2i = cc.i;
        auto scale = [&](int i) {
#pragma unroll
            for (int j = 0; j < 4; ++j) {
                const float br = acc[i][0][j], bi = acc[i][1][j];
                acc[i][0][j] = fma_(c2r, br, -(c2i * bi)); acc[i][1][j] = fma_(-c2r, bi, -(c2i * br));
            }
            tie(acc[i][0]); tie(acc[i][1]);
        };
        auto scales = [&](auto ac, auto bc) {
            static_for<CV(ac), CV(bc)>([&](auto ic) {
                constexpr int i = CV(ic);
                scale(i);
                if constexpr (i & 1) { sched(); hop<i == 1 ? 46 : i == 3 ? 16 : i == 5 ? 47 : 48>(); sched(); }
            });
        };
        constexpr int CA = CVR == 0 ? 0 : CVR == 4 ? 1 : 2;
        if constexpr (NB) {
            unsigned dn = 1;
            scales(IC(0), IC(DP));
            if constexpr (CA) { zero_chk<8>(dn, dna); static_for<0, CA>(c_unit); }
            if constexpr (SPR != 3) __builtin_amdgcn_s_setprio(SPR);
            TL(17);
            sched();
            scales(IC(DP), IC(ELP));
            if constexpr (!CA) zero_chk<8>(dn, dna);
        } else {
            scales(IC(0), IC(ELP));
            barrier();
            static_for<0, CA>(c_unit);
        }
        TL(18);
        u32x4 ev[4];
#pragma unroll
        for (int i = 0; i < 4; ++i) asm volatile("buffer_load_dwordx4 %0, %1, %2, 0 offen sc1" : "=v"(ev[i]) : "v"(eo[i]), "s"(rse) : "memory");
        asm volatile("" ::: "memory");
        sched();
        constexpr int CB = CVR == 0 ? 1 : CVR == 4 ? 2 : 4;
        static_for<CA, CB>(c_unit);
        scales(IC(ELP), IC(8));
        cf32 X = cfma(XC, p01, cf32{(g4 & 1) ? ev_.r : 0.f, (g4 & 1) ? ev_.i : 0.f});
        tie(X.r); tie(X.i);
        sched();
        hop<24>();
        if constexpr (SPR != 3) __builtin_amdgcn_s_setprio(3);
        if constexpr (CVR == 2 || CVR == 5) { wait_v<12>(); c_read(IC(0)); wait_l<0>(); c_tie(IC(0)); c_req(IC(2)); }
        wait_v<4 * (CB - CA) + 8 * (CVR == 2 || CVR == 5)>();
        TL(19);
        tie(ev[0]); tie(ev[1]); tie(ev[2]); tie(ev[3]);
        {
            const unsigned long long tag64 = (unsigned long long)tag | ((unsigned long long)~tag << 32);
#pragma unroll 1
            for (unsigned spins = 0; spins < SPIN_CAP; ++spins) {
                unsigned long long ok = ~0ull;
#pragma unroll
                for (int i = 0; i < 4; ++i) ok &= __builtin_amdgcn_ballot_w64(((unsigned long long)ev[i][2] | ((unsigned long long)ev[i][3] << 32)) == tag64) | none[i];
                TSV(11, spins);
                if (ok == ~0ull) break;
                asm volatile("buffer_load_dwordx4 %0, %4, %8, 0 offen sc1\n\tbuffer_load_dwordx4 %1, %5, %8, 0 offen sc1\n\tbuffer_load_dwordx4 %2, %6, %8, 0 offen sc1\n\t"
                             "buffer_load_dwordx4 %3, %7, %8, 0 offen sc1\n\ts_waitcnt vmcnt(0)" : "=&v"(ev[0]), "=&v"(ev[1]), "=&v"(ev[2]), "=&v"(ev[3])
                             : "v"(eo[0]), "v"(eo[1]), "v"(eo[2]), "v"(eo[3]), "s"(rse) : "memory");
            }
        }
        if constexpr (P18 < 0) page<18>();
        cf32 h;
        {
            auto e = [&](int i) { return cf32{u2f(ev[i][0]), u2f(ev[i][1])}; };
            cf32 c = cmul(NG, cfma(N8, cfma(N4, e(3), e(2)), cfma(N4, e(1), e(0)))), u, v;
            sw16(c.r, u.r, v.r); sw16(c.i, u.i, v.i);
            sw32(u.r + v.r, u.r, v.r); sw32(u.i + v.i, u.i, v.i);
            c = cfma(MG, cf32{u.r + v.r, u.i + v.i}, X);
            h = cf32{fma_(c2r, c.r, -(c2i * c.i)), fma_(-c2r, c.i, -(c2i * c.r))};
        }
        TSX(4);
        {
            const cf32 Lc = {L.r, -L.i};
            static_for<0, 4>([&](auto qc) {
                constexpr int q = CV(qc);
                if constexpr (q >= CB) c_unit(qc);
                if constexpr (q == 2) hop<20>();
                u32x4 wr, wi;
#pragma unroll
                for (int u = 0; u < 4; ++u) {
                    const int i = 2 * q + (u >> 1), j = 2 * (u & 1);
                    const cf32 h0 = cfma(Lc, h, cf32{acc[i][0][j], acc[i][1][j]});
                    h = cfma(Lc, h0, cf32{acc[i][0][j + 1], acc[i][1][j + 1]});
                    wr[u] = pack2(h0.r, h.r); wi[u] = pack2(h0.i, h.i);
                }
                if constexpr ((PRE == 2 || GQ >= 0) && q == GE) { tie(wr); tie(wi); dsw32<4>(lbase + DONE + wid * FST + lane * 4, 0u); }
                gst<0>(wr, ho, rh, q * 16384);
                gst<1024>(wi, ho, rh, q * 16384);
                if constexpr (q == P18) page<18>();
                asm volatile("s_nop 1" :: "v"(wr), "v"(wi));
                sched();
            });
        }
        TSX(10);
        auto flag = [&]() { gst<0>(u32x2{er[2], er[3]}, fv, rsf, hfo); TSX(5); };
        hop<22>();
        if constexpr (CVR == 5) {
            wait_v<16>();
            c_read(IC(1));
            wait_v<0>();
            flag();
            wait_l<0>();
            static_for<6, imax(6, 8 - CD)>(c_unit);
        } else if constexpr (CVR == 2) {
            wait_v<16>();
            c_read(IC(1));
            wait_l<0>();
            c_req(IC(3));
            wait_v<8>();
            flag();
        } else {
            if constexpr (CVR == 1) wait_v<8>(); else { wait_v<0>(); flag(); }
            c_read(IC(0)); c_read(IC(1));
            wait_l<8>();
            static_for<4, imin(6, 8 - CD)>(c_unit);
            wait_l<0>();
            static_for<6, imax(6, 8 - CD)>(c_unit);
            if constexpr (CVR == 1) { wait_v<16>(); flag(); }
        }
        c_tie(IC(0)); c_tie(IC(1));
        {
            unsigned ln = lane, pf, fso;
            tie(ln);
            const int wid = fresh(wid_), lw = wid, b = fresh(b_), idx = b >> 3, tq = (idx & 7) + JR(idx >> 3), qx = ((~b & 1) * 2) ^ JQ(idx >> 3);
            const unsigned nh = nr ^ ((idx & 2) << 12), hp = (idx & 7) * 16 + (b & 1) * 8;
            static_for<0, SU_D>([&](auto cc) { su(IC(0), cc, wid, b, ln); });
            wait_l<0>();
            static_for<SU_D, SU_N>([&](auto cc) { su(IC(0), cc, wid, b, ln); });
            unsigned e_h = wid == 3 ? ((ln & 1) ? ~tagg : tagg) : 0u, hsl = lbase + lw * 8192, hso = (hp + ((tq + wid) & 7)) * 65536 + nh + qx * 16384;
            unsigned a_h = wid == 3 ? ln * 4 : OOB, s_h = WS_F + F_H + nr + hp * 32, ca2[4], a_g = lbase + HGO + ln * 4;
            static_for<0, 4>([&](auto ic) { ca2[CV(ic)] = cad[CV(ic)] + 2 * BUF; tie(ca2[CV(ic)]); });
            tie(e_h); tie(a_h); tie(a_g); ties(hsl); ties(hso); ties(s_h);
                        sched();
            {
                auto look = [&]() { if constexpr (HA) pf_at<0>(pf, a_h, rws, s_h); else pf_ld(pf, a_h, rws, s_h); };
                static_for<8 - CD, 8>(c_unit);
                if constexpr (HA) pf_zero(pf);
                if constexpr (PW == 0) { look(); wait_v<1>(); }
                if constexpr (PW == 1) { wait_v<0>(); look(); }
                if constexpr (PW == 2) { wait_v<8>(); look(); wait_v<1>(); }
                static_for<16, 32>([&](auto ic) { constexpr int i = CV(ic); dsr128<((i >> 2) & 1) * 4096>(cf[i], (i & 8) ? cad[i & 3] : ca2[i & 3]); });
                wait_l<0>();
                static_for<16, 32>([&](auto ic) { tie(cf[CV(ic)]); });
                wait_v<0>();
                tag_chk<1, HA>(pf, e_h, a_h, rws, s_h);
                if (wid == 3) flag_set(a_g, 1u); else gate<1>(a_g, 1);
                TSX(6);
                hop<26>();
                static_for<0, 4>([&](auto ic) {
                    constexpr int i = CV(ic);
                    dma_x<(i >> 1) * 2 * BUF + (i & 1) * 4096, (i >> 1) * 262144 + (i & 1) * 4096, true>(fso, hsl, l16, rws, hso);
                    dma_n<1024>(fso, l16, rws); dma_n<2048>(fso, l16, rws); dma_n<3072>(fso, l16, rws);
                });
            }
        }
    } else {
        const int ch = xcd * 128 + (idx >> 2) * 16 + lw * 4 + (idx & 3);
        unsigned ln_ = lane; int lw_ = lw, bb_ = b;
        unsigned dw, nx; u32x4 ad;
        auto c_unit = [&](auto uc) { static_for<0, 4>([&](auto ic) { constexpr int u = CV(uc), i = CV(ic); dma4<(u & 1) * 4096, i>(cst, vc[i], rsc, c_off(lw, u)); }); };
        constexpr int XV = 4 * (EC == 1) + 8 * (PFQ == 2);
        auto ctouch = [&](int r) { const unsigned j = (fresh(idx * 4 + lw) + r * 37) & 127; stouch((pc0 & ~255ull) + (j < CODE_KB * 8 / 3 ? j : j - 64) * 384); };
        {
            const unsigned fb = lbase + FULL + lw * FST;
            auto piece = [&](auto sc, auto ic) {
                constexpr int s = CV(sc), i = CV(ic);
                dma4<g1_buf(s) + (i & 4) * 1024, i & 3>(lb, vo[i & 3], (i & 2) ? rs1 : rs0, (BL ? (((s * 128 + rp) ^ (s >= BL && s < 16 - BL ? mi : mo)) + rot) & 0x780 : ((s + rot) & 15) * 128) + (i >> 2) * rst);
            };
            auto mark = [&](auto sc) { dma1<(31 - CV(sc)) * 4>(fb, oob, rs0, 0); };
            auto feed = [&](auto kc, auto ic) {
                constexpr int k = CV(kc), i = CV(ic);
                if constexpr (i < 4) piece(IC(k + 3), IC(i + 4));
                if constexpr (i == 3) mark(IC(k + 3));
                if constexpr (i >= 4 && k < 12) piece(IC(k + 4), IC(i - 4));
            };
            __builtin_amdgcn_s_setprio(HP);
            TS(7);
            sld(tag, ws + WS_F + F_EP);
            sld(dw, ssmD + (ch & ~1));
            const int sb = idx * 2 + (lw >> 1), kh = lw & 1;
            u32x2 kk[11], vv[8]; u32x4 w4[2], kd[4], ke[4], kvo[4];
            const unsigned ko = g4 * 16384 + l15 * 8, so = sb * 65536 + xcd * 256 + kh * 128;
            auto kv_ld = [&](auto jc) {
                constexpr int j = CV(jc), i = j < 13 ? j - 5 : j - 13;
                if constexpr (j < 2) gld<16 * j>(w4[j], l15 * 32, mk_rsrc(sk, 8192), xcd * 1024 + kh * 512);
                else if constexpr (j < 5) gld<0>(kk[j - 2], ko + so - 6144 + (j - 2) * 2048, mk_rsrc(kin, 4 << 20), 0);
                else if constexpr (j < 13) gld<(i & 1) * 2048>(kk[3 + i], ko, mk_rsrc(kin, 4 << 20), so + (i >> 1) * 4096);
                else gld<(i & 1) * 2048>(vv[i], ko, mk_rsrc(vin, 4 << 20), so + (i >> 1) * 4096);
            };
            {
                static_for<0, KLP>(kv_ld);
                dma1<DSK>(lbase, lane * 4, mk_rsrc(dskip, 2048), dt * 256);
                dma<U0, false>(lbase + kh * 1024, oob, rs0, 0);
            }
            static_for<4, 8>([&](auto ic) { feed(IC(-2), ic); });
            const __amdgpu_buffer_rsrc_t rk = mk_brs(ws + WS_KVT + (size_t)((xcd & 3) * 32 + kh * 16) * 32768, 524288);
            const unsigned K = lbase + KC + lw * KCH;
            unsigned kfo;
            const unsigned n0 = (xcd & 4) << 11;
            auto st = [&](int c) { st16(kvo[c], rk, (l15 >> 2) * 131072 + g4 * 64 + (l15 & 3) * 16, c * 32768 + (sb >> 5) * 16384 + (KVS ? nx : n0 ^ ((sb & 32) << 8)) + (sb & 31) * 256); };
            float kf[7], pa[4], pb[4];
            auto kv_step = [&](auto uc, auto jc) {
                constexpr int u = CV(uc), j = CV(jc), c = u >> 1, h = u & 1;
                auto el = [&](const u32x2& q) -> float { return (c & 1) ? bfhi(q[c >> 1]) : bflo(q[c >> 1]); };
                auto tap = [&](int t) { const unsigned w = w4[c >> 1][(c & 1) * 2 + (t >> 1)]; return (t & 1) ? bfhi(w) : bflo(w); };
                auto all = [&](auto f) {
#pragma unroll
                    for (int i = 0; i < 4; ++i) f(i);
                };
                if constexpr (j == 0) {
#pragma unroll
                    for (int i = 0; i < 7; ++i) { kf[i] = (h && i < 3) ? kf[i + 4] : el(kk[4 * h + i]); tie(kf[i]); }
                }
                if constexpr (EXACT) {
                    if constexpr (j == 1) { const float t = tap(3); all([&](int i) { pa[i] = kf[i] * t; }); all([&](int i) { pa[i] = rbf(pa[i]); tie(pa[i]); }); }
                    if constexpr (j == 2 || j == 4 || j == 6) { const float t = tap(3 - j / 2); all([&](int i) { pb[i] = kf[i + j / 2] * t; }); all([&](int i) { pb[i] = rbf(pb[i]); tie(pb[i]); }); }
                    if constexpr (j == 3 || j == 5 || j == 7) { all([&](int i) { pa[i] += pb[i]; }); all([&](int i) { pa[i] = rbf(pa[i]); tie(pa[i]); }); }
                } else {
                    if constexpr (j == 1) { const float t = tap(3); all([&](int i) { pa[i] = kf[i] * t; tie(pa[i]); }); }
                    if constexpr (j >= 2 && j <= 4) { const float t = tap(4 - j); all([&](int i) { pa[i] = fma_(kf[i + j - 1], t, pa[i]); tie(pa[i]); }); }
                    if constexpr (j == 5) all([&](int i) { pa[i] = rbf(pa[i]); tie(pa[i]); });
                }
                if constexpr (j == (EXACT ? 7 : 6)) all([&](int i) { pb[i] = el(vv[4 * h + i]); tie(pb[i]); });
                if constexpr (j == 7) {
                    all([&](int i) { pa[i] *= pb[i]; });
                    kvo[c][3 - 2 * h] = pack2(pa[1], pa[0]); kvo[c][2 - 2 * h] = pack2(pa[3], pa[2]);
                    if constexpr (h && !KST && u >= kv_first(KSD)) st(c);
                    else if constexpr (u == 7) all(st);
                    else tie(kvo[c]);
                }
            };
            static_for<-1, 13>([&](auto kc) {
                constexpr int k = CV(kc);
                if constexpr (k == PGF && PGI == 0) page<1>();
                if constexpr (k == 2) hop<0>();
                if constexpr (k == 6) hop<6>();
                if constexpr (k == 8) hop<9>();
                if constexpr (k >= 0) barrier();
                if constexpr (k >= 0 && ((CTM >> k) & 1)) ctouch(k);
                if constexpr (k == KV0) {
                    wait_v<KLP == 21 ? 4 + hv_cnt(-1, k) : 9 - KLI - (KLI > 3) + hv_cnt(KLL + 1, k)>();
                    static_for<0, 11>([&](auto ic) { tie(kk[CV(ic)]); });
                    static_for<0, 8>([&](auto ic) { tie(vv[CV(ic)]); });
                    tie(w4[0]); tie(w4[1]); if constexpr (!LC) { keep(c_dt); keep(c_re); keep(c_im); }
                    TS(8);
                }
                if constexpr (k == KVP) {
                    wait_l<0>();
                    ties(tag); ties(dw);
                    tag += 0x01010101u;
                    __builtin_amdgcn_raw_buffer_store_b64(u32x2{tag, ~tag ^ vb}, rf, lane || vw >= 32 ? OOB : 0u, F_C + vw * 8, 16);
                }
                if constexpr (k == KCQ) dma1<POLL>(lbase + lw * 256, lane * 4, rsf, F_C);
                if constexpr (TM == 16 && k == KTI) stouch((unsigned long long)(ws + WS_E) + (TX ? (xcd * 128 + idx * 4 + lw) : b * 4 + lw) * 384);
                if constexpr (TM && TM < 16 && k == KTI) {
                    const unsigned wv = b * 4 + lw, a = lane < 32 ? ((TM & 1) ? wv * 4096 + lane * 128 : (TM & 4) && lane < 4 ? wv * 4096 + lane * 1024 + 896 : OOB) : lane < 35 && (TM & 2) ? (unsigned)(WS_E - WS_PP) + (wv * 3 + lane - 32) * 128 : OOB;
                    if constexpr (TM & 8) dma1<DUMP>(lbase + lw * 256, a, mk_rsrc(ws + WS_PP, WS_F + F_EP - WS_PP), 0);
                    else asm volatile("buffer_atomic_or %0, %1, %2, 0 offen sc1" :: "v"(0u), "v"(a), "s"(mk_rsrc(ws + WS_PP, WS_F + F_EP - WS_PP)) : "memory");
                }
                if constexpr (k == KCC) {
                    unsigned f, n, sp;
                    wait_v<hv_cnt(KCQ, k) - 1>();
                    dsr32<POLL - 131072>(f, lbase + 131072 + lw * 256 + lane * 4);
                    wait_l<0>();
                    vote_chk(f, (lane & 1) ? ~tag : tag, lane & 1, lbase + POLL + lw * 256 + lane * 4, lbase + POLL + lw * 256, lane * 4, rsf, F_C, sp);
                    nr = vote_cnt(f, tag, (xcd & 4) << 11, n);
                    nx = nr ^ ((xcd & 2) << 12);
                    kfo = F_K + (KVS ? nr : n0) + (((xcd & 3) * 2 + (idx >> 4)) * 64 + lw * 16) * 8;
                    TSV(11, n + 100 * (0x10000 - sp));
                }
                if constexpr (k == KFL) {
                    wait_v<hv_cnt(KVL + 1, k)>();
                    __builtin_amdgcn_raw_buffer_store_b64(u32x2{tag, ~tag}, rf, lane ? OOB : 0u, F_K + (KVS ? nr : n0) + (((xcd & 3) * 2 + kh) * 64 + sb) * 8, 16);
                    TSX(10);
                }
                if constexpr (k == 12) {
                    dma1<0>(K, oob, rs0, 0);
                    dma1<KCP>(K, oob, rs0, 0);
                }
                static_for<0, 8>([&](auto ic) {
                    constexpr int i = CV(ic), u0 = kv_first(k), nu = kv_first(k + 1) - u0;
                    static_for<KLP, 21>([&](auto jc) { if constexpr (ld_pos(CV(jc)) == 8 * k + i) kv_ld(jc); });
                    if constexpr (k == KPL && i == KPI) dma1<POLL>(lbase + wid * 256, (lane & 31) * 4, rsf, kfo);
                    if constexpr (k == PGF && PGI && i == PGI) page<1>();
                    feed(kc, ic);
                    if constexpr (k == 4 && i == 4) hop<3>();
                    if constexpr (!KST && k == KSD && (i & 1) && i / 2 < kv_first(KSD) / 2) st(i / 2);
                    if constexpr (nu == 1) kv_step(IC(u0), ic);
                    if constexpr (nu == 2) { kv_step(IC(u0 + i / 4), IC(2 * (i & 3))); kv_step(IC(u0 + i / 4), IC(2 * (i & 3) + 1)); }
                    if constexpr (!KDL && k == 12 && i >= 4) gld<(i - 4) * 1024>(kd[i - 4], lane * 16, mk_rsrc(ssmk, 4 << 20), ch * 4096);
                    if constexpr (k == 11 && i == 4) c_setup();
                    if constexpr (k == 10 && i >= 4) { cad[i - 4] = cst + frag(i & 1, (i >> 1) & 1); tie(cad[i - 4]); }
                    sched();
                });
                if constexpr (SUG == 3) {
                    auto go = [&](auto cc) { constexpr int c = CV(cc); if constexpr (c != 25 && c != 26 && (c < 29 || c > 32)) su(IC(1), cc, lw_, bb_, ln_); };
                    if constexpr (k == SV0) { ties(lw_); ties(bb_); tie(ln_); go(IC(0)); }
                    if constexpr (k >= SV0 && k < SV0 + 2) static_for<k == SV0 ? SU_S : 33, k == SV0 ? 33 : SU_D>(go);
                    if constexpr (k == SK0) { su(IC(1), IC(29), lw_, bb_, ln_); su(IC(1), IC(30), lw_, bb_, ln_); wait_l<0>(); tie(dsw_); }
                    if constexpr (k >= SK0 && k < SK1) static_for<1 + (k - SK0) * (SU_S - 1) / (SK1 - SK0), 1 + (k + 1 - SK0) * (SU_S - 1) / (SK1 - SK0)>(go);
                    sched();
                } else
                if constexpr (SUG && k >= SK0 && k < SK1) {
                    constexpr int nn = SUG == 1 ? SU_S : SU_D;
                    if constexpr (k == SK0) { ties(lw_); ties(bb_); tie(ln_); }
                    static_for<(k - SK0) * nn / (SK1 - SK0), (k + 1 - SK0) * nn / (SK1 - SK0)>([&](auto cc) {
                        constexpr int c = CV(cc);
                        if constexpr (c != 25 && c != 26 && c != 31 && c != 32) su(IC(1), cc, lw_, bb_, ln_);
                        if constexpr (c == 30) { wait_l<0>(); tie(dsw_); }
                    });
                    sched();
                }
            });
            page<12>();
            TS(1);
            if constexpr (EB == 2) barrier();
            unsigned pf;
            const unsigned fr = lbase + FULL + (lane & 3) * FST;
            wait_v<hv_cnt(KPL, 12) - (KPL < 12 ? 1 : 2) - (KPL == KFL) - KPI - (KPI > 3) + (KDL ? 7 : 11)>();
            dsr32<POLL - 131072>(pf, lbase + 131072 + wid * 256 + lane * 4);
            wait_l<0>();
            tag_chk<KSL>(pf, (lane & 1) ? ~tag : tag, (lane & 31) * 4, rsf, kfo);
            if constexpr (KDL) static_for<0, 4>([&](auto ic) { gld<CV(ic) * 1024>(kd[CV(ic)], lane * 16, mk_rsrc(ssmk, 4 << 20), ch * 4096); });
            auto b13 = [&]() { if constexpr (NB == 2) { unsigned f = 1; zero_chk<12, 1>(f, lbase + DONE + (lane & 3) * FST); } else if constexpr (NB < 2) barrier(); };
            if constexpr (EB == 1) b13();
            {
                const u32x4 rs = mk_rsrc(ws + WS_KVT + (size_t)((xcd & 3) * 32 + idx) * 32768, 32768);
                static_for<0, 4>([&](auto ic) { constexpr int i = CV(ic); dma<U0 + 2048 + i * 1024, true>(lbase + lw * 4096, (lane >> 2) * 64 + (((lane & 3) ^ g4) << 4), rs, (lw >> 1) * 16384 + (KVS ? nx : n0 ^ ((lw & 2) << 12)) + (lw & 1) * 4096 + i * 1024); });
            }
            mark(IC(16));
            auto warm = [&]() {
                const unsigned wo = g4 * 2048 + l15 * 16, dl = lbase + lw * 1024;
                const unsigned qo = (512 * (idx >> 3) + 128 * (idx & 1) + 32 * lw + 4 * ((idx >> 1) & 3)) * 2048 + xcd * 256, xo = (tm2 * 128 + 32 * lw + 8 * (qx & 2)) * 2048 + dt * 256;
                static_for<0, 4>([&](auto ic) { constexpr int i = CV(ic); dma<DUMP, false>(dl, wo, mk_rsrc(qin, 4 << 20), qo + ((i & 1) * 16 + (i >> 1) * 256) * 2048); });
                static_for<0, 4>([&](auto ic) { constexpr int i = CV(ic); dma<DUMP, false>(dl, wo, mk_rsrc(x, 4 << 20), xo + i * 8192); });
            };
            if constexpr (PFQ == 1) warm();
            if constexpr (EB == 0) b13();
            auto c_first = [&]() {
                if constexpr (CL <= 1) c_unit(IC(0));
                if constexpr (CL <= 0) c_unit(IC(1));
                if constexpr (EC == 1) static_for<0, 4>([&](auto ic) { constexpr int i = CV(ic); dma4<DUMP, i>(lbase, vc[i], mk_rsrc(cre + (size_t)dt * 131072, 262144), c_off(lw, 0)); });
                if constexpr (PFQ == 2) warm();
            };
            static_assert(NB != 3 || CL == 2, "");
            if constexpr (!KO) c_first();
            wait_v<5 + 8 * (PFQ == 1) + (KO ? 0 : 4 * (CL <= 1) + 4 * (CL <= 0) + XV)>();
            static_for<0, 4>([&](auto ic) { tie(kd[CV(ic)]); });
#pragma unroll
            for (int n = 0; n < 4; ++n) {
                const unsigned first = n ? __builtin_amdgcn_readlane(kd[n ? n - 1 : 0][3], 63) : 0u;
                const unsigned p = __builtin_amdgcn_update_dpp(first, kd[n][3], 0x138, 0xf, 0xf, false);
                ke[n] = u32x4{__builtin_amdgcn_alignbyte(kd[n][0], p, 2), __builtin_amdgcn_alignbyte(kd[n][1], kd[n][0], 2),
                              __builtin_amdgcn_alignbyte(kd[n][2], kd[n][1], 2), __builtin_amdgcn_alignbyte(kd[n][3], kd[n][2], 2)};
                tie(ke[n]);
            }
            if constexpr (ZM) {
                const unsigned dl = (ch & 1) ? dw >> 16 : dw & 0xffffu, dv = dl << ((~lane & 1) * 16);
                static_for<0, 4>([&](auto ic) { constexpr unsigned long long k = lag0_mask(CV(ic)); ad[CV(ic)] = sel0(dv, k); });
                tie(ad);
            }
            hop<14>();
            static_for<0, 4>([&](auto ic) { constexpr int i = CV(ic); dsw128<64 + i * 1024>(K + lane * 16, kd[i]); dsw128<KCP + 64 + i * 1024>(K + lane * 16, ke[i]); });
            if constexpr (KO) c_first();
            TS(9);
            static_for<0, -CL>([&](auto uc) {
                constexpr int u = CV(uc);
                wait_v<4 + (u < 2 ? XV : 0)>();
                static_for<0, 4>([&](auto ic) { constexpr int i = CV(ic); dsr128<(u & 1) * 4096>(cf[u * 4 + i], cad[i]); });
                wait_l<0>();
                static_for<0, 4>([&](auto ic) { tie(cf[u * 4 + CV(ic)]); });
                c_unit(IC(u + 2));
            });
            pf = 1;
            zero_chk<15 * 4>(pf, fr);
            if constexpr (PRE) { pf = 1; zero_chk<PRE == 1 ? 8 : 4>(pf, lbase + DONE + (lane & 3) * FST); }
            __builtin_amdgcn_s_setprio(0);
        }
        TS(2);
        TL(1);
        unsigned ka[4], ub[4];
#pragma unroll
        for (int i = 0; i < 4; ++i) {
            ka[i] = lbase + KC + lw * KCH + ((l15 & 1) ? 0 : KCP) + 84 + 2 * (l15 & 14) - 16 * g4 + i * 1024;
            const int e = 2 * l15 + g4 + 2;
            ub[i] = lbase + U0 + 64 * e + ((lw ^ (((e >> 2) + 3 - i) & 3)) << 4);
        }
        unsigned gad = lbase + DONE + lw * FST;
        if constexpr (GQ >= 0) tie(gad);
        f32x4 acc[8];
        bf16x4 FA[8][2]; bf16x8 FB[8];
        auto op = [&](auto gc) {
            constexpr Op o = cv_op(CV(gc));
            if constexpr (o.kind == CO_A) { constexpr int s = o.a >> 3, n = o.a & 7, d = (n & 1) * 128 + s * 16 + o.b * 2; dsr2<d, d + 1>(FA[n][o.b], ka[n >> 1]); }
            if constexpr (o.kind == CO_B) { constexpr int s = o.a >> 3, m = o.a & 7; dsr128<2048 * m + 1792 - 256 * s>(FB[m], ub[s & 3]); }
            if constexpr (o.kind == CO_C) dsr128<(o.a & 1) * 4096>(cf[o.a * 4 + o.b], cad[o.b]);
        };
        const __amdgpu_buffer_rsrc_t rz = mk_brs(ws + WS_ZT, 4 << 20);
        auto zst = [&](int jj) {
            tie(acc[2 * jj]); tie(acc[2 * jj + 1]);
            const f32x4 a = acc[2 * jj], c = acc[2 * jj + 1];
            st16(u32x4{pack2(a[0], a[1]), pack2(a[2], a[3]), pack2(c[0], c[1]), pack2(c[2], c[3])}, rz, lane * 16 + jj * 1024, (ch & 510) * 8192 + nx + (ch & 1) * 4096);
        };
        static_assert(!CT || (ZM && CL == 1 && cv_n(14) == 1 && cv_m(14) == 0 && cv_n(15) == 2 && cv_n(25) == 3 && cv_m(25) == 0 && cv_n(26) == 4), "");
        static_for<-36, 0>(op);
        static_for<0, 288>([&](auto gc) {
            constexpr int G = CV(gc), s = G / 36, q = G % 36, n = cv_n(q), m = cv_m(q);
            if constexpr (cv_wait(G) >= 0) wait_lr<cv_wait(G)>();
            if constexpr (q == 24 && s >= CL && s - CL < 8) static_for<0, 4>([&](auto ic) { tie(cf[(s - CL) * 4 + CV(ic)]); });
            if constexpr (ZM && G < 8) mfma<true>(acc[m], __builtin_bit_cast(bf16x8, ad), FB[m]);
            mfma<s == 0 && n == 0 && !ZM>(acc[n + m], cat(FA[n][0], FA[n][1]), FB[m]);
            sched();
            if constexpr (q == 11) TL(2 + 2 * s);
            if constexpr (q == 11 && s >= CL && s - CL < 8) wait_v<(s - CL < 7 ? 4 : 0) + (s - CL < (CL <= 1) + (CL <= 0) ? XV : 0) + 8 * (PFQ >= 3 && (s == PFQ - 2 || s == PFQ - 1))>();
            if constexpr (q == 11) TL(3 + 2 * s);
            op(gc);
            if constexpr (q == 30 && ((CTM >> (16 + s)) & 1)) ctouch(16 + s);
            if constexpr (!SUG && G == SU0) { ties(lw_); ties(bb_); }
            if constexpr (!SUG && G >= SU0 && G < SU0 + 2 * SU_S && !((G - SU0) & 1)) su(IC(1), IC((G - SU0) / 2), lw_, bb_, ln_);
            if constexpr (SUG < 2 && G == 259) tie(ln_);
            if constexpr (SUG < 2 && G >= 259 && G < 259 + SU_N - SU_S) su(IC(1), IC(G - 259 + SU_S), lw_, bb_, ln_);
            if constexpr (SUG >= 2 && G >= 270 && G < 288 && !(G & 1)) { constexpr int j = (G - 270) / 2; su(IC(1), IC(j < 2 ? 25 + j : j < 4 ? 29 + j : SU_D - 4 + j), lw_, bb_, ln_); }
            if constexpr (CT && s == 7 && (q == 17 || q == 29)) zst(q / 10 - 1);
            if constexpr (CT && s == 7 && q == 31) { wait_v<2>(); static_for<28, 32>([&](auto ic) { constexpr int i = CV(ic); dsr128<4096>(cf[i], cad[i & 3]); }); }
            if constexpr (G == 16) hop<17>();
            if constexpr (G == 66) hop<19>();
            if constexpr (G == PG1) page<21>();
            if constexpr (G == PG2) page<28>();
            if constexpr (G == 158) hop<23>();
            if constexpr (G == 209) hop<25>();
            if constexpr (G == 254) hop<27>();
            if constexpr (G == GQ) { unsigned f = 1; zero_chk<4, 1>(f, gad); }
            if constexpr (!NB && G == CBQ) barrier();
            if constexpr (NB == 3 && G == 23) { unsigned f = 1; zero_chk<8, 1>(f, lbase + DONE + (lane & 3) * FST); }
            if constexpr ((q == 24 || q == 25 || q == 29 || q == 34) && s + 2 - CL < 8) {
                constexpr int pc = q == 24 ? 0 : q == 25 ? 1 : q == 29 ? 2 : 3, u = s + 2 - CL;
                dma4<(u & 1) * 4096, pc>(cst, vc[pc], rsc, c_off(lw, u));
            }
            sched();
        });
        if constexpr (PG2 >= 288) page<28>();
        TL(18);
        TSX(3);
        {
            if constexpr (ZM) {
                asm volatile("s_nop 7");
#pragma unroll
                for (int jj = CT ? 2 : 0; jj < 4; ++jj) zst(jj);
            } else {
            u32x2 kvr[8];
            const unsigned kva = lbase + U0 + 2048 + 128 * l15 + 64 * (g4 >> 1) + ((lw ^ ((l15 >> 1) & 3)) << 4) + 8 * (1 - (g4 & 1));
            static_for<0, 8>([&](auto ic) { dsr64<CV(ic) * 2048>(kvr[CV(ic)], kva); });
            wait_l<0>();
            const float Dd = (ch & 1) ? bfhi(dw) : bflo(dw);
#pragma unroll
            for (int jj = 0; jj < 4; ++jj) {
                u32x4 o;
#pragma unroll
                for (int u = 0; u < 2; ++u) {
                    const int j = 2 * jj + u;
                    tie(kvr[j]); tie(acc[j]);
                    const float kv3 = bflo(kvr[j][0]), kv2 = bfhi(kvr[j][0]), kv1 = bflo(kvr[j][1]), kv0 = bfhi(kvr[j][1]);
                    o[2 * u] = pack2(rbf(acc[j][0]) + rbf(kv0 * Dd), rbf(acc[j][1]) + rbf(kv1 * Dd));
                    o[2 * u + 1] = pack2(rbf(acc[j][2]) + rbf(kv2 * Dd), rbf(acc[j][3]) + rbf(kv3 * Dd));
                }
                st16(o, rz, lane * 16 + jj * 1024, (ch & 510) * 8192 + nx + (ch & 1) * 4096);
                if (jj == 1) { sched(); hop<29>(); sched(); }
            }
            }
            TS(8);
            hop<30>();
            if constexpr (CL > 0) {
                if constexpr (!CT) {
                    wait_v<4>();
                    static_for<32 - 4 * CL, 32>([&](auto ic) { constexpr int i = CV(ic); dsr128<((i >> 2) & 1) * 4096>(cf[i], cad[i & 3]); });
                }
                wait_l<0>();
                static_for<32 - 4 * CL, 32>([&](auto ic) { tie(cf[CV(ic)]); });
            }
        }
        TL(19);
        TSX(4);
    }
    {
        unsigned pf, fso, xso;
        const int wid = fresh(wid_);
        static_assert(!PB && PK && KVS && P_W < -1, "");
        unsigned& a_p = l16; unsigned& a_w = l16;
        auto fill = [&](auto sc, auto pc) {
            constexpr int s = CV(sc), p = CV(pc);
            if constexpr (p == 0) dma_x<(2 * s % 3) * BUF, (s >> 1) * 16384 + (s & 1) * 262144, true>(fso, wl, l16, rws, hs0);
            else if constexpr (fill_m0(4 * s + p)) dma_m<(2 * s % 3) * BUF, p * 1024>(fso, wl, l16, rws);
            else dma_n<p * 1024>(fso, l16, rws);
        };
        bf16x4 hf[2][8];
        auto ldh = [&](auto nc, auto ic) {
            constexpr int n = CV(nc), i = CV(ic), j = (i >> 1) & 1, bo = (2 * (n >> 2) % 3) * BUF + (g2_unit(n, j) & 3) * 8192 + (i >> 2) * 4096, hi = bo >= 65536 ? 65536 : 0;
            dsr64tr<bo - hi>(hf[n & 1][i], ha[2 * g2_tile(n, j) + (i & 1) + (hi >> 14)]);
        };
        f32x4 yc[2][2][2];
        f32x4 im[2], ao, ob[2]; u32x4 pl[2], po;
        bf16x8 xf[2];
        u32x2 zv[4], qv[2];
        unsigned ck, ex, fail[2];
        float tq_[2], tz, sm[8], mx[2], cv[8];
        auto finB = [&](auto pc, auto jc) {
            constexpr int p = CV(pc), j = CV(jc);
            if constexpr (j < 8) { yc[p & 1][0][j & 1][j >> 1] += im[j & 1][j >> 1]; tie(yc[p & 1][0][j & 1]); }
            else {
                constexpr int i = (j - 8) >> 2, k = j & 3, dd = k & 1, h = k >> 1;
                ob[i][k] = sel8(yc[p & 1][0][dd][2 * i + 1 - h], yc[p & 1][0][dd][2 * i + h], h ? hi8 : lo8);
                if constexpr (k == 3 && (p == 3 || !(NOST & 4))) gst<0>(ob[i], a_ob, rso, s_b ^ ((p & 1) * 65536 + i * 16384));
                if constexpr (j == 12) keep(ob[0]);
            }
        };
        auto snd = [&](auto pc, auto jc) {
            constexpr int p = CV(pc), j = CV(jc);
            if constexpr (j < 8) { sm[j] = yc[p][0][j >> 2][j & 3] + im[j >> 2][j & 3]; tie(sm[j]); }
            if constexpr (j == 8) mx[0] = amax3(sm[0], sm[1], sm[2]);
            if constexpr (j == 9) mx[1] = amax3(sm[3], sm[4], sm[5]);
            if constexpr (j == 10) mx[0] = amax3(mx[0], sm[6], sm[7]);
            if constexpr (j == 11) mx[0] = amax3(mx[0], mx[1], mx[1]);
            if constexpr (j == 12) { ex = f2u(mx[0]) + 0x4000u; tie(ex); }
            if constexpr (j == 13) { ex &= 0x7f800000u; tie(ex); }
            if constexpr (j == 14) { mx[0] = u2f(ex + 0x04c00008u); tie(mx[0]); }
            if constexpr (j == 15) { ex >>= 23; tie(ex); }
            if constexpr (j >= 16 && j < 24) { sm[j - 16] += mx[0]; tie(sm[j - 16]); }
            if constexpr (j == 24) { ex |= ex << 12; tie(ex); }
            if constexpr (j >= 25 && j < 29) { po[j - 25] = __builtin_amdgcn_perm(f2u(sm[2 * (j - 25) + 1]), f2u(sm[2 * (j - 25)]), psel); tie(po); }
            if constexpr (j == 29) { po[0] = bfi_v(msk, po[0], ex); tie(po); }
            if constexpr (j >= 30 && j < NS) { po[j - 29] = bfi_s(msk, po[j - 29], tn_[j - 30]); if constexpr (j < NS - 1) tie(po); }
            if constexpr (j == NS - 1 && !(NOST & 2)) gst<p * 1024>(po, l16, rws, ppo);
        };
        auto rcv = [&](auto hc, auto jc) {
            constexpr int h = CV(hc), j = CV(jc);
            if constexpr (j < 3) { pl[h][j + 1] ^= tn_[j]; pp_tie<h>(pl[h]); }
            if constexpr (j == 3) { ck = pl[h][1] | pl[h][2] | pl[h][3]; tie(ck); }
            if constexpr (j == 4) { if constexpr (PB) ck = bfi_f(msk, fail[h], ck); else ck &= 0xf000fu; tie(ck); }
            if constexpr (j == 6 && !(NOST & 2)) pp_chk<h>(pl[h], ck, tagg, l16, rws, ppi, a_p);
            if constexpr (j == 7) { ex = pl[h][0] >> 12; tie(ex); }
            if constexpr (j == 8) ex = bfi_v(msk, ex, pl[h][0]);
            if constexpr (j == 9) { pl[h][0] &= msk; pp_tie<h>(pl[h]); }
            if constexpr (j == 10) { ex <<= 23; tie(ex); }
            if constexpr (j == 11) { ex &= 0x7f800000u; tie(ex); }
            if constexpr (j == 12) { mx[1] = u2f(ex) * 0x1p-14f; tie(mx[1]); }
            if constexpr (h == 0 && j >= 13) {
                constexpr int k = j - 13, a = (k + 1) / 2, e = k == 15 ? 7 : k / 2 - 1;
                if constexpr (k == 0 || ((k & 1) && k < 15)) cv[a] = cvt16<a & 1>(pl[h][a >> 1]);
                else { yc[0][0][e >> 2][e & 3] = fma_(cv[e], mx[1], yc[0][0][e >> 2][e & 3]); tie(yc[0][0][e >> 2]); }
            }
            if constexpr (h == 1 && j >= 13 && j < 21) cv[j - 13] = cvt16<(j - 13) & 1>(pl[h][(j - 13) >> 1]);
            if constexpr (h == 1 && j >= 21) { constexpr int e = j - 21; im[e >> 2][e & 3] = fma_(cv[e], mx[1], im[e >> 2][e & 3]); tie(im[e >> 2]); }
        };
        auto side = [&](auto pc) {
            constexpr int P = CV(pc), n = P >> 3, m = P & 7, p = n >> 3;
            if constexpr (P == P_X || P == P_X + 2) dma_x<ZST + (P != P_X) * 1024, (P != P_X) * 16384, false>(xso, w2k, a_x, rsx, s_x);
            if constexpr (P == P_W) pf_ld(pf, a_w, rws, ppo);
            if constexpr (!NOA) {
            if constexpr (P == P_Q || P == P_Q + 2) dma_x<QST + (P != P_Q) * 1024, (P != P_Q) * 524288, false>(xso, w2k, a_q, rsq, s_q);
            if constexpr (P == P_T) gst<0>(tg2, a_t, rws, zfo);
            if constexpr (P == AT(ZP, 4)) { if constexpr (P_W >= 0) pf_keep(pf); pf_ld(pf, a_f, rws, zfo); }
            if constexpr (P == P_ZC) { wait_v<g2_allow(P, AT(ZP, 4))>(); tag_chk(pf, expect, a_f, rws, zfo); }
            if constexpr (P == P_Z || P == P_Z + 2) dma_x<ZST + (P != P_Z) * 1024, (P != P_Z) * 65536, true>(xso, w2k, P == P_Z ? a_z : a_z2, rws, s_z);
            }
            if constexpr (P == AT(7, 5) || P == AT(7, 7)) dsr128<(m - 5) * 512>(xf[(m - 5) / 2], a_xr);
            if constexpr (P == AT(8, 4)) { tie(xf[0]); tie(xf[1]); }
            if constexpr (P == AT(8, 5) || P == AT(9, 3)) static_for<0, 2>([&](auto dc) { mfma<false>(yc[n & 1][0][CV(dc)], xf[n & 1], __builtin_bit_cast(bf16x8, dg[CV(dc)])); });
            if constexpr ((n & 7) == 6 && m == XW && !(DBG & 2)) dsw128<0>(a_iw, yc[p & 1][1][0]);
            if constexpr ((n & 7) == 6 && m == XW2 && !(DBG & 2)) dsw128<1024>(a_iw, yc[p & 1][1][1]);
            if constexpr ((n & 7) == 7 && (m & 5) == (n < 31 ? 4 : 0)) dsr128<(m & 2) * 512>(im[(m >> 1) & 1], a_ir);
            if constexpr (n >= 8 && (n & 7) == 0 && m == 4) { tie(im[0]); tie(im[1]); }
            if constexpr (P >= P_PP0 && P <= E_PP0) snd(IC(0), IC(P - P_PP0));
            if constexpr (P >= P_PP1 && P <= E_PP1) static_for<0, D1>([&](auto ic) { snd(IC(1), IC((P - P_PP1) * D1 + CV(ic))); });
            if constexpr (PB && (P == P_B0 - 1 || P == P_B1 - 1)) pf_zero(pf);
            if constexpr (PB && (P == P_B0 || P == P_B1)) pf_at<(P == P_B1) * 1024>(pf, a_p, rws, ppi);
            if constexpr (P == P_L0 || P == P_L1) {
                unsigned so = ppi;
                if constexpr (PB) { wait_v<g2_allow(P, P - QB) + (g2_fill(P) >= 0)>(); pp_sel(so, fail[P == P_L1], pf, tn_[2], ppi, ppo); }
                pp_ld<P == P_L1>(pl[P == P_L1], l16, rws, so);
            }
            if constexpr (P == P_R0 || P == P_R1) { wait_v<g2_allow(P, P == P_R0 ? imax(P_L0, XL ? P_X + 2 : 0) : P_L1)>(); pp_tie<P == P_R1>(pl[P == P_R1]); }
            if constexpr (P >= P_R0 && P <= E_R0) static_for<0, D0>([&](auto ic) { if constexpr ((P - P_R0) * D0 + CV(ic) < NR) rcv(IC(0), IC((P - P_R0) * D0 + CV(ic))); });
            if constexpr (P >= P_R1 && P < P_R1 + 21) rcv(IC(1), IC(P - P_R1));
            if constexpr (!NOA) {
            if constexpr (n == ZW && m >= 4) dsr64<(m - 4) * 128>(zv[m - 4], a_zr);
            static_for<0, 4>([&](auto rc) {
                constexpr int r = CV(rc);
                if constexpr (P == q_tie(r)) tie(qv[r & 1]);
                if constexpr (P == q_rd(r)) dsr64<r * 256>(qv[r & 1], a_qr);
            });
            if constexpr (P == AT(ZW + 1, 4)) { tie(zv[0]); tie(zv[1]); tie(zv[2]); tie(zv[3]); }
            }
            if constexpr (a_step(P) >= 0) {
                constexpr int s = a_step(P), e = s == 0 ? 0 : s % 3 == 2 ? (s + 1) / 3 : s % 3 == 1 ? s / 3 : s / 3 - 1, r = e >> 2, c = e & 3;
                if constexpr (s == 0 || (s % 3 == 2 && e < 16)) { tq_[e & 1] = (c & 1) ? bfhi(qv[r & 1][c >> 1]) : bflo(qv[r & 1][c >> 1]); tie(tq_[e & 1]); }
                else if constexpr (s % 3 == 1) { tz = (r & 1) ? bfhi(zv[c][r >> 1]) : bflo(zv[c][r >> 1]); tie(tz); }
                else if constexpr (s % 3 == 0) {
                    ao[c] = tq_[e & 1] * tz;
                    if constexpr (c == 3 && !(NOST & 1)) gst<0>(ao, a_oa, rso, s_a + r * 8192);
                    else tie(ao);
                }
            }
            if constexpr (P >= P_F2 && P < P_F2 + 16) finB(IC(2), IC(P - P_F2));
            if constexpr (a_step(P - 1) > 0 && a_step(P - 1) % 12 == 0) keep(ao);
            if constexpr (P == E_PP0 + 1 || P == E_PP1 + 1) keep(po);
            if constexpr (P == P_F2 + 16) keep(ob[1]);
        };
        wait_v<8>();
        if (wid < 4) TS(7);
        TG_DECL
        barrier();
        static_for<0, 8>([&](auto ic) { ldh(IC(0), ic); });
        static_for<0, 32>([&](auto nc) {
            constexpr int n = CV(nc), p = n >> 3;
            if constexpr (n == PG3) page<33>();
            if constexpr (n == PG4) page<37>();
            wait_lr<(n & 3) == 3 ? 0 : 4 + g2_lx(n - 1)>();
            if constexpr (n == 31) barrier();
            if constexpr ((n & 3) == 3 && n < 31) {
                constexpr int need = g2_need(n);
                TL(21 + n / 4 * 3);
                wait_v<g2_allow(8 * n, need)>();
                TL(22 + n / 4 * 3);
                barrier();
                TL(23 + n / 4 * 3);
            }
            if constexpr (n == 1) hop<31>();
            if constexpr (n == 4) hop<32>();
            if constexpr (n == 11) hop<34>();
            if constexpr (n == 15) hop<35>();
            if constexpr (n == 19) hop<36>();
            if constexpr (n == 27) hop<38>();
            if constexpr (n == 31) hop<39>();
            if constexpr (n == 0) TL(20);
            TG(20 + n);
            if constexpr (n == 31) TL(42);
            if constexpr (PRG) __builtin_amdgcn_s_setprio(n == 31 ? 0 : 3 - ((n + 1) & 3));
            sched();
            static_for<0, 8>([&](auto mc) {
                constexpr int m = CV(mc), ks = m >> 2, tt = (m >> 1) & 1, dd = m & 1, P = n * 8 + m;
                if constexpr (m == 4 && (n & 3) != 3) wait_lr<(rd1(n) ? 8 : 4) + ((n & 7) == 6 ? (XW < 4) + (XW2 < 4) : 0)>();
                mfma<(n & 7) == 0 && ks == 0>(yc[p & 1][(DBG & 1) ? tt : g2_tile(n, tt)][dd], cat(hf[n & 1][4 * ks + 2 * tt], hf[n & 1][4 * ks + 2 * tt + 1]), cf[g2_unit(n, tt) * 4 + 2 * ks + dd]);
                sched();
                if constexpr (n < 31 && rd1(n) && m < 4) { ldh(IC(n + 1), IC(2 * m)); ldh(IC(n + 1), IC(2 * m + 1)); }
                if constexpr (n < 31 && !rd1(n)) ldh(IC(n + 1), mc);
                if constexpr (g2_fill(P) >= 0) fill(IC(g2_fill(P) >> 2), IC(g2_fill(P) & 3));
                side(IC(P));
                sched();
            });
        });
        TG(52);
        if (wid >= 4) TSX(5); else TS(8);
        TG(53);
        static_for<256 - P_R1, 21>([&](auto jc) { rcv(IC(1), jc); });
        wait_l<0>();
        tie(im[0]); tie(im[1]);
        static_for<21, NR>([&](auto jc) { rcv(IC(1), jc); });
        asm volatile("s_nop 3");
        sched();
        static_for<0, 16>([&](auto jc) { finB(IC(3), jc); });
        if (lastw == 0) gst<0>(u32x2{tagg, tagg}, l16 ? OOB : 0u, rws, WS_F + F_EP);
    }
}

}

#if defined(__HIP_DEVICE_COMPILE__)
#pragma clang attribute pop
#endif

extern "C" void kernel_launch(void* const* d_in, const int* in_sizes, int n_in, void* d_out, int out_size, void* d_ws, size_t ws_size,
                              hipStream_t stream) {
    (void)in_sizes; (void)n_in; (void)out_size; (void)ws_size;
    using namespace fk;
    const uint16_t* q = (const uint16_t*)d_in[0];
    const uint16_t* kin = (const uint16_t*)d_in[1];
    const uint16_t* vin = (const uint16_t*)d_in[2];
    const uint16_t* sk = (const uint16_t*)d_in[3];
    const uint16_t* ssmk = (const uint16_t*)d_in[4];
    const uint16_t* ssmD = (const uint16_t*)d_in[5];
    const uint16_t* x = (const uint16_t*)d_in[6];
    const uint16_t* lre = (const uint16_t*)d_in[7];
    const uint16_t* lim = (const uint16_t*)d_in[8];
    const uint16_t* bre = (const uint16_t*)d_in[9];
    const uint16_t* bim = (const uint16_t*)d_in[10];
    const uint16_t* cre = (const uint16_t*)d_in[11];
    const uint16_t* cim = (const uint16_t*)d_in[12];
    const uint16_t* logdt = (const uint16_t*)d_in[13];
    const uint16_t* dskip = (const uint16_t*)d_in[14];
#define WOFF 0
    char* ws = (char*)(((uintptr_t)d_ws + ((size_t)WOFF << 20) + 0x1fffff) & ~(uintptr_t)0x1fffff);
    hipLaunchKernelGGL(k1, dim3(256), dim3(512), 0, stream, x, bre, bim, lre, lim, logdt, ws, cre, cim, dskip, ssmk, ssmD, q, kin, vin, sk, (float*)d_out TS_PASS(0));
}
```
